```python
import torch

def _softmax(x, axis=-1):
    return torch.softmax(x, dim=axis)
def _gelu(x):
    return torch.nn.functional.gelu(x, approximate="tanh")
def _silu(x):
    return torch.nn.functional.silu(x)
def _sigmoid(x):
    return torch.sigmoid(x)
def _layernorm(x, axis=-1, eps=1e-5):
    m = x.mean(dim=axis, keepdim=True)
    v = x.var(dim=axis, keepdim=True, unbiased=False)
    return (x - m) / torch.sqrt(v + eps)
def _scatter_add(tgt, idx, src, axis=0):
    return tgt.clone().index_add_(axis, idx, src)
def _segment_sum(src, seg, n):
    out = torch.zeros((n,) + src.shape[1:], dtype=src.dtype)
    return out.index_add_(0, seg, src)
def _pad(x, pw):
    flat = tuple(v for pair in reversed(pw) for v in pair)
    return torch.nn.functional.pad(x, flat)

kernel_name = 'standalone__gqa_softmax__near_ridge'

def setup_inputs():

    _w = (lambda a: (lambda t: t.to(torch.bfloat16) if t.is_floating_point() else t)(torch.as_tensor(a)))
    B, Hq, Hkv, Sq, Sk, D = 8, 32, 32, 336, 2048, 128
    return {
        "q": _w((lambda *s: torch.randn(*s, dtype=torch.bfloat16) * 0.8)(B, Hq, Sq, D)),
        "k": _w((lambda *s: torch.randn(*s, dtype=torch.bfloat16) * 0.8)(B, Hkv, Sk, D)),
        "v": _w((lambda *s: torch.randn(*s, dtype=torch.bfloat16) * 0.05)(B, Hkv, Sk, D)),
    }

def reference(q, k, v):
    B, Hq, Sq, D = q.shape
    Hkv = k.shape[1]
    rep = Hq // Hkv
    k = (lambda x, n, axis: torch.repeat_interleave(x, n, dim=axis))(k, rep, axis=1)
    v = (lambda x, n, axis: torch.repeat_interleave(x, n, dim=axis))(v, rep, axis=1)
    scale = D ** -0.5
    scores = torch.einsum("bhqd,bhkd->bhqk", q, k) * scale
    p = _softmax(scores, axis=-1)
    return torch.einsum("bhqk,bhkd->bhqd", p, v)
```

```cpp
#include <hip/hip_runtime.h>
#include <hip/hip_bf16.h>
typedef short  s16x8 __attribute__((ext_vector_type(8)));
typedef short  s16x4 __attribute__((ext_vector_type(4)));
typedef __bf16 bf16x8 __attribute__((ext_vector_type(8)));
typedef __bf16 bf16x4 __attribute__((ext_vector_type(4)));
typedef __bf16 bf16x2 __attribute__((ext_vector_type(2)));
typedef float  f32x4 __attribute__((ext_vector_type(4)));
typedef float  f32x2 __attribute__((ext_vector_type(2)));
typedef unsigned u32x4 __attribute__((ext_vector_type(4)));
typedef unsigned u32x2 __attribute__((ext_vector_type(2)));
constexpr int SQ = 336, SK = 2048, D = 128;
__device__ __forceinline__ unsigned cvtpk(float lo, float hi) { f32x2 v = {lo, hi}; bf16x2 b = __builtin_convertvector(v, bf16x2); return __builtin_bit_cast(unsigned, b); }

__device__ __forceinline__ void safe_attn(const unsigned short* __restrict__ Q, const unsigned short* __restrict__ K, const unsigned short* __restrict__ V,
                          unsigned short* __restrict__ O, char* lds) {
  const int tid = threadIdx.x, lane = tid & 63, w = tid >> 6, i16 = lane & 15, g = lane >> 4;
  const float C = 0.12751743082459868f;
  char* Ks = lds; char* Vs = lds + 32 * 272;
  for (int round = 0; round < 6; ++round) {
    int tile = 4 * round + w; const bool live = tile < 21; if (!live) tile = 20;
    bf16x8 qf[4];
#pragma unroll
    for (int ds = 0; ds < 4; ++ds) qf[ds] = *(const bf16x8*)(Q + (size_t)(tile * 16 + i16) * D + 32 * ds + 8 * g);
    f32x4 o[8];
#pragma unroll
    for (int db = 0; db < 8; ++db) o[db] = f32x4{0.f, 0.f, 0.f, 0.f};
    float m = -INFINITY, l = 0.f;
    for (int k0 = 0; k0 < SK; k0 += 32) {
      __syncthreads();
#pragma unroll
      for (int it = 0; it < 2; ++it) {
        const int c = tid + 256 * it, row = c >> 4, ch = c & 15;
        *(u32x4*)(Ks + row * 272 + ch * 16) = *(const u32x4*)(K + (size_t)(k0 + row) * D + ch * 8);
        *(u32x4*)(Vs + row * 272 + ch * 16) = *(const u32x4*)(V + (size_t)(k0 + row) * D + ch * 8);
      }
      __syncthreads();
      f32x4 s[2];
#pragma unroll
      for (int st = 0; st < 2; ++st) {
        s[st] = f32x4{0.f, 0.f, 0.f, 0.f};
#pragma unroll
        for (int ds = 0; ds < 4; ++ds) {
          const bf16x8 kf = *(const bf16x8*)(Ks + (16 * st + i16) * 272 + 64 * ds + 16 * g);
          s[st] = __builtin_amdgcn_mfma_f32_16x16x32_bf16(kf, qf[ds], s[st], 0, 0, 0);
        }
      }
      float mx = -INFINITY;
#pragma unroll
      for (int st = 0; st < 2; ++st)
#pragma unroll
        for (int r = 0; r < 4; ++r) { s[st][r] *= C; mx = fmaxf(mx, s[st][r]); }
      mx = fmaxf(mx, __shfl_xor(mx, 16)); mx = fmaxf(mx, __shfl_xor(mx, 32));
      const float mn = fmaxf(m, mx), alpha = __builtin_amdgcn_exp2f(m - mn); m = mn;
      float ps = 0.f;
#pragma unroll
      for (int st = 0; st < 2; ++st)
#pragma unroll
        for (int r = 0; r < 4; ++r) { s[st][r] = __builtin_amdgcn_exp2f(s[st][r] - mn); ps += s[st][r]; }
      l = l * alpha + ps;
      u32x4 pw = {cvtpk(s[0][0], s[0][1]), cvtpk(s[0][2], s[0][3]), cvtpk(s[1][0], s[1][1]), cvtpk(s[1][2], s[1][3])};
      const bf16x8 pf = __builtin_bit_cast(bf16x8, pw);
#pragma unroll
      for (int db = 0; db < 8; ++db) {
        s16x8 vv;
#pragma unroll
        for (int j = 0; j < 8; ++j) { const int key = (j < 4) ? 4 * g + j : 16 + 4 * g + (j - 4); vv[j] = *(const short*)(Vs + key * 272 + (16 * db + i16) * 2); }
        f32x4 t = o[db] * alpha;
        o[db] = __builtin_amdgcn_mfma_f32_16x16x32_bf16(__builtin_bit_cast(bf16x8, vv), pf, t, 0, 0, 0);
      }
    }
    l += __shfl_xor(l, 16); l += __shfl_xor(l, 32);
    const float il = 1.f / l;
    if (live) {
#pragma unroll
      for (int db = 0; db < 8; ++db) {
        u32x2 wv = {cvtpk(o[db][0] * il, o[db][1] * il), cvtpk(o[db][2] * il, o[db][3] * il)};
        *(u32x2*)(O + (size_t)(tile * 16 + i16) * D + 16 * db + 4 * g) = wv;
      }
    }
  }
}

__global__ __launch_bounds__(256, 1) void attn_base(const unsigned short* __restrict__ Q, const unsigned short* __restrict__ K, const unsigned short* __restrict__ V, unsigned short* __restrict__ O) {
  __shared__ __attribute__((aligned(16))) char lds[2 * 32 * 272];
  const size_t bh = blockIdx.x;
  safe_attn(Q + bh * SQ * D, K + bh * SK * D, V + bh * SK * D, O + bh * SQ * D, lds);
}

extern "C" void kernel_launch(void* const* d_in, const int* in_sizes, int n_in, void* d_out, int out_size, void* d_ws, size_t ws_size, hipStream_t stream) {
  attn_base<<<256, 256, 0, stream>>>((const unsigned short*)d_in[0], (const unsigned short*)d_in[1], (const unsigned short*)d_in[2], (unsigned short*)d_out);
}
```

```cpp
#include <hip/hip_runtime.h>
#include <hip/hip_bf16.h>
typedef short  s16x8 __attribute__((ext_vector_type(8)));
typedef __bf16 bf16x8 __attribute__((ext_vector_type(8)));
typedef __bf16 bf16x2 __attribute__((ext_vector_type(2)));
typedef float  f32x4 __attribute__((ext_vector_type(4)));
typedef float  f32x2 __attribute__((ext_vector_type(2)));
typedef unsigned u32x4 __attribute__((ext_vector_type(4)));
typedef unsigned u32x2 __attribute__((ext_vector_type(2)));
constexpr int SQ = 336, SK = 2048, D = 128;
__device__ __forceinline__ unsigned cvtpk(float lo, float hi) { f32x2 v = {lo, hi}; bf16x2 b = __builtin_convertvector(v, bf16x2); return __builtin_bit_cast(unsigned, b); }

__device__ __forceinline__ void safe_attn(const unsigned short* __restrict__ Q, const unsigned short* __restrict__ K, const unsigned short* __restrict__ V,
                                          unsigned short* __restrict__ O, char* Vs, int w, int lane) {
  const int i16 = lane & 15, g = lane >> 4;
  const float C = 0.12751743082459868f;
  for (int round = 0; round < 6; ++round) {
    const int row0 = round < 5 ? 80 * w + 16 * round : 320;
    bf16x8 qf[4];
#pragma unroll
    for (int ds = 0; ds < 4; ++ds) qf[ds] = *(const bf16x8*)(Q + (size_t)(row0 + i16) * D + 32 * ds + 8 * g);
    f32x4 o[8];
#pragma unroll
    for (int db = 0; db < 8; ++db) o[db] = f32x4{0.f, 0.f, 0.f, 0.f};
    float m = -INFINITY, l = 0.f;
    for (int k0 = 0; k0 < SK; k0 += 32) {
      asm volatile("s_waitcnt lgkmcnt(0)" ::: "memory");
#pragma unroll
      for (int it = 0; it < 8; ++it) {
        const int c = lane + 64 * it, row = c >> 4, ch = c & 15;
        *(u32x4*)(Vs + row * 272 + ch * 16) = *(const u32x4*)(V + (size_t)(k0 + row) * D + ch * 8);
      }
      f32x4 s[2];
#pragma unroll
      for (int st = 0; st < 2; ++st) {
        s[st] = f32x4{0.f, 0.f, 0.f, 0.f};
#pragma unroll
        for (int ds = 0; ds < 4; ++ds) {
          const bf16x8 kf = *(const bf16x8*)(K + (size_t)(k0 + 16 * st + i16) * D + 32 * ds + 8 * g);
          s[st] = __builtin_amdgcn_mfma_f32_16x16x32_bf16(kf, qf[ds], s[st], 0, 0, 0);
        }
      }
      float mx = -INFINITY;
#pragma unroll
      for (int st = 0; st < 2; ++st)
#pragma unroll
        for (int r = 0; r < 4; ++r) { s[st][r] *= C; mx = fmaxf(mx, s[st][r]); }
      mx = fmaxf(mx, __shfl_xor(mx, 16)); mx = fmaxf(mx, __shfl_xor(mx, 32));
      const float mn = fmaxf(m, mx), alpha = __builtin_amdgcn_exp2f(m - mn); m = mn;
      float ps = 0.f;
#pragma unroll
      for (int st = 0; st < 2; ++st)
#pragma unroll
        for (int r = 0; r < 4; ++r) { s[st][r] = __builtin_amdgcn_exp2f(s[st][r] - mn); ps += s[st][r]; }
      l = l * alpha + ps;
      u32x4 pw = {cvtpk(s[0][0], s[0][1]), cvtpk(s[0][2], s[0][3]), cvtpk(s[1][0], s[1][1]), cvtpk(s[1][2], s[1][3])};
      const bf16x8 pf = __builtin_bit_cast(bf16x8, pw);
      asm volatile("s_waitcnt lgkmcnt(0)" ::: "memory");
#pragma unroll
      for (int db = 0; db < 8; ++db) {
        s16x8 vv;
#pragma unroll
        for (int j = 0; j < 8; ++j) { const int key = (j < 4) ? 4 * g + j : 16 + 4 * g + (j - 4); vv[j] = *(const short*)(Vs + key * 272 + (16 * db + i16) * 2); }
        f32x4 t = o[db] * alpha;
        o[db] = __builtin_amdgcn_mfma_f32_16x16x32_bf16(__builtin_bit_cast(bf16x8, vv), pf, t, 0, 0, 0);
      }
    }
    l += __shfl_xor(l, 16); l += __shfl_xor(l, 32);
    const float il = 1.f / l;
#pragma unroll
    for (int db = 0; db < 8; ++db) {
      if (round < 5 || (db >> 1) == w) {
        u32x2 wv = {cvtpk(o[db][0] * il, o[db][1] * il), cvtpk(o[db][2] * il, o[db][3] * il)};
        *(u32x2*)(O + (size_t)(row0 + i16) * D + 16 * db + 4 * g) = wv;
      }
    }
  }
}

__global__ __launch_bounds__(256, 1) void attn_fast(const unsigned short* __restrict__ Q, const unsigned short* __restrict__ K,
                                                 const unsigned short* __restrict__ V, unsigned short* __restrict__ O) {
  __shared__ __attribute__((aligned(16))) char lds[139520];
  const unsigned wave = __builtin_amdgcn_readfirstlane(threadIdx.x >> 6);
  const unsigned ldsb = (unsigned)(size_t)lds;
  const unsigned bh = blockIdx.x;
  unsigned redo;
  asm volatile(
    "s_lshl_b32 s61, %5, 19\n\t"
    "s_add_u32 s40, %8, s61\n\t"
    "s_addc_u32 s41, %9, 0\n\t"
    "s_and_b32 s41, s41, 0xffff\n\t"
    "s_mov_b32 s42, 524288\n\t"
    "s_mov_b32 s43, 0x20000\n\t"
    "v_mbcnt_lo_u32_b32 v1, -1, 0\n\t"
    "v_mbcnt_hi_u32_b32 v1, -1, v1\n\t"
    "s_mul_i32 s55, %6, 1056\n\t"
    "s_add_u32 s55, s55, %7\n\t"
    "v_lshlrev_b32 v239, 4, v1\n\t"
    "s_lshl_b32 s64, %6, 10\n\t"
    "s_lshr_b32 s59, %5, 3\n\t"
    "s_and_b32 s59, s59, 31\n\t"
    "s_lshl_b32 s59, s59, 14\n\t"
    "s_add_u32 s64, s64, s59\n\t"
    "s_add_u32 m0, s55, 0\n\t"
    "s_mul_i32 s61, %5, 86016\n\t"
    "buffer_load_dwordx4 v239, s[40:43], s64 offen nt lds\n\t"
    "s_add_u32 s64, s64, 4096\n\t"
    "s_add_u32 m0, s55, 4224\n\t"
    "s_mov_b32 s38, 86016\n\t"
    "buffer_load_dwordx4 v239, s[40:43], s64 offen nt lds\n\t"
    "s_add_u32 s64, s64, 4096\n\t"
    "s_add_u32 m0, s55, 8448\n\t"
    "s_mov_b32 s39, 0x20000\n\t"
    "buffer_load_dwordx4 v239, s[40:43], s64 offen nt lds\n\t"
    "s_add_u32 s64, s64, 4096\n\t"
    "s_add_u32 m0, s55, 12672\n\t"
    "v_and_b32 v2, 15, v1\n\t"
    "buffer_load_dwordx4 v239, s[40:43], s64 offen nt lds\n\t"
    "s_add_u32 s36, %10, s61\n\t"
    "s_addc_u32 s37, %11, 0\n\t"
    "s_and_b32 s37, s37, 0xffff\n\t"
    "v_lshrrev_b32 v255, 4, v1\n\t"
    "v_lshlrev_b32 v240, 8, v2\n\t"
    "v_lshl_add_u32 v240, v255, 4, v240\n\t"
    "s_mul_i32 s64, %6, 20480\n\t"
    "buffer_load_dwordx4 v[100:103], v240, s[36:39], s64 offen offset:0\n\t"
    "buffer_load_dwordx4 v[104:107], v240, s[36:39], s64 offen offset:64\n\t"
    "buffer_load_dwordx4 v[108:111], v240, s[36:39], s64 offen offset:128\n\t"
    "buffer_load_dwordx4 v[112:115], v240, s[36:39], s64 offen offset:192\n\t"
    "s_getpc_b64 s[96:97]\n\t"
    "PC0_%=:\n\t"
    "s_mov_b32 s98, WS0_%=-PC0_%=\n\t"
    "s_cmp_eq_u32 %6, 1\n\t"
    "s_cselect_b32 s98, WS1_%=-PC0_%=, s98\n\t"
    "s_cmp_eq_u32 %6, 2\n\t"
    "s_cselect_b32 s98, WS2_%=-PC0_%=, s98\n\t"
    "s_cmp_eq_u32 %6, 3\n\t"
    "s_cselect_b32 s98, WS3_%=-PC0_%=, s98\n\t"
    "s_add_u32 s96, s96, s98\n\t"
    "s_addc_u32 s97, s97, 0\n\t"
    "s_and_b32 s97, s97, 0xffff\n\t"
    "s_mov_b32 s98, WEND0_%=-WS0_%=\n\t"
    "s_mov_b32 s99, 0x20000\n\t"
    "s_lshr_b32 s61, %5, 3\n\t"
    "s_mul_i32 s61, s61, 640\n\t"
    "v_lshlrev_b32 v3, 7, v1\n\t"
    "v_cmp_gt_u32 vcc, 5, v1\n\t"
    "s_nop 1\n\t"
    "v_cndmask_b32 v3, -1, v3, vcc\n\t"
    "buffer_load_dword v4, v3, s[96:99], s61 offen\n\t"
    "s_mov_b64 s[44:45], %3\n\t"
    "s_mul_i32 s61, %5, 524288\n\t"
    "s_add_u32 s44, s44, s61\n\t"
    "s_addc_u32 s45, s45, 0\n\t"
    "s_and_b32 s45, s45, 0xffff\n\t"
    "s_mov_b32 s46, 524288\n\t"
    "s_mov_b32 s47, 0x20000\n\t"
    "s_mov_b64 s[48:49], %4\n\t"
    "s_mul_i32 s61, %5, 86016\n\t"
    "s_add_u32 s48, s48, s61\n\t"
    "s_addc_u32 s49, s49, 0\n\t"
    "s_and_b32 s49, s49, 0xffff\n\t"
    "s_mov_b32 s50, 86016\n\t"
    "s_mov_b32 s51, 0x20000\n\t"
    "s_mov_b32 s52, %6\n\t"
    "s_mov_b32 s53, %7\n\t"
    "s_cmp_eq_u32 s52, 1\n\t"
    "s_cbranch_scc1 W1_%=\n\t"
    "s_cmp_eq_u32 s52, 2\n\t"
    "s_cbranch_scc1 W2_%=\n\t"
    "s_cmp_eq_u32 s52, 3\n\t"
    "s_cbranch_scc1 W3_%=\n\t"
    "WS0_%=:\n\t"
    "CPF0_%=:\n\t"
    "s_add_u32 s56, s53, 67584\n\t"
    "v_add_u32 v226, 4096, v239\n\t"
    "v_add_u32 v227, 8192, v239\n\t"
    "v_add_u32 v3, 12288, v239\n\t"
    "s_mov_b32 s58, 33792\n\t"
    "s_mov_b32 s60, 14\n\t"
    "s_mov_b32 s65, 0x3e0293ee\n\t"
    "s_mov_b64 s[62:63], 0\n\t"
    "v_bfe_u32 v242, v2, 2, 1\n\t"
    "v_and_b32 v243, 3, v2\n\t"
    "v_lshl_add_u32 v242, v242, 2, v243\n\t"
    "v_mul_u32_u24 v242, 0x420, v242\n\t"
    "v_lshrrev_b32 v243, 3, v2\n\t"
    "v_lshl_add_u32 v242, v243, 8, v242\n\t"
    "v_lshl_add_u32 v242, v255, 4, v242\n\t"
    "s_add_u32 s61, s53, 0\n\t"
    "v_add_u32 v236, s61, v242\n\t"
    "v_and_b32 v242, 1, v255\n\t"
    "v_bfe_u32 v243, v1, 2, 2\n\t"
    "v_lshl_add_u32 v242, v242, 2, v243\n\t"
    "v_mul_u32_u24 v242, 0x420, v242\n\t"
    "v_lshrrev_b32 v243, 1, v255\n\t"
    "v_lshl_add_u32 v242, v243, 8, v242\n\t"
    "v_and_b32 v243, 3, v1\n\t"
    "v_lshl_add_u32 v242, v243, 4, v242\n\t"
    "v_bfe_u32 v243, v1, 2, 1\n\t"
    "v_mul_u32_u24 v243, 24, v243\n\t"
    "v_sub_u32 v242, v242, v243\n\t"
    "s_add_u32 s61, s53, 67584\n\t"
    "v_add_u32 v237, s61, v242\n\t"
    "s_add_u32 s61, s53, 135168\n\t"
    "v_add_u32 v238, s61, v239\n\t"
    "v_lshlrev_b32 v241, 8, v2\n\t"
    "v_lshl_add_u32 v241, v255, 4, v241\n\t"
    "v_mov_b32 v242, 0x3f803f80\n\t"
    "v_accvgpr_write_b32 a248, v242\n\t"
    "v_accvgpr_write_b32 a249, v242\n\t"
    "v_accvgpr_write_b32 a250, v242\n\t"
    "v_accvgpr_write_b32 a251, v242\n\t"
    "v_mov_b32 v132, 0\n\t"
    "v_mov_b32 v133, 0\n\t"
    "v_mov_b32 v134, 0\n\t"
    "v_mov_b32 v135, 0\n\t"
    "v_mov_b32 v136, 0\n\t"
    "v_mov_b32 v137, 0\n\t"
    "v_mov_b32 v138, 0\n\t"
    "v_mov_b32 v139, 0\n\t"
    "v_mov_b32 v140, 0\n\t"
    "v_mov_b32 v141, 0\n\t"
    "v_mov_b32 v142, 0\n\t"
    "v_mov_b32 v143, 0\n\t"
    "v_mov_b32 v144, 0\n\t"
    "v_mov_b32 v145, 0\n\t"
    "v_mov_b32 v146, 0\n\t"
    "v_mov_b32 v147, 0\n\t"
    "v_mov_b32 v148, 0\n\t"
    "v_mov_b32 v149, 0\n\t"
    "v_mov_b32 v150, 0\n\t"
    "v_mov_b32 v151, 0\n\t"
    "v_mov_b32 v152, 0\n\t"
    "v_mov_b32 v153, 0\n\t"
    "v_mov_b32 v154, 0\n\t"
    "v_mov_b32 v155, 0\n\t"
    "v_mov_b32 v156, 0\n\t"
    "v_mov_b32 v157, 0\n\t"
    "v_mov_b32 v158, 0\n\t"
    "v_mov_b32 v159, 0\n\t"
    "v_mov_b32 v160, 0\n\t"
    "v_mov_b32 v161, 0\n\t"
    "v_mov_b32 v162, 0\n\t"
    "v_mov_b32 v163, 0\n\t"
    "v_mov_b32 v164, 0\n\t"
    "v_mov_b32 v165, 0\n\t"
    "v_mov_b32 v166, 0\n\t"
    "v_mov_b32 v167, 0\n\t"
    "v_mov_b32 v168, 0\n\t"
    "v_mov_b32 v169, 0\n\t"
    "v_mov_b32 v170, 0\n\t"
    "v_mov_b32 v171, 0\n\t"
    "v_mov_b32 v250, 0\n\t"
    "v_mov_b32 v251, 0\n\t"
    "v_mov_b32 v252, 0\n\t"
    "v_mov_b32 v253, 0\n\t"
    "v_mov_b32 v254, 0\n\t"
    "v_mov_b32 v242, 1.0\n\t"
    "v_accvgpr_write_b32 a252, v242\n\t"
    "s_mov_b32 s64, 4096\n\t"
    "buffer_load_dwordx4 v[116:119], v240, s[36:39], s64 offen offset:0\n\t"
    "buffer_load_dwordx4 v[120:123], v240, s[36:39], s64 offen offset:64\n\t"
    "buffer_load_dwordx4 v[124:127], v240, s[36:39], s64 offen offset:128\n\t"
    "buffer_load_dwordx4 v[128:131], v240, s[36:39], s64 offen offset:192\n\t"
    "s_waitcnt vmcnt(9)\n\t"
    "s_barrier\n\t"
    "ds_read_b128 a[96:99], v236 offset:0\n\t"
    "ds_read_b128 a[100:103], v236 offset:64\n\t"
    "ds_read_b128 a[104:107], v236 offset:128\n\t"
    "ds_read_b128 a[108:111], v236 offset:192\n\t"
    "ds_read_b128 a[112:115], v236 offset:512\n\t"
    "ds_read_b128 a[116:119], v236 offset:576\n\t"
    "ds_read_b128 a[120:123], v236 offset:640\n\t"
    "ds_read_b128 a[124:127], v236 offset:704\n\t"
    "ds_read_b128 a[128:131], v236 offset:8448\n\t"
    "ds_read_b128 a[132:135], v236 offset:8512\n\t"
    "ds_read_b128 a[136:139], v236 offset:8576\n\t"
    "ds_read_b128 a[140:143], v236 offset:8640\n\t"
    "ds_read_b128 a[144:147], v236 offset:8960\n\t"
    "ds_read_b128 a[148:151], v236 offset:9024\n\t"
    "ds_read_b128 a[152:155], v236 offset:9088\n\t"
    "ds_read_b128 a[156:159], v236 offset:9152\n\t"
    "s_add_u32 m0, s56, 0\n\t"
    "s_add_u32 s61, s59, 0\n\t"
    "s_and_b32 s61, s61, 0x7ffff\n\t"
    "buffer_load_dwordx4 v239, s[44:47], s61 offen offset:0 lds\n\t"
    "s_add_u32 m0, s56, 4224\n\t"
    "s_nop 0\n\t"
    "buffer_load_dwordx4 v226, s[44:47], s61 offen offset:0 lds\n\t"
    "s_add_u32 m0, s56, 8448\n\t"
    "s_nop 0\n\t"
    "buffer_load_dwordx4 v227, s[44:47], s61 offen offset:0 lds\n\t"
    "s_add_u32 m0, s56, 12672\n\t"
    "s_nop 0\n\t"
    "buffer_load_dwordx4 v3, s[44:47], s61 offen offset:0 lds\n\t"
    "s_waitcnt vmcnt(12)\n\t"
    "v_lshlrev_b32 v242, 16, v100\n\t"
    "v_and_b32 v243, 0xffff0000, v100\n\t"
    "v_mul_f32 v242, s65, v242\n\t"
    "v_mul_f32 v243, s65, v243\n\t"
    "v_cvt_pk_bf16_f32 v242, v242, v243\n\t"
    "v_accvgpr_write_b32 a0, v242\n\t"
    "s_mov_b32 s64, 81920\n\t"
    "buffer_load_dwordx4 v[68:71], v240, s[36:39], s64 offen offset:0\n\t"
    "buffer_load_dwordx4 v[72:75], v240, s[36:39], s64 offen offset:64\n\t"
    "buffer_load_dwordx4 v[76:79], v240, s[36:39], s64 offen offset:128\n\t"
    "buffer_load_dwordx4 v[80:83], v240, s[36:39], s64 offen offset:192\n\t"
    "v_lshlrev_b32 v244, 16, v101\n\t"
    "v_and_b32 v245, 0xffff0000, v101\n\t"
    "v_mul_f32 v244, s65, v244\n\t"
    "v_mul_f32 v245, s65, v245\n\t"
    "v_cvt_pk_bf16_f32 v244, v244, v245\n\t"
    "v_accvgpr_write_b32 a1, v244\n\t"
    "v_lshlrev_b32 v246, 16, v102\n\t"
    "v_and_b32 v247, 0xffff0000, v102\n\t"
    "v_mul_f32 v246, s65, v246\n\t"
    "v_mul_f32 v247, s65, v247\n\t"
    "v_cvt_pk_bf16_f32 v246, v246, v247\n\t"
    "v_accvgpr_write_b32 a2, v246\n\t"
    "v_lshlrev_b32 v248, 16, v103\n\t"
    "v_and_b32 v249, 0xffff0000, v103\n\t"
    "v_mul_f32 v248, s65, v248\n\t"
    "v_mul_f32 v249, s65, v249\n\t"
    "v_cvt_pk_bf16_f32 v248, v248, v249\n\t"
    "v_accvgpr_write_b32 a3, v248\n\t"
    "s_waitcnt vmcnt(15)\n\t"
    "v_lshlrev_b32 v242, 16, v104\n\t"
    "v_and_b32 v243, 0xffff0000, v104\n\t"
    "v_mul_f32 v242, s65, v242\n\t"
    "v_mul_f32 v243, s65, v243\n\t"
    "v_cvt_pk_bf16_f32 v242, v242, v243\n\t"
    "v_accvgpr_write_b32 a4, v242\n\t"
    "v_lshlrev_b32 v244, 16, v105\n\t"
    "v_and_b32 v245, 0xffff0000, v105\n\t"
    "v_mul_f32 v244, s65, v244\n\t"
    "v_mul_f32 v245, s65, v245\n\t"
    "v_cvt_pk_bf16_f32 v244, v244, v245\n\t"
    "v_accvgpr_write_b32 a5, v244\n\t"
    "v_lshlrev_b32 v246, 16, v106\n\t"
    "v_and_b32 v247, 0xffff0000, v106\n\t"
    "v_mul_f32 v246, s65, v246\n\t"
    "v_mul_f32 v247, s65, v247\n\t"
    "v_cvt_pk_bf16_f32 v246, v246, v247\n\t"
    "v_accvgpr_write_b32 a6, v246\n\t"
    "v_lshlrev_b32 v248, 16, v107\n\t"
    "v_and_b32 v249, 0xffff0000, v107\n\t"
    "v_mul_f32 v248, s65, v248\n\t"
    "v_mul_f32 v249, s65, v249\n\t"
    "v_cvt_pk_bf16_f32 v248, v248, v249\n\t"
    "v_accvgpr_write_b32 a7, v248\n\t"
    "s_waitcnt vmcnt(14)\n\t"
    "v_lshlrev_b32 v242, 16, v108\n\t"
    "v_and_b32 v243, 0xffff0000, v108\n\t"
    "v_mul_f32 v242, s65, v242\n\t"
    "v_mul_f32 v243, s65, v243\n\t"
    "v_cvt_pk_bf16_f32 v242, v242, v243\n\t"
    "v_accvgpr_write_b32 a8, v242\n\t"
    "v_lshlrev_b32 v244, 16, v109\n\t"
    "v_and_b32 v245, 0xffff0000, v109\n\t"
    "v_mul_f32 v244, s65, v244\n\t"
    "v_mul_f32 v245, s65, v245\n\t"
    "v_cvt_pk_bf16_f32 v244, v244, v245\n\t"
    "v_accvgpr_write_b32 a9, v244\n\t"
    "v_lshlrev_b32 v246, 16, v110\n\t"
    "v_and_b32 v247, 0xffff0000, v110\n\t"
    "v_mul_f32 v246, s65, v246\n\t"
    "v_mul_f32 v247, s65, v247\n\t"
    "v_cvt_pk_bf16_f32 v246, v246, v247\n\t"
    "v_accvgpr_write_b32 a10, v246\n\t"
    "v_lshlrev_b32 v248, 16, v111\n\t"
    "v_and_b32 v249, 0xffff0000, v111\n\t"
    "v_mul_f32 v248, s65, v248\n\t"
    "v_mul_f32 v249, s65, v249\n\t"
    "v_cvt_pk_bf16_f32 v248, v248, v249\n\t"
    "v_accvgpr_write_b32 a11, v248\n\t"
    "s_waitcnt vmcnt(13)\n\t"
    "v_lshlrev_b32 v242, 16, v112\n\t"
    "v_and_b32 v243, 0xffff0000, v112\n\t"
    "v_mul_f32 v242, s65, v242\n\t"
    "v_mul_f32 v243, s65, v243\n\t"
    "v_cvt_pk_bf16_f32 v242, v242, v243\n\t"
    "v_accvgpr_write_b32 a12, v242\n\t"
    "v_lshlrev_b32 v244, 16, v113\n\t"
    "v_and_b32 v245, 0xffff0000, v113\n\t"
    "v_mul_f32 v244, s65, v244\n\t"
    "v_mul_f32 v245, s65, v245\n\t"
    "v_cvt_pk_bf16_f32 v244, v244, v245\n\t"
    "v_accvgpr_write_b32 a13, v244\n\t"
    "v_lshlrev_b32 v246, 16, v114\n\t"
    "v_and_b32 v247, 0xffff0000, v114\n\t"
    "v_mul_f32 v246, s65, v246\n\t"
    "v_mul_f32 v247, s65, v247\n\t"
    "v_cvt_pk_bf16_f32 v246, v246, v247\n\t"
    "v_accvgpr_write_b32 a14, v246\n\t"
    "v_lshlrev_b32 v248, 16, v115\n\t"
    "v_and_b32 v249, 0xffff0000, v115\n\t"
    "v_mul_f32 v248, s65, v248\n\t"
    "v_mul_f32 v249, s65, v249\n\t"
    "v_cvt_pk_bf16_f32 v248, v248, v249\n\t"
    "v_accvgpr_write_b32 a15, v248\n\t"
    "s_waitcnt lgkmcnt(15)\n\t"
    "v_mfma_f32_16x16x32_bf16 v[172:175], a[96:99], a[0:3], 0\n\t"
    "s_waitcnt lgkmcnt(14)\n\t"
    "v_mfma_f32_16x16x32_bf16 v[172:175], a[100:103], a[4:7], v[172:175]\n\t"
    "s_waitcnt lgkmcnt(13)\n\t"
    "s_waitcnt vmcnt(11)\n\t"
    "v_lshlrev_b32 v242, 16, v116\n\t"
    "v_and_b32 v243, 0xffff0000, v116\n\t"
    "v_mul_f32 v242, s65, v242\n\t"
    "v_mul_f32 v243, s65, v243\n\t"
    "v_cvt_pk_bf16_f32 v242, v242, v243\n\t"
    "v_accvgpr_write_b32 a16, v242\n\t"
    "s_mov_b32 s64, 8192\n\t"
    "buffer_load_dwordx4 v[84:87], v240, s[36:39], s64 offen offset:0\n\t"
    "buffer_load_dwordx4 v[88:91], v240, s[36:39], s64 offen offset:64\n\t"
    "buffer_load_dwordx4 v[92:95], v240, s[36:39], s64 offen offset:128\n\t"
    "buffer_load_dwordx4 v[96:99], v240, s[36:39], s64 offen offset:192\n\t"
    "v_mfma_f32_16x16x32_bf16 v[172:175], a[104:107], a[8:11], v[172:175]\n\t"
    "s_waitcnt lgkmcnt(12)\n\t"
    "v_lshlrev_b32 v244, 16, v117\n\t"
    "v_and_b32 v245, 0xffff0000, v117\n\t"
    "v_mul_f32 v244, s65, v244\n\t"
    "v_mul_f32 v245, s65, v245\n\t"
    "v_cvt_pk_bf16_f32 v244, v244, v245\n\t"
    "v_accvgpr_write_b32 a17, v244\n\t"
    "v_mfma_f32_16x16x32_bf16 v[172:175], a[108:111], a[12:15], v[172:175]\n\t"
    "s_waitcnt lgkmcnt(11)\n\t"
    "v_lshlrev_b32 v246, 16, v118\n\t"
    "v_and_b32 v247, 0xffff0000, v118\n\t"
    "v_mul_f32 v246, s65, v246\n\t"
    "v_mul_f32 v247, s65, v247\n\t"
    "v_cvt_pk_bf16_f32 v246, v246, v247\n\t"
    "v_accvgpr_write_b32 a18, v246\n\t"
    "v_lshlrev_b32 v248, 16, v119\n\t"
    "v_mfma_f32_16x16x32_bf16 v[176:179], a[112:115], a[0:3], 0\n\t"
    "s_waitcnt lgkmcnt(10)\n\t"
    "v_and_b32 v249, 0xffff0000, v119\n\t"
    "v_mul_f32 v248, s65, v248\n\t"
    "v_mul_f32 v249, s65, v249\n\t"
    "v_cvt_pk_bf16_f32 v248, v248, v249\n\t"
    "v_accvgpr_write_b32 a19, v248\n\t"
    "s_waitcnt vmcnt(14)\n\t"
    "v_lshlrev_b32 v242, 16, v120\n\t"
    "v_mfma_f32_16x16x32_bf16 v[176:179], a[116:119], a[4:7], v[176:179]\n\t"
    "s_waitcnt lgkmcnt(9)\n\t"
    "v_and_b32 v243, 0xffff0000, v120\n\t"
    "v_mul_f32 v242, s65, v242\n\t"
    "v_mul_f32 v243, s65, v243\n\t"
    "v_cvt_pk_bf16_f32 v242, v242, v243\n\t"
    "v_accvgpr_write_b32 a20, v242\n\t"
    "v_lshlrev_b32 v244, 16, v121\n\t"
    "v_and_b32 v245, 0xffff0000, v121\n\t"
    "v_mfma_f32_16x16x32_bf16 v[176:179], a[120:123], a[8:11], v[176:179]\n\t"
    "s_waitcnt lgkmcnt(0)\n\t"
    "v_mul_f32 v244, s65, v244\n\t"
    "v_mul_f32 v245, s65, v245\n\t"
    "v_cvt_pk_bf16_f32 v244, v244, v245\n\t"
    "v_accvgpr_write_b32 a21, v244\n\t"
    "v_lshlrev_b32 v246, 16, v122\n\t"
    "v_and_b32 v247, 0xffff0000, v122\n\t"
    "v_mfma_f32_16x16x32_bf16 v[176:179], a[124:127], a[12:15], v[176:179]\n\t"
    "v_mul_f32 v246, s65, v246\n\t"
    "v_mul_f32 v247, s65, v247\n\t"
    "v_cvt_pk_bf16_f32 v246, v246, v247\n\t"
    "v_accvgpr_write_b32 a22, v246\n\t"
    "v_lshlrev_b32 v248, 16, v123\n\t"
    "v_and_b32 v249, 0xffff0000, v123\n\t"
    "v_mfma_f32_16x16x32_bf16 v[180:183], a[128:131], a[0:3], 0\n\t"
    "v_mul_f32 v248, s65, v248\n\t"
    "v_mul_f32 v249, s65, v249\n\t"
    "v_cvt_pk_bf16_f32 v248, v248, v249\n\t"
    "v_accvgpr_write_b32 a23, v248\n\t"
    "s_waitcnt vmcnt(13)\n\t"
    "v_lshlrev_b32 v242, 16, v124\n\t"
    "v_and_b32 v243, 0xffff0000, v124\n\t"
    "v_mul_f32 v242, s65, v242\n\t"
    "v_mfma_f32_16x16x32_bf16 v[180:183], a[132:135], a[4:7], v[180:183]\n\t"
    "v_mul_f32 v243, s65, v243\n\t"
    "v_cvt_pk_bf16_f32 v242, v242, v243\n\t"
    "v_accvgpr_write_b32 a24, v242\n\t"
    "v_lshlrev_b32 v244, 16, v125\n\t"
    "v_and_b32 v245, 0xffff0000, v125\n\t"
    "v_mul_f32 v244, s65, v244\n\t"
    "v_mfma_f32_16x16x32_bf16 v[180:183], a[136:139], a[8:11], v[180:183]\n\t"
    "v_mul_f32 v245, s65, v245\n\t"
    "v_cvt_pk_bf16_f32 v244, v244, v245\n\t"
    "v_accvgpr_write_b32 a25, v244\n\t"
    "v_lshlrev_b32 v246, 16, v126\n\t"
    "v_and_b32 v247, 0xffff0000, v126\n\t"
    "v_mul_f32 v246, s65, v246\n\t"
    "v_mul_f32 v247, s65, v247\n\t"
    "v_mfma_f32_16x16x32_bf16 v[180:183], a[140:143], a[12:15], v[180:183]\n\t"
    "v_cvt_pk_bf16_f32 v246, v246, v247\n\t"
    "v_accvgpr_write_b32 a26, v246\n\t"
    "v_lshlrev_b32 v248, 16, v127\n\t"
    "v_and_b32 v249, 0xffff0000, v127\n\t"
    "v_mul_f32 v248, s65, v248\n\t"
    "v_mul_f32 v249, s65, v249\n\t"
    "v_exp_f32 v172, v172\n\t"
    "v_mfma_f32_16x16x32_bf16 v[184:187], a[144:147], a[0:3], 0\n\t"
    "v_cvt_pk_bf16_f32 v248, v248, v249\n\t"
    "v_accvgpr_write_b32 a27, v248\n\t"
    "s_waitcnt vmcnt(12)\n\t"
    "v_lshlrev_b32 v242, 16, v128\n\t"
    "v_and_b32 v243, 0xffff0000, v128\n\t"
    "v_mul_f32 v242, s65, v242\n\t"
    "v_mul_f32 v243, s65, v243\n\t"
    "v_exp_f32 v173, v173\n\t"
    "v_mfma_f32_16x16x32_bf16 v[184:187], a[148:151], a[4:7], v[184:187]\n\t"
    "v_cvt_pk_bf16_f32 v242, v242, v243\n\t"
    "v_accvgpr_write_b32 a28, v242\n\t"
    "v_lshlrev_b32 v244, 16, v129\n\t"
    "v_and_b32 v245, 0xffff0000, v129\n\t"
    "v_mul_f32 v244, s65, v244\n\t"
    "v_mul_f32 v245, s65, v245\n\t"
    "v_cvt_pk_bf16_f32 v244, v244, v245\n\t"
    "v_cvt_pk_bf16_f32 v204, v172, v173\n\t"
    "v_add_f32 v250, v250, v172\n\t"
    "v_mfma_f32_16x16x32_bf16 v[184:187], a[152:155], a[8:11], v[184:187]\n\t"
    "v_accvgpr_write_b32 a29, v244\n\t"
    "v_lshlrev_b32 v246, 16, v130\n\t"
    "v_and_b32 v247, 0xffff0000, v130\n\t"
    "v_mul_f32 v246, s65, v246\n\t"
    "v_mul_f32 v247, s65, v247\n\t"
    "v_cvt_pk_bf16_f32 v246, v246, v247\n\t"
    "v_exp_f32 v174, v174\n\t"
    "v_mfma_f32_16x16x32_bf16 v[184:187], a[156:159], a[12:15], v[184:187]\n\t"
    "v_accvgpr_write_b32 a30, v246\n\t"
    "v_lshlrev_b32 v248, 16, v131\n\t"
    "v_and_b32 v249, 0xffff0000, v131\n\t"
    "v_mul_f32 v248, s65, v248\n\t"
    "v_mul_f32 v249, s65, v249\n\t"
    "v_cvt_pk_bf16_f32 v248, v248, v249\n\t"
    "v_accvgpr_write_b32 a31, v248\n\t"
    "v_exp_f32 v175, v175\n\t"
    "s_waitcnt vmcnt(8)\n\t"
    "s_barrier\n\t"
    "v_cvt_pk_bf16_f32 v205, v174, v175\n\t"
    "v_add_f32 v250, v250, v173\n\t"
    "s_mov_b32 s64, 12288\n\t"
    "buffer_load_dwordx4 v[100:103], v240, s[36:39], s64 offen offset:0\n\t"
    "buffer_load_dwordx4 v[104:107], v240, s[36:39], s64 offen offset:64\n\t"
    "buffer_load_dwordx4 v[108:111], v240, s[36:39], s64 offen offset:128\n\t"
    "buffer_load_dwordx4 v[112:115], v240, s[36:39], s64 offen offset:192\n\t"
    "v_exp_f32 v176, v176\n\t"
    "v_exp_f32 v177, v177\n\t"
    "ds_read_b64_tr_b16 a[160:161], v237 offset:0\n\t"
    "ds_read_b64_tr_b16 a[162:163], v237 offset:512\n\t"
    "v_cvt_pk_bf16_f32 v206, v176, v177\n\t"
    "v_add_f32 v250, v250, v174\n\t"
    "s_waitcnt vmcnt(11)\n\t"
    "v_lshlrev_b32 v242, 16, v68\n\t"
    "v_and_b32 v243, 0xffff0000, v68\n\t"
    "v_mul_f32 v242, s65, v242\n\t"
    "v_mul_f32 v243, s65, v243\n\t"
    "v_cvt_pk_bf16_f32 v242, v242, v243\n\t"
    "v_accvgpr_write_b32 a80, v242\n\t"
    "s_add_u32 m0, s55, 16896\n\t"
    "s_add_u32 s61, s59, 16384\n\t"
    "s_and_b32 s61, s61, 0x7ffff\n\t"
    "buffer_load_dwordx4 v239, s[40:43], s61 offen offset:0 nt lds\n\t"
    "s_add_u32 m0, s55, 21120\n\t"
    "s_nop 0\n\t"
    "buffer_load_dwordx4 v226, s[40:43], s61 offen offset:0 nt lds\n\t"
    "s_add_u32 m0, s55, 25344\n\t"
    "s_nop 0\n\t"
    "buffer_load_dwordx4 v227, s[40:43], s61 offen offset:0 nt lds\n\t"
    "s_add_u32 m0, s55, 29568\n\t"
    "s_nop 0\n\t"
    "buffer_load_dwordx4 v3, s[40:43], s61 offen offset:0 nt lds\n\t"
    "ds_read_b64_tr_b16 a[192:193], v237 offset:8448\n\t"
    "ds_read_b64_tr_b16 a[194:195], v237 offset:8960\n\t"
    "v_lshlrev_b32 v244, 16, v69\n\t"
    "v_and_b32 v245, 0xffff0000, v69\n\t"
    "v_mul_f32 v244, s65, v244\n\t"
    "v_mul_f32 v245, s65, v245\n\t"
    "v_cvt_pk_bf16_f32 v244, v244, v245\n\t"
    "v_accvgpr_write_b32 a81, v244\n\t"
    "v_lshlrev_b32 v246, 16, v70\n\t"
    "v_exp_f32 v178, v178\n\t"
    "v_and_b32 v247, 0xffff0000, v70\n\t"
    "v_mul_f32 v246, s65, v246\n\t"
    "v_mul_f32 v247, s65, v247\n\t"
    "v_cvt_pk_bf16_f32 v246, v246, v247\n\t"
    "v_accvgpr_write_b32 a82, v246\n\t"
    "v_lshlrev_b32 v248, 16, v71\n\t"
    "v_and_b32 v249, 0xffff0000, v71\n\t"
    "ds_read_b64_tr_b16 a[164:165], v237 offset:8\n\t"
    "ds_read_b64_tr_b16 a[166:167], v237 offset:520\n\t"
    "v_mul_f32 v248, s65, v248\n\t"
    "v_mul_f32 v249, s65, v249\n\t"
    "v_cvt_pk_bf16_f32 v248, v248, v249\n\t"
    "v_accvgpr_write_b32 a83, v248\n\t"
    "s_waitcnt vmcnt(14)\n\t"
    "v_lshlrev_b32 v242, 16, v72\n\t"
    "v_and_b32 v243, 0xffff0000, v72\n\t"
    "v_mul_f32 v242, s65, v242\n\t"
    "v_exp_f32 v179, v179\n\t"
    "v_mul_f32 v243, s65, v243\n\t"
    "v_cvt_pk_bf16_f32 v242, v242, v243\n\t"
    "v_accvgpr_write_b32 a84, v242\n\t"
    "v_lshlrev_b32 v244, 16, v73\n\t"
    "v_and_b32 v245, 0xffff0000, v73\n\t"
    "v_mul_f32 v244, s65, v244\n\t"
    "v_mul_f32 v245, s65, v245\n\t"
    "ds_read_b64_tr_b16 a[196:197], v237 offset:8456\n\t"
    "ds_read_b64_tr_b16 a[198:199], v237 offset:8968\n\t"
    "v_cvt_pk_bf16_f32 v244, v244, v245\n\t"
    "v_accvgpr_write_b32 a85, v244\n\t"
    "v_lshlrev_b32 v246, 16, v74\n\t"
    "v_and_b32 v247, 0xffff0000, v74\n\t"
    "v_mul_f32 v246, s65, v246\n\t"
    "v_mul_f32 v247, s65, v247\n\t"
    "v_cvt_pk_bf16_f32 v246, v246, v247\n\t"
    "v_cvt_pk_bf16_f32 v207, v178, v179\n\t"
    "v_add_f32 v250, v250, v175\n\t"
    "v_accvgpr_write_b32 a86, v246\n\t"
    "v_lshlrev_b32 v248, 16, v75\n\t"
    "v_and_b32 v249, 0xffff0000, v75\n\t"
    "v_mul_f32 v248, s65, v248\n\t"
    "v_mul_f32 v249, s65, v249\n\t"
    "v_cvt_pk_bf16_f32 v248, v248, v249\n\t"
    "v_accvgpr_write_b32 a87, v248\n\t"
    "ds_read_b64_tr_b16 a[168:169], v237 offset:64\n\t"
    "ds_read_b64_tr_b16 a[170:171], v237 offset:576\n\t"
    "s_waitcnt vmcnt(13)\n\t"
    "v_lshlrev_b32 v242, 16, v76\n\t"
    "v_and_b32 v243, 0xffff0000, v76\n\t"
    "v_mul_f32 v242, s65, v242\n\t"
    "v_mul_f32 v243, s65, v243\n\t"
    "v_cvt_pk_bf16_f32 v242, v242, v243\n\t"
    "v_accvgpr_write_b32 a88, v242\n\t"
    "v_exp_f32 v180, v180\n\t"
    "v_lshlrev_b32 v244, 16, v77\n\t"
    "v_and_b32 v245, 0xffff0000, v77\n\t"
    "v_mul_f32 v244, s65, v244\n\t"
    "v_mul_f32 v245, s65, v245\n\t"
    "v_cvt_pk_bf16_f32 v244, v244, v245\n\t"
    "v_accvgpr_write_b32 a89, v244\n\t"
    "v_lshlrev_b32 v246, 16, v78\n\t"
    "ds_read_b64_tr_b16 a[200:201], v237 offset:8512\n\t"
    "ds_read_b64_tr_b16 a[202:203], v237 offset:9024\n\t"
    "v_and_b32 v247, 0xffff0000, v78\n\t"
    "v_mul_f32 v246, s65, v246\n\t"
    "v_mul_f32 v247, s65, v247\n\t"
    "v_cvt_pk_bf16_f32 v246, v246, v247\n\t"
    "v_accvgpr_write_b32 a90, v246\n\t"
    "v_lshlrev_b32 v248, 16, v79\n\t"
    "v_and_b32 v249, 0xffff0000, v79\n\t"
    "v_exp_f32 v181, v181\n\t"
    "v_mul_f32 v248, s65, v248\n\t"
    "v_mul_f32 v249, s65, v249\n\t"
    "v_cvt_pk_bf16_f32 v248, v248, v249\n\t"
    "v_accvgpr_write_b32 a91, v248\n\t"
    "s_waitcnt vmcnt(12)\n\t"
    "v_lshlrev_b32 v242, 16, v80\n\t"
    "v_and_b32 v243, 0xffff0000, v80\n\t"
    "v_mul_f32 v242, s65, v242\n\t"
    "ds_read_b64_tr_b16 a[172:173], v237 offset:72\n\t"
    "ds_read_b64_tr_b16 a[174:175], v237 offset:584\n\t"
    "v_mul_f32 v243, s65, v243\n\t"
    "v_cvt_pk_bf16_f32 v242, v242, v243\n\t"
    "v_accvgpr_write_b32 a92, v242\n\t"
    "v_lshlrev_b32 v244, 16, v81\n\t"
    "v_and_b32 v245, 0xffff0000, v81\n\t"
    "v_mul_f32 v244, s65, v244\n\t"
    "v_mul_f32 v245, s65, v245\n\t"
    "v_cvt_pk_bf16_f32 v208, v180, v181\n\t"
    "v_add_f32 v250, v250, v176\n\t"
    "v_cvt_pk_bf16_f32 v244, v244, v245\n\t"
    "v_accvgpr_write_b32 a93, v244\n\t"
    "v_lshlrev_b32 v246, 16, v82\n\t"
    "v_and_b32 v247, 0xffff0000, v82\n\t"
    "v_mul_f32 v246, s65, v246\n\t"
    "v_mul_f32 v247, s65, v247\n\t"
    "v_cvt_pk_bf16_f32 v246, v246, v247\n\t"
    "v_accvgpr_write_b32 a94, v246\n\t"
    "v_lshlrev_b32 v248, 16, v83\n\t"
    "v_and_b32 v249, 0xffff0000, v83\n\t"
    "v_mul_f32 v248, s65, v248\n\t"
    "v_mul_f32 v249, s65, v249\n\t"
    "v_cvt_pk_bf16_f32 v248, v248, v249\n\t"
    "v_accvgpr_write_b32 a95, v248\n\t"
    "v_exp_f32 v182, v182\n\t"
    "v_mfma_f32_16x16x32_bf16 v[188:191], a[96:99], a[16:19], 0\n\t"
    "ds_read_b64_tr_b16 a[204:205], v237 offset:8520\n\t"
    "ds_read_b64_tr_b16 a[206:207], v237 offset:9032\n\t"
    "v_mfma_f32_16x16x32_bf16 v[188:191], a[100:103], a[20:23], v[188:191]\n\t"
    "v_exp_f32 v183, v183\n\t"
    "v_mfma_f32_16x16x32_bf16 v[188:191], a[104:107], a[24:27], v[188:191]\n\t"
    "s_waitcnt vmcnt(11)\n\t"
    "v_lshlrev_b32 v242, 16, v84\n\t"
    "v_and_b32 v243, 0xffff0000, v84\n\t"
    "v_mul_f32 v242, s65, v242\n\t"
    "s_add_u32 m0, s56, 16896\n\t"
    "s_add_u32 s61, s59, 16384\n\t"
    "s_and_b32 s61, s61, 0x7ffff\n\t"
    "buffer_load_dwordx4 v239, s[44:47], s61 offen offset:0 lds\n\t"
    "s_add_u32 m0, s56, 21120\n\t"
    "s_nop 0\n\t"
    "buffer_load_dwordx4 v226, s[44:47], s61 offen offset:0 lds\n\t"
    "s_add_u32 m0, s56, 25344\n\t"
    "s_nop 0\n\t"
    "buffer_load_dwordx4 v227, s[44:47], s61 offen offset:0 lds\n\t"
    "s_add_u32 m0, s56, 29568\n\t"
    "s_nop 0\n\t"
    "buffer_load_dwordx4 v3, s[44:47], s61 offen offset:0 lds\n\t"
    "ds_read_b64_tr_b16 a[176:177], v237 offset:128\n\t"
    "ds_read_b64_tr_b16 a[178:179], v237 offset:640\n\t"
    "v_mfma_f32_16x16x32_bf16 v[188:191], a[108:111], a[28:31], v[188:191]\n\t"
    "v_mul_f32 v243, s65, v243\n\t"
    "v_cvt_pk_bf16_f32 v242, v242, v243\n\t"
    "v_accvgpr_write_b32 a32, v242\n\t"
    "v_cvt_pk_bf16_f32 v209, v182, v183\n\t"
    "v_add_f32 v250, v250, v177\n\t"
    "v_mfma_f32_16x16x32_bf16 v[192:195], a[112:115], a[16:19], 0\n\t"
    "v_lshlrev_b32 v244, 16, v85\n\t"
    "v_and_b32 v245, 0xffff0000, v85\n\t"
    "v_mul_f32 v244, s65, v244\n\t"
    "ds_read_b64_tr_b16 a[208:209], v237 offset:8576\n\t"
    "ds_read_b64_tr_b16 a[210:211], v237 offset:9088\n\t"
    "v_mfma_f32_16x16x32_bf16 v[192:195], a[116:119], a[20:23], v[192:195]\n\t"
    "v_mul_f32 v245, s65, v245\n\t"
    "v_cvt_pk_bf16_f32 v244, v244, v245\n\t"
    "v_accvgpr_write_b32 a33, v244\n\t"
    "v_exp_f32 v184, v184\n\t"
    "v_mfma_f32_16x16x32_bf16 v[192:195], a[120:123], a[24:27], v[192:195]\n\t"
    "v_lshlrev_b32 v246, 16, v86\n\t"
    "v_and_b32 v247, 0xffff0000, v86\n\t"
    "v_mul_f32 v246, s65, v246\n\t"
    "ds_read_b64_tr_b16 a[180:181], v237 offset:136\n\t"
    "ds_read_b64_tr_b16 a[182:183], v237 offset:648\n\t"
    "v_mfma_f32_16x16x32_bf16 v[192:195], a[124:127], a[28:31], v[192:195]\n\t"
    "v_mul_f32 v247, s65, v247\n\t"
    "v_cvt_pk_bf16_f32 v246, v246, v247\n\t"
    "v_accvgpr_write_b32 a34, v246\n\t"
    "v_exp_f32 v185, v185\n\t"
    "v_mfma_f32_16x16x32_bf16 v[196:199], a[128:131], a[16:19], 0\n\t"
    "v_lshlrev_b32 v248, 16, v87\n\t"
    "v_and_b32 v249, 0xffff0000, v87\n\t"
    "v_mul_f32 v248, s65, v248\n\t"
    "ds_read_b64_tr_b16 a[212:213], v237 offset:8584\n\t"
    "ds_read_b64_tr_b16 a[214:215], v237 offset:9096\n\t"
    "v_mfma_f32_16x16x32_bf16 v[196:199], a[132:135], a[20:23], v[196:199]\n\t"
    "v_mul_f32 v249, s65, v249\n\t"
    "v_cvt_pk_bf16_f32 v248, v248, v249\n\t"
    "v_accvgpr_write_b32 a35, v248\n\t"
    "v_cvt_pk_bf16_f32 v210, v184, v185\n\t"
    "v_add_f32 v250, v250, v178\n\t"
    "v_mfma_f32_16x16x32_bf16 v[196:199], a[136:139], a[24:27], v[196:199]\n\t"
    "s_waitcnt vmcnt(14)\n\t"
    "v_lshlrev_b32 v242, 16, v88\n\t"
    "v_and_b32 v243, 0xffff0000, v88\n\t"
    "v_mul_f32 v242, s65, v242\n\t"
    "ds_read_b64_tr_b16 a[184:185], v237 offset:192\n\t"
    "ds_read_b64_tr_b16 a[186:187], v237 offset:704\n\t"
    "v_mfma_f32_16x16x32_bf16 v[196:199], a[140:143], a[28:31], v[196:199]\n\t"
    "v_mul_f32 v243, s65, v243\n\t"
    "v_cvt_pk_bf16_f32 v242, v242, v243\n\t"
    "v_accvgpr_write_b32 a36, v242\n\t"
    "v_exp_f32 v186, v186\n\t"
    "v_mfma_f32_16x16x32_bf16 v[200:203], a[144:147], a[16:19], 0\n\t"
    "v_lshlrev_b32 v244, 16, v89\n\t"
    "v_and_b32 v245, 0xffff0000, v89\n\t"
    "v_mul_f32 v244, s65, v244\n\t"
    "v_mul_f32 v245, s65, v245\n\t"
    "ds_read_b64_tr_b16 a[216:217], v237 offset:8640\n\t"
    "ds_read_b64_tr_b16 a[218:219], v237 offset:9152\n\t"
    "v_mfma_f32_16x16x32_bf16 v[200:203], a[148:151], a[20:23], v[200:203]\n\t"
    "v_cvt_pk_bf16_f32 v244, v244, v245\n\t"
    "v_accvgpr_write_b32 a37, v244\n\t"
    "v_lshlrev_b32 v246, 16, v90\n\t"
    "v_exp_f32 v187, v187\n\t"
    "v_mfma_f32_16x16x32_bf16 v[200:203], a[152:155], a[24:27], v[200:203]\n\t"
    "v_and_b32 v247, 0xffff0000, v90\n\t"
    "v_mul_f32 v246, s65, v246\n\t"
    "v_mul_f32 v247, s65, v247\n\t"
    "ds_read_b64_tr_b16 a[188:189], v237 offset:200\n\t"
    "ds_read_b64_tr_b16 a[190:191], v237 offset:712\n\t"
    "v_mfma_f32_16x16x32_bf16 v[200:203], a[156:159], a[28:31], v[200:203]\n\t"
    "v_cvt_pk_bf16_f32 v246, v246, v247\n\t"
    "v_accvgpr_write_b32 a38, v246\n\t"
    "v_lshlrev_b32 v248, 16, v91\n\t"
    "v_cvt_pk_bf16_f32 v211, v186, v187\n\t"
    "v_add_f32 v250, v250, v179\n\t"
    "v_mfma_f32_16x16x32_bf16 v[220:223], a[96:99], a[80:83], 0\n\t"
    "v_and_b32 v249, 0xffff0000, v91\n\t"
    "v_mul_f32 v248, s65, v248\n\t"
    "v_mul_f32 v249, s65, v249\n\t"
    "ds_read_b64_tr_b16 a[220:221], v237 offset:8648\n\t"
    "ds_read_b64_tr_b16 a[222:223], v237 offset:9160\n\t"
    "v_mfma_f32_16x16x32_bf16 v[220:223], a[100:103], a[84:87], v[220:223]\n\t"
    "v_cvt_pk_bf16_f32 v248, v248, v249\n\t"
    "v_accvgpr_write_b32 a39, v248\n\t"
    "s_waitcnt vmcnt(13)\n\t"
    "v_lshlrev_b32 v242, 16, v92\n\t"
    "v_exp_f32 v188, v188\n\t"
    "v_mfma_f32_16x16x32_bf16 v[220:223], a[104:107], a[88:91], v[220:223]\n\t"
    "v_and_b32 v243, 0xffff0000, v92\n\t"
    "v_mul_f32 v242, s65, v242\n\t"
    "v_mul_f32 v243, s65, v243\n\t"
    "v_mfma_f32_16x16x32_bf16 v[220:223], a[108:111], a[92:95], v[220:223]\n\t"
    "s_waitcnt lgkmcnt(4)\n\t"
    "v_cvt_pk_bf16_f32 v242, v242, v243\n\t"
    "v_accvgpr_write_b32 a40, v242\n\t"
    "v_lshlrev_b32 v244, 16, v93\n\t"
    "v_exp_f32 v189, v189\n\t"
    "v_mfma_f32_16x16x32_bf16 v[4:7], a[160:163], v[204:207], 0\n\t"
    "v_and_b32 v245, 0xffff0000, v93\n\t"
    "v_mul_f32 v244, s65, v244\n\t"
    "v_mul_f32 v245, s65, v245\n\t"
    "v_cvt_pk_bf16_f32 v212, v188, v189\n\t"
    "v_add_f32 v250, v250, v180\n\t"
    "v_mfma_f32_16x16x32_bf16 v[4:7], a[192:195], v[208:211], v[4:7]\n\t"
    "v_cvt_pk_bf16_f32 v244, v244, v245\n\t"
    "v_accvgpr_write_b32 a41, v244\n\t"
    "v_lshlrev_b32 v246, 16, v94\n\t"
    "v_exp_f32 v190, v190\n\t"
    "v_mfma_f32_16x16x32_bf16 v[8:11], a[164:167], v[204:207], 0\n\t"
    "v_and_b32 v247, 0xffff0000, v94\n\t"
    "v_mul_f32 v246, s65, v246\n\t"
    "v_mul_f32 v247, s65, v247\n\t"
    "v_cvt_pk_bf16_f32 v246, v246, v247\n\t"
    "v_exp_f32 v191, v191\n\t"
    "v_mfma_f32_16x16x32_bf16 v[8:11], a[196:199], v[208:211], v[8:11]\n\t"
    "v_accvgpr_write_b32 a42, v246\n\t"
    "v_lshlrev_b32 v248, 16, v95\n\t"
    "v_and_b32 v249, 0xffff0000, v95\n\t"
    "v_cvt_pk_bf16_f32 v213, v190, v191\n\t"
    "v_add_f32 v250, v250, v181\n\t"
    "v_mfma_f32_16x16x32_bf16 v[12:15], a[168:171], v[204:207], 0\n\t"
    "v_mul_f32 v248, s65, v248\n\t"
    "v_mul_f32 v249, s65, v249\n\t"
    "v_cvt_pk_bf16_f32 v248, v248, v249\n\t"
    "v_exp_f32 v192, v192\n\t"
    "v_mfma_f32_16x16x32_bf16 v[12:15], a[200:203], v[208:211], v[12:15]\n\t"
    "v_accvgpr_write_b32 a43, v248\n\t"
    "s_waitcnt vmcnt(12)\n\t"
    "v_lshlrev_b32 v242, 16, v96\n\t"
    "v_and_b32 v243, 0xffff0000, v96\n\t"
    "v_exp_f32 v193, v193\n\t"
    "v_mfma_f32_16x16x32_bf16 v[16:19], a[172:175], v[204:207], 0\n\t"
    "v_mul_f32 v242, s65, v242\n\t"
    "v_mul_f32 v243, s65, v243\n\t"
    "v_cvt_pk_bf16_f32 v242, v242, v243\n\t"
    "v_cvt_pk_bf16_f32 v214, v192, v193\n\t"
    "v_add_f32 v250, v250, v182\n\t"
    "v_mfma_f32_16x16x32_bf16 v[16:19], a[204:207], v[208:211], v[16:19]\n\t"
    "v_accvgpr_write_b32 a44, v242\n\t"
    "v_lshlrev_b32 v244, 16, v97\n\t"
    "v_and_b32 v245, 0xffff0000, v97\n\t"
    "v_exp_f32 v194, v194\n\t"
    "v_mfma_f32_16x16x32_bf16 v[20:23], a[176:179], v[204:207], 0\n\t"
    "v_mul_f32 v244, s65, v244\n\t"
    "v_mul_f32 v245, s65, v245\n\t"
    "v_cvt_pk_bf16_f32 v244, v244, v245\n\t"
    "v_exp_f32 v195, v195\n\t"
    "v_mfma_f32_16x16x32_bf16 v[20:23], a[208:211], v[208:211], v[20:23]\n\t"
    "v_accvgpr_write_b32 a45, v244\n\t"
    "v_lshlrev_b32 v246, 16, v98\n\t"
    "v_and_b32 v247, 0xffff0000, v98\n\t"
    "v_cvt_pk_bf16_f32 v215, v194, v195\n\t"
    "v_add_f32 v250, v250, v183\n\t"
    "v_mfma_f32_16x16x32_bf16 v[24:27], a[180:183], v[204:207], 0\n\t"
    "v_mul_f32 v246, s65, v246\n\t"
    "v_mul_f32 v247, s65, v247\n\t"
    "v_cvt_pk_bf16_f32 v246, v246, v247\n\t"
    "v_mfma_f32_16x16x32_bf16 v[24:27], a[212:215], v[208:211], v[24:27]\n\t"
    "v_accvgpr_write_b32 a46, v246\n\t"
    "v_lshlrev_b32 v248, 16, v99\n\t"
    "v_and_b32 v249, 0xffff0000, v99\n\t"
    "v_exp_f32 v196, v196\n\t"
    "v_mfma_f32_16x16x32_bf16 v[28:31], a[184:187], v[204:207], 0\n\t"
    "v_mul_f32 v248, s65, v248\n\t"
    "v_mul_f32 v249, s65, v249\n\t"
    "v_cvt_pk_bf16_f32 v248, v248, v249\n\t"
    "v_accvgpr_write_b32 a47, v248\n\t"
    "v_exp_f32 v197, v197\n\t"
    "v_mfma_f32_16x16x32_bf16 v[28:31], a[216:219], v[208:211], v[28:31]\n\t"
    "s_waitcnt lgkmcnt(0)\n\t"
    "v_cvt_pk_bf16_f32 v216, v196, v197\n\t"
    "v_add_f32 v250, v250, v184\n\t"
    "v_mfma_f32_16x16x32_bf16 v[32:35], a[188:191], v[204:207], 0\n\t"
    "v_exp_f32 v198, v198\n\t"
    "v_mfma_f32_16x16x32_bf16 v[32:35], a[220:223], v[208:211], v[32:35]\n\t"
    "v_exp_f32 v199, v199\n\t"
    "v_mfma_f32_16x16x32_bf16 v[172:175], a[96:99], a[32:35], 0\n\t"
    "v_cvt_pk_bf16_f32 v217, v198, v199\n\t"
    "v_add_f32 v250, v250, v185\n\t"
    "v_mfma_f32_16x16x32_bf16 v[172:175], a[100:103], a[36:39], v[172:175]\n\t"
    "s_waitcnt vmcnt(11)\n\t"
    "v_lshlrev_b32 v242, 16, v100\n\t"
    "v_and_b32 v243, 0xffff0000, v100\n\t"
    "v_mul_f32 v242, s65, v242\n\t"
    "s_mov_b32 s64, 16384\n\t"
    "buffer_load_dwordx4 v[116:119], v240, s[36:39], s64 offen offset:0\n\t"
    "buffer_load_dwordx4 v[120:123], v240, s[36:39], s64 offen offset:64\n\t"
    "buffer_load_dwordx4 v[124:127], v240, s[36:39], s64 offen offset:128\n\t"
    "buffer_load_dwordx4 v[128:131], v240, s[36:39], s64 offen offset:192\n\t"
    "v_exp_f32 v200, v200\n\t"
    "v_mfma_f32_16x16x32_bf16 v[172:175], a[104:107], a[40:43], v[172:175]\n\t"
    "v_mul_f32 v243, s65, v243\n\t"
    "v_cvt_pk_bf16_f32 v242, v242, v243\n\t"
    "v_accvgpr_write_b32 a48, v242\n\t"
    "v_exp_f32 v220, v220\n\t"
    "v_mfma_f32_16x16x32_bf16 v[172:175], a[108:111], a[44:47], v[172:175]\n\t"
    "v_lshlrev_b32 v244, 16, v101\n\t"
    "v_and_b32 v245, 0xffff0000, v101\n\t"
    "v_mul_f32 v244, s65, v244\n\t"
    "v_exp_f32 v201, v201\n\t"
    "v_mfma_f32_16x16x32_bf16 v[176:179], a[112:115], a[32:35], 0\n\t"
    "v_mul_f32 v245, s65, v245\n\t"
    "v_cvt_pk_bf16_f32 v244, v244, v245\n\t"
    "v_accvgpr_write_b32 a49, v244\n\t"
    "v_exp_f32 v221, v221\n\t"
    "v_mfma_f32_16x16x32_bf16 v[176:179], a[116:119], a[36:39], v[176:179]\n\t"
    "v_lshlrev_b32 v246, 16, v102\n\t"
    "v_and_b32 v247, 0xffff0000, v102\n\t"
    "v_mul_f32 v246, s65, v246\n\t"
    "v_add_f32 v250, v250, v186\n\t"
    "v_add_f32 v250, v250, v187\n\t"
    "v_mfma_f32_16x16x32_bf16 v[176:179], a[120:123], a[40:43], v[176:179]\n\t"
    "v_mul_f32 v247, s65, v247\n\t"
    "v_cvt_pk_bf16_f32 v246, v246, v247\n\t"
    "v_accvgpr_write_b32 a50, v246\n\t"
    "v_cvt_pk_bf16_f32 v218, v200, v201\n\t"
    "v_cvt_pk_bf16_f32 v224, v220, v221\n\t"
    "v_mfma_f32_16x16x32_bf16 v[176:179], a[124:127], a[44:47], v[176:179]\n\t"
    "v_lshlrev_b32 v248, 16, v103\n\t"
    "v_and_b32 v249, 0xffff0000, v103\n\t"
    "v_mul_f32 v248, s65, v248\n\t"
    "v_exp_f32 v202, v202\n\t"
    "v_mfma_f32_16x16x32_bf16 v[180:183], a[128:131], a[32:35], 0\n\t"
    "v_mul_f32 v249, s65, v249\n\t"
    "v_cvt_pk_bf16_f32 v248, v248, v249\n\t"
    "v_accvgpr_write_b32 a51, v248\n\t"
    "v_exp_f32 v222, v222\n\t"
    "v_mfma_f32_16x16x32_bf16 v[180:183], a[132:135], a[36:39], v[180:183]\n\t"
    "s_waitcnt vmcnt(14)\n\t"
    "v_lshlrev_b32 v242, 16, v104\n\t"
    "v_and_b32 v243, 0xffff0000, v104\n\t"
    "v_mul_f32 v242, s65, v242\n\t"
    "v_exp_f32 v203, v203\n\t"
    "v_mfma_f32_16x16x32_bf16 v[180:183], a[136:139], a[40:43], v[180:183]\n\t"
    "v_mul_f32 v243, s65, v243\n\t"
    "v_cvt_pk_bf16_f32 v242, v242, v243\n\t"
    "v_accvgpr_write_b32 a52, v242\n\t"
    "v_mfma_f32_16x16x32_bf16 v[180:183], a[140:143], a[44:47], v[180:183]\n\t"
    "v_lshlrev_b32 v244, 16, v105\n\t"
    "v_and_b32 v245, 0xffff0000, v105\n\t"
    "v_mul_f32 v244, s65, v244\n\t"
    "v_mul_f32 v245, s65, v245\n\t"
    "v_exp_f32 v223, v223\n\t"
    "v_mfma_f32_16x16x32_bf16 v[184:187], a[144:147], a[32:35], 0\n\t"
    "v_cvt_pk_bf16_f32 v244, v244, v245\n\t"
    "v_accvgpr_write_b32 a53, v244\n\t"
    "v_lshlrev_b32 v246, 16, v106\n\t"
    "v_cvt_pk_bf16_f32 v225, v222, v223\n\t"
    "v_cvt_pk_bf16_f32 v219, v202, v203\n\t"
    "v_mfma_f32_16x16x32_bf16 v[184:187], a[148:151], a[36:39], v[184:187]\n\t"
    "v_and_b32 v247, 0xffff0000, v106\n\t"
    "v_mul_f32 v246, s65, v246\n\t"
    "v_mul_f32 v247, s65, v247\n\t"
    "ds_write_b64 v238, v[224:225] offset:0\n\t"
    "v_mfma_f32_16x16x32_bf16 v[184:187], a[152:155], a[40:43], v[184:187]\n\t"
    "v_cvt_pk_bf16_f32 v246, v246, v247\n\t"
    "v_accvgpr_write_b32 a54, v246\n\t"
    "v_lshlrev_b32 v248, 16, v107\n\t"
    "v_exp_f32 v172, v172\n\t"
    "v_mfma_f32_16x16x32_bf16 v[184:187], a[156:159], a[44:47], v[184:187]\n\t"
    "v_and_b32 v249, 0xffff0000, v107\n\t"
    "v_mul_f32 v248, s65, v248\n\t"
    "v_mul_f32 v249, s65, v249\n\t"
    "v_exp_f32 v173, v173\n\t"
    "v_mfma_f32_16x16x16_bf16 a[244:247], a[248:249], v[224:225], 0\n\t"
    "v_cvt_pk_bf16_f32 v248, v248, v249\n\t"
    "v_accvgpr_write_b32 a55, v248\n\t"
    "s_waitcnt vmcnt(13)\n\t"
    "v_lshlrev_b32 v242, 16, v108\n\t"
    "v_cvt_pk_bf16_f32 v204, v172, v173\n\t"
    "v_add_f32 v251, v251, v196\n\t"
    "v_mfma_f32_16x16x32_bf16 v[36:39], a[160:163], v[212:215], 0\n\t"
    "v_and_b32 v243, 0xffff0000, v108\n\t"
    "v_mul_f32 v242, s65, v242\n\t"
    "v_mul_f32 v243, s65, v243\n\t"
    "v_exp_f32 v174, v174\n\t"
    "v_mfma_f32_16x16x32_bf16 v[36:39], a[192:195], v[216:219], v[36:39]\n\t"
    "v_cvt_pk_bf16_f32 v242, v242, v243\n\t"
    "v_accvgpr_write_b32 a56, v242\n\t"
    "v_lshlrev_b32 v244, 16, v109\n\t"
    "v_exp_f32 v175, v175\n\t"
    "v_mfma_f32_16x16x32_bf16 v[40:43], a[164:167], v[212:215], 0\n\t"
    "v_and_b32 v245, 0xffff0000, v109\n\t"
    "v_mul_f32 v244, s65, v244\n\t"
    "v_mul_f32 v245, s65, v245\n\t"
    "v_cvt_pk_bf16_f32 v205, v174, v175\n\t"
    "v_add_f32 v251, v251, v197\n\t"
    "v_mfma_f32_16x16x32_bf16 v[40:43], a[196:199], v[216:219], v[40:43]\n\t"
    "v_cvt_pk_bf16_f32 v244, v244, v245\n\t"
    "v_accvgpr_write_b32 a57, v244\n\t"
    "v_lshlrev_b32 v246, 16, v110\n\t"
    "v_exp_f32 v176, v176\n\t"
    "v_mfma_f32_16x16x32_bf16 v[44:47], a[168:171], v[212:215], 0\n\t"
    "v_and_b32 v247, 0xffff0000, v110\n\t"
    "v_mul_f32 v246, s65, v246\n\t"
    "v_mul_f32 v247, s65, v247\n\t"
    "v_cvt_pk_bf16_f32 v246, v246, v247\n\t"
    "v_exp_f32 v177, v177\n\t"
    "v_mfma_f32_16x16x32_bf16 v[44:47], a[200:203], v[216:219], v[44:47]\n\t"
    "v_accvgpr_write_b32 a58, v246\n\t"
    "v_lshlrev_b32 v248, 16, v111\n\t"
    "v_and_b32 v249, 0xffff0000, v111\n\t"
    "v_cvt_pk_bf16_f32 v206, v176, v177\n\t"
    "v_add_f32 v251, v251, v198\n\t"
    "v_mfma_f32_16x16x32_bf16 v[48:51], a[172:175], v[212:215], 0\n\t"
    "v_mul_f32 v248, s65, v248\n\t"
    "v_mul_f32 v249, s65, v249\n\t"
    "v_cvt_pk_bf16_f32 v248, v248, v249\n\t"
    "v_exp_f32 v178, v178\n\t"
    "v_mfma_f32_16x16x32_bf16 v[48:51], a[204:207], v[216:219], v[48:51]\n\t"
    "v_accvgpr_write_b32 a59, v248\n\t"
    "s_waitcnt vmcnt(12)\n\t"
    "v_lshlrev_b32 v242, 16, v112\n\t"
    "v_and_b32 v243, 0xffff0000, v112\n\t"
    "v_exp_f32 v179, v179\n\t"
    "v_mfma_f32_16x16x32_bf16 v[52:55], a[176:179], v[212:215], 0\n\t"
    "v_mul_f32 v242, s65, v242\n\t"
    "v_mul_f32 v243, s65, v243\n\t"
    "v_cvt_pk_bf16_f32 v242, v242, v243\n\t"
    "v_cvt_pk_bf16_f32 v207, v178, v179\n\t"
    "v_add_f32 v251, v251, v199\n\t"
    "v_mfma_f32_16x16x32_bf16 v[52:55], a[208:211], v[216:219], v[52:55]\n\t"
    "v_accvgpr_write_b32 a60, v242\n\t"
    "v_lshlrev_b32 v244, 16, v113\n\t"
    "v_and_b32 v245, 0xffff0000, v113\n\t"
    "v_mfma_f32_16x16x32_bf16 v[56:59], a[180:183], v[212:215], 0\n\t"
    "v_mul_f32 v244, s65, v244\n\t"
    "v_mul_f32 v245, s65, v245\n\t"
    "v_cvt_pk_bf16_f32 v244, v244, v245\n\t"
    "v_exp_f32 v180, v180\n\t"
    "v_mfma_f32_16x16x32_bf16 v[56:59], a[212:215], v[216:219], v[56:59]\n\t"
    "v_accvgpr_write_b32 a61, v244\n\t"
    "v_lshlrev_b32 v246, 16, v114\n\t"
    "v_and_b32 v247, 0xffff0000, v114\n\t"
    "v_exp_f32 v181, v181\n\t"
    "v_mfma_f32_16x16x32_bf16 v[60:63], a[184:187], v[212:215], 0\n\t"
    "v_mul_f32 v246, s65, v246\n\t"
    "v_mul_f32 v247, s65, v247\n\t"
    "v_cvt_pk_bf16_f32 v246, v246, v247\n\t"
    "v_cvt_pk_bf16_f32 v208, v180, v181\n\t"
    "v_add_f32 v251, v251, v200\n\t"
    "v_mfma_f32_16x16x32_bf16 v[60:63], a[216:219], v[216:219], v[60:63]\n\t"
    "v_accvgpr_write_b32 a62, v246\n\t"
    "v_lshlrev_b32 v248, 16, v115\n\t"
    "v_and_b32 v249, 0xffff0000, v115\n\t"
    "v_exp_f32 v182, v182\n\t"
    "v_mfma_f32_16x16x32_bf16 v[64:67], a[188:191], v[212:215], 0\n\t"
    "v_mul_f32 v248, s65, v248\n\t"
    "v_mul_f32 v249, s65, v249\n\t"
    "v_cvt_pk_bf16_f32 v248, v248, v249\n\t"
    "v_accvgpr_write_b32 a63, v248\n\t"
    "v_exp_f32 v183, v183\n\t"
    "v_mfma_f32_16x16x32_bf16 v[64:67], a[220:223], v[216:219], v[64:67]\n\t"
    "v_cvt_pk_bf16_f32 v209, v182, v183\n\t"
    "v_add_f32 v251, v251, v201\n\t"
    "v_mfma_f32_16x16x32_bf16 a[228:231], a[248:251], v[212:215], 0\n\t"
    "v_exp_f32 v184, v184\n\t"
    "v_mfma_f32_16x16x32_bf16 v[188:191], a[96:99], a[48:51], 0\n\t"
    "v_exp_f32 v185, v185\n\t"
    "v_mfma_f32_16x16x32_bf16 v[188:191], a[100:103], a[52:55], v[188:191]\n\t"
    "s_waitcnt vmcnt(3)\n\t"
    "v_lshlrev_b32 v242, 16, v116\n\t"
    "v_and_b32 v243, 0xffff0000, v116\n\t"
    "v_mul_f32 v242, s65, v242\n\t"
    "v_cvt_pk_bf16_f32 v210, v184, v185\n\t"
    "v_add_f32 v251, v251, v202\n\t"
    "v_mfma_f32_16x16x32_bf16 v[188:191], a[104:107], a[56:59], v[188:191]\n\t"
    "v_mul_f32 v243, s65, v243\n\t"
    "v_cvt_pk_bf16_f32 v242, v242, v243\n\t"
    "v_accvgpr_write_b32 a64, v242\n\t"
    "s_add_u32 m0, s55, 33792\n\t"
    "s_add_u32 s61, s59, 32768\n\t"
    "s_and_b32 s61, s61, 0x7ffff\n\t"
    "buffer_load_dwordx4 v239, s[40:43], s61 offen offset:0 nt lds\n\t"
    "s_add_u32 m0, s55, 38016\n\t"
    "s_nop 0\n\t"
    "buffer_load_dwordx4 v226, s[40:43], s61 offen offset:0 nt lds\n\t"
    "s_add_u32 m0, s55, 42240\n\t"
    "s_nop 0\n\t"
    "buffer_load_dwordx4 v227, s[40:43], s61 offen offset:0 nt lds\n\t"
    "s_add_u32 m0, s55, 46464\n\t"
    "s_nop 0\n\t"
    "buffer_load_dwordx4 v3, s[40:43], s61 offen offset:0 nt lds\n\t"
    "v_exp_f32 v186, v186\n\t"
    "v_mfma_f32_16x16x32_bf16 v[188:191], a[108:111], a[60:63], v[188:191]\n\t"
    "v_lshlrev_b32 v244, 16, v117\n\t"
    "v_and_b32 v245, 0xffff0000, v117\n\t"
    "v_mul_f32 v244, s65, v244\n\t"
    "v_add_f32 v251, v251, v203\n\t"
    "v_mfma_f32_16x16x32_bf16 v[192:195], a[112:115], a[48:51], 0\n\t"
    "v_mul_f32 v245, s65, v245\n\t"
    "v_cvt_pk_bf16_f32 v244, v244, v245\n\t"
    "v_accvgpr_write_b32 a65, v244\n\t"
    "v_exp_f32 v187, v187\n\t"
    "v_mfma_f32_16x16x32_bf16 v[192:195], a[116:119], a[52:55], v[192:195]\n\t"
    "v_lshlrev_b32 v246, 16, v118\n\t"
    "v_and_b32 v247, 0xffff0000, v118\n\t"
    "v_mul_f32 v246, s65, v246\n\t"
    "v_mul_f32 v247, s65, v247\n\t"
    "v_cvt_pk_bf16_f32 v211, v186, v187\n\t"
    "v_mfma_f32_16x16x32_bf16 v[192:195], a[120:123], a[56:59], v[192:195]\n\t"
    "v_cvt_pk_bf16_f32 v246, v246, v247\n\t"
    "v_accvgpr_write_b32 a66, v246\n\t"
    "v_lshlrev_b32 v248, 16, v119\n\t"
    "v_add_f32 v252, v252, v180\n\t"
    "v_add_f32 v252, v252, v181\n\t"
    "v_mfma_f32_16x16x32_bf16 v[192:195], a[124:127], a[60:63], v[192:195]\n\t"
    "v_and_b32 v249, 0xffff0000, v119\n\t"
    "v_mul_f32 v248, s65, v248\n\t"
    "v_mul_f32 v249, s65, v249\n\t"
    "v_add_f32 v252, v252, v182\n\t"
    "v_add_f32 v252, v252, v183\n\t"
    "v_mfma_f32_16x16x32_bf16 v[196:199], a[128:131], a[48:51], 0\n\t"
    "v_cvt_pk_bf16_f32 v248, v248, v249\n\t"
    "v_accvgpr_write_b32 a67, v248\n\t"
    "s_waitcnt vmcnt(6)\n\t"
    "v_lshlrev_b32 v242, 16, v120\n\t"
    "v_mfma_f32_16x16x32_bf16 v[196:199], a[132:135], a[52:55], v[196:199]\n\t"
    "v_and_b32 v243, 0xffff0000, v120\n\t"
    "v_mul_f32 v242, s65, v242\n\t"
    "v_mul_f32 v243, s65, v243\n\t"
    "s_add_u32 m0, s56, 33792\n\t"
    "s_add_u32 s61, s59, 32768\n\t"
    "s_and_b32 s61, s61, 0x7ffff\n\t"
    "buffer_load_dwordx4 v239, s[44:47], s61 offen offset:0 lds\n\t"
    "s_add_u32 m0, s56, 38016\n\t"
    "s_nop 0\n\t"
    "buffer_load_dwordx4 v226, s[44:47], s61 offen offset:0 lds\n\t"
    "s_add_u32 m0, s56, 42240\n\t"
    "s_nop 0\n\t"
    "buffer_load_dwordx4 v227, s[44:47], s61 offen offset:0 lds\n\t"
    "s_add_u32 m0, s56, 46464\n\t"
    "s_nop 0\n\t"
    "buffer_load_dwordx4 v3, s[44:47], s61 offen offset:0 lds\n\t"
    "v_add_f32 v252, v252, v184\n\t"
    "v_add_f32 v252, v252, v185\n\t"
    "v_mfma_f32_16x16x32_bf16 v[196:199], a[136:139], a[56:59], v[196:199]\n\t"
    "v_cvt_pk_bf16_f32 v242, v242, v243\n\t"
    "v_accvgpr_write_b32 a68, v242\n\t"
    "v_lshlrev_b32 v244, 16, v121\n\t"
    "v_and_b32 v245, 0xffff0000, v121\n\t"
    "v_add_f32 v252, v252, v186\n\t"
    "v_add_f32 v252, v252, v187\n\t"
    "v_mfma_f32_16x16x32_bf16 v[196:199], a[140:143], a[60:63], v[196:199]\n\t"
    "v_mul_f32 v244, s65, v244\n\t"
    "v_mul_f32 v245, s65, v245\n\t"
    "v_cvt_pk_bf16_f32 v244, v244, v245\n\t"
    "v_exp_f32 v188, v188\n\t"
    "v_mfma_f32_16x16x32_bf16 v[200:203], a[144:147], a[48:51], 0\n\t"
    "v_accvgpr_write_b32 a69, v244\n\t"
    "v_lshlrev_b32 v246, 16, v122\n\t"
    "v_and_b32 v247, 0xffff0000, v122\n\t"
    "v_exp_f32 v189, v189\n\t"
    "v_mfma_f32_16x16x32_bf16 v[200:203], a[148:151], a[52:55], v[200:203]\n\t"
    "v_mul_f32 v246, s65, v246\n\t"
    "v_mul_f32 v247, s65, v247\n\t"
    "v_cvt_pk_bf16_f32 v246, v246, v247\n\t"
    "v_cvt_pk_bf16_f32 v212, v188, v189\n\t"
    "v_mfma_f32_16x16x32_bf16 v[200:203], a[152:155], a[56:59], v[200:203]\n\t"
    "v_accvgpr_write_b32 a70, v246\n\t"
    "v_lshlrev_b32 v248, 16, v123\n\t"
    "v_and_b32 v249, 0xffff0000, v123\n\t"
    "v_exp_f32 v190, v190\n\t"
    "v_mfma_f32_16x16x32_bf16 v[200:203], a[156:159], a[60:63], v[200:203]\n\t"
    "v_mul_f32 v248, s65, v248\n\t"
    "v_mul_f32 v249, s65, v249\n\t"
    "v_cvt_pk_bf16_f32 v248, v248, v249\n\t"
    "v_accvgpr_write_b32 a71, v248\n\t"
    "v_exp_f32 v191, v191\n\t"
    "v_mfma_f32_16x16x32_bf16 v[68:71], a[160:163], v[204:207], 0\n\t"
    "s_waitcnt vmcnt(9)\n\t"
    "v_lshlrev_b32 v242, 16, v124\n\t"
    "v_and_b32 v243, 0xffff0000, v124\n\t"
    "v_mul_f32 v242, s65, v242\n\t"
    "v_cvt_pk_bf16_f32 v213, v190, v191\n\t"
    "v_mfma_f32_16x16x32_bf16 v[68:71], a[192:195], v[208:211], v[68:71]\n\t"
    "v_mul_f32 v243, s65, v243\n\t"
    "v_cvt_pk_bf16_f32 v242, v242, v243\n\t"
    "v_accvgpr_write_b32 a72, v242\n\t"
    "v_exp_f32 v192, v192\n\t"
    "v_mfma_f32_16x16x32_bf16 v[72:75], a[164:167], v[204:207], 0\n\t"
    "v_lshlrev_b32 v244, 16, v125\n\t"
    "v_and_b32 v245, 0xffff0000, v125\n\t"
    "v_mul_f32 v244, s65, v244\n\t"
    "v_exp_f32 v193, v193\n\t"
    "v_mfma_f32_16x16x32_bf16 v[72:75], a[196:199], v[208:211], v[72:75]\n\t"
    "v_mul_f32 v245, s65, v245\n\t"
    "v_cvt_pk_bf16_f32 v244, v244, v245\n\t"
    "v_accvgpr_write_b32 a73, v244\n\t"
    "v_cvt_pk_bf16_f32 v214, v192, v193\n\t"
    "v_mfma_f32_16x16x32_bf16 v[76:79], a[168:171], v[204:207], 0\n\t"
    "v_lshlrev_b32 v246, 16, v126\n\t"
    "v_and_b32 v247, 0xffff0000, v126\n\t"
    "v_mul_f32 v246, s65, v246\n\t"
    "v_mul_f32 v247, s65, v247\n\t"
    "v_exp_f32 v194, v194\n\t"
    "v_mfma_f32_16x16x32_bf16 v[76:79], a[200:203], v[208:211], v[76:79]\n\t"
    "v_cvt_pk_bf16_f32 v246, v246, v247\n\t"
    "v_accvgpr_write_b32 a74, v246\n\t"
    "v_lshlrev_b32 v248, 16, v127\n\t"
    "v_exp_f32 v195, v195\n\t"
    "v_mfma_f32_16x16x32_bf16 v[80:83], a[172:175], v[204:207], 0\n\t"
    "v_and_b32 v249, 0xffff0000, v127\n\t"
    "v_mul_f32 v248, s65, v248\n\t"
    "v_mul_f32 v249, s65, v249\n\t"
    "v_cvt_pk_bf16_f32 v215, v194, v195\n\t"
    "v_mfma_f32_16x16x32_bf16 v[80:83], a[204:207], v[208:211], v[80:83]\n\t"
    "v_cvt_pk_bf16_f32 v248, v248, v249\n\t"
    "v_accvgpr_write_b32 a75, v248\n\t"
    "s_waitcnt vmcnt(8)\n\t"
    "v_lshlrev_b32 v242, 16, v128\n\t"
    "v_exp_f32 v196, v196\n\t"
    "v_mfma_f32_16x16x32_bf16 v[84:87], a[176:179], v[204:207], 0\n\t"
    "v_and_b32 v243, 0xffff0000, v128\n\t"
    "v_mul_f32 v242, s65, v242\n\t"
    "v_mul_f32 v243, s65, v243\n\t"
    "v_exp_f32 v197, v197\n\t"
    "v_mfma_f32_16x16x32_bf16 v[84:87], a[208:211], v[208:211], v[84:87]\n\t"
    "v_cvt_pk_bf16_f32 v242, v242, v243\n\t"
    "v_accvgpr_write_b32 a76, v242\n\t"
    "v_lshlrev_b32 v244, 16, v129\n\t"
    "v_and_b32 v245, 0xffff0000, v129\n\t"
    "v_cvt_pk_bf16_f32 v216, v196, v197\n\t"
    "v_add_f32 v253, v253, v196\n\t"
    "v_mfma_f32_16x16x32_bf16 v[88:91], a[180:183], v[204:207], 0\n\t"
    "v_mul_f32 v244, s65, v244\n\t"
    "v_mul_f32 v245, s65, v245\n\t"
    "v_cvt_pk_bf16_f32 v244, v244, v245\n\t"
    "v_exp_f32 v198, v198\n\t"
    "v_mfma_f32_16x16x32_bf16 v[88:91], a[212:215], v[208:211], v[88:91]\n\t"
    "v_accvgpr_write_b32 a77, v244\n\t"
    "v_lshlrev_b32 v246, 16, v130\n\t"
    "v_and_b32 v247, 0xffff0000, v130\n\t"
    "v_exp_f32 v199, v199\n\t"
    "v_mfma_f32_16x16x32_bf16 v[92:95], a[184:187], v[204:207], 0\n\t"
    "v_mul_f32 v246, s65, v246\n\t"
    "v_mul_f32 v247, s65, v247\n\t"
    "v_cvt_pk_bf16_f32 v246, v246, v247\n\t"
    "v_cvt_pk_bf16_f32 v217, v198, v199\n\t"
    "v_add_f32 v253, v253, v197\n\t"
    "v_mfma_f32_16x16x32_bf16 v[92:95], a[216:219], v[208:211], v[92:95]\n\t"
    "v_accvgpr_write_b32 a78, v246\n\t"
    "v_lshlrev_b32 v248, 16, v131\n\t"
    "v_and_b32 v249, 0xffff0000, v131\n\t"
    "v_exp_f32 v200, v200\n\t"
    "v_mfma_f32_16x16x32_bf16 v[96:99], a[188:191], v[204:207], 0\n\t"
    "v_mul_f32 v248, s65, v248\n\t"
    "v_mul_f32 v249, s65, v249\n\t"
    "v_cvt_pk_bf16_f32 v248, v248, v249\n\t"
    "v_accvgpr_write_b32 a79, v248\n\t"
    "v_exp_f32 v201, v201\n\t"
    "v_mfma_f32_16x16x32_bf16 v[96:99], a[220:223], v[208:211], v[96:99]\n\t"
    "s_add_u32 s54, s59, 65536\n\t"
    "s_and_b32 s54, s54, 0x7ffff\n\t"
    "s_mov_b32 s57, 33792\n\t"
    "v_cvt_pk_bf16_f32 v218, v200, v201\n\t"
    "v_add_f32 v253, v253, v198\n\t"
    "v_mfma_f32_16x16x32_bf16 a[232:235], a[248:251], v[204:207], 0\n\t"
    "s_waitcnt lgkmcnt(0)\n\t"
    "s_barrier\n\t"
    "v_mfma_f32_16x16x32_bf16 v[172:175], a[96:99], a[64:67], 0\n\t"
    "ds_read_b128 a[96:99], v236 offset:16896\n\t"
    "v_add_f32 v253, v253, v199\n\t"
    "v_mfma_f32_16x16x32_bf16 v[172:175], a[100:103], a[68:71], v[172:175]\n\t"
    "v_exp_f32 v202, v202\n\t"
    "v_mfma_f32_16x16x32_bf16 v[172:175], a[104:107], a[72:75], v[172:175]\n\t"
    "s_add_u32 m0, s55, 50688\n\t"
    "s_add_u32 s61, s59, 49152\n\t"
    "s_and_b32 s61, s61, 0x7ffff\n\t"
    "buffer_load_dwordx4 v239, s[40:43], s61 offen offset:0 nt lds\n\t"
    "s_add_u32 m0, s55, 54912\n\t"
    "s_nop 0\n\t"
    "buffer_load_dwordx4 v226, s[40:43], s61 offen offset:0 nt lds\n\t"
    "s_add_u32 m0, s55, 59136\n\t"
    "s_nop 0\n\t"
    "buffer_load_dwordx4 v227, s[40:43], s61 offen offset:0 nt lds\n\t"
    "s_add_u32 m0, s55, 63360\n\t"
    "s_nop 0\n\t"
    "buffer_load_dwordx4 v3, s[40:43], s61 offen offset:0 nt lds\n\t"
    "ds_read_b128 a[100:103], v236 offset:16960\n\t"
    "ds_read_b128 a[104:107], v236 offset:17024\n\t"
    "v_mfma_f32_16x16x32_bf16 v[172:175], a[108:111], a[76:79], v[172:175]\n\t"
    "v_exp_f32 v203, v203\n\t"
    "v_mfma_f32_16x16x32_bf16 v[176:179], a[112:115], a[64:67], 0\n\t"
    "ds_read_b128 a[108:111], v236 offset:17088\n\t"
    "ds_read_b128 a[112:115], v236 offset:17408\n\t"
    "v_mfma_f32_16x16x32_bf16 v[176:179], a[116:119], a[68:71], v[176:179]\n\t"
    "v_cvt_pk_bf16_f32 v219, v202, v203\n\t"
    "v_add_f32 v253, v253, v200\n\t"
    "v_mfma_f32_16x16x32_bf16 v[176:179], a[120:123], a[72:75], v[176:179]\n\t"
    "ds_read_b128 a[116:119], v236 offset:17472\n\t"
    "ds_read_b128 a[120:123], v236 offset:17536\n\t"
    "v_mfma_f32_16x16x32_bf16 v[176:179], a[124:127], a[76:79], v[176:179]\n\t"
    "v_add_f32 v253, v253, v201\n\t"
    "v_add_f32 v253, v253, v202\n\t"
    "v_mfma_f32_16x16x32_bf16 v[180:183], a[128:131], a[64:67], 0\n\t"
    "ds_read_b128 a[124:127], v236 offset:17600\n\t"
    "v_add_f32 v253, v253, v203\n\t"
    "v_mfma_f32_16x16x32_bf16 v[180:183], a[132:135], a[68:71], v[180:183]\n\t"
    "ds_read_b128 a[128:131], v236 offset:25344\n\t"
    "v_mfma_f32_16x16x32_bf16 v[180:183], a[136:139], a[72:75], v[180:183]\n\t"
    "ds_read_b128 a[132:135], v236 offset:25408\n\t"
    "v_mfma_f32_16x16x32_bf16 v[180:183], a[140:143], a[76:79], v[180:183]\n\t"
    "v_exp_f32 v172, v172\n\t"
    "v_mfma_f32_16x16x32_bf16 v[184:187], a[144:147], a[64:67], 0\n\t"
    "ds_read_b128 a[136:139], v236 offset:25472\n\t"
    "ds_read_b128 a[140:143], v236 offset:25536\n\t"
    "v_mfma_f32_16x16x32_bf16 v[184:187], a[148:151], a[68:71], v[184:187]\n\t"
    "v_exp_f32 v173, v173\n\t"
    "v_mfma_f32_16x16x32_bf16 v[184:187], a[152:155], a[72:75], v[184:187]\n\t"
    "ds_read_b128 a[144:147], v236 offset:25856\n\t"
    "ds_read_b128 a[148:151], v236 offset:25920\n\t"
    "v_mfma_f32_16x16x32_bf16 v[184:187], a[156:159], a[76:79], v[184:187]\n\t"
    "s_add_u32 m0, s56, 50688\n\t"
    "s_add_u32 s61, s59, 49152\n\t"
    "s_and_b32 s61, s61, 0x7ffff\n\t"
    "buffer_load_dwordx4 v239, s[44:47], s61 offen offset:0 lds\n\t"
    "s_add_u32 m0, s56, 54912\n\t"
    "s_nop 0\n\t"
    "buffer_load_dwordx4 v226, s[44:47], s61 offen offset:0 lds\n\t"
    "s_add_u32 m0, s56, 59136\n\t"
    "s_nop 0\n\t"
    "buffer_load_dwordx4 v227, s[44:47], s61 offen offset:0 lds\n\t"
    "s_add_u32 m0, s56, 63360\n\t"
    "s_nop 0\n\t"
    "buffer_load_dwordx4 v3, s[44:47], s61 offen offset:0 lds\n\t"
    "v_cvt_pk_bf16_f32 v204, v172, v173\n\t"
    "v_add_f32 v254, v254, v172\n\t"
    "v_mfma_f32_16x16x32_bf16 v[100:103], a[160:163], v[212:215], 0\n\t"
    "ds_read_b128 a[152:155], v236 offset:25984\n\t"
    "ds_read_b128 a[156:159], v236 offset:26048\n\t"
    "v_mfma_f32_16x16x32_bf16 v[100:103], a[192:195], v[216:219], v[100:103]\n\t"
    "v_exp_f32 v174, v174\n\t"
    "v_mfma_f32_16x16x32_bf16 v[104:107], a[164:167], v[212:215], 0\n\t"
    "ds_read_b128 v[228:231], v238 offset:0\n\t"
    "ds_read_b128 v[232:235], v238 offset:1024\n\t"
    "v_mfma_f32_16x16x32_bf16 v[104:107], a[196:199], v[216:219], v[104:107]\n\t"
    "v_exp_f32 v175, v175\n\t"
    "v_mfma_f32_16x16x32_bf16 v[108:111], a[168:171], v[212:215], 0\n\t"
    "v_cvt_pk_bf16_f32 v205, v174, v175\n\t"
    "v_add_f32 v254, v254, v173\n\t"
    "v_mfma_f32_16x16x32_bf16 v[108:111], a[200:203], v[216:219], v[108:111]\n\t"
    "v_exp_f32 v176, v176\n\t"
    "v_mfma_f32_16x16x32_bf16 v[112:115], a[172:175], v[212:215], 0\n\t"
    "v_exp_f32 v177, v177\n\t"
    "v_mfma_f32_16x16x32_bf16 v[112:115], a[204:207], v[216:219], v[112:115]\n\t"
    "v_cvt_pk_bf16_f32 v206, v176, v177\n\t"
    "v_add_f32 v254, v254, v174\n\t"
    "v_mfma_f32_16x16x32_bf16 v[116:119], a[176:179], v[212:215], 0\n\t"
    "v_exp_f32 v178, v178\n\t"
    "v_mfma_f32_16x16x32_bf16 v[116:119], a[208:211], v[216:219], v[116:119]\n\t"
    "v_exp_f32 v179, v179\n\t"
    "v_mfma_f32_16x16x32_bf16 v[120:123], a[180:183], v[212:215], 0\n\t"
    "v_cvt_pk_bf16_f32 v207, v178, v179\n\t"
    "v_add_f32 v254, v254, v175\n\t"
    "v_mfma_f32_16x16x32_bf16 v[120:123], a[212:215], v[216:219], v[120:123]\n\t"
    "v_exp_f32 v180, v180\n\t"
    "v_mfma_f32_16x16x32_bf16 v[124:127], a[184:187], v[212:215], 0\n\t"
    "v_exp_f32 v181, v181\n\t"
    "v_mfma_f32_16x16x32_bf16 v[124:127], a[216:219], v[216:219], v[124:127]\n\t"
    "v_cvt_pk_bf16_f32 v208, v180, v181\n\t"
    "v_add_f32 v254, v254, v176\n\t"
    "v_mfma_f32_16x16x32_bf16 v[128:131], a[188:191], v[212:215], 0\n\t"
    "v_exp_f32 v182, v182\n\t"
    "v_mfma_f32_16x16x32_bf16 v[128:131], a[220:223], v[216:219], v[128:131]\n\t"
    "v_exp_f32 v183, v183\n\t"
    "v_mfma_f32_16x16x32_bf16 a[236:239], a[248:251], v[212:215], 0\n\t"
    "s_waitcnt lgkmcnt(0)\n\t"
    "v_cvt_pk_bf16_f32 v209, v182, v183\n\t"
    "v_add_f32 v254, v254, v177\n\t"
    "LOOP0_%=:\n\t"
    "s_cmp_lg_u32 s60, 0\n\t"
    "s_cbranch_scc1 NOPF0_%=\n\t"
    "s_mov_b32 s42, 0\n\t"
    "s_mov_b32 s46, 0\n\t"
    "s_mov_b32 s61, TAIL0_%=-CPF0_%=\n\t"
    "v_lshlrev_b32 v2, 7, v1\n\t"
    "buffer_load_dword v255, v2, s[96:99], s61 offen\n\t"
    "NOPF0_%=:\n\t"
    "v_mfma_f32_16x16x32_bf16 v[188:191], a[96:99], a[0:3], 0\n\t"
    "v_exp_f32 v184, v184\n\t"
    "v_mfma_f32_16x16x32_bf16 v[188:191], a[100:103], a[4:7], v[188:191]\n\t"
    "s_add_u32 m0, s55, 0\n\t"
    "v_exp_f32 v185, v185\n\t"
    "v_mfma_f32_16x16x32_bf16 v[188:191], a[104:107], a[8:11], v[188:191]\n\t"
    "buffer_load_dwordx4 v239, s[40:43], s54 offen offset:0 nt lds\n\t"
    "v_mfma_f32_16x16x32_bf16 v[188:191], a[108:111], a[12:15], v[188:191]\n\t"
    "v_cvt_pk_bf16_f32 v210, v184, v185\n\t"
    "v_add_f32 v254, v254, v178\n\t"
    "v_mfma_f32_16x16x32_bf16 v[192:195], a[112:115], a[0:3], 0\n\t"
    "v_exp_f32 v186, v186\n\t"
    "v_mfma_f32_16x16x32_bf16 v[192:195], a[116:119], a[4:7], v[192:195]\n\t"
    "v_exp_f32 v187, v187\n\t"
    "v_mfma_f32_16x16x32_bf16 v[192:195], a[120:123], a[8:11], v[192:195]\n\t"
    "v_cvt_pk_bf16_f32 v211, v186, v187\n\t"
    "v_add_f32 v254, v254, v179\n\t"
    "v_mfma_f32_16x16x32_bf16 v[192:195], a[124:127], a[12:15], v[192:195]\n\t"
    "v_add_f32 v254, v254, v180\n\t"
    "v_add_f32 v254, v254, v181\n\t"
    "v_mfma_f32_16x16x32_bf16 v[196:199], a[128:131], a[0:3], 0\n\t"
    "v_add_f32 v254, v254, v182\n\t"
    "v_add_f32 v254, v254, v183\n\t"
    "v_mfma_f32_16x16x32_bf16 v[196:199], a[132:135], a[4:7], v[196:199]\n\t"
    "v_add_f32 v254, v254, v184\n\t"
    "v_add_f32 v254, v254, v185\n\t"
    "v_mfma_f32_16x16x32_bf16 v[196:199], a[136:139], a[8:11], v[196:199]\n\t"
    "v_add_f32 v254, v254, v186\n\t"
    "v_add_f32 v254, v254, v187\n\t"
    "v_mfma_f32_16x16x32_bf16 v[196:199], a[140:143], a[12:15], v[196:199]\n\t"
    "v_exp_f32 v188, v188\n\t"
    "v_mfma_f32_16x16x32_bf16 v[200:203], a[144:147], a[0:3], 0\n\t"
    "v_exp_f32 v189, v189\n\t"
    "v_mfma_f32_16x16x32_bf16 v[200:203], a[148:151], a[4:7], v[200:203]\n\t"
    "v_cvt_pk_bf16_f32 v212, v188, v189\n\t"
    "v_add_f32 v250, v250, v188\n\t"
    "v_mfma_f32_16x16x32_bf16 v[200:203], a[152:155], a[8:11], v[200:203]\n\t"
    "v_exp_f32 v190, v190\n\t"
    "v_mfma_f32_16x16x32_bf16 v[200:203], a[156:159], a[12:15], v[200:203]\n\t"
    "v_exp_f32 v191, v191\n\t"
    "v_mfma_f32_16x16x32_bf16 v[164:167], a[160:163], v[228:231], v[164:167]\n\t"
    "v_cvt_pk_bf16_f32 v213, v190, v191\n\t"
    "v_add_f32 v250, v250, v189\n\t"
    "v_mfma_f32_16x16x32_bf16 v[164:167], a[192:195], v[232:235], v[164:167]\n\t"
    "s_add_u32 m0, s56, 0\n\t"
    "v_exp_f32 v192, v192\n\t"
    "v_mfma_f32_16x16x32_bf16 v[168:171], a[164:167], v[228:231], v[168:171]\n\t"
    "buffer_load_dwordx4 v239, s[44:47], s54 offen offset:0 lds\n\t"
    "v_mfma_f32_16x16x32_bf16 v[168:171], a[196:199], v[232:235], v[168:171]\n\t"
    "v_exp_f32 v193, v193\n\t"
    "v_mfma_f32_16x16x32_bf16 v[132:135], a[160:163], v[204:207], v[132:135]\n\t"
    "ds_read_b64_tr_b16 a[160:161], v237 offset:16896\n\t"
    "ds_read_b64_tr_b16 a[162:163], v237 offset:17408\n\t"
    "v_mfma_f32_16x16x32_bf16 v[132:135], a[192:195], v[208:211], v[132:135]\n\t"
    "v_cvt_pk_bf16_f32 v214, v192, v193\n\t"
    "v_add_f32 v250, v250, v190\n\t"
    "v_mfma_f32_16x16x32_bf16 v[136:139], a[164:167], v[204:207], v[136:139]\n\t"
    "ds_read_b64_tr_b16 a[192:193], v237 offset:25344\n\t"
    "ds_read_b64_tr_b16 a[194:195], v237 offset:25856\n\t"
    "v_mfma_f32_16x16x32_bf16 v[136:139], a[196:199], v[208:211], v[136:139]\n\t"
    "v_exp_f32 v194, v194\n\t"
    "v_mfma_f32_16x16x32_bf16 v[140:143], a[168:171], v[204:207], v[140:143]\n\t"
    "ds_read_b64_tr_b16 a[164:165], v237 offset:16904\n\t"
    "ds_read_b64_tr_b16 a[166:167], v237 offset:17416\n\t"
    "v_mfma_f32_16x16x32_bf16 v[140:143], a[200:203], v[208:211], v[140:143]\n\t"
    "v_exp_f32 v195, v195\n\t"
    "v_mfma_f32_16x16x32_bf16 v[144:147], a[172:175], v[204:207], v[144:147]\n\t"
    "ds_read_b64_tr_b16 a[196:197], v237 offset:25352\n\t"
    "ds_read_b64_tr_b16 a[198:199], v237 offset:25864\n\t"
    "v_mfma_f32_16x16x32_bf16 v[144:147], a[204:207], v[208:211], v[144:147]\n\t"
    "v_cvt_pk_bf16_f32 v215, v194, v195\n\t"
    "v_add_f32 v250, v250, v191\n\t"
    "v_mfma_f32_16x16x32_bf16 v[148:151], a[176:179], v[204:207], v[148:151]\n\t"
    "ds_read_b64_tr_b16 a[168:169], v237 offset:16960\n\t"
    "ds_read_b64_tr_b16 a[170:171], v237 offset:17472\n\t"
    "v_mfma_f32_16x16x32_bf16 v[148:151], a[208:211], v[208:211], v[148:151]\n\t"
    "v_exp_f32 v196, v196\n\t"
    "v_mfma_f32_16x16x32_bf16 v[152:155], a[180:183], v[204:207], v[152:155]\n\t"
    "ds_read_b64_tr_b16 a[200:201], v237 offset:25408\n\t"
    "ds_read_b64_tr_b16 a[202:203], v237 offset:25920\n\t"
    "v_mfma_f32_16x16x32_bf16 v[152:155], a[212:215], v[208:211], v[152:155]\n\t"
    "v_exp_f32 v197, v197\n\t"
    "v_mfma_f32_16x16x32_bf16 v[156:159], a[184:187], v[204:207], v[156:159]\n\t"
    "ds_read_b64_tr_b16 a[172:173], v237 offset:16968\n\t"
    "ds_read_b64_tr_b16 a[174:175], v237 offset:17480\n\t"
    "v_mfma_f32_16x16x32_bf16 v[156:159], a[216:219], v[208:211], v[156:159]\n\t"
    "s_add_u32 m0, s55, 4224\n\t"
    "v_cvt_pk_bf16_f32 v216, v196, v197\n\t"
    "v_add_f32 v250, v250, v192\n\t"
    "v_mfma_f32_16x16x32_bf16 v[160:163], a[188:191], v[204:207], v[160:163]\n\t"
    "buffer_load_dwordx4 v226, s[40:43], s54 offen offset:0 nt lds\n\t"
    "v_mfma_f32_16x16x32_bf16 v[160:163], a[220:223], v[208:211], v[160:163]\n\t"
    "v_exp_f32 v198, v198\n\t"
    "v_mfma_f32_16x16x32_bf16 v[172:175], a[96:99], a[16:19], 0\n\t"
    "ds_read_b64_tr_b16 a[204:205], v237 offset:25416\n\t"
    "ds_read_b64_tr_b16 a[206:207], v237 offset:25928\n\t"
    "v_mfma_f32_16x16x32_bf16 v[172:175], a[100:103], a[20:23], v[172:175]\n\t"
    "v_exp_f32 v199, v199\n\t"
    "v_mfma_f32_16x16x32_bf16 v[172:175], a[104:107], a[24:27], v[172:175]\n\t"
    "ds_read_b64_tr_b16 a[176:177], v237 offset:17024\n\t"
    "ds_read_b64_tr_b16 a[178:179], v237 offset:17536\n\t"
    "v_mfma_f32_16x16x32_bf16 v[172:175], a[108:111], a[28:31], v[172:175]\n\t"
    "v_cvt_pk_bf16_f32 v217, v198, v199\n\t"
    "v_add_f32 v250, v250, v193\n\t"
    "v_mfma_f32_16x16x32_bf16 v[176:179], a[112:115], a[16:19], 0\n\t"
    "ds_read_b64_tr_b16 a[208:209], v237 offset:25472\n\t"
    "ds_read_b64_tr_b16 a[210:211], v237 offset:25984\n\t"
    "v_mfma_f32_16x16x32_bf16 v[176:179], a[116:119], a[20:23], v[176:179]\n\t"
    "v_exp_f32 v200, v200\n\t"
    "v_mfma_f32_16x16x32_bf16 v[176:179], a[120:123], a[24:27], v[176:179]\n\t"
    "ds_read_b64_tr_b16 a[180:181], v237 offset:17032\n\t"
    "ds_read_b64_tr_b16 a[182:183], v237 offset:17544\n\t"
    "v_mfma_f32_16x16x32_bf16 v[176:179], a[124:127], a[28:31], v[176:179]\n\t"
    "v_exp_f32 v201, v201\n\t"
    "v_mfma_f32_16x16x32_bf16 v[180:183], a[128:131], a[16:19], 0\n\t"
    "ds_read_b64_tr_b16 a[212:213], v237 offset:25480\n\t"
    "ds_read_b64_tr_b16 a[214:215], v237 offset:25992\n\t"
    "v_mfma_f32_16x16x32_bf16 v[180:183], a[132:135], a[20:23], v[180:183]\n\t"
    "v_cvt_pk_bf16_f32 v218, v200, v201\n\t"
    "v_add_f32 v250, v250, v194\n\t"
    "v_mfma_f32_16x16x32_bf16 v[180:183], a[136:139], a[24:27], v[180:183]\n\t"
    "ds_read_b64_tr_b16 a[184:185], v237 offset:17088\n\t"
    "ds_read_b64_tr_b16 a[186:187], v237 offset:17600\n\t"
    "v_mfma_f32_16x16x32_bf16 v[180:183], a[140:143], a[28:31], v[180:183]\n\t"
    "v_exp_f32 v202, v202\n\t"
    "v_mfma_f32_16x16x32_bf16 v[184:187], a[144:147], a[16:19], 0\n\t"
    "ds_read_b64_tr_b16 a[216:217], v237 offset:25536\n\t"
    "ds_read_b64_tr_b16 a[218:219], v237 offset:26048\n\t"
    "v_mfma_f32_16x16x32_bf16 v[184:187], a[148:151], a[20:23], v[184:187]\n\t"
    "v_exp_f32 v203, v203\n\t"
    "v_mfma_f32_16x16x32_bf16 v[184:187], a[152:155], a[24:27], v[184:187]\n\t"
    "ds_read_b64_tr_b16 a[188:189], v237 offset:17096\n\t"
    "ds_read_b64_tr_b16 a[190:191], v237 offset:17608\n\t"
    "v_mfma_f32_16x16x32_bf16 v[184:187], a[156:159], a[28:31], v[184:187]\n\t"
    "v_cvt_pk_bf16_f32 v219, v202, v203\n\t"
    "v_add_f32 v250, v250, v195\n\t"
    "v_mfma_f32_16x16x32_bf16 v[220:223], a[96:99], a[80:83], 0\n\t"
    "ds_read_b64_tr_b16 a[220:221], v237 offset:25544\n\t"
    "ds_read_b64_tr_b16 a[222:223], v237 offset:26056\n\t"
    "v_mfma_f32_16x16x32_bf16 v[220:223], a[100:103], a[84:87], v[220:223]\n\t"
    "s_add_u32 m0, s56, 4224\n\t"
    "v_exp_f32 v172, v172\n\t"
    "v_mfma_f32_16x16x32_bf16 v[220:223], a[104:107], a[88:91], v[220:223]\n\t"
    "buffer_load_dwordx4 v226, s[44:47], s54 offen offset:0 lds\n\t"
    "v_mfma_f32_16x16x32_bf16 v[220:223], a[108:111], a[92:95], v[220:223]\n\t"
    "s_waitcnt lgkmcnt(4)\n\t"
    "v_exp_f32 v173, v173\n\t"
    "v_mfma_f32_16x16x32_bf16 v[4:7], a[160:163], v[212:215], v[4:7]\n\t"
    "v_cvt_pk_bf16_f32 v204, v172, v173\n\t"
    "v_add_f32 v250, v250, v196\n\t"
    "v_mfma_f32_16x16x32_bf16 v[4:7], a[192:195], v[216:219], v[4:7]\n\t"
    "v_exp_f32 v174, v174\n\t"
    "v_mfma_f32_16x16x32_bf16 v[8:11], a[164:167], v[212:215], v[8:11]\n\t"
    "v_exp_f32 v175, v175\n\t"
    "v_mfma_f32_16x16x32_bf16 v[8:11], a[196:199], v[216:219], v[8:11]\n\t"
    "v_cvt_pk_bf16_f32 v205, v174, v175\n\t"
    "v_add_f32 v250, v250, v197\n\t"
    "v_mfma_f32_16x16x32_bf16 v[12:15], a[168:171], v[212:215], v[12:15]\n\t"
    "v_exp_f32 v176, v176\n\t"
    "v_mfma_f32_16x16x32_bf16 v[12:15], a[200:203], v[216:219], v[12:15]\n\t"
    "v_exp_f32 v177, v177\n\t"
    "v_mfma_f32_16x16x32_bf16 v[16:19], a[172:175], v[212:215], v[16:19]\n\t"
    "v_cvt_pk_bf16_f32 v206, v176, v177\n\t"
    "v_add_f32 v250, v250, v198\n\t"
    "v_mfma_f32_16x16x32_bf16 v[16:19], a[204:207], v[216:219], v[16:19]\n\t"
    "v_exp_f32 v178, v178\n\t"
    "v_mfma_f32_16x16x32_bf16 v[20:23], a[176:179], v[212:215], v[20:23]\n\t"
    "v_exp_f32 v179, v179\n\t"
    "v_mfma_f32_16x16x32_bf16 v[20:23], a[208:211], v[216:219], v[20:23]\n\t"
    "s_add_u32 m0, s55, 8448\n\t"
    "v_cvt_pk_bf16_f32 v207, v178, v179\n\t"
    "v_add_f32 v250, v250, v199\n\t"
    "v_mfma_f32_16x16x32_bf16 v[24:27], a[180:183], v[212:215], v[24:27]\n\t"
    "buffer_load_dwordx4 v227, s[40:43], s54 offen offset:0 nt lds\n\t"
    "v_mfma_f32_16x16x32_bf16 v[24:27], a[212:215], v[216:219], v[24:27]\n\t"
    "v_exp_f32 v180, v180\n\t"
    "v_mfma_f32_16x16x32_bf16 v[28:31], a[184:187], v[212:215], v[28:31]\n\t"
    "v_exp_f32 v181, v181\n\t"
    "v_mfma_f32_16x16x32_bf16 v[28:31], a[216:219], v[216:219], v[28:31]\n\t"
    "s_waitcnt lgkmcnt(0)\n\t"
    "v_cvt_pk_bf16_f32 v208, v180, v181\n\t"
    "v_add_f32 v250, v250, v200\n\t"
    "v_mfma_f32_16x16x32_bf16 v[32:35], a[188:191], v[212:215], v[32:35]\n\t"
    "v_exp_f32 v182, v182\n\t"
    "v_mfma_f32_16x16x32_bf16 v[32:35], a[220:223], v[216:219], v[32:35]\n\t"
    "v_exp_f32 v183, v183\n\t"
    "v_mfma_f32_16x16x32_bf16 v[188:191], a[96:99], a[32:35], 0\n\t"
    "v_cvt_pk_bf16_f32 v209, v182, v183\n\t"
    "v_add_f32 v250, v250, v201\n\t"
    "v_mfma_f32_16x16x32_bf16 v[188:191], a[100:103], a[36:39], v[188:191]\n\t"
    "v_exp_f32 v184, v184\n\t"
    "v_mfma_f32_16x16x32_bf16 v[188:191], a[104:107], a[40:43], v[188:191]\n\t"
    "v_exp_f32 v220, v220\n\t"
    "v_mfma_f32_16x16x32_bf16 v[188:191], a[108:111], a[44:47], v[188:191]\n\t"
    "v_exp_f32 v185, v185\n\t"
    "v_mfma_f32_16x16x32_bf16 v[192:195], a[112:115], a[32:35], 0\n\t"
    "v_exp_f32 v221, v221\n\t"
    "v_mfma_f32_16x16x32_bf16 v[192:195], a[116:119], a[36:39], v[192:195]\n\t"
    "v_add_f32 v250, v250, v202\n\t"
    "v_add_f32 v250, v250, v203\n\t"
    "v_mfma_f32_16x16x32_bf16 v[192:195], a[120:123], a[40:43], v[192:195]\n\t"
    "v_cvt_pk_bf16_f32 v210, v184, v185\n\t"
    "v_cvt_pk_bf16_f32 v224, v220, v221\n\t"
    "v_mfma_f32_16x16x32_bf16 v[192:195], a[124:127], a[44:47], v[192:195]\n\t"
    "v_exp_f32 v186, v186\n\t"
    "v_mfma_f32_16x16x32_bf16 v[196:199], a[128:131], a[32:35], 0\n\t"
    "v_exp_f32 v222, v222\n\t"
    "v_mfma_f32_16x16x32_bf16 v[196:199], a[132:135], a[36:39], v[196:199]\n\t"
    "s_add_u32 m0, s56, 8448\n\t"
    "v_exp_f32 v187, v187\n\t"
    "v_mfma_f32_16x16x32_bf16 v[196:199], a[136:139], a[40:43], v[196:199]\n\t"
    "buffer_load_dwordx4 v227, s[44:47], s54 offen offset:0 lds\n\t"
    "v_mfma_f32_16x16x32_bf16 v[196:199], a[140:143], a[44:47], v[196:199]\n\t"
    "v_exp_f32 v223, v223\n\t"
    "v_mfma_f32_16x16x32_bf16 v[200:203], a[144:147], a[32:35], 0\n\t"
    "v_cvt_pk_bf16_f32 v225, v222, v223\n\t"
    "v_cvt_pk_bf16_f32 v211, v186, v187\n\t"
    "v_mfma_f32_16x16x32_bf16 v[200:203], a[148:151], a[36:39], v[200:203]\n\t"
    "ds_write_b64 v238, v[224:225] offset:2048\n\t"
    "v_mfma_f32_16x16x32_bf16 v[200:203], a[152:155], a[40:43], v[200:203]\n\t"
    "v_exp_f32 v188, v188\n\t"
    "v_mfma_f32_16x16x32_bf16 v[200:203], a[156:159], a[44:47], v[200:203]\n\t"
    "v_exp_f32 v189, v189\n\t"
    "v_mfma_f32_16x16x16_bf16 a[244:247], a[248:249], v[224:225], a[244:247]\n\t"
    "v_cvt_pk_bf16_f32 v212, v188, v189\n\t"
    "v_add_f32 v251, v251, v180\n\t"
    "v_mfma_f32_16x16x32_bf16 v[36:39], a[160:163], v[204:207], v[36:39]\n\t"
    "v_exp_f32 v190, v190\n\t"
    "v_mfma_f32_16x16x32_bf16 v[36:39], a[192:195], v[208:211], v[36:39]\n\t"
    "v_exp_f32 v191, v191\n\t"
    "v_mfma_f32_16x16x32_bf16 v[40:43], a[164:167], v[204:207], v[40:43]\n\t"
    "v_cvt_pk_bf16_f32 v213, v190, v191\n\t"
    "v_add_f32 v251, v251, v181\n\t"
    "v_mfma_f32_16x16x32_bf16 v[40:43], a[196:199], v[208:211], v[40:43]\n\t"
    "v_exp_f32 v192, v192\n\t"
    "v_mfma_f32_16x16x32_bf16 v[44:47], a[168:171], v[204:207], v[44:47]\n\t"
    "v_exp_f32 v193, v193\n\t"
    "v_mfma_f32_16x16x32_bf16 v[44:47], a[200:203], v[208:211], v[44:47]\n\t"
    "v_cvt_pk_bf16_f32 v214, v192, v193\n\t"
    "v_add_f32 v251, v251, v182\n\t"
    "v_mfma_f32_16x16x32_bf16 v[48:51], a[172:175], v[204:207], v[48:51]\n\t"
    "v_exp_f32 v194, v194\n\t"
    "v_mfma_f32_16x16x32_bf16 v[48:51], a[204:207], v[208:211], v[48:51]\n\t"
    "v_exp_f32 v195, v195\n\t"
    "v_mfma_f32_16x16x32_bf16 v[52:55], a[176:179], v[204:207], v[52:55]\n\t"
    "s_add_u32 m0, s55, 12672\n\t"
    "v_cvt_pk_bf16_f32 v215, v194, v195\n\t"
    "v_add_f32 v251, v251, v183\n\t"
    "v_mfma_f32_16x16x32_bf16 v[52:55], a[208:211], v[208:211], v[52:55]\n\t"
    "buffer_load_dwordx4 v3, s[40:43], s54 offen offset:0 nt lds\n\t"
    "v_mfma_f32_16x16x32_bf16 v[56:59], a[180:183], v[204:207], v[56:59]\n\t"
    "v_exp_f32 v196, v196\n\t"
    "v_mfma_f32_16x16x32_bf16 v[56:59], a[212:215], v[208:211], v[56:59]\n\t"
    "v_exp_f32 v197, v197\n\t"
    "v_mfma_f32_16x16x32_bf16 v[60:63], a[184:187], v[204:207], v[60:63]\n\t"
    "v_cvt_pk_bf16_f32 v216, v196, v197\n\t"
    "v_add_f32 v251, v251, v184\n\t"
    "v_mfma_f32_16x16x32_bf16 v[60:63], a[216:219], v[208:211], v[60:63]\n\t"
    "v_exp_f32 v198, v198\n\t"
    "v_mfma_f32_16x16x32_bf16 v[64:67], a[188:191], v[204:207], v[64:67]\n\t"
    "v_exp_f32 v199, v199\n\t"
    "v_mfma_f32_16x16x32_bf16 v[64:67], a[220:223], v[208:211], v[64:67]\n\t"
    "v_cvt_pk_bf16_f32 v217, v198, v199\n\t"
    "v_add_f32 v251, v251, v185\n\t"
    "v_mfma_f32_16x16x32_bf16 a[228:231], a[248:251], v[204:207], a[228:231]\n\t"
    "v_exp_f32 v200, v200\n\t"
    "v_mfma_f32_16x16x32_bf16 v[172:175], a[96:99], a[48:51], 0\n\t"
    "v_exp_f32 v201, v201\n\t"
    "v_mfma_f32_16x16x32_bf16 v[172:175], a[100:103], a[52:55], v[172:175]\n\t"
    "v_cvt_pk_bf16_f32 v218, v200, v201\n\t"
    "v_add_f32 v251, v251, v186\n\t"
    "v_mfma_f32_16x16x32_bf16 v[172:175], a[104:107], a[56:59], v[172:175]\n\t"
    "v_exp_f32 v202, v202\n\t"
    "v_mfma_f32_16x16x32_bf16 v[172:175], a[108:111], a[60:63], v[172:175]\n\t"
    "v_add_f32 v251, v251, v187\n\t"
    "v_add_u32 v236, s58, v236\n\t"
    "v_mfma_f32_16x16x32_bf16 v[176:179], a[112:115], a[48:51], 0\n\t"
    "v_exp_f32 v203, v203\n\t"
    "v_mfma_f32_16x16x32_bf16 v[176:179], a[116:119], a[52:55], v[176:179]\n\t"
    "v_cvt_pk_bf16_f32 v219, v202, v203\n\t"
    "v_add_u32 v237, s58, v237\n\t"
    "v_mfma_f32_16x16x32_bf16 v[176:179], a[120:123], a[56:59], v[176:179]\n\t"
    "v_add_f32 v252, v252, v196\n\t"
    "v_add_f32 v252, v252, v197\n\t"
    "v_mfma_f32_16x16x32_bf16 v[176:179], a[124:127], a[60:63], v[176:179]\n\t"
    "s_add_u32 m0, s56, 12672\n\t"
    "v_add_f32 v252, v252, v198\n\t"
    "v_add_f32 v252, v252, v199\n\t"
    "v_mfma_f32_16x16x32_bf16 v[180:183], a[128:131], a[48:51], 0\n\t"
    "buffer_load_dwordx4 v3, s[44:47], s54 offen offset:0 lds\n\t"
    "v_mfma_f32_16x16x32_bf16 v[180:183], a[132:135], a[52:55], v[180:183]\n\t"
    "v_add_f32 v252, v252, v200\n\t"
    "v_add_f32 v252, v252, v201\n\t"
    "v_mfma_f32_16x16x32_bf16 v[180:183], a[136:139], a[56:59], v[180:183]\n\t"
    "v_add_f32 v252, v252, v202\n\t"
    "v_add_f32 v252, v252, v203\n\t"
    "v_mfma_f32_16x16x32_bf16 v[180:183], a[140:143], a[60:63], v[180:183]\n\t"
    "v_exp_f32 v172, v172\n\t"
    "v_mfma_f32_16x16x32_bf16 v[184:187], a[144:147], a[48:51], 0\n\t"
    "v_exp_f32 v173, v173\n\t"
    "v_mfma_f32_16x16x32_bf16 v[184:187], a[148:151], a[52:55], v[184:187]\n\t"
    "v_cvt_pk_bf16_f32 v204, v172, v173\n\t"
    "v_mfma_f32_16x16x32_bf16 v[184:187], a[152:155], a[56:59], v[184:187]\n\t"
    "v_exp_f32 v174, v174\n\t"
    "v_mfma_f32_16x16x32_bf16 v[184:187], a[156:159], a[60:63], v[184:187]\n\t"
    "v_exp_f32 v175, v175\n\t"
    "v_mfma_f32_16x16x32_bf16 v[68:71], a[160:163], v[212:215], v[68:71]\n\t"
    "v_cvt_pk_bf16_f32 v205, v174, v175\n\t"
    "v_mfma_f32_16x16x32_bf16 v[68:71], a[192:195], v[216:219], v[68:71]\n\t"
    "v_exp_f32 v176, v176\n\t"
    "v_mfma_f32_16x16x32_bf16 v[72:75], a[164:167], v[212:215], v[72:75]\n\t"
    "v_exp_f32 v177, v177\n\t"
    "v_mfma_f32_16x16x32_bf16 v[72:75], a[196:199], v[216:219], v[72:75]\n\t"
    "v_cvt_pk_bf16_f32 v206, v176, v177\n\t"
    "v_mfma_f32_16x16x32_bf16 v[76:79], a[168:171], v[212:215], v[76:79]\n\t"
    "v_exp_f32 v178, v178\n\t"
    "v_mfma_f32_16x16x32_bf16 v[76:79], a[200:203], v[216:219], v[76:79]\n\t"
    "v_exp_f32 v179, v179\n\t"
    "v_mfma_f32_16x16x32_bf16 v[80:83], a[172:175], v[212:215], v[80:83]\n\t"
    "v_cvt_pk_bf16_f32 v207, v178, v179\n\t"
    "v_mfma_f32_16x16x32_bf16 v[80:83], a[204:207], v[216:219], v[80:83]\n\t"
    "v_exp_f32 v180, v180\n\t"
    "v_mfma_f32_16x16x32_bf16 v[84:87], a[176:179], v[212:215], v[84:87]\n\t"
    "v_exp_f32 v181, v181\n\t"
    "v_mfma_f32_16x16x32_bf16 v[84:87], a[208:211], v[216:219], v[84:87]\n\t"
    "v_cvt_pk_bf16_f32 v208, v180, v181\n\t"
    "v_add_f32 v253, v253, v180\n\t"
    "v_mfma_f32_16x16x32_bf16 v[88:91], a[180:183], v[212:215], v[88:91]\n\t"
    "v_exp_f32 v182, v182\n\t"
    "v_mfma_f32_16x16x32_bf16 v[88:91], a[212:215], v[216:219], v[88:91]\n\t"
    "v_exp_f32 v183, v183\n\t"
    "v_mfma_f32_16x16x32_bf16 v[92:95], a[184:187], v[212:215], v[92:95]\n\t"
    "v_cvt_pk_bf16_f32 v209, v182, v183\n\t"
    "v_add_f32 v253, v253, v181\n\t"
    "v_mfma_f32_16x16x32_bf16 v[92:95], a[216:219], v[216:219], v[92:95]\n\t"
    "v_exp_f32 v184, v184\n\t"
    "v_mfma_f32_16x16x32_bf16 v[96:99], a[188:191], v[212:215], v[96:99]\n\t"
    "v_exp_f32 v185, v185\n\t"
    "v_mfma_f32_16x16x32_bf16 v[96:99], a[220:223], v[216:219], v[96:99]\n\t"
    "s_add_u32 s54, s54, 16384\n\t"
    "v_cvt_pk_bf16_f32 v210, v184, v185\n\t"
    "v_add_f32 v253, v253, v182\n\t"
    "v_mfma_f32_16x16x32_bf16 a[232:235], a[248:251], v[212:215], a[232:235]\n\t"
    "s_waitcnt vmcnt(16) lgkmcnt(0)\n\t"
    "s_barrier\n\t"
    "v_mfma_f32_16x16x32_bf16 v[188:191], a[96:99], a[64:67], 0\n\t"
    "ds_read_b128 a[96:99], v236 offset:0\n\t"
    "v_add_f32 v253, v253, v183\n\t"
    "v_mfma_f32_16x16x32_bf16 v[188:191], a[100:103], a[68:71], v[188:191]\n\t"
    "s_sub_u32 s58, 0, s58\n\t"
    "v_exp_f32 v186, v186\n\t"
    "v_mfma_f32_16x16x32_bf16 v[188:191], a[104:107], a[72:75], v[188:191]\n\t"
    "ds_read_b128 a[100:103], v236 offset:64\n\t"
    "ds_read_b128 a[104:107], v236 offset:128\n\t"
    "v_mfma_f32_16x16x32_bf16 v[188:191], a[108:111], a[76:79], v[188:191]\n\t"
    "s_and_b32 s54, s54, 0x7ffff\n\t"
    "v_exp_f32 v187, v187\n\t"
    "v_mfma_f32_16x16x32_bf16 v[192:195], a[112:115], a[64:67], 0\n\t"
    "ds_read_b128 a[108:111], v236 offset:192\n\t"
    "ds_read_b128 a[112:115], v236 offset:512\n\t"
    "v_mfma_f32_16x16x32_bf16 v[192:195], a[116:119], a[68:71], v[192:195]\n\t"
    "v_cvt_pk_bf16_f32 v211, v186, v187\n\t"
    "v_add_f32 v253, v253, v184\n\t"
    "v_mfma_f32_16x16x32_bf16 v[192:195], a[120:123], a[72:75], v[192:195]\n\t"
    "ds_read_b128 a[116:119], v236 offset:576\n\t"
    "ds_read_b128 a[120:123], v236 offset:640\n\t"
    "v_mfma_f32_16x16x32_bf16 v[192:195], a[124:127], a[76:79], v[192:195]\n\t"
    "v_add_f32 v253, v253, v185\n\t"
    "v_add_f32 v253, v253, v186\n\t"
    "v_mfma_f32_16x16x32_bf16 v[196:199], a[128:131], a[64:67], 0\n\t"
    "ds_read_b128 a[124:127], v236 offset:704\n\t"
    "v_add_f32 v253, v253, v187\n\t"
    "v_mfma_f32_16x16x32_bf16 v[196:199], a[132:135], a[68:71], v[196:199]\n\t"
    "ds_read_b128 a[128:131], v236 offset:8448\n\t"
    "v_mfma_f32_16x16x32_bf16 v[196:199], a[136:139], a[72:75], v[196:199]\n\t"
    "ds_read_b128 a[132:135], v236 offset:8512\n\t"
    "v_mfma_f32_16x16x32_bf16 v[196:199], a[140:143], a[76:79], v[196:199]\n\t"
    "v_exp_f32 v188, v188\n\t"
    "v_mfma_f32_16x16x32_bf16 v[200:203], a[144:147], a[64:67], 0\n\t"
    "ds_read_b128 a[136:139], v236 offset:8576\n\t"
    "ds_read_b128 a[140:143], v236 offset:8640\n\t"
    "v_mfma_f32_16x16x32_bf16 v[200:203], a[148:151], a[68:71], v[200:203]\n\t"
    "v_exp_f32 v189, v189\n\t"
    "v_mfma_f32_16x16x32_bf16 v[200:203], a[152:155], a[72:75], v[200:203]\n\t"
    "ds_read_b128 a[144:147], v236 offset:8960\n\t"
    "ds_read_b128 a[148:151], v236 offset:9024\n\t"
    "v_mfma_f32_16x16x32_bf16 v[200:203], a[156:159], a[76:79], v[200:203]\n\t"
    "v_cvt_pk_bf16_f32 v212, v188, v189\n\t"
    "v_add_f32 v254, v254, v188\n\t"
    "v_mfma_f32_16x16x32_bf16 v[100:103], a[160:163], v[204:207], v[100:103]\n\t"
    "ds_read_b128 a[152:155], v236 offset:9088\n\t"
    "ds_read_b128 a[156:159], v236 offset:9152\n\t"
    "v_mfma_f32_16x16x32_bf16 v[100:103], a[192:195], v[208:211], v[100:103]\n\t"
    "v_exp_f32 v190, v190\n\t"
    "v_mfma_f32_16x16x32_bf16 v[104:107], a[164:167], v[204:207], v[104:107]\n\t"
    "ds_read_b128 v[228:231], v238 offset:2048\n\t"
    "ds_read_b128 v[232:235], v238 offset:3072\n\t"
    "v_mfma_f32_16x16x32_bf16 v[104:107], a[196:199], v[208:211], v[104:107]\n\t"
    "v_exp_f32 v191, v191\n\t"
    "v_mfma_f32_16x16x32_bf16 v[108:111], a[168:171], v[204:207], v[108:111]\n\t"
    "v_cvt_pk_bf16_f32 v213, v190, v191\n\t"
    "v_add_f32 v254, v254, v189\n\t"
    "v_mfma_f32_16x16x32_bf16 v[108:111], a[200:203], v[208:211], v[108:111]\n\t"
    "v_exp_f32 v192, v192\n\t"
    "v_mfma_f32_16x16x32_bf16 v[112:115], a[172:175], v[204:207], v[112:115]\n\t"
    "v_exp_f32 v193, v193\n\t"
    "v_mfma_f32_16x16x32_bf16 v[112:115], a[204:207], v[208:211], v[112:115]\n\t"
    "v_cvt_pk_bf16_f32 v214, v192, v193\n\t"
    "v_add_f32 v254, v254, v190\n\t"
    "v_mfma_f32_16x16x32_bf16 v[116:119], a[176:179], v[204:207], v[116:119]\n\t"
    "v_exp_f32 v194, v194\n\t"
    "v_mfma_f32_16x16x32_bf16 v[116:119], a[208:211], v[208:211], v[116:119]\n\t"
    "v_exp_f32 v195, v195\n\t"
    "v_mfma_f32_16x16x32_bf16 v[120:123], a[180:183], v[204:207], v[120:123]\n\t"
    "v_cvt_pk_bf16_f32 v215, v194, v195\n\t"
    "v_add_f32 v254, v254, v191\n\t"
    "v_mfma_f32_16x16x32_bf16 v[120:123], a[212:215], v[208:211], v[120:123]\n\t"
    "v_exp_f32 v196, v196\n\t"
    "v_mfma_f32_16x16x32_bf16 v[124:127], a[184:187], v[204:207], v[124:127]\n\t"
    "v_exp_f32 v197, v197\n\t"
    "v_mfma_f32_16x16x32_bf16 v[124:127], a[216:219], v[208:211], v[124:127]\n\t"
    "v_cvt_pk_bf16_f32 v216, v196, v197\n\t"
    "v_add_f32 v254, v254, v192\n\t"
    "v_mfma_f32_16x16x32_bf16 v[128:131], a[188:191], v[204:207], v[128:131]\n\t"
    "v_exp_f32 v198, v198\n\t"
    "v_mfma_f32_16x16x32_bf16 v[128:131], a[220:223], v[208:211], v[128:131]\n\t"
    "v_exp_f32 v199, v199\n\t"
    "v_mfma_f32_16x16x32_bf16 a[236:239], a[248:251], v[204:207], a[236:239]\n\t"
    "s_waitcnt lgkmcnt(0)\n\t"
    "v_cvt_pk_bf16_f32 v217, v198, v199\n\t"
    "v_add_f32 v254, v254, v193\n\t"
    "v_mfma_f32_16x16x32_bf16 v[172:175], a[96:99], a[0:3], 0\n\t"
    "v_exp_f32 v200, v200\n\t"
    "v_mfma_f32_16x16x32_bf16 v[172:175], a[100:103], a[4:7], v[172:175]\n\t"
    "s_add_u32 m0, s55, 16896\n\t"
    "v_exp_f32 v201, v201\n\t"
    "v_mfma_f32_16x16x32_bf16 v[172:175], a[104:107], a[8:11], v[172:175]\n\t"
    "buffer_load_dwordx4 v239, s[40:43], s54 offen offset:0 nt lds\n\t"
    "v_mfma_f32_16x16x32_bf16 v[172:175], a[108:111], a[12:15], v[172:175]\n\t"
    "v_cvt_pk_bf16_f32 v218, v200, v201\n\t"
    "v_add_f32 v254, v254, v194\n\t"
    "v_mfma_f32_16x16x32_bf16 v[176:179], a[112:115], a[0:3], 0\n\t"
    "v_exp_f32 v202, v202\n\t"
    "v_mfma_f32_16x16x32_bf16 v[176:179], a[116:119], a[4:7], v[176:179]\n\t"
    "v_exp_f32 v203, v203\n\t"
    "v_mfma_f32_16x16x32_bf16 v[176:179], a[120:123], a[8:11], v[176:179]\n\t"
    "v_cvt_pk_bf16_f32 v219, v202, v203\n\t"
    "v_add_f32 v254, v254, v195\n\t"
    "v_mfma_f32_16x16x32_bf16 v[176:179], a[124:127], a[12:15], v[176:179]\n\t"
    "v_add_f32 v254, v254, v196\n\t"
    "v_add_f32 v254, v254, v197\n\t"
    "v_mfma_f32_16x16x32_bf16 v[180:183], a[128:131], a[0:3], 0\n\t"
    "v_add_f32 v254, v254, v198\n\t"
    "v_add_f32 v254, v254, v199\n\t"
    "v_mfma_f32_16x16x32_bf16 v[180:183], a[132:135], a[4:7], v[180:183]\n\t"
    "v_add_f32 v254, v254, v200\n\t"
    "v_add_f32 v254, v254, v201\n\t"
    "v_mfma_f32_16x16x32_bf16 v[180:183], a[136:139], a[8:11], v[180:183]\n\t"
    "v_add_f32 v254, v254, v202\n\t"
    "v_add_f32 v254, v254, v203\n\t"
    "v_mfma_f32_16x16x32_bf16 v[180:183], a[140:143], a[12:15], v[180:183]\n\t"
    "v_exp_f32 v172, v172\n\t"
    "v_mfma_f32_16x16x32_bf16 v[184:187], a[144:147], a[0:3], 0\n\t"
    "v_exp_f32 v173, v173\n\t"
    "v_mfma_f32_16x16x32_bf16 v[184:187], a[148:151], a[4:7], v[184:187]\n\t"
    "v_cvt_pk_bf16_f32 v204, v172, v173\n\t"
    "v_add_f32 v250, v250, v172\n\t"
    "v_mfma_f32_16x16x32_bf16 v[184:187], a[152:155], a[8:11], v[184:187]\n\t"
    "v_exp_f32 v174, v174\n\t"
    "v_mfma_f32_16x16x32_bf16 v[184:187], a[156:159], a[12:15], v[184:187]\n\t"
    "v_exp_f32 v175, v175\n\t"
    "v_mfma_f32_16x16x32_bf16 v[164:167], a[160:163], v[228:231], v[164:167]\n\t"
    "v_cvt_pk_bf16_f32 v205, v174, v175\n\t"
    "v_add_f32 v250, v250, v173\n\t"
    "v_mfma_f32_16x16x32_bf16 v[164:167], a[192:195], v[232:235], v[164:167]\n\t"
    "s_add_u32 m0, s56, 16896\n\t"
    "v_exp_f32 v176, v176\n\t"
    "v_mfma_f32_16x16x32_bf16 v[168:171], a[164:167], v[228:231], v[168:171]\n\t"
    "buffer_load_dwordx4 v239, s[44:47], s54 offen offset:0 lds\n\t"
    "v_mfma_f32_16x16x32_bf16 v[168:171], a[196:199], v[232:235], v[168:171]\n\t"
    "v_exp_f32 v177, v177\n\t"
    "v_mfma_f32_16x16x32_bf16 v[132:135], a[160:163], v[212:215], v[132:135]\n\t"
    "ds_read_b64_tr_b16 a[160:161], v237 offset:0\n\t"
    "ds_read_b64_tr_b16 a[162:163], v237 offset:512\n\t"
    "v_mfma_f32_16x16x32_bf16 v[132:135], a[192:195], v[216:219], v[132:135]\n\t"
    "v_cvt_pk_bf16_f32 v206, v176, v177\n\t"
    "v_add_f32 v250, v250, v174\n\t"
    "v_mfma_f32_16x16x32_bf16 v[136:139], a[164:167], v[212:215], v[136:139]\n\t"
    "ds_read_b64_tr_b16 a[192:193], v237 offset:8448\n\t"
    "ds_read_b64_tr_b16 a[194:195], v237 offset:8960\n\t"
    "v_mfma_f32_16x16x32_bf16 v[136:139], a[196:199], v[216:219], v[136:139]\n\t"
    "v_exp_f32 v178, v178\n\t"
    "v_mfma_f32_16x16x32_bf16 v[140:143], a[168:171], v[212:215], v[140:143]\n\t"
    "ds_read_b64_tr_b16 a[164:165], v237 offset:8\n\t"
    "ds_read_b64_tr_b16 a[166:167], v237 offset:520\n\t"
    "v_mfma_f32_16x16x32_bf16 v[140:143], a[200:203], v[216:219], v[140:143]\n\t"
    "v_exp_f32 v179, v179\n\t"
    "v_mfma_f32_16x16x32_bf16 v[144:147], a[172:175], v[212:215], v[144:147]\n\t"
    "ds_read_b64_tr_b16 a[196:197], v237 offset:8456\n\t"
    "ds_read_b64_tr_b16 a[198:199], v237 offset:8968\n\t"
    "v_mfma_f32_16x16x32_bf16 v[144:147], a[204:207], v[216:219], v[144:147]\n\t"
    "v_cvt_pk_bf16_f32 v207, v178, v179\n\t"
    "v_add_f32 v250, v250, v175\n\t"
    "v_mfma_f32_16x16x32_bf16 v[148:151], a[176:179], v[212:215], v[148:151]\n\t"
    "ds_read_b64_tr_b16 a[168:169], v237 offset:64\n\t"
    "ds_read_b64_tr_b16 a[170:171], v237 offset:576\n\t"
    "v_mfma_f32_16x16x32_bf16 v[148:151], a[208:211], v[216:219], v[148:151]\n\t"
    "v_exp_f32 v180, v180\n\t"
    "v_mfma_f32_16x16x32_bf16 v[152:155], a[180:183], v[212:215], v[152:155]\n\t"
    "ds_read_b64_tr_b16 a[200:201], v237 offset:8512\n\t"
    "ds_read_b64_tr_b16 a[202:203], v237 offset:9024\n\t"
    "v_mfma_f32_16x16x32_bf16 v[152:155], a[212:215], v[216:219], v[152:155]\n\t"
    "v_exp_f32 v181, v181\n\t"
    "v_mfma_f32_16x16x32_bf16 v[156:159], a[184:187], v[212:215], v[156:159]\n\t"
    "ds_read_b64_tr_b16 a[172:173], v237 offset:72\n\t"
    "ds_read_b64_tr_b16 a[174:175], v237 offset:584\n\t"
    "v_mfma_f32_16x16x32_bf16 v[156:159], a[216:219], v[216:219], v[156:159]\n\t"
    "s_add_u32 m0, s55, 21120\n\t"
    "v_cvt_pk_bf16_f32 v208, v180, v181\n\t"
    "v_add_f32 v250, v250, v176\n\t"
    "v_mfma_f32_16x16x32_bf16 v[160:163], a[188:191], v[212:215], v[160:163]\n\t"
    "buffer_load_dwordx4 v226, s[40:43], s54 offen offset:0 nt lds\n\t"
    "v_mfma_f32_16x16x32_bf16 v[160:163], a[220:223], v[216:219], v[160:163]\n\t"
    "v_exp_f32 v182, v182\n\t"
    "v_mfma_f32_16x16x32_bf16 v[188:191], a[96:99], a[16:19], 0\n\t"
    "ds_read_b64_tr_b16 a[204:205], v237 offset:8520\n\t"
    "ds_read_b64_tr_b16 a[206:207], v237 offset:9032\n\t"
    "v_mfma_f32_16x16x32_bf16 v[188:191], a[100:103], a[20:23], v[188:191]\n\t"
    "v_exp_f32 v183, v183\n\t"
    "v_mfma_f32_16x16x32_bf16 v[188:191], a[104:107], a[24:27], v[188:191]\n\t"
    "ds_read_b64_tr_b16 a[176:177], v237 offset:128\n\t"
    "ds_read_b64_tr_b16 a[178:179], v237 offset:640\n\t"
    "v_mfma_f32_16x16x32_bf16 v[188:191], a[108:111], a[28:31], v[188:191]\n\t"
    "v_cvt_pk_bf16_f32 v209, v182, v183\n\t"
    "v_add_f32 v250, v250, v177\n\t"
    "v_mfma_f32_16x16x32_bf16 v[192:195], a[112:115], a[16:19], 0\n\t"
    "ds_read_b64_tr_b16 a[208:209], v237 offset:8576\n\t"
    "ds_read_b64_tr_b16 a[210:211], v237 offset:9088\n\t"
    "v_mfma_f32_16x16x32_bf16 v[192:195], a[116:119], a[20:23], v[192:195]\n\t"
    "v_exp_f32 v184, v184\n\t"
    "v_mfma_f32_16x16x32_bf16 v[192:195], a[120:123], a[24:27], v[192:195]\n\t"
    "ds_read_b64_tr_b16 a[180:181], v237 offset:136\n\t"
    "ds_read_b64_tr_b16 a[182:183], v237 offset:648\n\t"
    "v_mfma_f32_16x16x32_bf16 v[192:195], a[124:127], a[28:31], v[192:195]\n\t"
    "v_exp_f32 v185, v185\n\t"
    "v_mfma_f32_16x16x32_bf16 v[196:199], a[128:131], a[16:19], 0\n\t"
    "ds_read_b64_tr_b16 a[212:213], v237 offset:8584\n\t"
    "ds_read_b64_tr_b16 a[214:215], v237 offset:9096\n\t"
    "v_mfma_f32_16x16x32_bf16 v[196:199], a[132:135], a[20:23], v[196:199]\n\t"
    "v_cvt_pk_bf16_f32 v210, v184, v185\n\t"
    "v_add_f32 v250, v250, v178\n\t"
    "v_mfma_f32_16x16x32_bf16 v[196:199], a[136:139], a[24:27], v[196:199]\n\t"
    "ds_read_b64_tr_b16 a[184:185], v237 offset:192\n\t"
    "ds_read_b64_tr_b16 a[186:187], v237 offset:704\n\t"
    "v_mfma_f32_16x16x32_bf16 v[196:199], a[140:143], a[28:31], v[196:199]\n\t"
    "v_exp_f32 v186, v186\n\t"
    "v_mfma_f32_16x16x32_bf16 v[200:203], a[144:147], a[16:19], 0\n\t"
    "ds_read_b64_tr_b16 a[216:217], v237 offset:8640\n\t"
    "ds_read_b64_tr_b16 a[218:219], v237 offset:9152\n\t"
    "v_mfma_f32_16x16x32_bf16 v[200:203], a[148:151], a[20:23], v[200:203]\n\t"
    "v_exp_f32 v187, v187\n\t"
    "v_mfma_f32_16x16x32_bf16 v[200:203], a[152:155], a[24:27], v[200:203]\n\t"
    "ds_read_b64_tr_b16 a[188:189], v237 offset:200\n\t"
    "ds_read_b64_tr_b16 a[190:191], v237 offset:712\n\t"
    "v_mfma_f32_16x16x32_bf16 v[200:203], a[156:159], a[28:31], v[200:203]\n\t"
    "v_cvt_pk_bf16_f32 v211, v186, v187\n\t"
    "v_add_f32 v250, v250, v179\n\t"
    "v_mfma_f32_16x16x32_bf16 v[220:223], a[96:99], a[80:83], 0\n\t"
    "ds_read_b64_tr_b16 a[220:221], v237 offset:8648\n\t"
    "ds_read_b64_tr_b16 a[222:223], v237 offset:9160\n\t"
    "v_mfma_f32_16x16x32_bf16 v[220:223], a[100:103], a[84:87], v[220:223]\n\t"
    "s_add_u32 m0, s56, 21120\n\t"
    "v_exp_f32 v188, v188\n\t"
    "v_mfma_f32_16x16x32_bf16 v[220:223], a[104:107], a[88:91], v[220:223]\n\t"
    "buffer_load_dwordx4 v226, s[44:47], s54 offen offset:0 lds\n\t"
    "v_mfma_f32_16x16x32_bf16 v[220:223], a[108:111], a[92:95], v[220:223]\n\t"
    "s_waitcnt lgkmcnt(4)\n\t"
    "v_exp_f32 v189, v189\n\t"
    "v_mfma_f32_16x16x32_bf16 v[4:7], a[160:163], v[204:207], v[4:7]\n\t"
    "v_cvt_pk_bf16_f32 v212, v188, v189\n\t"
    "v_add_f32 v250, v250, v180\n\t"
    "v_mfma_f32_16x16x32_bf16 v[4:7], a[192:195], v[208:211], v[4:7]\n\t"
    "v_exp_f32 v190, v190\n\t"
    "v_mfma_f32_16x16x32_bf16 v[8:11], a[164:167], v[204:207], v[8:11]\n\t"
    "v_exp_f32 v191, v191\n\t"
    "v_mfma_f32_16x16x32_bf16 v[8:11], a[196:199], v[208:211], v[8:11]\n\t"
    "v_cvt_pk_bf16_f32 v213, v190, v191\n\t"
    "v_add_f32 v250, v250, v181\n\t"
    "v_mfma_f32_16x16x32_bf16 v[12:15], a[168:171], v[204:207], v[12:15]\n\t"
    "v_exp_f32 v192, v192\n\t"
    "v_mfma_f32_16x16x32_bf16 v[12:15], a[200:203], v[208:211], v[12:15]\n\t"
    "v_exp_f32 v193, v193\n\t"
    "v_mfma_f32_16x16x32_bf16 v[16:19], a[172:175], v[204:207], v[16:19]\n\t"
    "v_cvt_pk_bf16_f32 v214, v192, v193\n\t"
    "v_add_f32 v250, v250, v182\n\t"
    "v_mfma_f32_16x16x32_bf16 v[16:19], a[204:207], v[208:211], v[16:19]\n\t"
    "v_exp_f32 v194, v194\n\t"
    "v_mfma_f32_16x16x32_bf16 v[20:23], a[176:179], v[204:207], v[20:23]\n\t"
    "v_exp_f32 v195, v195\n\t"
    "v_mfma_f32_16x16x32_bf16 v[20:23], a[208:211], v[208:211], v[20:23]\n\t"
    "s_add_u32 m0, s55, 25344\n\t"
    "v_cvt_pk_bf16_f32 v215, v194, v195\n\t"
    "v_add_f32 v250, v250, v183\n\t"
    "v_mfma_f32_16x16x32_bf16 v[24:27], a[180:183], v[204:207], v[24:27]\n\t"
    "buffer_load_dwordx4 v227, s[40:43], s54 offen offset:0 nt lds\n\t"
    "v_mfma_f32_16x16x32_bf16 v[24:27], a[212:215], v[208:211], v[24:27]\n\t"
    "v_exp_f32 v196, v196\n\t"
    "v_mfma_f32_16x16x32_bf16 v[28:31], a[184:187], v[204:207], v[28:31]\n\t"
    "v_exp_f32 v197, v197\n\t"
    "v_mfma_f32_16x16x32_bf16 v[28:31], a[216:219], v[208:211], v[28:31]\n\t"
    "s_waitcnt lgkmcnt(0)\n\t"
    "v_cvt_pk_bf16_f32 v216, v196, v197\n\t"
    "v_add_f32 v250, v250, v184\n\t"
    "v_mfma_f32_16x16x32_bf16 v[32:35], a[188:191], v[204:207], v[32:35]\n\t"
    "v_exp_f32 v198, v198\n\t"
    "v_mfma_f32_16x16x32_bf16 v[32:35], a[220:223], v[208:211], v[32:35]\n\t"
    "v_exp_f32 v199, v199\n\t"
    "v_mfma_f32_16x16x32_bf16 v[172:175], a[96:99], a[32:35], 0\n\t"
    "v_cvt_pk_bf16_f32 v217, v198, v199\n\t"
    "v_add_f32 v250, v250, v185\n\t"
    "v_mfma_f32_16x16x32_bf16 v[172:175], a[100:103], a[36:39], v[172:175]\n\t"
    "v_exp_f32 v200, v200\n\t"
    "v_mfma_f32_16x16x32_bf16 v[172:175], a[104:107], a[40:43], v[172:175]\n\t"
    "v_exp_f32 v220, v220\n\t"
    "v_mfma_f32_16x16x32_bf16 v[172:175], a[108:111], a[44:47], v[172:175]\n\t"
    "v_exp_f32 v201, v201\n\t"
    "v_mfma_f32_16x16x32_bf16 v[176:179], a[112:115], a[32:35], 0\n\t"
    "v_exp_f32 v221, v221\n\t"
    "v_mfma_f32_16x16x32_bf16 v[176:179], a[116:119], a[36:39], v[176:179]\n\t"
    "v_add_f32 v250, v250, v186\n\t"
    "v_add_f32 v250, v250, v187\n\t"
    "v_mfma_f32_16x16x32_bf16 v[176:179], a[120:123], a[40:43], v[176:179]\n\t"
    "v_cvt_pk_bf16_f32 v218, v200, v201\n\t"
    "v_cvt_pk_bf16_f32 v224, v220, v221\n\t"
    "v_mfma_f32_16x16x32_bf16 v[176:179], a[124:127], a[44:47], v[176:179]\n\t"
    "v_exp_f32 v202, v202\n\t"
    "v_mfma_f32_16x16x32_bf16 v[180:183], a[128:131], a[32:35], 0\n\t"
    "v_exp_f32 v222, v222\n\t"
    "v_mfma_f32_16x16x32_bf16 v[180:183], a[132:135], a[36:39], v[180:183]\n\t"
    "s_add_u32 m0, s56, 25344\n\t"
    "v_exp_f32 v203, v203\n\t"
    "v_mfma_f32_16x16x32_bf16 v[180:183], a[136:139], a[40:43], v[180:183]\n\t"
    "buffer_load_dwordx4 v227, s[44:47], s54 offen offset:0 lds\n\t"
    "v_mfma_f32_16x16x32_bf16 v[180:183], a[140:143], a[44:47], v[180:183]\n\t"
    "v_exp_f32 v223, v223\n\t"
    "v_mfma_f32_16x16x32_bf16 v[184:187], a[144:147], a[32:35], 0\n\t"
    "v_cvt_pk_bf16_f32 v225, v222, v223\n\t"
    "v_cvt_pk_bf16_f32 v219, v202, v203\n\t"
    "v_mfma_f32_16x16x32_bf16 v[184:187], a[148:151], a[36:39], v[184:187]\n\t"
    "ds_write_b64 v238, v[224:225] offset:0\n\t"
    "v_mfma_f32_16x16x32_bf16 v[184:187], a[152:155], a[40:43], v[184:187]\n\t"
    "v_exp_f32 v172, v172\n\t"
    "v_mfma_f32_16x16x32_bf16 v[184:187], a[156:159], a[44:47], v[184:187]\n\t"
    "v_exp_f32 v173, v173\n\t"
    "v_mfma_f32_16x16x16_bf16 a[244:247], a[248:249], v[224:225], a[244:247]\n\t"
    "v_cvt_pk_bf16_f32 v204, v172, v173\n\t"
    "v_add_f32 v251, v251, v196\n\t"
    "v_mfma_f32_16x16x32_bf16 v[36:39], a[160:163], v[212:215], v[36:39]\n\t"
    "v_exp_f32 v174, v174\n\t"
    "v_mfma_f32_16x16x32_bf16 v[36:39], a[192:195], v[216:219], v[36:39]\n\t"
    "v_exp_f32 v175, v175\n\t"
    "v_mfma_f32_16x16x32_bf16 v[40:43], a[164:167], v[212:215], v[40:43]\n\t"
    "v_cvt_pk_bf16_f32 v205, v174, v175\n\t"
    "v_add_f32 v251, v251, v197\n\t"
    "v_mfma_f32_16x16x32_bf16 v[40:43], a[196:199], v[216:219], v[40:43]\n\t"
    "v_exp_f32 v176, v176\n\t"
    "v_mfma_f32_16x16x32_bf16 v[44:47], a[168:171], v[212:215], v[44:47]\n\t"
    "v_exp_f32 v177, v177\n\t"
    "v_mfma_f32_16x16x32_bf16 v[44:47], a[200:203], v[216:219], v[44:47]\n\t"
    "v_cvt_pk_bf16_f32 v206, v176, v177\n\t"
    "v_add_f32 v251, v251, v198\n\t"
    "v_mfma_f32_16x16x32_bf16 v[48:51], a[172:175], v[212:215], v[48:51]\n\t"
    "v_exp_f32 v178, v178\n\t"
    "v_mfma_f32_16x16x32_bf16 v[48:51], a[204:207], v[216:219], v[48:51]\n\t"
    "v_exp_f32 v179, v179\n\t"
    "v_mfma_f32_16x16x32_bf16 v[52:55], a[176:179], v[212:215], v[52:55]\n\t"
    "s_add_u32 m0, s55, 29568\n\t"
    "v_cvt_pk_bf16_f32 v207, v178, v179\n\t"
    "v_add_f32 v251, v251, v199\n\t"
    "v_mfma_f32_16x16x32_bf16 v[52:55], a[208:211], v[216:219], v[52:55]\n\t"
    "buffer_load_dwordx4 v3, s[40:43], s54 offen offset:0 nt lds\n\t"
    "v_mfma_f32_16x16x32_bf16 v[56:59], a[180:183], v[212:215], v[56:59]\n\t"
    "v_exp_f32 v180, v180\n\t"
    "v_mfma_f32_16x16x32_bf16 v[56:59], a[212:215], v[216:219], v[56:59]\n\t"
    "v_exp_f32 v181, v181\n\t"
    "v_mfma_f32_16x16x32_bf16 v[60:63], a[184:187], v[212:215], v[60:63]\n\t"
    "v_cvt_pk_bf16_f32 v208, v180, v181\n\t"
    "v_add_f32 v251, v251, v200\n\t"
    "v_mfma_f32_16x16x32_bf16 v[60:63], a[216:219], v[216:219], v[60:63]\n\t"
    "v_exp_f32 v182, v182\n\t"
    "v_mfma_f32_16x16x32_bf16 v[64:67], a[188:191], v[212:215], v[64:67]\n\t"
    "v_exp_f32 v183, v183\n\t"
    "v_mfma_f32_16x16x32_bf16 v[64:67], a[220:223], v[216:219], v[64:67]\n\t"
    "v_cvt_pk_bf16_f32 v209, v182, v183\n\t"
    "v_add_f32 v251, v251, v201\n\t"
    "v_mfma_f32_16x16x32_bf16 a[228:231], a[248:251], v[212:215], a[228:231]\n\t"
    "v_exp_f32 v184, v184\n\t"
    "v_mfma_f32_16x16x32_bf16 v[188:191], a[96:99], a[48:51], 0\n\t"
    "v_exp_f32 v185, v185\n\t"
    "v_mfma_f32_16x16x32_bf16 v[188:191], a[100:103], a[52:55], v[188:191]\n\t"
    "v_cvt_pk_bf16_f32 v210, v184, v185\n\t"
    "v_add_f32 v251, v251, v202\n\t"
    "v_mfma_f32_16x16x32_bf16 v[188:191], a[104:107], a[56:59], v[188:191]\n\t"
    "v_exp_f32 v186, v186\n\t"
    "v_mfma_f32_16x16x32_bf16 v[188:191], a[108:111], a[60:63], v[188:191]\n\t"
    "v_add_f32 v251, v251, v203\n\t"
    "v_mfma_f32_16x16x32_bf16 v[192:195], a[112:115], a[48:51], 0\n\t"
    "v_exp_f32 v187, v187\n\t"
    "v_mfma_f32_16x16x32_bf16 v[192:195], a[116:119], a[52:55], v[192:195]\n\t"
    "v_cvt_pk_bf16_f32 v211, v186, v187\n\t"
    "v_mfma_f32_16x16x32_bf16 v[192:195], a[120:123], a[56:59], v[192:195]\n\t"
    "v_add_f32 v252, v252, v180\n\t"
    "v_add_f32 v252, v252, v181\n\t"
    "v_mfma_f32_16x16x32_bf16 v[192:195], a[124:127], a[60:63], v[192:195]\n\t"
    "s_add_u32 m0, s56, 29568\n\t"
    "v_add_f32 v252, v252, v182\n\t"
    "v_add_f32 v252, v252, v183\n\t"
    "v_mfma_f32_16x16x32_bf16 v[196:199], a[128:131], a[48:51], 0\n\t"
    "buffer_load_dwordx4 v3, s[44:47], s54 offen offset:0 lds\n\t"
    "v_mfma_f32_16x16x32_bf16 v[196:199], a[132:135], a[52:55], v[196:199]\n\t"
    "v_add_f32 v252, v252, v184\n\t"
    "v_add_f32 v252, v252, v185\n\t"
    "v_mfma_f32_16x16x32_bf16 v[196:199], a[136:139], a[56:59], v[196:199]\n\t"
    "v_add_f32 v252, v252, v186\n\t"
    "v_add_f32 v252, v252, v187\n\t"
    "v_mfma_f32_16x16x32_bf16 v[196:199], a[140:143], a[60:63], v[196:199]\n\t"
    "v_exp_f32 v188, v188\n\t"
    "v_mfma_f32_16x16x32_bf16 v[200:203], a[144:147], a[48:51], 0\n\t"
    "v_exp_f32 v189, v189\n\t"
    "v_mfma_f32_16x16x32_bf16 v[200:203], a[148:151], a[52:55], v[200:203]\n\t"
    "v_cvt_pk_bf16_f32 v212, v188, v189\n\t"
    "v_mfma_f32_16x16x32_bf16 v[200:203], a[152:155], a[56:59], v[200:203]\n\t"
    "v_exp_f32 v190, v190\n\t"
    "v_mfma_f32_16x16x32_bf16 v[200:203], a[156:159], a[60:63], v[200:203]\n\t"
    "v_exp_f32 v191, v191\n\t"
    "v_mfma_f32_16x16x32_bf16 v[68:71], a[160:163], v[204:207], v[68:71]\n\t"
    "v_cvt_pk_bf16_f32 v213, v190, v191\n\t"
    "v_mfma_f32_16x16x32_bf16 v[68:71], a[192:195], v[208:211], v[68:71]\n\t"
    "v_exp_f32 v192, v192\n\t"
    "v_mfma_f32_16x16x32_bf16 v[72:75], a[164:167], v[204:207], v[72:75]\n\t"
    "v_exp_f32 v193, v193\n\t"
    "v_mfma_f32_16x16x32_bf16 v[72:75], a[196:199], v[208:211], v[72:75]\n\t"
    "v_cvt_pk_bf16_f32 v214, v192, v193\n\t"
    "v_mfma_f32_16x16x32_bf16 v[76:79], a[168:171], v[204:207], v[76:79]\n\t"
    "v_exp_f32 v194, v194\n\t"
    "v_mfma_f32_16x16x32_bf16 v[76:79], a[200:203], v[208:211], v[76:79]\n\t"
    "v_exp_f32 v195, v195\n\t"
    "v_mfma_f32_16x16x32_bf16 v[80:83], a[172:175], v[204:207], v[80:83]\n\t"
    "v_cvt_pk_bf16_f32 v215, v194, v195\n\t"
    "v_mfma_f32_16x16x32_bf16 v[80:83], a[204:207], v[208:211], v[80:83]\n\t"
    "v_exp_f32 v196, v196\n\t"
    "v_mfma_f32_16x16x32_bf16 v[84:87], a[176:179], v[204:207], v[84:87]\n\t"
    "v_exp_f32 v197, v197\n\t"
    "v_mfma_f32_16x16x32_bf16 v[84:87], a[208:211], v[208:211], v[84:87]\n\t"
    "v_cvt_pk_bf16_f32 v216, v196, v197\n\t"
    "v_add_f32 v253, v253, v196\n\t"
    "v_mfma_f32_16x16x32_bf16 v[88:91], a[180:183], v[204:207], v[88:91]\n\t"
    "v_exp_f32 v198, v198\n\t"
    "v_mfma_f32_16x16x32_bf16 v[88:91], a[212:215], v[208:211], v[88:91]\n\t"
    "v_exp_f32 v199, v199\n\t"
    "v_mfma_f32_16x16x32_bf16 v[92:95], a[184:187], v[204:207], v[92:95]\n\t"
    "v_cvt_pk_bf16_f32 v217, v198, v199\n\t"
    "v_add_f32 v253, v253, v197\n\t"
    "v_mfma_f32_16x16x32_bf16 v[92:95], a[216:219], v[208:211], v[92:95]\n\t"
    "v_exp_f32 v200, v200\n\t"
    "v_mfma_f32_16x16x32_bf16 v[96:99], a[188:191], v[204:207], v[96:99]\n\t"
    "v_exp_f32 v201, v201\n\t"
    "v_mfma_f32_16x16x32_bf16 v[96:99], a[220:223], v[208:211], v[96:99]\n\t"
    "s_add_u32 s54, s54, 16384\n\t"
    "v_cvt_pk_bf16_f32 v218, v200, v201\n\t"
    "v_add_f32 v253, v253, v198\n\t"
    "v_mfma_f32_16x16x32_bf16 a[232:235], a[248:251], v[204:207], a[232:235]\n\t"
    "s_waitcnt vmcnt(16) lgkmcnt(0)\n\t"
    "s_barrier\n\t"
    "v_mfma_f32_16x16x32_bf16 v[172:175], a[96:99], a[64:67], 0\n\t"
    "ds_read_b128 a[96:99], v236 offset:16896\n\t"
    "v_add_f32 v253, v253, v199\n\t"
    "v_mfma_f32_16x16x32_bf16 v[172:175], a[100:103], a[68:71], v[172:175]\n\t"
    "v_exp_f32 v202, v202\n\t"
    "v_mfma_f32_16x16x32_bf16 v[172:175], a[104:107], a[72:75], v[172:175]\n\t"
    "ds_read_b128 a[100:103], v236 offset:16960\n\t"
    "ds_read_b128 a[104:107], v236 offset:17024\n\t"
    "v_mfma_f32_16x16x32_bf16 v[172:175], a[108:111], a[76:79], v[172:175]\n\t"
    "s_and_b32 s54, s54, 0x7ffff\n\t"
    "v_exp_f32 v203, v203\n\t"
    "v_mfma_f32_16x16x32_bf16 v[176:179], a[112:115], a[64:67], 0\n\t"
    "ds_read_b128 a[108:111], v236 offset:17088\n\t"
    "ds_read_b128 a[112:115], v236 offset:17408\n\t"
    "v_mfma_f32_16x16x32_bf16 v[176:179], a[116:119], a[68:71], v[176:179]\n\t"
    "v_cvt_pk_bf16_f32 v219, v202, v203\n\t"
    "v_add_f32 v253, v253, v200\n\t"
    "v_mfma_f32_16x16x32_bf16 v[176:179], a[120:123], a[72:75], v[176:179]\n\t"
    "ds_read_b128 a[116:119], v236 offset:17472\n\t"
    "ds_read_b128 a[120:123], v236 offset:17536\n\t"
    "v_mfma_f32_16x16x32_bf16 v[176:179], a[124:127], a[76:79], v[176:179]\n\t"
    "v_add_f32 v253, v253, v201\n\t"
    "v_add_f32 v253, v253, v202\n\t"
    "v_mfma_f32_16x16x32_bf16 v[180:183], a[128:131], a[64:67], 0\n\t"
    "ds_read_b128 a[124:127], v236 offset:17600\n\t"
    "v_add_f32 v253, v253, v203\n\t"
    "v_mfma_f32_16x16x32_bf16 v[180:183], a[132:135], a[68:71], v[180:183]\n\t"
    "ds_read_b128 a[128:131], v236 offset:25344\n\t"
    "v_mfma_f32_16x16x32_bf16 v[180:183], a[136:139], a[72:75], v[180:183]\n\t"
    "ds_read_b128 a[132:135], v236 offset:25408\n\t"
    "v_mfma_f32_16x16x32_bf16 v[180:183], a[140:143], a[76:79], v[180:183]\n\t"
    "s_add_u32 s55, s55, s57\n\t"
    "v_exp_f32 v172, v172\n\t"
    "v_mfma_f32_16x16x32_bf16 v[184:187], a[144:147], a[64:67], 0\n\t"
    "ds_read_b128 a[136:139], v236 offset:25472\n\t"
    "ds_read_b128 a[140:143], v236 offset:25536\n\t"
    "v_mfma_f32_16x16x32_bf16 v[184:187], a[148:151], a[68:71], v[184:187]\n\t"
    "s_add_u32 s56, s56, s57\n\t"
    "v_exp_f32 v173, v173\n\t"
    "v_mfma_f32_16x16x32_bf16 v[184:187], a[152:155], a[72:75], v[184:187]\n\t"
    "ds_read_b128 a[144:147], v236 offset:25856\n\t"
    "ds_read_b128 a[148:151], v236 offset:25920\n\t"
    "v_mfma_f32_16x16x32_bf16 v[184:187], a[156:159], a[76:79], v[184:187]\n\t"
    "s_sub_u32 s57, 0, s57\n\t"
    "v_cvt_pk_bf16_f32 v204, v172, v173\n\t"
    "v_add_f32 v254, v254, v172\n\t"
    "v_mfma_f32_16x16x32_bf16 v[100:103], a[160:163], v[212:215], v[100:103]\n\t"
    "ds_read_b128 a[152:155], v236 offset:25984\n\t"
    "ds_read_b128 a[156:159], v236 offset:26048\n\t"
    "v_mfma_f32_16x16x32_bf16 v[100:103], a[192:195], v[216:219], v[100:103]\n\t"
    "v_exp_f32 v174, v174\n\t"
    "v_mfma_f32_16x16x32_bf16 v[104:107], a[164:167], v[212:215], v[104:107]\n\t"
    "ds_read_b128 v[228:231], v238 offset:0\n\t"
    "ds_read_b128 v[232:235], v238 offset:1024\n\t"
    "v_mfma_f32_16x16x32_bf16 v[104:107], a[196:199], v[216:219], v[104:107]\n\t"
    "v_exp_f32 v175, v175\n\t"
    "v_mfma_f32_16x16x32_bf16 v[108:111], a[168:171], v[212:215], v[108:111]\n\t"
    "v_cvt_pk_bf16_f32 v205, v174, v175\n\t"
    "v_add_f32 v254, v254, v173\n\t"
    "v_mfma_f32_16x16x32_bf16 v[108:111], a[200:203], v[216:219], v[108:111]\n\t"
    "v_exp_f32 v176, v176\n\t"
    "v_mfma_f32_16x16x32_bf16 v[112:115], a[172:175], v[212:215], v[112:115]\n\t"
    "v_exp_f32 v177, v177\n\t"
    "v_mfma_f32_16x16x32_bf16 v[112:115], a[204:207], v[216:219], v[112:115]\n\t"
    "v_cvt_pk_bf16_f32 v206, v176, v177\n\t"
    "v_add_f32 v254, v254, v174\n\t"
    "v_mfma_f32_16x16x32_bf16 v[116:119], a[176:179], v[212:215], v[116:119]\n\t"
    "v_exp_f32 v178, v178\n\t"
    "v_mfma_f32_16x16x32_bf16 v[116:119], a[208:211], v[216:219], v[116:119]\n\t"
    "v_exp_f32 v179, v179\n\t"
    "v_mfma_f32_16x16x32_bf16 v[120:123], a[180:183], v[212:215], v[120:123]\n\t"
    "v_cvt_pk_bf16_f32 v207, v178, v179\n\t"
    "v_add_f32 v254, v254, v175\n\t"
    "v_mfma_f32_16x16x32_bf16 v[120:123], a[212:215], v[216:219], v[120:123]\n\t"
    "v_exp_f32 v180, v180\n\t"
    "v_mfma_f32_16x16x32_bf16 v[124:127], a[184:187], v[212:215], v[124:127]\n\t"
    "v_exp_f32 v181, v181\n\t"
    "v_mfma_f32_16x16x32_bf16 v[124:127], a[216:219], v[216:219], v[124:127]\n\t"
    "v_cvt_pk_bf16_f32 v208, v180, v181\n\t"
    "v_add_f32 v254, v254, v176\n\t"
    "v_mfma_f32_16x16x32_bf16 v[128:131], a[188:191], v[212:215], v[128:131]\n\t"
    "v_exp_f32 v182, v182\n\t"
    "v_mfma_f32_16x16x32_bf16 v[128:131], a[220:223], v[216:219], v[128:131]\n\t"
    "v_exp_f32 v183, v183\n\t"
    "v_mfma_f32_16x16x32_bf16 a[236:239], a[248:251], v[212:215], a[236:239]\n\t"
    "s_waitcnt lgkmcnt(0)\n\t"
    "v_cvt_pk_bf16_f32 v209, v182, v183\n\t"
    "v_add_f32 v254, v254, v177\n\t"
    "s_sub_u32 s60, s60, 1\n\t"
    "s_cbranch_scc0 LOOP0_%=\n\t"
    "TAIL0_%=:\n\t"
    "v_mfma_f32_16x16x32_bf16 v[188:191], a[96:99], a[0:3], 0\n\t"
    "v_exp_f32 v184, v184\n\t"
    "v_mfma_f32_16x16x32_bf16 v[188:191], a[100:103], a[4:7], v[188:191]\n\t"
    "v_exp_f32 v185, v185\n\t"
    "v_mfma_f32_16x16x32_bf16 v[188:191], a[104:107], a[8:11], v[188:191]\n\t"
    "v_mfma_f32_16x16x32_bf16 v[188:191], a[108:111], a[12:15], v[188:191]\n\t"
    "v_cvt_pk_bf16_f32 v210, v184, v185\n\t"
    "v_add_f32 v254, v254, v178\n\t"
    "v_mfma_f32_16x16x32_bf16 v[192:195], a[112:115], a[0:3], 0\n\t"
    "v_exp_f32 v186, v186\n\t"
    "v_mfma_f32_16x16x32_bf16 v[192:195], a[116:119], a[4:7], v[192:195]\n\t"
    "v_exp_f32 v187, v187\n\t"
    "v_mfma_f32_16x16x32_bf16 v[192:195], a[120:123], a[8:11], v[192:195]\n\t"
    "v_cvt_pk_bf16_f32 v211, v186, v187\n\t"
    "v_add_f32 v254, v254, v179\n\t"
    "v_mfma_f32_16x16x32_bf16 v[192:195], a[124:127], a[12:15], v[192:195]\n\t"
    "v_add_f32 v254, v254, v180\n\t"
    "v_add_f32 v254, v254, v181\n\t"
    "v_mfma_f32_16x16x32_bf16 v[196:199], a[128:131], a[0:3], 0\n\t"
    "v_add_f32 v254, v254, v182\n\t"
    "v_add_f32 v254, v254, v183\n\t"
    "v_mfma_f32_16x16x32_bf16 v[196:199], a[132:135], a[4:7], v[196:199]\n\t"
    "v_add_f32 v254, v254, v184\n\t"
    "v_add_f32 v254, v254, v185\n\t"
    "v_mfma_f32_16x16x32_bf16 v[196:199], a[136:139], a[8:11], v[196:199]\n\t"
    "v_add_f32 v254, v254, v186\n\t"
    "v_add_f32 v254, v254, v187\n\t"
    "v_mfma_f32_16x16x32_bf16 v[196:199], a[140:143], a[12:15], v[196:199]\n\t"
    "v_exp_f32 v188, v188\n\t"
    "v_mfma_f32_16x16x32_bf16 v[200:203], a[144:147], a[0:3], 0\n\t"
    "v_exp_f32 v189, v189\n\t"
    "v_mfma_f32_16x16x32_bf16 v[200:203], a[148:151], a[4:7], v[200:203]\n\t"
    "v_cvt_pk_bf16_f32 v212, v188, v189\n\t"
    "v_add_f32 v250, v250, v188\n\t"
    "v_mfma_f32_16x16x32_bf16 v[200:203], a[152:155], a[8:11], v[200:203]\n\t"
    "v_exp_f32 v190, v190\n\t"
    "v_mfma_f32_16x16x32_bf16 v[200:203], a[156:159], a[12:15], v[200:203]\n\t"
    "v_exp_f32 v191, v191\n\t"
    "v_mfma_f32_16x16x32_bf16 v[164:167], a[160:163], v[228:231], v[164:167]\n\t"
    "v_cvt_pk_bf16_f32 v213, v190, v191\n\t"
    "v_add_f32 v250, v250, v189\n\t"
    "v_mfma_f32_16x16x32_bf16 v[164:167], a[192:195], v[232:235], v[164:167]\n\t"
    "v_exp_f32 v192, v192\n\t"
    "v_mfma_f32_16x16x32_bf16 v[168:171], a[164:167], v[228:231], v[168:171]\n\t"
    "v_mfma_f32_16x16x32_bf16 v[168:171], a[196:199], v[232:235], v[168:171]\n\t"
    "v_exp_f32 v193, v193\n\t"
    "v_mfma_f32_16x16x32_bf16 v[132:135], a[160:163], v[204:207], v[132:135]\n\t"
    "ds_read_b64_tr_b16 a[160:161], v237 offset:16896\n\t"
    "ds_read_b64_tr_b16 a[162:163], v237 offset:17408\n\t"
    "v_accvgpr_write_b32 a0, 0\n\t"
    "v_mfma_f32_16x16x32_bf16 v[132:135], a[192:195], v[208:211], v[132:135]\n\t"
    "v_cvt_pk_bf16_f32 v214, v192, v193\n\t"
    "v_add_f32 v250, v250, v190\n\t"
    "v_accvgpr_write_b32 a1, 0\n\t"
    "v_mfma_f32_16x16x32_bf16 v[136:139], a[164:167], v[204:207], v[136:139]\n\t"
    "ds_read_b64_tr_b16 a[192:193], v237 offset:25344\n\t"
    "ds_read_b64_tr_b16 a[194:195], v237 offset:25856\n\t"
    "v_accvgpr_write_b32 a2, 0\n\t"
    "v_mfma_f32_16x16x32_bf16 v[136:139], a[196:199], v[208:211], v[136:139]\n\t"
    "v_exp_f32 v194, v194\n\t"
    "v_accvgpr_write_b32 a3, 0\n\t"
    "v_mfma_f32_16x16x32_bf16 v[140:143], a[168:171], v[204:207], v[140:143]\n\t"
    "ds_read_b64_tr_b16 a[164:165], v237 offset:16904\n\t"
    "ds_read_b64_tr_b16 a[166:167], v237 offset:17416\n\t"
    "v_accvgpr_write_b32 a4, 0\n\t"
    "v_mfma_f32_16x16x32_bf16 v[140:143], a[200:203], v[208:211], v[140:143]\n\t"
    "v_exp_f32 v195, v195\n\t"
    "v_mfma_f32_16x16x32_bf16 v[144:147], a[172:175], v[204:207], v[144:147]\n\t"
    "ds_read_b64_tr_b16 a[196:197], v237 offset:25352\n\t"
    "ds_read_b64_tr_b16 a[198:199], v237 offset:25864\n\t"
    "v_mfma_f32_16x16x32_bf16 v[144:147], a[204:207], v[208:211], v[144:147]\n\t"
    "v_cvt_pk_bf16_f32 v215, v194, v195\n\t"
    "v_add_f32 v250, v250, v191\n\t"
    "v_mfma_f32_16x16x32_bf16 v[148:151], a[176:179], v[204:207], v[148:151]\n\t"
    "ds_read_b64_tr_b16 a[168:169], v237 offset:16960\n\t"
    "ds_read_b64_tr_b16 a[170:171], v237 offset:17472\n\t"
    "v_mfma_f32_16x16x32_bf16 v[148:151], a[208:211], v[208:211], v[148:151]\n\t"
    "v_exp_f32 v196, v196\n\t"
    "v_mfma_f32_16x16x32_bf16 v[152:155], a[180:183], v[204:207], v[152:155]\n\t"
    "ds_read_b64_tr_b16 a[200:201], v237 offset:25408\n\t"
    "ds_read_b64_tr_b16 a[202:203], v237 offset:25920\n\t"
    "v_mfma_f32_16x16x32_bf16 v[152:155], a[212:215], v[208:211], v[152:155]\n\t"
    "v_exp_f32 v197, v197\n\t"
    "v_mfma_f32_16x16x32_bf16 v[156:159], a[184:187], v[204:207], v[156:159]\n\t"
    "ds_read_b64_tr_b16 a[172:173], v237 offset:16968\n\t"
    "ds_read_b64_tr_b16 a[174:175], v237 offset:17480\n\t"
    "v_mfma_f32_16x16x32_bf16 v[156:159], a[216:219], v[208:211], v[156:159]\n\t"
    "v_cvt_pk_bf16_f32 v216, v196, v197\n\t"
    "v_add_f32 v250, v250, v192\n\t"
    "v_mfma_f32_16x16x32_bf16 v[160:163], a[188:191], v[204:207], v[160:163]\n\t"
    "v_mfma_f32_16x16x32_bf16 v[160:163], a[220:223], v[208:211], v[160:163]\n\t"
    "v_exp_f32 v198, v198\n\t"
    "v_mfma_f32_16x16x32_bf16 v[172:175], a[96:99], a[16:19], 0\n\t"
    "ds_read_b64_tr_b16 a[204:205], v237 offset:25416\n\t"
    "ds_read_b64_tr_b16 a[206:207], v237 offset:25928\n\t"
    "v_mfma_f32_16x16x32_bf16 v[172:175], a[100:103], a[20:23], v[172:175]\n\t"
    "v_exp_f32 v199, v199\n\t"
    "v_mfma_f32_16x16x32_bf16 v[172:175], a[104:107], a[24:27], v[172:175]\n\t"
    "ds_read_b64_tr_b16 a[176:177], v237 offset:17024\n\t"
    "ds_read_b64_tr_b16 a[178:179], v237 offset:17536\n\t"
    "v_mfma_f32_16x16x32_bf16 v[172:175], a[108:111], a[28:31], v[172:175]\n\t"
    "v_cvt_pk_bf16_f32 v217, v198, v199\n\t"
    "v_add_f32 v250, v250, v193\n\t"
    "v_mfma_f32_16x16x32_bf16 v[176:179], a[112:115], a[16:19], 0\n\t"
    "ds_read_b64_tr_b16 a[208:209], v237 offset:25472\n\t"
    "ds_read_b64_tr_b16 a[210:211], v237 offset:25984\n\t"
    "v_mfma_f32_16x16x32_bf16 v[176:179], a[116:119], a[20:23], v[176:179]\n\t"
    "v_exp_f32 v200, v200\n\t"
    "v_mfma_f32_16x16x32_bf16 v[176:179], a[120:123], a[24:27], v[176:179]\n\t"
    "ds_read_b64_tr_b16 a[180:181], v237 offset:17032\n\t"
    "ds_read_b64_tr_b16 a[182:183], v237 offset:17544\n\t"
    "v_mfma_f32_16x16x32_bf16 v[176:179], a[124:127], a[28:31], v[176:179]\n\t"
    "v_exp_f32 v201, v201\n\t"
    "v_mfma_f32_16x16x32_bf16 v[180:183], a[128:131], a[16:19], 0\n\t"
    "ds_read_b64_tr_b16 a[212:213], v237 offset:25480\n\t"
    "ds_read_b64_tr_b16 a[214:215], v237 offset:25992\n\t"
    "v_mfma_f32_16x16x32_bf16 v[180:183], a[132:135], a[20:23], v[180:183]\n\t"
    "v_cvt_pk_bf16_f32 v218, v200, v201\n\t"
    "v_add_f32 v250, v250, v194\n\t"
    "v_mfma_f32_16x16x32_bf16 v[180:183], a[136:139], a[24:27], v[180:183]\n\t"
    "ds_read_b64_tr_b16 a[184:185], v237 offset:17088\n\t"
    "ds_read_b64_tr_b16 a[186:187], v237 offset:17600\n\t"
    "v_mfma_f32_16x16x32_bf16 v[180:183], a[140:143], a[28:31], v[180:183]\n\t"
    "v_exp_f32 v202, v202\n\t"
    "v_mfma_f32_16x16x32_bf16 v[184:187], a[144:147], a[16:19], 0\n\t"
    "ds_read_b64_tr_b16 a[216:217], v237 offset:25536\n\t"
    "ds_read_b64_tr_b16 a[218:219], v237 offset:26048\n\t"
    "v_mfma_f32_16x16x32_bf16 v[184:187], a[148:151], a[20:23], v[184:187]\n\t"
    "v_exp_f32 v203, v203\n\t"
    "v_mfma_f32_16x16x32_bf16 v[184:187], a[152:155], a[24:27], v[184:187]\n\t"
    "ds_read_b64_tr_b16 a[188:189], v237 offset:17096\n\t"
    "ds_read_b64_tr_b16 a[190:191], v237 offset:17608\n\t"
    "v_mfma_f32_16x16x32_bf16 v[184:187], a[156:159], a[28:31], v[184:187]\n\t"
    "v_cvt_pk_bf16_f32 v219, v202, v203\n\t"
    "v_add_f32 v250, v250, v195\n\t"
    "v_mfma_f32_16x16x32_bf16 v[220:223], a[96:99], a[80:83], 0\n\t"
    "ds_read_b64_tr_b16 a[220:221], v237 offset:25544\n\t"
    "ds_read_b64_tr_b16 a[222:223], v237 offset:26056\n\t"
    "v_mfma_f32_16x16x32_bf16 v[220:223], a[100:103], a[84:87], v[220:223]\n\t"
    "v_exp_f32 v172, v172\n\t"
    "v_mfma_f32_16x16x32_bf16 v[220:223], a[104:107], a[88:91], v[220:223]\n\t"
    "v_mfma_f32_16x16x32_bf16 v[220:223], a[108:111], a[92:95], v[220:223]\n\t"
    "s_waitcnt lgkmcnt(4)\n\t"
    "v_exp_f32 v173, v173\n\t"
    "v_mfma_f32_16x16x32_bf16 v[4:7], a[160:163], v[212:215], v[4:7]\n\t"
    "v_cvt_pk_bf16_f32 v204, v172, v173\n\t"
    "v_add_f32 v250, v250, v196\n\t"
    "v_mfma_f32_16x16x32_bf16 v[4:7], a[192:195], v[216:219], v[4:7]\n\t"
    "v_exp_f32 v174, v174\n\t"
    "v_mfma_f32_16x16x32_bf16 v[8:11], a[164:167], v[212:215], v[8:11]\n\t"
    "v_exp_f32 v175, v175\n\t"
    "v_mfma_f32_16x16x32_bf16 v[8:11], a[196:199], v[216:219], v[8:11]\n\t"
    "v_cvt_pk_bf16_f32 v205, v174, v175\n\t"
    "v_add_f32 v250, v250, v197\n\t"
    "v_mfma_f32_16x16x32_bf16 v[12:15], a[168:171], v[212:215], v[12:15]\n\t"
    "v_exp_f32 v176, v176\n\t"
    "v_mfma_f32_16x16x32_bf16 v[12:15], a[200:203], v[216:219], v[12:15]\n\t"
    "v_exp_f32 v177, v177\n\t"
    "v_mfma_f32_16x16x32_bf16 v[16:19], a[172:175], v[212:215], v[16:19]\n\t"
    "v_cvt_pk_bf16_f32 v206, v176, v177\n\t"
    "v_add_f32 v250, v250, v198\n\t"
    "v_mfma_f32_16x16x32_bf16 v[16:19], a[204:207], v[216:219], v[16:19]\n\t"
    "v_exp_f32 v178, v178\n\t"
    "v_mfma_f32_16x16x32_bf16 v[20:23], a[176:179], v[212:215], v[20:23]\n\t"
    "v_exp_f32 v179, v179\n\t"
    "v_mfma_f32_16x16x32_bf16 v[20:23], a[208:211], v[216:219], v[20:23]\n\t"
    "v_cvt_pk_bf16_f32 v207, v178, v179\n\t"
    "v_add_f32 v250, v250, v199\n\t"
    "v_mfma_f32_16x16x32_bf16 v[24:27], a[180:183], v[212:215], v[24:27]\n\t"
    "v_mfma_f32_16x16x32_bf16 v[24:27], a[212:215], v[216:219], v[24:27]\n\t"
    "v_exp_f32 v180, v180\n\t"
    "v_mfma_f32_16x16x32_bf16 v[28:31], a[184:187], v[212:215], v[28:31]\n\t"
    "v_exp_f32 v181, v181\n\t"
    "v_mfma_f32_16x16x32_bf16 v[28:31], a[216:219], v[216:219], v[28:31]\n\t"
    "s_waitcnt lgkmcnt(0)\n\t"
    "v_cvt_pk_bf16_f32 v208, v180, v181\n\t"
    "v_add_f32 v250, v250, v200\n\t"
    "v_mfma_f32_16x16x32_bf16 v[32:35], a[188:191], v[212:215], v[32:35]\n\t"
    "v_exp_f32 v182, v182\n\t"
    "v_mfma_f32_16x16x32_bf16 v[32:35], a[220:223], v[216:219], v[32:35]\n\t"
    "v_exp_f32 v183, v183\n\t"
    "v_mfma_f32_16x16x32_bf16 v[188:191], a[96:99], a[32:35], 0\n\t"
    "v_cvt_pk_bf16_f32 v209, v182, v183\n\t"
    "v_add_f32 v250, v250, v201\n\t"
    "v_mfma_f32_16x16x32_bf16 v[188:191], a[100:103], a[36:39], v[188:191]\n\t"
    "v_exp_f32 v184, v184\n\t"
    "v_mfma_f32_16x16x32_bf16 v[188:191], a[104:107], a[40:43], v[188:191]\n\t"
    "v_exp_f32 v220, v220\n\t"
    "v_mfma_f32_16x16x32_bf16 v[188:191], a[108:111], a[44:47], v[188:191]\n\t"
    "v_exp_f32 v185, v185\n\t"
    "v_mfma_f32_16x16x32_bf16 v[192:195], a[112:115], a[32:35], 0\n\t"
    "v_exp_f32 v221, v221\n\t"
    "v_mfma_f32_16x16x32_bf16 v[192:195], a[116:119], a[36:39], v[192:195]\n\t"
    "v_add_f32 v250, v250, v202\n\t"
    "v_add_f32 v250, v250, v203\n\t"
    "v_mfma_f32_16x16x32_bf16 v[192:195], a[120:123], a[40:43], v[192:195]\n\t"
    "v_cvt_pk_bf16_f32 v210, v184, v185\n\t"
    "v_cvt_pk_bf16_f32 v224, v220, v221\n\t"
    "v_mfma_f32_16x16x4_f32 a[224:227], a252, v250, 0\n\t"
    "v_mfma_f32_16x16x32_bf16 v[192:195], a[124:127], a[44:47], v[192:195]\n\t"
    "v_exp_f32 v186, v186\n\t"
    "v_mfma_f32_16x16x32_bf16 v[196:199], a[128:131], a[32:35], 0\n\t"
    "v_exp_f32 v222, v222\n\t"
    "v_mfma_f32_16x16x32_bf16 v[196:199], a[132:135], a[36:39], v[196:199]\n\t"
    "v_exp_f32 v187, v187\n\t"
    "v_mfma_f32_16x16x32_bf16 v[196:199], a[136:139], a[40:43], v[196:199]\n\t"
    "s_nop 2\n\t"
    "v_accvgpr_read_b32 v242, a224\n\t"
    "v_subrev_u32 v246, 0x2b800000, v242\n\t"
    "v_cmp_lt_u32 vcc, 0x28000000, v246\n\t"
    "s_nop 1\n\t"
    "s_or_b64 s[62:63], s[62:63], vcc\n\t"
    "v_rcp_f32 v243, v242\n\t"
    "s_mov_b32 s64, 0\n\t"
    "v_mul_f32 v4, v4, v243\n\t"
    "v_mfma_f32_16x16x32_bf16 v[196:199], a[140:143], a[44:47], v[196:199]\n\t"
    "v_exp_f32 v223, v223\n\t"
    "v_mul_f32 v5, v5, v243\n\t"
    "v_mul_f32 v6, v6, v243\n\t"
    "v_mul_f32 v7, v7, v243\n\t"
    "v_mul_f32 v8, v8, v243\n\t"
    "v_mfma_f32_16x16x32_bf16 v[200:203], a[144:147], a[32:35], 0\n\t"
    "v_cvt_pk_bf16_f32 v225, v222, v223\n\t"
    "v_cvt_pk_bf16_f32 v211, v186, v187\n\t"
    "v_mul_f32 v9, v9, v243\n\t"
    "v_mul_f32 v10, v10, v243\n\t"
    "v_mul_f32 v11, v11, v243\n\t"
    "v_cvt_pk_bf16_f32 v4, v4, v5\n\t"
    "v_mfma_f32_16x16x32_bf16 v[200:203], a[148:151], a[36:39], v[200:203]\n\t"
    "ds_write_b64 v238, v[224:225] offset:2048\n\t"
    "v_cvt_pk_bf16_f32 v5, v6, v7\n\t"
    "v_cvt_pk_bf16_f32 v6, v8, v9\n\t"
    "v_cvt_pk_bf16_f32 v7, v10, v11\n\t"
    "s_nop 1\n\t"
    "v_mfma_f32_16x16x32_bf16 a[4:7], a[0:3], v[4:7], a[4:7]\n\t"
    "v_mfma_f32_16x16x32_bf16 v[200:203], a[152:155], a[40:43], v[200:203]\n\t"
    "v_exp_f32 v188, v188\n\t"
    "v_mul_f32 v12, v12, v243\n\t"
    "v_mul_f32 v13, v13, v243\n\t"
    "v_mul_f32 v14, v14, v243\n\t"
    "v_mul_f32 v15, v15, v243\n\t"
    "v_mfma_f32_16x16x32_bf16 v[200:203], a[156:159], a[44:47], v[200:203]\n\t"
    "buffer_store_dwordx4 v[4:7], v241, s[48:51], s64 offen offset:0 sc0 sc1\n\t"
    "v_exp_f32 v189, v189\n\t"
    "v_mul_f32 v16, v16, v243\n\t"
    "v_mul_f32 v17, v17, v243\n\t"
    "v_mul_f32 v18, v18, v243\n\t"
    "v_mul_f32 v19, v19, v243\n\t"
    "v_mfma_f32_16x16x16_bf16 a[244:247], a[248:249], v[224:225], a[244:247]\n\t"
    "v_cvt_pk_bf16_f32 v212, v188, v189\n\t"
    "v_add_f32 v251, v251, v180\n\t"
    "v_cvt_pk_bf16_f32 v12, v12, v13\n\t"
    "v_cvt_pk_bf16_f32 v13, v14, v15\n\t"
    "v_cvt_pk_bf16_f32 v14, v16, v17\n\t"
    "v_cvt_pk_bf16_f32 v15, v18, v19\n\t"
    "v_mfma_f32_16x16x32_bf16 v[36:39], a[160:163], v[204:207], v[36:39]\n\t"
    "v_exp_f32 v190, v190\n\t"
    "v_mfma_f32_16x16x32_bf16 a[4:7], a[0:3], v[12:15], a[4:7]\n\t"
    "v_mul_f32 v20, v20, v243\n\t"
    "v_mul_f32 v21, v21, v243\n\t"
    "v_mul_f32 v22, v22, v243\n\t"
    "v_mfma_f32_16x16x32_bf16 v[36:39], a[192:195], v[208:211], v[36:39]\n\t"
    "v_exp_f32 v191, v191\n\t"
    "v_mul_f32 v23, v23, v243\n\t"
    "v_mul_f32 v24, v24, v243\n\t"
    "v_mul_f32 v25, v25, v243\n\t"
    "v_mul_f32 v26, v26, v243\n\t"
    "v_mfma_f32_16x16x32_bf16 v[40:43], a[164:167], v[204:207], v[40:43]\n\t"
    "buffer_store_dwordx4 v[12:15], v241, s[48:51], s64 offen offset:64 sc0 sc1\n\t"
    "v_cvt_pk_bf16_f32 v213, v190, v191\n\t"
    "v_add_f32 v251, v251, v181\n\t"
    "v_mul_f32 v27, v27, v243\n\t"
    "v_cvt_pk_bf16_f32 v20, v20, v21\n\t"
    "v_cvt_pk_bf16_f32 v21, v22, v23\n\t"
    "v_mfma_f32_16x16x32_bf16 v[40:43], a[196:199], v[208:211], v[40:43]\n\t"
    "v_exp_f32 v192, v192\n\t"
    "v_cvt_pk_bf16_f32 v22, v24, v25\n\t"
    "v_cvt_pk_bf16_f32 v23, v26, v27\n\t"
    "s_nop 1\n\t"
    "v_mfma_f32_16x16x32_bf16 a[4:7], a[0:3], v[20:23], a[4:7]\n\t"
    "v_mul_f32 v28, v28, v243\n\t"
    "v_mfma_f32_16x16x32_bf16 v[44:47], a[168:171], v[204:207], v[44:47]\n\t"
    "v_exp_f32 v193, v193\n\t"
    "v_mul_f32 v29, v29, v243\n\t"
    "v_mul_f32 v30, v30, v243\n\t"
    "v_mul_f32 v31, v31, v243\n\t"
    "v_mul_f32 v32, v32, v243\n\t"
    "v_mfma_f32_16x16x32_bf16 v[44:47], a[200:203], v[208:211], v[44:47]\n\t"
    "buffer_store_dwordx4 v[20:23], v241, s[48:51], s64 offen offset:128 sc0 sc1\n\t"
    "v_cvt_pk_bf16_f32 v214, v192, v193\n\t"
    "v_add_f32 v251, v251, v182\n\t"
    "v_mul_f32 v33, v33, v243\n\t"
    "v_mul_f32 v34, v34, v243\n\t"
    "v_mul_f32 v35, v35, v243\n\t"
    "v_mfma_f32_16x16x32_bf16 v[48:51], a[172:175], v[204:207], v[48:51]\n\t"
    "v_exp_f32 v194, v194\n\t"
    "v_cvt_pk_bf16_f32 v28, v28, v29\n\t"
    "v_cvt_pk_bf16_f32 v29, v30, v31\n\t"
    "v_cvt_pk_bf16_f32 v30, v32, v33\n\t"
    "v_cvt_pk_bf16_f32 v31, v34, v35\n\t"
    "v_mfma_f32_16x16x32_bf16 v[48:51], a[204:207], v[208:211], v[48:51]\n\t"
    "v_exp_f32 v195, v195\n\t"
    "v_mfma_f32_16x16x32_bf16 a[4:7], a[0:3], v[28:31], a[4:7]\n\t"
    "v_mfma_f32_16x16x32_bf16 v[52:55], a[176:179], v[204:207], v[52:55]\n\t"
    "v_cvt_pk_bf16_f32 v215, v194, v195\n\t"
    "v_add_f32 v251, v251, v183\n\t"
    "v_and_b32 v220, 15, v1\n\t"
    "v_mfma_f32_16x16x32_bf16 v[52:55], a[208:211], v[208:211], v[52:55]\n\t"
    "buffer_store_dwordx4 v[28:31], v241, s[48:51], s64 offen offset:192 sc0 sc1\n\t"
    "v_lshlrev_b32 v220, 2, v220\n\t"
    "v_mfma_f32_16x16x32_bf16 v[56:59], a[180:183], v[204:207], v[56:59]\n\t"
    "s_add_u32 s61, s53, 139264\n\t"
    "v_exp_f32 v196, v196\n\t"
    "v_add_u32 v220, s61, v220\n\t"
    "v_mfma_f32_16x16x32_bf16 v[56:59], a[212:215], v[208:211], v[56:59]\n\t"
    "v_exp_f32 v197, v197\n\t"
    "v_accvgpr_read_b32 v221, a244\n\t"
    "v_mfma_f32_16x16x32_bf16 v[60:63], a[184:187], v[204:207], v[60:63]\n\t"
    "ds_write_b32 v220, v221 offset:0\n\t"
    "v_cvt_pk_bf16_f32 v216, v196, v197\n\t"
    "v_add_f32 v251, v251, v184\n\t"
    "v_mfma_f32_16x16x32_bf16 v[60:63], a[216:219], v[208:211], v[60:63]\n\t"
    "v_exp_f32 v198, v198\n\t"
    "v_mfma_f32_16x16x32_bf16 v[64:67], a[188:191], v[204:207], v[64:67]\n\t"
    "v_exp_f32 v199, v199\n\t"
    "v_mfma_f32_16x16x32_bf16 v[64:67], a[220:223], v[208:211], v[64:67]\n\t"
    "v_cvt_pk_bf16_f32 v217, v198, v199\n\t"
    "v_add_f32 v251, v251, v185\n\t"
    "v_mfma_f32_16x16x32_bf16 a[228:231], a[248:251], v[204:207], a[228:231]\n\t"
    "v_exp_f32 v200, v200\n\t"
    "v_mfma_f32_16x16x32_bf16 v[172:175], a[96:99], a[48:51], 0\n\t"
    "v_exp_f32 v201, v201\n\t"
    "v_mfma_f32_16x16x32_bf16 v[172:175], a[100:103], a[52:55], v[172:175]\n\t"
    "v_cvt_pk_bf16_f32 v218, v200, v201\n\t"
    "v_add_f32 v251, v251, v186\n\t"
    "v_mfma_f32_16x16x32_bf16 v[172:175], a[104:107], a[56:59], v[172:175]\n\t"
    "v_exp_f32 v202, v202\n\t"
    "v_mfma_f32_16x16x32_bf16 v[172:175], a[108:111], a[60:63], v[172:175]\n\t"
    "v_add_f32 v251, v251, v187\n\t"
    "v_mfma_f32_16x16x32_bf16 v[176:179], a[112:115], a[48:51], 0\n\t"
    "v_exp_f32 v203, v203\n\t"
    "v_mfma_f32_16x16x32_bf16 v[176:179], a[116:119], a[52:55], v[176:179]\n\t"
    "v_cvt_pk_bf16_f32 v219, v202, v203\n\t"
    "v_mfma_f32_16x16x32_bf16 v[176:179], a[120:123], a[56:59], v[176:179]\n\t"
    "v_add_f32 v252, v252, v196\n\t"
    "v_add_f32 v252, v252, v197\n\t"
    "v_mfma_f32_16x16x4_f32 a[228:231], a252, v251, a[228:231]\n\t"
    "v_mfma_f32_16x16x32_bf16 v[176:179], a[124:127], a[60:63], v[176:179]\n\t"
    "v_add_f32 v252, v252, v198\n\t"
    "v_add_f32 v252, v252, v199\n\t"
    "v_mfma_f32_16x16x32_bf16 v[180:183], a[128:131], a[48:51], 0\n\t"
    "v_mfma_f32_16x16x32_bf16 v[180:183], a[132:135], a[52:55], v[180:183]\n\t"
    "v_add_f32 v252, v252, v200\n\t"
    "v_add_f32 v252, v252, v201\n\t"
    "v_mfma_f32_16x16x32_bf16 v[180:183], a[136:139], a[56:59], v[180:183]\n\t"
    "v_add_f32 v252, v252, v202\n\t"
    "v_add_f32 v252, v252, v203\n\t"
    "v_accvgpr_read_b32 v244, a228\n\t"
    "v_subrev_u32 v246, 0x2b800000, v244\n\t"
    "v_cmp_lt_u32 vcc, 0x28000000, v246\n\t"
    "s_nop 1\n\t"
    "s_or_b64 s[62:63], s[62:63], vcc\n\t"
    "v_mfma_f32_16x16x32_bf16 v[180:183], a[140:143], a[60:63], v[180:183]\n\t"
    "v_exp_f32 v172, v172\n\t"
    "v_rcp_f32 v245, v244\n\t"
    "s_mov_b32 s64, 4096\n\t"
    "v_mul_f32 v36, v36, v245\n\t"
    "v_mul_f32 v37, v37, v245\n\t"
    "v_mfma_f32_16x16x32_bf16 v[184:187], a[144:147], a[48:51], 0\n\t"
    "v_exp_f32 v173, v173\n\t"
    "v_mul_f32 v38, v38, v245\n\t"
    "v_mul_f32 v39, v39, v245\n\t"
    "v_mul_f32 v40, v40, v245\n\t"
    "v_mul_f32 v41, v41, v245\n\t"
    "v_mfma_f32_16x16x32_bf16 v[184:187], a[148:151], a[52:55], v[184:187]\n\t"
    "v_cvt_pk_bf16_f32 v204, v172, v173\n\t"
    "v_mul_f32 v42, v42, v245\n\t"
    "v_mul_f32 v43, v43, v245\n\t"
    "v_cvt_pk_bf16_f32 v36, v36, v37\n\t"
    "v_cvt_pk_bf16_f32 v37, v38, v39\n\t"
    "v_cvt_pk_bf16_f32 v38, v40, v41\n\t"
    "v_mfma_f32_16x16x32_bf16 v[184:187], a[152:155], a[56:59], v[184:187]\n\t"
    "v_exp_f32 v174, v174\n\t"
    "v_cvt_pk_bf16_f32 v39, v42, v43\n\t"
    "s_nop 1\n\t"
    "v_mfma_f32_16x16x32_bf16 a[4:7], a[0:3], v[36:39], a[4:7]\n\t"
    "v_mul_f32 v44, v44, v245\n\t"
    "v_mul_f32 v45, v45, v245\n\t"
    "v_mfma_f32_16x16x32_bf16 v[184:187], a[156:159], a[60:63], v[184:187]\n\t"
    "v_exp_f32 v175, v175\n\t"
    "v_mul_f32 v46, v46, v245\n\t"
    "v_mul_f32 v47, v47, v245\n\t"
    "v_mul_f32 v48, v48, v245\n\t"
    "v_mul_f32 v49, v49, v245\n\t"
    "v_mfma_f32_16x16x32_bf16 v[68:71], a[160:163], v[212:215], v[68:71]\n\t"
    "buffer_store_dwordx4 v[36:39], v241, s[48:51], s64 offen offset:0 sc0 sc1\n\t"
    "v_cvt_pk_bf16_f32 v205, v174, v175\n\t"
    "v_mul_f32 v50, v50, v245\n\t"
    "v_mul_f32 v51, v51, v245\n\t"
    "v_cvt_pk_bf16_f32 v44, v44, v45\n\t"
    "v_cvt_pk_bf16_f32 v45, v46, v47\n\t"
    "v_mfma_f32_16x16x32_bf16 v[68:71], a[192:195], v[216:219], v[68:71]\n\t"
    "v_exp_f32 v176, v176\n\t"
    "v_cvt_pk_bf16_f32 v46, v48, v49\n\t"
    "v_cvt_pk_bf16_f32 v47, v50, v51\n\t"
    "s_nop 1\n\t"
    "v_mfma_f32_16x16x32_bf16 a[4:7], a[0:3], v[44:47], a[4:7]\n\t"
    "v_mul_f32 v52, v52, v245\n\t"
    "v_mfma_f32_16x16x32_bf16 v[72:75], a[164:167], v[212:215], v[72:75]\n\t"
    "v_exp_f32 v177, v177\n\t"
    "v_mul_f32 v53, v53, v245\n\t"
    "v_mul_f32 v54, v54, v245\n\t"
    "v_mul_f32 v55, v55, v245\n\t"
    "v_mul_f32 v56, v56, v245\n\t"
    "v_mfma_f32_16x16x32_bf16 v[72:75], a[196:199], v[216:219], v[72:75]\n\t"
    "buffer_store_dwordx4 v[44:47], v241, s[48:51], s64 offen offset:64 sc0 sc1\n\t"
    "v_cvt_pk_bf16_f32 v206, v176, v177\n\t"
    "v_mul_f32 v57, v57, v245\n\t"
    "v_mul_f32 v58, v58, v245\n\t"
    "v_mul_f32 v59, v59, v245\n\t"
    "v_cvt_pk_bf16_f32 v52, v52, v53\n\t"
    "v_mfma_f32_16x16x32_bf16 v[76:79], a[168:171], v[212:215], v[76:79]\n\t"
    "v_exp_f32 v178, v178\n\t"
    "v_cvt_pk_bf16_f32 v53, v54, v55\n\t"
    "v_cvt_pk_bf16_f32 v54, v56, v57\n\t"
    "v_cvt_pk_bf16_f32 v55, v58, v59\n\t"
    "s_nop 1\n\t"
    "v_mfma_f32_16x16x32_bf16 a[4:7], a[0:3], v[52:55], a[4:7]\n\t"
    "v_mfma_f32_16x16x32_bf16 v[76:79], a[200:203], v[216:219], v[76:79]\n\t"
    "v_exp_f32 v179, v179\n\t"
    "v_mul_f32 v60, v60, v245\n\t"
    "v_mul_f32 v61, v61, v245\n\t"
    "v_mul_f32 v62, v62, v245\n\t"
    "v_mul_f32 v63, v63, v245\n\t"
    "v_mfma_f32_16x16x32_bf16 v[80:83], a[172:175], v[212:215], v[80:83]\n\t"
    "buffer_store_dwordx4 v[52:55], v241, s[48:51], s64 offen offset:128 sc0 sc1\n\t"
    "v_cvt_pk_bf16_f32 v207, v178, v179\n\t"
    "v_mul_f32 v64, v64, v245\n\t"
    "v_mul_f32 v65, v65, v245\n\t"
    "v_mul_f32 v66, v66, v245\n\t"
    "v_mul_f32 v67, v67, v245\n\t"
    "v_mfma_f32_16x16x32_bf16 v[80:83], a[204:207], v[216:219], v[80:83]\n\t"
    "v_exp_f32 v180, v180\n\t"
    "v_cvt_pk_bf16_f32 v60, v60, v61\n\t"
    "v_cvt_pk_bf16_f32 v61, v62, v63\n\t"
    "v_cvt_pk_bf16_f32 v62, v64, v65\n\t"
    "v_cvt_pk_bf16_f32 v63, v66, v67\n\t"
    "v_mfma_f32_16x16x32_bf16 v[84:87], a[176:179], v[212:215], v[84:87]\n\t"
    "v_exp_f32 v181, v181\n\t"
    "v_mfma_f32_16x16x32_bf16 a[4:7], a[0:3], v[60:63], a[4:7]\n\t"
    "v_mfma_f32_16x16x32_bf16 v[84:87], a[208:211], v[216:219], v[84:87]\n\t"
    "v_cvt_pk_bf16_f32 v208, v180, v181\n\t"
    "v_add_f32 v253, v253, v180\n\t"
    "v_mfma_f32_16x16x32_bf16 v[88:91], a[180:183], v[212:215], v[88:91]\n\t"
    "buffer_store_dwordx4 v[60:63], v241, s[48:51], s64 offen offset:192 sc0 sc1\n\t"
    "v_exp_f32 v182, v182\n\t"
    "v_mfma_f32_16x16x32_bf16 v[88:91], a[212:215], v[216:219], v[88:91]\n\t"
    "v_exp_f32 v183, v183\n\t"
    "v_mfma_f32_16x16x32_bf16 v[92:95], a[184:187], v[212:215], v[92:95]\n\t"
    "v_cvt_pk_bf16_f32 v209, v182, v183\n\t"
    "v_add_f32 v253, v253, v181\n\t"
    "v_mfma_f32_16x16x32_bf16 v[92:95], a[216:219], v[216:219], v[92:95]\n\t"
    "v_exp_f32 v184, v184\n\t"
    "v_mfma_f32_16x16x32_bf16 v[96:99], a[188:191], v[212:215], v[96:99]\n\t"
    "v_exp_f32 v185, v185\n\t"
    "v_mfma_f32_16x16x32_bf16 v[96:99], a[220:223], v[216:219], v[96:99]\n\t"
    "v_cvt_pk_bf16_f32 v210, v184, v185\n\t"
    "v_add_f32 v253, v253, v182\n\t"
    "v_mfma_f32_16x16x32_bf16 a[232:235], a[248:251], v[212:215], a[232:235]\n\t"
    "s_waitcnt vmcnt(8) lgkmcnt(0)\n\t"
    "s_barrier\n\t"
    "v_mfma_f32_16x16x32_bf16 v[188:191], a[96:99], a[64:67], 0\n\t"
    "v_add_f32 v253, v253, v183\n\t"
    "v_mfma_f32_16x16x32_bf16 v[188:191], a[100:103], a[68:71], v[188:191]\n\t"
    "v_exp_f32 v186, v186\n\t"
    "v_mfma_f32_16x16x32_bf16 v[188:191], a[104:107], a[72:75], v[188:191]\n\t"
    "v_mfma_f32_16x16x32_bf16 v[188:191], a[108:111], a[76:79], v[188:191]\n\t"
    "v_exp_f32 v187, v187\n\t"
    "v_mfma_f32_16x16x32_bf16 v[192:195], a[112:115], a[64:67], 0\n\t"
    "v_mfma_f32_16x16x32_bf16 v[192:195], a[116:119], a[68:71], v[192:195]\n\t"
    "v_cvt_pk_bf16_f32 v211, v186, v187\n\t"
    "v_add_f32 v253, v253, v184\n\t"
    "v_mfma_f32_16x16x32_bf16 v[192:195], a[120:123], a[72:75], v[192:195]\n\t"
    "v_mfma_f32_16x16x4_f32 a[232:235], a252, v252, a[232:235]\n\t"
    "v_mfma_f32_16x16x32_bf16 v[192:195], a[124:127], a[76:79], v[192:195]\n\t"
    "v_add_f32 v253, v253, v185\n\t"
    "v_add_f32 v253, v253, v186\n\t"
    "v_mfma_f32_16x16x32_bf16 v[196:199], a[128:131], a[64:67], 0\n\t"
    "v_add_f32 v253, v253, v187\n\t"
    "v_mfma_f32_16x16x32_bf16 v[196:199], a[132:135], a[68:71], v[196:199]\n\t"
    "v_mfma_f32_16x16x32_bf16 v[196:199], a[136:139], a[72:75], v[196:199]\n\t"
    "s_nop 2\n\t"
    "v_accvgpr_read_b32 v242, a232\n\t"
    "v_subrev_u32 v246, 0x2b800000, v242\n\t"
    "v_cmp_lt_u32 vcc, 0x28000000, v246\n\t"
    "s_nop 1\n\t"
    "s_or_b64 s[62:63], s[62:63], vcc\n\t"
    "v_rcp_f32 v243, v242\n\t"
    "s_mov_b32 s64, 8192\n\t"
    "v_mul_f32 v68, v68, v243\n\t"
    "v_mfma_f32_16x16x32_bf16 v[196:199], a[140:143], a[76:79], v[196:199]\n\t"
    "v_exp_f32 v188, v188\n\t"
    "v_mul_f32 v69, v69, v243\n\t"
    "v_mul_f32 v70, v70, v243\n\t"
    "v_mul_f32 v71, v71, v243\n\t"
    "v_mul_f32 v72, v72, v243\n\t"
    "v_mfma_f32_16x16x32_bf16 v[200:203], a[144:147], a[64:67], 0\n\t"
    "v_mul_f32 v73, v73, v243\n\t"
    "v_mul_f32 v74, v74, v243\n\t"
    "v_mul_f32 v75, v75, v243\n\t"
    "v_cvt_pk_bf16_f32 v68, v68, v69\n\t"
    "v_cvt_pk_bf16_f32 v69, v70, v71\n\t"
    "v_cvt_pk_bf16_f32 v70, v72, v73\n\t"
    "v_mfma_f32_16x16x32_bf16 v[200:203], a[148:151], a[68:71], v[200:203]\n\t"
    "v_exp_f32 v189, v189\n\t"
    "v_cvt_pk_bf16_f32 v71, v74, v75\n\t"
    "s_nop 1\n\t"
    "v_mfma_f32_16x16x32_bf16 a[4:7], a[0:3], v[68:71], a[4:7]\n\t"
    "v_mul_f32 v76, v76, v243\n\t"
    "v_mul_f32 v77, v77, v243\n\t"
    "v_mfma_f32_16x16x32_bf16 v[200:203], a[152:155], a[72:75], v[200:203]\n\t"
    "v_mul_f32 v78, v78, v243\n\t"
    "v_mul_f32 v79, v79, v243\n\t"
    "v_mul_f32 v80, v80, v243\n\t"
    "v_mul_f32 v81, v81, v243\n\t"
    "v_mul_f32 v82, v82, v243\n\t"
    "v_mul_f32 v83, v83, v243\n\t"
    "v_mfma_f32_16x16x32_bf16 v[200:203], a[156:159], a[76:79], v[200:203]\n\t"
    "buffer_store_dwordx4 v[68:71], v241, s[48:51], s64 offen offset:0 sc0 sc1\n\t"
    "v_cvt_pk_bf16_f32 v212, v188, v189\n\t"
    "v_add_f32 v254, v254, v188\n\t"
    "v_cvt_pk_bf16_f32 v76, v76, v77\n\t"
    "v_cvt_pk_bf16_f32 v77, v78, v79\n\t"
    "v_cvt_pk_bf16_f32 v78, v80, v81\n\t"
    "v_mfma_f32_16x16x32_bf16 v[100:103], a[160:163], v[204:207], v[100:103]\n\t"
    "v_cvt_pk_bf16_f32 v79, v82, v83\n\t"
    "s_nop 1\n\t"
    "v_mfma_f32_16x16x32_bf16 a[4:7], a[0:3], v[76:79], a[4:7]\n\t"
    "v_mul_f32 v84, v84, v243\n\t"
    "v_mul_f32 v85, v85, v243\n\t"
    "v_mul_f32 v86, v86, v243\n\t"
    "v_mul_f32 v87, v87, v243\n\t"
    "v_mfma_f32_16x16x32_bf16 v[100:103], a[192:195], v[208:211], v[100:103]\n\t"
    "v_exp_f32 v190, v190\n\t"
    "v_mul_f32 v88, v88, v243\n\t"
    "v_mul_f32 v89, v89, v243\n\t"
    "v_mul_f32 v90, v90, v243\n\t"
    "v_mul_f32 v91, v91, v243\n\t"
    "v_mfma_f32_16x16x32_bf16 v[104:107], a[164:167], v[204:207], v[104:107]\n\t"
    "ds_read_b128 v[228:231], v238 offset:2048\n\t"
    "ds_read_b128 v[232:235], v238 offset:3072\n\t"
    "v_cvt_pk_bf16_f32 v84, v84, v85\n\t"
    "v_cvt_pk_bf16_f32 v85, v86, v87\n\t"
    "v_cvt_pk_bf16_f32 v86, v88, v89\n\t"
    "v_cvt_pk_bf16_f32 v87, v90, v91\n\t"
    "v_mfma_f32_16x16x32_bf16 v[104:107], a[196:199], v[208:211], v[104:107]\n\t"
    "buffer_store_dwordx4 v[76:79], v241, s[48:51], s64 offen offset:64 sc0 sc1\n\t"
    "v_exp_f32 v191, v191\n\t"
    "v_mfma_f32_16x16x32_bf16 a[4:7], a[0:3], v[84:87], a[4:7]\n\t"
    "v_mul_f32 v92, v92, v243\n\t"
    "v_mul_f32 v93, v93, v243\n\t"
    "v_mul_f32 v94, v94, v243\n\t"
    "v_mfma_f32_16x16x32_bf16 v[108:111], a[168:171], v[204:207], v[108:111]\n\t"
    "v_cvt_pk_bf16_f32 v213, v190, v191\n\t"
    "v_add_f32 v254, v254, v189\n\t"
    "v_mul_f32 v95, v95, v243\n\t"
    "v_mul_f32 v96, v96, v243\n\t"
    "v_mul_f32 v97, v97, v243\n\t"
    "v_mul_f32 v98, v98, v243\n\t"
    "v_mfma_f32_16x16x32_bf16 v[108:111], a[200:203], v[208:211], v[108:111]\n\t"
    "buffer_store_dwordx4 v[84:87], v241, s[48:51], s64 offen offset:128 sc0 sc1\n\t"
    "v_exp_f32 v192, v192\n\t"
    "v_mul_f32 v99, v99, v243\n\t"
    "v_cvt_pk_bf16_f32 v92, v92, v93\n\t"
    "v_cvt_pk_bf16_f32 v93, v94, v95\n\t"
    "v_cvt_pk_bf16_f32 v94, v96, v97\n\t"
    "v_mfma_f32_16x16x32_bf16 v[112:115], a[172:175], v[204:207], v[112:115]\n\t"
    "v_exp_f32 v193, v193\n\t"
    "v_cvt_pk_bf16_f32 v95, v98, v99\n\t"
    "s_nop 1\n\t"
    "v_mfma_f32_16x16x32_bf16 a[4:7], a[0:3], v[92:95], a[4:7]\n\t"
    "ds_read_b32 v222, v220 offset:0\n\t"
    "ds_read_b32 v223, v220 offset:64\n\t"
    "v_mfma_f32_16x16x32_bf16 v[112:115], a[204:207], v[208:211], v[112:115]\n\t"
    "v_cvt_pk_bf16_f32 v214, v192, v193\n\t"
    "v_add_f32 v254, v254, v190\n\t"
    "ds_read_b32 v224, v220 offset:128\n\t"
    "ds_read_b32 v225, v220 offset:192\n\t"
    "s_waitcnt lgkmcnt(2)\n\t"
    "v_add_f32 v222, v222, v223\n\t"
    "s_waitcnt lgkmcnt(0)\n\t"
    "v_add_f32 v224, v224, v225\n\t"
    "v_mfma_f32_16x16x32_bf16 v[116:119], a[176:179], v[204:207], v[116:119]\n\t"
    "buffer_store_dwordx4 v[92:95], v241, s[48:51], s64 offen offset:192 sc0 sc1\n\t"
    "v_exp_f32 v194, v194\n\t"
    "v_add_f32 v248, v222, v224\n\t"
    "v_subrev_u32 v223, 0x2b800000, v248\n\t"
    "v_cmp_lt_u32 vcc, 0x28000000, v223\n\t"
    "s_nop 1\n\t"
    "s_or_b64 s[62:63], s[62:63], vcc\n\t"
    "v_mfma_f32_16x16x32_bf16 v[116:119], a[208:211], v[208:211], v[116:119]\n\t"
    "v_exp_f32 v195, v195\n\t"
    "v_rcp_f32 v249, v248\n\t"
    "v_mfma_f32_16x16x32_bf16 v[120:123], a[180:183], v[204:207], v[120:123]\n\t"
    "v_cvt_pk_bf16_f32 v215, v194, v195\n\t"
    "v_add_f32 v254, v254, v191\n\t"
    "v_mfma_f32_16x16x32_bf16 v[120:123], a[212:215], v[208:211], v[120:123]\n\t"
    "v_exp_f32 v196, v196\n\t"
    "v_mfma_f32_16x16x32_bf16 v[124:127], a[184:187], v[204:207], v[124:127]\n\t"
    "v_exp_f32 v197, v197\n\t"
    "v_mfma_f32_16x16x32_bf16 v[124:127], a[216:219], v[208:211], v[124:127]\n\t"
    "v_cvt_pk_bf16_f32 v216, v196, v197\n\t"
    "v_add_f32 v254, v254, v192\n\t"
    "v_mfma_f32_16x16x32_bf16 v[128:131], a[188:191], v[204:207], v[128:131]\n\t"
    "v_exp_f32 v198, v198\n\t"
    "v_mfma_f32_16x16x32_bf16 v[128:131], a[220:223], v[208:211], v[128:131]\n\t"
    "v_exp_f32 v199, v199\n\t"
    "v_mfma_f32_16x16x32_bf16 a[236:239], a[248:251], v[204:207], a[236:239]\n\t"
    "v_cvt_pk_bf16_f32 v217, v198, v199\n\t"
    "v_add_f32 v254, v254, v193\n\t"
    "v_exp_f32 v200, v200\n\t"
    "v_exp_f32 v201, v201\n\t"
    "s_nop 0\n\t"
    "v_cvt_pk_bf16_f32 v218, v200, v201\n\t"
    "v_add_f32 v254, v254, v194\n\t"
    "v_exp_f32 v202, v202\n\t"
    "v_exp_f32 v203, v203\n\t"
    "s_nop 0\n\t"
    "v_cvt_pk_bf16_f32 v219, v202, v203\n\t"
    "v_add_f32 v254, v254, v195\n\t"
    "v_mfma_f32_16x16x4_f32 a[236:239], a252, v253, a[236:239]\n\t"
    "v_add_f32 v254, v254, v196\n\t"
    "v_add_f32 v254, v254, v197\n\t"
    "v_add_f32 v254, v254, v198\n\t"
    "v_add_f32 v254, v254, v199\n\t"
    "v_add_f32 v254, v254, v200\n\t"
    "v_add_f32 v254, v254, v201\n\t"
    "v_add_f32 v254, v254, v202\n\t"
    "v_add_f32 v254, v254, v203\n\t"
    "s_nop 1\n\t"
    "v_accvgpr_read_b32 v244, a236\n\t"
    "v_subrev_u32 v246, 0x2b800000, v244\n\t"
    "v_cmp_lt_u32 vcc, 0x28000000, v246\n\t"
    "s_nop 1\n\t"
    "s_or_b64 s[62:63], s[62:63], vcc\n\t"
    "v_rcp_f32 v245, v244\n\t"
    "s_mov_b32 s64, 12288\n\t"
    "v_mul_f32 v100, v100, v245\n\t"
    "v_mul_f32 v101, v101, v245\n\t"
    "v_mul_f32 v102, v102, v245\n\t"
    "v_mul_f32 v103, v103, v245\n\t"
    "v_mul_f32 v104, v104, v245\n\t"
    "v_mul_f32 v105, v105, v245\n\t"
    "v_mul_f32 v106, v106, v245\n\t"
    "v_mul_f32 v107, v107, v245\n\t"
    "v_cvt_pk_bf16_f32 v100, v100, v101\n\t"
    "v_cvt_pk_bf16_f32 v101, v102, v103\n\t"
    "v_cvt_pk_bf16_f32 v102, v104, v105\n\t"
    "v_cvt_pk_bf16_f32 v103, v106, v107\n\t"
    "buffer_store_dwordx4 v[100:103], v241, s[48:51], s64 offen offset:0 sc0 sc1\n\t"
    "s_nop 0\n\t"
    "v_mfma_f32_16x16x32_bf16 a[4:7], a[0:3], v[100:103], a[4:7]\n\t"
    "v_mul_f32 v108, v108, v245\n\t"
    "v_mul_f32 v109, v109, v245\n\t"
    "v_mul_f32 v110, v110, v245\n\t"
    "v_mul_f32 v111, v111, v245\n\t"
    "v_mul_f32 v112, v112, v245\n\t"
    "v_mul_f32 v113, v113, v245\n\t"
    "v_mul_f32 v114, v114, v245\n\t"
    "v_mul_f32 v115, v115, v245\n\t"
    "v_cvt_pk_bf16_f32 v108, v108, v109\n\t"
    "v_cvt_pk_bf16_f32 v109, v110, v111\n\t"
    "v_cvt_pk_bf16_f32 v110, v112, v113\n\t"
    "v_cvt_pk_bf16_f32 v111, v114, v115\n\t"
    "buffer_store_dwordx4 v[108:111], v241, s[48:51], s64 offen offset:64 sc0 sc1\n\t"
    "s_nop 0\n\t"
    "v_mfma_f32_16x16x32_bf16 a[4:7], a[0:3], v[108:111], a[4:7]\n\t"
    "v_mul_f32 v116, v116, v245\n\t"
    "v_mul_f32 v117, v117, v245\n\t"
    "v_mul_f32 v118, v118, v245\n\t"
    "v_mfma_f32_16x16x32_bf16 v[164:167], a[160:163], v[228:231], v[164:167]\n\t"
    "v_mul_f32 v119, v119, v245\n\t"
    "v_mul_f32 v120, v120, v245\n\t"
    "v_mul_f32 v121, v121, v245\n\t"
    "v_mul_f32 v122, v122, v245\n\t"
    "v_mul_f32 v123, v123, v245\n\t"
    "v_cvt_pk_bf16_f32 v116, v116, v117\n\t"
    "v_mfma_f32_16x16x32_bf16 v[164:167], a[192:195], v[232:235], v[164:167]\n\t"
    "v_cvt_pk_bf16_f32 v117, v118, v119\n\t"
    "v_cvt_pk_bf16_f32 v118, v120, v121\n\t"
    "v_cvt_pk_bf16_f32 v119, v122, v123\n\t"
    "s_nop 1\n\t"
    "v_mfma_f32_16x16x32_bf16 a[4:7], a[0:3], v[116:119], a[4:7]\n\t"
    "v_mul_f32 v124, v124, v245\n\t"
    "v_mul_f32 v125, v125, v245\n\t"
    "v_mfma_f32_16x16x32_bf16 v[168:171], a[164:167], v[228:231], v[168:171]\n\t"
    "v_mul_f32 v126, v126, v245\n\t"
    "v_mul_f32 v127, v127, v245\n\t"
    "v_mul_f32 v128, v128, v245\n\t"
    "v_mul_f32 v129, v129, v245\n\t"
    "v_mul_f32 v130, v130, v245\n\t"
    "v_mul_f32 v131, v131, v245\n\t"
    "v_mfma_f32_16x16x32_bf16 v[168:171], a[196:199], v[232:235], v[168:171]\n\t"
    "buffer_store_dwordx4 v[116:119], v241, s[48:51], s64 offen offset:128 sc0 sc1\n\t"
    "v_cvt_pk_bf16_f32 v124, v124, v125\n\t"
    "v_cvt_pk_bf16_f32 v125, v126, v127\n\t"
    "v_cvt_pk_bf16_f32 v126, v128, v129\n\t"
    "v_cvt_pk_bf16_f32 v127, v130, v131\n\t"
    "s_nop 1\n\t"
    "v_mfma_f32_16x16x32_bf16 a[4:7], a[0:3], v[124:127], a[4:7]\n\t"
    "v_mfma_f32_16x16x32_bf16 v[132:135], a[160:163], v[212:215], v[132:135]\n\t"
    "v_mfma_f32_16x16x4_f32 a[240:243], a252, v254, 0\n\t"
    "v_mfma_f32_16x16x32_bf16 v[132:135], a[192:195], v[216:219], v[132:135]\n\t"
    "buffer_store_dwordx4 v[124:127], v241, s[48:51], s64 offen offset:192 sc0 sc1\n\t"
    "v_mfma_f32_16x16x32_bf16 v[136:139], a[164:167], v[212:215], v[136:139]\n\t"
    "v_mfma_f32_16x16x32_bf16 v[136:139], a[196:199], v[216:219], v[136:139]\n\t"
    "v_mfma_f32_16x16x32_bf16 v[140:143], a[168:171], v[212:215], v[140:143]\n\t"
    "v_mfma_f32_16x16x32_bf16 v[140:143], a[200:203], v[216:219], v[140:143]\n\t"
    "v_mfma_f32_16x16x32_bf16 v[144:147], a[172:175], v[212:215], v[144:147]\n\t"
    "v_mfma_f32_16x16x32_bf16 v[144:147], a[204:207], v[216:219], v[144:147]\n\t"
    "v_mfma_f32_16x16x32_bf16 v[148:151], a[176:179], v[212:215], v[148:151]\n\t"
    "s_mov_b32 s61, 81920\n\t"
    "v_mul_f32 v164, v164, v249\n\t"
    "v_mul_f32 v165, v165, v249\n\t"
    "v_mul_f32 v166, v166, v249\n\t"
    "v_mul_f32 v167, v167, v249\n\t"
    "v_mul_f32 v168, v168, v249\n\t"
    "v_mul_f32 v169, v169, v249\n\t"
    "v_mfma_f32_16x16x32_bf16 v[148:151], a[208:211], v[216:219], v[148:151]\n\t"
    "v_mul_f32 v170, v170, v249\n\t"
    "v_mul_f32 v171, v171, v249\n\t"
    "v_cvt_pk_bf16_f32 v164, v164, v165\n\t"
    "v_cvt_pk_bf16_f32 v165, v166, v167\n\t"
    "v_cvt_pk_bf16_f32 v166, v168, v169\n\t"
    "v_cvt_pk_bf16_f32 v167, v170, v171\n\t"
    "v_mfma_f32_16x16x32_bf16 v[152:155], a[180:183], v[212:215], v[152:155]\n\t"
    "s_nop 0\n\t"
    "v_mfma_f32_16x16x32_bf16 a[4:7], a[0:3], v[164:167], a[4:7]\n\t"
    "v_accvgpr_read_b32 v242, a240\n\t"
    "v_subrev_u32 v246, 0x2b800000, v242\n\t"
    "v_cmp_lt_u32 vcc, 0x28000000, v246\n\t"
    "s_nop 1\n\t"
    "s_or_b64 s[62:63], s[62:63], vcc\n\t"
    "v_rcp_f32 v243, v242\n\t"
    "v_mfma_f32_16x16x32_bf16 v[152:155], a[212:215], v[216:219], v[152:155]\n\t"
    "v_mfma_f32_16x16x32_bf16 v[156:159], a[184:187], v[212:215], v[156:159]\n\t"
    "buffer_store_dwordx4 v[164:167], v241, s[48:51], s61 offen offset:0 sc0 sc1\n\t"
    "s_mov_b32 s64, 16384\n\t"
    "v_mfma_f32_16x16x32_bf16 v[156:159], a[216:219], v[216:219], v[156:159]\n\t"
    "v_mfma_f32_16x16x32_bf16 v[160:163], a[188:191], v[212:215], v[160:163]\n\t"
    "v_mfma_f32_16x16x32_bf16 v[160:163], a[220:223], v[216:219], v[160:163]\n\t"
    "v_mul_f32 v132, v132, v243\n\t"
    "v_mul_f32 v133, v133, v243\n\t"
    "v_mul_f32 v134, v134, v243\n\t"
    "v_mul_f32 v135, v135, v243\n\t"
    "v_mul_f32 v136, v136, v243\n\t"
    "v_mul_f32 v137, v137, v243\n\t"
    "v_mul_f32 v138, v138, v243\n\t"
    "v_mul_f32 v139, v139, v243\n\t"
    "v_cvt_pk_bf16_f32 v132, v132, v133\n\t"
    "v_cvt_pk_bf16_f32 v133, v134, v135\n\t"
    "v_cvt_pk_bf16_f32 v134, v136, v137\n\t"
    "v_cvt_pk_bf16_f32 v135, v138, v139\n\t"
    "v_mul_f32 v140, v140, v243\n\t"
    "v_mul_f32 v141, v141, v243\n\t"
    "v_mul_f32 v142, v142, v243\n\t"
    "v_mul_f32 v143, v143, v243\n\t"
    "v_mul_f32 v144, v144, v243\n\t"
    "v_mul_f32 v145, v145, v243\n\t"
    "v_mul_f32 v146, v146, v243\n\t"
    "v_mul_f32 v147, v147, v243\n\t"
    "v_cvt_pk_bf16_f32 v140, v140, v141\n\t"
    "v_cvt_pk_bf16_f32 v141, v142, v143\n\t"
    "v_cvt_pk_bf16_f32 v142, v144, v145\n\t"
    "v_cvt_pk_bf16_f32 v143, v146, v147\n\t"
    "buffer_store_dwordx4 v[132:135], v241, s[48:51], s64 offen offset:0 sc0 sc1\n\t"
    "v_mfma_f32_16x16x32_bf16 a[4:7], a[0:3], v[132:135], a[4:7]\n\t"
    "v_mul_f32 v148, v148, v243\n\t"
    "v_mul_f32 v149, v149, v243\n\t"
    "v_mul_f32 v150, v150, v243\n\t"
    "v_mul_f32 v151, v151, v243\n\t"
    "v_mul_f32 v152, v152, v243\n\t"
    "v_mul_f32 v153, v153, v243\n\t"
    "v_mul_f32 v154, v154, v243\n\t"
    "v_mul_f32 v155, v155, v243\n\t"
    "v_cvt_pk_bf16_f32 v148, v148, v149\n\t"
    "v_cvt_pk_bf16_f32 v149, v150, v151\n\t"
    "v_cvt_pk_bf16_f32 v150, v152, v153\n\t"
    "v_cvt_pk_bf16_f32 v151, v154, v155\n\t"
    "buffer_store_dwordx4 v[140:143], v241, s[48:51], s64 offen offset:64 sc0 sc1\n\t"
    "v_mfma_f32_16x16x32_bf16 a[4:7], a[0:3], v[140:143], a[4:7]\n\t"
    "v_mul_f32 v156, v156, v243\n\t"
    "v_mul_f32 v157, v157, v243\n\t"
    "v_mul_f32 v158, v158, v243\n\t"
    "v_mul_f32 v159, v159, v243\n\t"
    "v_mul_f32 v160, v160, v243\n\t"
    "v_mul_f32 v161, v161, v243\n\t"
    "v_mul_f32 v162, v162, v243\n\t"
    "v_mul_f32 v163, v163, v243\n\t"
    "v_cvt_pk_bf16_f32 v156, v156, v157\n\t"
    "v_cvt_pk_bf16_f32 v157, v158, v159\n\t"
    "v_cvt_pk_bf16_f32 v158, v160, v161\n\t"
    "v_cvt_pk_bf16_f32 v159, v162, v163\n\t"
    "buffer_store_dwordx4 v[148:151], v241, s[48:51], s64 offen offset:128 sc0 sc1\n\t"
    "v_mfma_f32_16x16x32_bf16 a[4:7], a[0:3], v[148:151], a[4:7]\n\t"
    "buffer_store_dwordx4 v[156:159], v241, s[48:51], s64 offen offset:192 sc0 sc1\n\t"
    "v_mfma_f32_16x16x32_bf16 a[4:7], a[0:3], v[156:159], a[4:7]\n\t"
    "s_nop 7\n\t"
    "v_accvgpr_read_b32 v246, a4\n\t"
    "v_cmp_u_f32 vcc, v246, v246\n\t"
    "s_nop 1\n\t"
    "s_or_b64 s[62:63], s[62:63], vcc\n\t"
    "s_cmp_lg_u64 s[62:63], 0\n\t"
    "s_cselect_b32 %0, 1, 0\n\t"
    "s_branch END_%=\n\t"
    "WEND0_%=:\n\t"
    "W1_%=:\n\t"
    "WS1_%=:\n\t"
    "CPF1_%=:\n\t"
    "s_add_u32 s56, s53, 68616\n\t"
    "v_add_u32 v226, 4096, v239\n\t"
    "v_add_u32 v227, 8192, v239\n\t"
    "v_add_u32 v3, 12288, v239\n\t"
    "s_mov_b32 s58, 33792\n\t"
    "s_mov_b32 s60, 14\n\t"
    "s_mov_b32 s65, 0x3e0293ee\n\t"
    "s_mov_b64 s[62:63], 0\n\t"
    "v_bfe_u32 v242, v2, 2, 1\n\t"
    "v_and_b32 v243, 3, v2\n\t"
    "v_lshl_add_u32 v242, v242, 2, v243\n\t"
    "v_mul_u32_u24 v242, 0x420, v242\n\t"
    "v_lshrrev_b32 v243, 3, v2\n\t"
    "v_lshl_add_u32 v242, v243, 8, v242\n\t"
    "v_lshl_add_u32 v242, v255, 4, v242\n\t"
    "s_add_u32 s61, s53, 0\n\t"
    "v_add_u32 v236, s61, v242\n\t"
    "v_and_b32 v242, 1, v255\n\t"
    "v_bfe_u32 v243, v1, 2, 2\n\t"
    "v_lshl_add_u32 v242, v242, 2, v243\n\t"
    "v_mul_u32_u24 v242, 0x420, v242\n\t"
    "v_lshrrev_b32 v243, 1, v255\n\t"
    "v_lshl_add_u32 v242, v243, 8, v242\n\t"
    "v_and_b32 v243, 3, v1\n\t"
    "v_lshl_add_u32 v242, v243, 4, v242\n\t"
    "v_bfe_u32 v243, v1, 2, 1\n\t"
    "v_mul_u32_u24 v243, 24, v243\n\t"
    "v_sub_u32 v242, v242, v243\n\t"
    "s_add_u32 s61, s53, 67584\n\t"
    "v_add_u32 v237, s61, v242\n\t"
    "s_add_u32 s61, s53, 135168\n\t"
    "v_add_u32 v238, s61, v239\n\t"
    "v_lshlrev_b32 v241, 8, v2\n\t"
    "v_lshl_add_u32 v241, v255, 4, v241\n\t"
    "v_mov_b32 v242, 0x3f803f80\n\t"
    "v_accvgpr_write_b32 a248, v242\n\t"
    "v_accvgpr_write_b32 a249, v242\n\t"
    "v_accvgpr_write_b32 a250, v242\n\t"
    "v_accvgpr_write_b32 a251, v242\n\t"
    "v_mov_b32 v132, 0\n\t"
    "v_mov_b32 v133, 0\n\t"
    "v_mov_b32 v134, 0\n\t"
    "v_mov_b32 v135, 0\n\t"
    "v_mov_b32 v136, 0\n\t"
    "v_mov_b32 v137, 0\n\t"
    "v_mov_b32 v138, 0\n\t"
    "v_mov_b32 v139, 0\n\t"
    "v_mov_b32 v140, 0\n\t"
    "v_mov_b32 v141, 0\n\t"
    "v_mov_b32 v142, 0\n\t"
    "v_mov_b32 v143, 0\n\t"
    "v_mov_b32 v144, 0\n\t"
    "v_mov_b32 v145, 0\n\t"
    "v_mov_b32 v146, 0\n\t"
    "v_mov_b32 v147, 0\n\t"
    "v_mov_b32 v148, 0\n\t"
    "v_mov_b32 v149, 0\n\t"
    "v_mov_b32 v150, 0\n\t"
    "v_mov_b32 v151, 0\n\t"
    "v_mov_b32 v152, 0\n\t"
    "v_mov_b32 v153, 0\n\t"
    "v_mov_b32 v154, 0\n\t"
    "v_mov_b32 v155, 0\n\t"
    "v_mov_b32 v156, 0\n\t"
    "v_mov_b32 v157, 0\n\t"
    "v_mov_b32 v158, 0\n\t"
    "v_mov_b32 v159, 0\n\t"
    "v_mov_b32 v160, 0\n\t"
    "v_mov_b32 v161, 0\n\t"
    "v_mov_b32 v162, 0\n\t"
    "v_mov_b32 v163, 0\n\t"
    "v_mov_b32 v164, 0\n\t"
    "v_mov_b32 v165, 0\n\t"
    "v_mov_b32 v166, 0\n\t"
    "v_mov_b32 v167, 0\n\t"
    "v_mov_b32 v168, 0\n\t"
    "v_mov_b32 v169, 0\n\t"
    "v_mov_b32 v170, 0\n\t"
    "v_mov_b32 v171, 0\n\t"
    "v_mov_b32 v250, 0\n\t"
    "v_mov_b32 v251, 0\n\t"
    "v_mov_b32 v252, 0\n\t"
    "v_mov_b32 v253, 0\n\t"
    "v_mov_b32 v254, 0\n\t"
    "v_mov_b32 v242, 1.0\n\t"
    "v_accvgpr_write_b32 a252, v242\n\t"
    "s_mov_b32 s64, 24576\n\t"
    "buffer_load_dwordx4 v[116:119], v240, s[36:39], s64 offen offset:0\n\t"
    "buffer_load_dwordx4 v[120:123], v240, s[36:39], s64 offen offset:64\n\t"
    "buffer_load_dwordx4 v[124:127], v240, s[36:39], s64 offen offset:128\n\t"
    "buffer_load_dwordx4 v[128:131], v240, s[36:39], s64 offen offset:192\n\t"
    "s_waitcnt vmcnt(9)\n\t"
    "s_barrier\n\t"
    "ds_read_b128 a[96:99], v236 offset:0\n\t"
    "ds_read_b128 a[100:103], v236 offset:64\n\t"
    "ds_read_b128 a[104:107], v236 offset:128\n\t"
    "ds_read_b128 a[108:111], v236 offset:192\n\t"
    "ds_read_b128 a[112:115], v236 offset:512\n\t"
    "ds_read_b128 a[116:119], v236 offset:576\n\t"
    "ds_read_b128 a[120:123], v236 offset:640\n\t"
    "ds_read_b128 a[124:127], v236 offset:704\n\t"
    "ds_read_b128 a[128:131], v236 offset:8448\n\t"
    "ds_read_b128 a[132:135], v236 offset:8512\n\t"
    "ds_read_b128 a[136:139], v236 offset:8576\n\t"
    "ds_read_b128 a[140:143], v236 offset:8640\n\t"
    "ds_read_b128 a[144:147], v236 offset:8960\n\t"
    "ds_read_b128 a[148:151], v236 offset:9024\n\t"
    "ds_read_b128 a[152:155], v236 offset:9088\n\t"
    "ds_read_b128 a[156:159], v236 offset:9152\n\t"
    "s_add_u32 m0, s56, 0\n\t"
    "s_add_u32 s61, s59, 1024\n\t"
    "s_and_b32 s61, s61, 0x7ffff\n\t"
    "buffer_load_dwordx4 v239, s[44:47], s61 offen offset:0 lds\n\t"
    "s_add_u32 m0, s56, 4224\n\t"
    "s_nop 0\n\t"
    "buffer_load_dwordx4 v226, s[44:47], s61 offen offset:0 lds\n\t"
    "s_add_u32 m0, s56, 8448\n\t"
    "s_nop 0\n\t"
    "buffer_load_dwordx4 v227, s[44:47], s61 offen offset:0 lds\n\t"
    "s_add_u32 m0, s56, 12672\n\t"
    "s_nop 0\n\t"
    "buffer_load_dwordx4 v3, s[44:47], s61 offen offset:0 lds\n\t"
    "s_waitcnt vmcnt(12)\n\t"
    "v_lshlrev_b32 v242, 16, v100\n\t"
    "v_and_b32 v243, 0xffff0000, v100\n\t"
    "v_mul_f32 v242, s65, v242\n\t"
    "v_mul_f32 v243, s65, v243\n\t"
    "v_cvt_pk_bf16_f32 v242, v242, v243\n\t"
    "v_accvgpr_write_b32 a0, v242\n\t"
    "s_mov_b32 s64, 81920\n\t"
    "buffer_load_dwordx4 v[68:71], v240, s[36:39], s64 offen offset:0\n\t"
    "buffer_load_dwordx4 v[72:75], v240, s[36:39], s64 offen offset:64\n\t"
    "buffer_load_dwordx4 v[76:79], v240, s[36:39], s64 offen offset:128\n\t"
    "buffer_load_dwordx4 v[80:83], v240, s[36:39], s64 offen offset:192\n\t"
    "v_lshlrev_b32 v244, 16, v101\n\t"
    "v_and_b32 v245, 0xffff0000, v101\n\t"
    "v_mul_f32 v244, s65, v244\n\t"
    "v_mul_f32 v245, s65, v245\n\t"
    "v_cvt_pk_bf16_f32 v244, v244, v245\n\t"
    "v_accvgpr_write_b32 a1, v244\n\t"
    "v_lshlrev_b32 v246, 16, v102\n\t"
    "v_and_b32 v247, 0xffff0000, v102\n\t"
    "v_mul_f32 v246, s65, v246\n\t"
    "v_mul_f32 v247, s65, v247\n\t"
    "v_cvt_pk_bf16_f32 v246, v246, v247\n\t"
    "v_accvgpr_write_b32 a2, v246\n\t"
    "v_lshlrev_b32 v248, 16, v103\n\t"
    "v_and_b32 v249, 0xffff0000, v103\n\t"
    "v_mul_f32 v248, s65, v248\n\t"
    "v_mul_f32 v249, s65, v249\n\t"
    "v_cvt_pk_bf16_f32 v248, v248, v249\n\t"
    "v_accvgpr_write_b32 a3, v248\n\t"
    "s_waitcnt vmcnt(15)\n\t"
    "v_lshlrev_b32 v242, 16, v104\n\t"
    "v_and_b32 v243, 0xffff0000, v104\n\t"
    "v_mul_f32 v242, s65, v242\n\t"
    "v_mul_f32 v243, s65, v243\n\t"
    "v_cvt_pk_bf16_f32 v242, v242, v243\n\t"
    "v_accvgpr_write_b32 a4, v242\n\t"
    "v_lshlrev_b32 v244, 16, v105\n\t"
    "v_and_b32 v245, 0xffff0000, v105\n\t"
    "v_mul_f32 v244, s65, v244\n\t"
    "v_mul_f32 v245, s65, v245\n\t"
    "v_cvt_pk_bf16_f32 v244, v244, v245\n\t"
    "v_accvgpr_write_b32 a5, v244\n\t"
    "v_lshlrev_b32 v246, 16, v106\n\t"
    "v_and_b32 v247, 0xffff0000, v106\n\t"
    "v_mul_f32 v246, s65, v246\n\t"
    "v_mul_f32 v247, s65, v247\n\t"
    "v_cvt_pk_bf16_f32 v246, v246, v247\n\t"
    "v_accvgpr_write_b32 a6, v246\n\t"
    "v_lshlrev_b32 v248, 16, v107\n\t"
    "v_and_b32 v249, 0xffff0000, v107\n\t"
    "v_mul_f32 v248, s65, v248\n\t"
    "v_mul_f32 v249, s65, v249\n\t"
    "v_cvt_pk_bf16_f32 v248, v248, v249\n\t"
    "v_accvgpr_write_b32 a7, v248\n\t"
    "s_waitcnt vmcnt(14)\n\t"
    "v_lshlrev_b32 v242, 16, v108\n\t"
    "v_and_b32 v243, 0xffff0000, v108\n\t"
    "v_mul_f32 v242, s65, v242\n\t"
    "v_mul_f32 v243, s65, v243\n\t"
    "v_cvt_pk_bf16_f32 v242, v242, v243\n\t"
    "v_accvgpr_write_b32 a8, v242\n\t"
    "v_lshlrev_b32 v244, 16, v109\n\t"
    "v_and_b32 v245, 0xffff0000, v109\n\t"
    "v_mul_f32 v244, s65, v244\n\t"
    "v_mul_f32 v245, s65, v245\n\t"
    "v_cvt_pk_bf16_f32 v244, v244, v245\n\t"
    "v_accvgpr_write_b32 a9, v244\n\t"
    "v_lshlrev_b32 v246, 16, v110\n\t"
    "v_and_b32 v247, 0xffff0000, v110\n\t"
    "v_mul_f32 v246, s65, v246\n\t"
    "v_mul_f32 v247, s65, v247\n\t"
    "v_cvt_pk_bf16_f32 v246, v246, v247\n\t"
    "v_accvgpr_write_b32 a10, v246\n\t"
    "v_lshlrev_b32 v248, 16, v111\n\t"
    "v_and_b32 v249, 0xffff0000, v111\n\t"
    "v_mul_f32 v248, s65, v248\n\t"
    "v_mul_f32 v249, s65, v249\n\t"
    "v_cvt_pk_bf16_f32 v248, v248, v249\n\t"
    "v_accvgpr_write_b32 a11, v248\n\t"
    "s_waitcnt vmcnt(13)\n\t"
    "v_lshlrev_b32 v242, 16, v112\n\t"
    "v_and_b32 v243, 0xffff0000, v112\n\t"
    "v_mul_f32 v242, s65, v242\n\t"
    "v_mul_f32 v243, s65, v243\n\t"
    "v_cvt_pk_bf16_f32 v242, v242, v243\n\t"
    "v_accvgpr_write_b32 a12, v242\n\t"
    "v_lshlrev_b32 v244, 16, v113\n\t"
    "v_and_b32 v245, 0xffff0000, v113\n\t"
    "v_mul_f32 v244, s65, v244\n\t"
    "v_mul_f32 v245, s65, v245\n\t"
    "v_cvt_pk_bf16_f32 v244, v244, v245\n\t"
    "v_accvgpr_write_b32 a13, v244\n\t"
    "v_lshlrev_b32 v246, 16, v114\n\t"
    "v_and_b32 v247, 0xffff0000, v114\n\t"
    "v_mul_f32 v246, s65, v246\n\t"
    "v_mul_f32 v247, s65, v247\n\t"
    "v_cvt_pk_bf16_f32 v246, v246, v247\n\t"
    "v_accvgpr_write_b32 a14, v246\n\t"
    "v_lshlrev_b32 v248, 16, v115\n\t"
    "v_and_b32 v249, 0xffff0000, v115\n\t"
    "v_mul_f32 v248, s65, v248\n\t"
    "v_mul_f32 v249, s65, v249\n\t"
    "v_cvt_pk_bf16_f32 v248, v248, v249\n\t"
    "v_accvgpr_write_b32 a15, v248\n\t"
    "s_waitcnt lgkmcnt(15)\n\t"
    "v_mfma_f32_16x16x32_bf16 v[172:175], a[96:99], a[0:3], 0\n\t"
    "s_waitcnt lgkmcnt(14)\n\t"
    "v_mfma_f32_16x16x32_bf16 v[172:175], a[100:103], a[4:7], v[172:175]\n\t"
    "s_waitcnt lgkmcnt(13)\n\t"
    "s_waitcnt vmcnt(11)\n\t"
    "v_lshlrev_b32 v242, 16, v116\n\t"
    "v_and_b32 v243, 0xffff0000, v116\n\t"
    "v_mul_f32 v242, s65, v242\n\t"
    "v_mul_f32 v243, s65, v243\n\t"
    "v_cvt_pk_bf16_f32 v242, v242, v243\n\t"
    "v_accvgpr_write_b32 a16, v242\n\t"
    "s_mov_b32 s64, 28672\n\t"
    "buffer_load_dwordx4 v[84:87], v240, s[36:39], s64 offen offset:0\n\t"
    "buffer_load_dwordx4 v[88:91], v240, s[36:39], s64 offen offset:64\n\t"
    "buffer_load_dwordx4 v[92:95], v240, s[36:39], s64 offen offset:128\n\t"
    "buffer_load_dwordx4 v[96:99], v240, s[36:39], s64 offen offset:192\n\t"
    "v_mfma_f32_16x16x32_bf16 v[172:175], a[104:107], a[8:11], v[172:175]\n\t"
    "s_waitcnt lgkmcnt(12)\n\t"
    "v_lshlrev_b32 v244, 16, v117\n\t"
    "v_and_b32 v245, 0xffff0000, v117\n\t"
    "v_mul_f32 v244, s65, v244\n\t"
    "v_mul_f32 v245, s65, v245\n\t"
    "v_cvt_pk_bf16_f32 v244, v244, v245\n\t"
    "v_accvgpr_write_b32 a17, v244\n\t"
    "v_mfma_f32_16x16x32_bf16 v[172:175], a[108:111], a[12:15], v[172:175]\n\t"
    "s_waitcnt lgkmcnt(11)\n\t"
    "v_lshlrev_b32 v246, 16, v118\n\t"
    "v_and_b32 v247, 0xffff0000, v118\n\t"
    "v_mul_f32 v246, s65, v246\n\t"
    "v_mul_f32 v247, s65, v247\n\t"
    "v_cvt_pk_bf16_f32 v246, v246, v247\n\t"
    "v_accvgpr_write_b32 a18, v246\n\t"
    "v_lshlrev_b32 v248, 16, v119\n\t"
    "v_mfma_f32_16x16x32_bf16 v[176:179], a[112:115], a[0:3], 0\n\t"
    "s_waitcnt lgkmcnt(10)\n\t"
    "v_and_b32 v249, 0xffff0000, v119\n\t"
    "v_mul_f32 v248, s65, v248\n\t"
    "v_mul_f32 v249, s65, v249\n\t"
    "v_cvt_pk_bf16_f32 v248, v248, v249\n\t"
    "v_accvgpr_write_b32 a19, v248\n\t"
    "s_waitcnt vmcnt(14)\n\t"
    "v_lshlrev_b32 v242, 16, v120\n\t"
    "v_mfma_f32_16x16x32_bf16 v[176:179], a[116:119], a[4:7], v[176:179]\n\t"
    "s_waitcnt lgkmcnt(9)\n\t"
    "v_and_b32 v243, 0xffff0000, v120\n\t"
    "v_mul_f32 v242, s65, v242\n\t"
    "v_mul_f32 v243, s65, v243\n\t"
    "v_cvt_pk_bf16_f32 v242, v242, v243\n\t"
    "v_accvgpr_write_b32 a20, v242\n\t"
    "v_lshlrev_b32 v244, 16, v121\n\t"
    "v_and_b32 v245, 0xffff0000, v121\n\t"
    "v_mfma_f32_16x16x32_bf16 v[176:179], a[120:123], a[8:11], v[176:179]\n\t"
    "s_waitcnt lgkmcnt(0)\n\t"
    "v_mul_f32 v244, s65, v244\n\t"
    "v_mul_f32 v245, s65, v245\n\t"
    "v_cvt_pk_bf16_f32 v244, v244, v245\n\t"
    "v_accvgpr_write_b32 a21, v244\n\t"
    "v_lshlrev_b32 v246, 16, v122\n\t"
    "v_and_b32 v247, 0xffff0000, v122\n\t"
    "v_mfma_f32_16x16x32_bf16 v[176:179], a[124:127], a[12:15], v[176:179]\n\t"
    "v_mul_f32 v246, s65, v246\n\t"
    "v_mul_f32 v247, s65, v247\n\t"
    "v_cvt_pk_bf16_f32 v246, v246, v247\n\t"
    "v_accvgpr_write_b32 a22, v246\n\t"
    "v_lshlrev_b32 v248, 16, v123\n\t"
    "v_and_b32 v249, 0xffff0000, v123\n\t"
    "v_mfma_f32_16x16x32_bf16 v[180:183], a[128:131], a[0:3], 0\n\t"
    "v_mul_f32 v248, s65, v248\n\t"
    "v_mul_f32 v249, s65, v249\n\t"
    "v_cvt_pk_bf16_f32 v248, v248, v249\n\t"
    "v_accvgpr_write_b32 a23, v248\n\t"
    "s_waitcnt vmcnt(13)\n\t"
    "v_lshlrev_b32 v242, 16, v124\n\t"
    "v_and_b32 v243, 0xffff0000, v124\n\t"
    "v_mul_f32 v242, s65, v242\n\t"
    "v_mfma_f32_16x16x32_bf16 v[180:183], a[132:135], a[4:7], v[180:183]\n\t"
    "v_mul_f32 v243, s65, v243\n\t"
    "v_cvt_pk_bf16_f32 v242, v242, v243\n\t"
    "v_accvgpr_write_b32 a24, v242\n\t"
    "v_lshlrev_b32 v244, 16, v125\n\t"
    "v_and_b32 v245, 0xffff0000, v125\n\t"
    "v_mul_f32 v244, s65, v244\n\t"
    "v_mfma_f32_16x16x32_bf16 v[180:183], a[136:139], a[8:11], v[180:183]\n\t"
    "v_mul_f32 v245, s65, v245\n\t"
    "v_cvt_pk_bf16_f32 v244, v244, v245\n\t"
    "v_accvgpr_write_b32 a25, v244\n\t"
    "v_lshlrev_b32 v246, 16, v126\n\t"
    "v_and_b32 v247, 0xffff0000, v126\n\t"
    "v_mul_f32 v246, s65, v246\n\t"
    "v_mul_f32 v247, s65, v247\n\t"
    "v_mfma_f32_16x16x32_bf16 v[180:183], a[140:143], a[12:15], v[180:183]\n\t"
    "v_cvt_pk_bf16_f32 v246, v246, v247\n\t"
    "v_accvgpr_write_b32 a26, v246\n\t"
    "v_lshlrev_b32 v248, 16, v127\n\t"
    "v_and_b32 v249, 0xffff0000, v127\n\t"
    "v_mul_f32 v248, s65, v248\n\t"
    "v_mul_f32 v249, s65, v249\n\t"
    "v_exp_f32 v172, v172\n\t"
    "v_mfma_f32_16x16x32_bf16 v[184:187], a[144:147], a[0:3], 0\n\t"
    "v_cvt_pk_bf16_f32 v248, v248, v249\n\t"
    "v_accvgpr_write_b32 a27, v248\n\t"
    "s_waitcnt vmcnt(12)\n\t"
    "v_lshlrev_b32 v242, 16, v128\n\t"
    "v_and_b32 v243, 0xffff0000, v128\n\t"
    "v_mul_f32 v242, s65, v242\n\t"
    "v_mul_f32 v243, s65, v243\n\t"
    "v_exp_f32 v173, v173\n\t"
    "v_mfma_f32_16x16x32_bf16 v[184:187], a[148:151], a[4:7], v[184:187]\n\t"
    "v_cvt_pk_bf16_f32 v242, v242, v243\n\t"
    "v_accvgpr_write_b32 a28, v242\n\t"
    "v_lshlrev_b32 v244, 16, v129\n\t"
    "v_and_b32 v245, 0xffff0000, v129\n\t"
    "v_mul_f32 v244, s65, v244\n\t"
    "v_mul_f32 v245, s65, v245\n\t"
    "v_cvt_pk_bf16_f32 v244, v244, v245\n\t"
    "v_cvt_pk_bf16_f32 v204, v172, v173\n\t"
    "v_add_f32 v250, v250, v172\n\t"
    "v_mfma_f32_16x16x32_bf16 v[184:187], a[152:155], a[8:11], v[184:187]\n\t"
    "v_accvgpr_write_b32 a29, v244\n\t"
    "v_lshlrev_b32 v246, 16, v130\n\t"
    "v_and_b32 v247, 0xffff0000, v130\n\t"
    "v_mul_f32 v246, s65, v246\n\t"
    "v_mul_f32 v247, s65, v247\n\t"
    "v_cvt_pk_bf16_f32 v246, v246, v247\n\t"
    "v_exp_f32 v174, v174\n\t"
    "v_mfma_f32_16x16x32_bf16 v[184:187], a[156:159], a[12:15], v[184:187]\n\t"
    "v_accvgpr_write_b32 a30, v246\n\t"
    "v_lshlrev_b32 v248, 16, v131\n\t"
    "v_and_b32 v249, 0xffff0000, v131\n\t"
    "v_mul_f32 v248, s65, v248\n\t"
    "v_mul_f32 v249, s65, v249\n\t"
    "v_cvt_pk_bf16_f32 v248, v248, v249\n\t"
    "v_accvgpr_write_b32 a31, v248\n\t"
    "v_exp_f32 v175, v175\n\t"
    "s_waitcnt vmcnt(8)\n\t"
    "s_barrier\n\t"
    "v_cvt_pk_bf16_f32 v205, v174, v175\n\t"
    "v_add_f32 v250, v250, v173\n\t"
    "s_mov_b32 s64, 32768\n\t"
    "buffer_load_dwordx4 v[100:103], v240, s[36:39], s64 offen offset:0\n\t"
    "buffer_load_dwordx4 v[104:107], v240, s[36:39], s64 offen offset:64\n\t"
    "buffer_load_dwordx4 v[108:111], v240, s[36:39], s64 offen offset:128\n\t"
    "buffer_load_dwordx4 v[112:115], v240, s[36:39], s64 offen offset:192\n\t"
    "v_exp_f32 v176, v176\n\t"
    "v_exp_f32 v177, v177\n\t"
    "ds_read_b64_tr_b16 a[160:161], v237 offset:0\n\t"
    "ds_read_b64_tr_b16 a[162:163], v237 offset:512\n\t"
    "v_cvt_pk_bf16_f32 v206, v176, v177\n\t"
    "v_add_f32 v250, v250, v174\n\t"
    "s_waitcnt vmcnt(11)\n\t"
    "v_lshlrev_b32 v242, 16, v68\n\t"
    "v_and_b32 v243, 0xffff0000, v68\n\t"
    "v_mul_f32 v242, s65, v242\n\t"
    "v_mul_f32 v243, s65, v243\n\t"
    "v_cvt_pk_bf16_f32 v242, v242, v243\n\t"
    "v_accvgpr_write_b32 a80, v242\n\t"
    "s_add_u32 m0, s55, 16896\n\t"
    "s_add_u32 s61, s59, 17408\n\t"
    "s_and_b32 s61, s61, 0x7ffff\n\t"
    "buffer_load_dwordx4 v239, s[40:43], s61 offen offset:0 nt lds\n\t"
    "s_add_u32 m0, s55, 21120\n\t"
    "s_nop 0\n\t"
    "buffer_load_dwordx4 v226, s[40:43], s61 offen offset:0 nt lds\n\t"
    "s_add_u32 m0, s55, 25344\n\t"
    "s_nop 0\n\t"
    "buffer_load_dwordx4 v227, s[40:43], s61 offen offset:0 nt lds\n\t"
    "s_add_u32 m0, s55, 29568\n\t"
    "s_nop 0\n\t"
    "buffer_load_dwordx4 v3, s[40:43], s61 offen offset:0 nt lds\n\t"
    "ds_read_b64_tr_b16 a[192:193], v237 offset:8448\n\t"
    "ds_read_b64_tr_b16 a[194:195], v237 offset:8960\n\t"
    "v_lshlrev_b32 v244, 16, v69\n\t"
    "v_and_b32 v245, 0xffff0000, v69\n\t"
    "v_mul_f32 v244, s65, v244\n\t"
    "v_mul_f32 v245, s65, v245\n\t"
    "v_cvt_pk_bf16_f32 v244, v244, v245\n\t"
    "v_accvgpr_write_b32 a81, v244\n\t"
    "v_lshlrev_b32 v246, 16, v70\n\t"
    "v_exp_f32 v178, v178\n\t"
    "v_and_b32 v247, 0xffff0000, v70\n\t"
    "v_mul_f32 v246, s65, v246\n\t"
    "v_mul_f32 v247, s65, v247\n\t"
    "v_cvt_pk_bf16_f32 v246, v246, v247\n\t"
    "v_accvgpr_write_b32 a82, v246\n\t"
    "v_lshlrev_b32 v248, 16, v71\n\t"
    "v_and_b32 v249, 0xffff0000, v71\n\t"
    "ds_read_b64_tr_b16 a[164:165], v237 offset:8\n\t"
    "ds_read_b64_tr_b16 a[166:167], v237 offset:520\n\t"
    "v_mul_f32 v248, s65, v248\n\t"
    "v_mul_f32 v249, s65, v249\n\t"
    "v_cvt_pk_bf16_f32 v248, v248, v249\n\t"
    "v_accvgpr_write_b32 a83, v248\n\t"
    "s_waitcnt vmcnt(14)\n\t"
    "v_lshlrev_b32 v242, 16, v72\n\t"
    "v_and_b32 v243, 0xffff0000, v72\n\t"
    "v_mul_f32 v242, s65, v242\n\t"
    "v_exp_f32 v179, v179\n\t"
    "v_mul_f32 v243, s65, v243\n\t"
    "v_cvt_pk_bf16_f32 v242, v242, v243\n\t"
    "v_accvgpr_write_b32 a84, v242\n\t"
    "v_lshlrev_b32 v244, 16, v73\n\t"
    "v_and_b32 v245, 0xffff0000, v73\n\t"
    "v_mul_f32 v244, s65, v244\n\t"
    "v_mul_f32 v245, s65, v245\n\t"
    "ds_read_b64_tr_b16 a[196:197], v237 offset:8456\n\t"
    "ds_read_b64_tr_b16 a[198:199], v237 offset:8968\n\t"
    "v_cvt_pk_bf16_f32 v244, v244, v245\n\t"
    "v_accvgpr_write_b32 a85, v244\n\t"
    "v_lshlrev_b32 v246, 16, v74\n\t"
    "v_and_b32 v247, 0xffff0000, v74\n\t"
    "v_mul_f32 v246, s65, v246\n\t"
    "v_mul_f32 v247, s65, v247\n\t"
    "v_cvt_pk_bf16_f32 v246, v246, v247\n\t"
    "v_cvt_pk_bf16_f32 v207, v178, v179\n\t"
    "v_add_f32 v250, v250, v175\n\t"
    "v_accvgpr_write_b32 a86, v246\n\t"
    "v_lshlrev_b32 v248, 16, v75\n\t"
    "v_and_b32 v249, 0xffff0000, v75\n\t"
    "v_mul_f32 v248, s65, v248\n\t"
    "v_mul_f32 v249, s65, v249\n\t"
    "v_cvt_pk_bf16_f32 v248, v248, v249\n\t"
    "v_accvgpr_write_b32 a87, v248\n\t"
    "ds_read_b64_tr_b16 a[168:169], v237 offset:64\n\t"
    "ds_read_b64_tr_b16 a[170:171], v237 offset:576\n\t"
    "s_waitcnt vmcnt(13)\n\t"
    "v_lshlrev_b32 v242, 16, v76\n\t"
    "v_and_b32 v243, 0xffff0000, v76\n\t"
    "v_mul_f32 v242, s65, v242\n\t"
    "v_mul_f32 v243, s65, v243\n\t"
    "v_cvt_pk_bf16_f32 v242, v242, v243\n\t"
    "v_accvgpr_write_b32 a88, v242\n\t"
    "v_exp_f32 v180, v180\n\t"
    "v_lshlrev_b32 v244, 16, v77\n\t"
    "v_and_b32 v245, 0xffff0000, v77\n\t"
    "v_mul_f32 v244, s65, v244\n\t"
    "v_mul_f32 v245, s65, v245\n\t"
    "v_cvt_pk_bf16_f32 v244, v244, v245\n\t"
    "v_accvgpr_write_b32 a89, v244\n\t"
    "v_lshlrev_b32 v246, 16, v78\n\t"
    "ds_read_b64_tr_b16 a[200:201], v237 offset:8512\n\t"
    "ds_read_b64_tr_b16 a[202:203], v237 offset:9024\n\t"
    "v_and_b32 v247, 0xffff0000, v78\n\t"
    "v_mul_f32 v246, s65, v246\n\t"
    "v_mul_f32 v247, s65, v247\n\t"
    "v_cvt_pk_bf16_f32 v246, v246, v247\n\t"
    "v_accvgpr_write_b32 a90, v246\n\t"
    "v_lshlrev_b32 v248, 16, v79\n\t"
    "v_and_b32 v249, 0xffff0000, v79\n\t"
    "v_exp_f32 v181, v181\n\t"
    "v_mul_f32 v248, s65, v248\n\t"
    "v_mul_f32 v249, s65, v249\n\t"
    "v_cvt_pk_bf16_f32 v248, v248, v249\n\t"
    "v_accvgpr_write_b32 a91, v248\n\t"
    "s_waitcnt vmcnt(12)\n\t"
    "v_lshlrev_b32 v242, 16, v80\n\t"
    "v_and_b32 v243, 0xffff0000, v80\n\t"
    "v_mul_f32 v242, s65, v242\n\t"
    "ds_read_b64_tr_b16 a[172:173], v237 offset:72\n\t"
    "ds_read_b64_tr_b16 a[174:175], v237 offset:584\n\t"
    "v_mul_f32 v243, s65, v243\n\t"
    "v_cvt_pk_bf16_f32 v242, v242, v243\n\t"
    "v_accvgpr_write_b32 a92, v242\n\t"
    "v_lshlrev_b32 v244, 16, v81\n\t"
    "v_and_b32 v245, 0xffff0000, v81\n\t"
    "v_mul_f32 v244, s65, v244\n\t"
    "v_mul_f32 v245, s65, v245\n\t"
    "v_cvt_pk_bf16_f32 v208, v180, v181\n\t"
    "v_add_f32 v250, v250, v176\n\t"
    "v_cvt_pk_bf16_f32 v244, v244, v245\n\t"
    "v_accvgpr_write_b32 a93, v244\n\t"
    "v_lshlrev_b32 v246, 16, v82\n\t"
    "v_and_b32 v247, 0xffff0000, v82\n\t"
    "v_mul_f32 v246, s65, v246\n\t"
    "v_mul_f32 v247, s65, v247\n\t"
    "v_cvt_pk_bf16_f32 v246, v246, v247\n\t"
    "v_accvgpr_write_b32 a94, v246\n\t"
    "v_lshlrev_b32 v248, 16, v83\n\t"
    "v_and_b32 v249, 0xffff0000, v83\n\t"
    "v_mul_f32 v248, s65, v248\n\t"
    "v_mul_f32 v249, s65, v249\n\t"
    "v_cvt_pk_bf16_f32 v248, v248, v249\n\t"
    "v_accvgpr_write_b32 a95, v248\n\t"
    "v_exp_f32 v182, v182\n\t"
    "v_mfma_f32_16x16x32_bf16 v[188:191], a[96:99], a[16:19], 0\n\t"
    "ds_read_b64_tr_b16 a[204:205], v237 offset:8520\n\t"
    "ds_read_b64_tr_b16 a[206:207], v237 offset:9032\n\t"
    "v_mfma_f32_16x16x32_bf16 v[188:191], a[100:103], a[20:23], v[188:191]\n\t"
    "v_exp_f32 v183, v183\n\t"
    "v_mfma_f32_16x16x32_bf16 v[188:191], a[104:107], a[24:27], v[188:191]\n\t"
    "s_waitcnt vmcnt(11)\n\t"
    "v_lshlrev_b32 v242, 16, v84\n\t"
    "v_and_b32 v243, 0xffff0000, v84\n\t"
    "v_mul_f32 v242, s65, v242\n\t"
    "s_add_u32 m0, s56, 16896\n\t"
    "s_add_u32 s61, s59, 17408\n\t"
    "s_and_b32 s61, s61, 0x7ffff\n\t"
    "buffer_load_dwordx4 v239, s[44:47], s61 offen offset:0 lds\n\t"
    "s_add_u32 m0, s56, 21120\n\t"
    "s_nop 0\n\t"
    "buffer_load_dwordx4 v226, s[44:47], s61 offen offset:0 lds\n\t"
    "s_add_u32 m0, s56, 25344\n\t"
    "s_nop 0\n\t"
    "buffer_load_dwordx4 v227, s[44:47], s61 offen offset:0 lds\n\t"
    "s_add_u32 m0, s56, 29568\n\t"
    "s_nop 0\n\t"
    "buffer_load_dwordx4 v3, s[44:47], s61 offen offset:0 lds\n\t"
    "ds_read_b64_tr_b16 a[176:177], v237 offset:128\n\t"
    "ds_read_b64_tr_b16 a[178:179], v237 offset:640\n\t"
    "v_mfma_f32_16x16x32_bf16 v[188:191], a[108:111], a[28:31], v[188:191]\n\t"
    "v_mul_f32 v243, s65, v243\n\t"
    "v_cvt_pk_bf16_f32 v242, v242, v243\n\t"
    "v_accvgpr_write_b32 a32, v242\n\t"
    "v_cvt_pk_bf16_f32 v209, v182, v183\n\t"
    "v_add_f32 v250, v250, v177\n\t"
    "v_mfma_f32_16x16x32_bf16 v[192:195], a[112:115], a[16:19], 0\n\t"
    "v_lshlrev_b32 v244, 16, v85\n\t"
    "v_and_b32 v245, 0xffff0000, v85\n\t"
    "v_mul_f32 v244, s65, v244\n\t"
    "ds_read_b64_tr_b16 a[208:209], v237 offset:8576\n\t"
    "ds_read_b64_tr_b16 a[210:211], v237 offset:9088\n\t"
    "v_mfma_f32_16x16x32_bf16 v[192:195], a[116:119], a[20:23], v[192:195]\n\t"
    "v_mul_f32 v245, s65, v245\n\t"
    "v_cvt_pk_bf16_f32 v244, v244, v245\n\t"
    "v_accvgpr_write_b32 a33, v244\n\t"
    "v_exp_f32 v184, v184\n\t"
    "v_mfma_f32_16x16x32_bf16 v[192:195], a[120:123], a[24:27], v[192:195]\n\t"
    "v_lshlrev_b32 v246, 16, v86\n\t"
    "v_and_b32 v247, 0xffff0000, v86\n\t"
    "v_mul_f32 v246, s65, v246\n\t"
    "ds_read_b64_tr_b16 a[180:181], v237 offset:136\n\t"
    "ds_read_b64_tr_b16 a[182:183], v237 offset:648\n\t"
    "v_mfma_f32_16x16x32_bf16 v[192:195], a[124:127], a[28:31], v[192:195]\n\t"
    "v_mul_f32 v247, s65, v247\n\t"
    "v_cvt_pk_bf16_f32 v246, v246, v247\n\t"
    "v_accvgpr_write_b32 a34, v246\n\t"
    "v_exp_f32 v185, v185\n\t"
    "v_mfma_f32_16x16x32_bf16 v[196:199], a[128:131], a[16:19], 0\n\t"
    "v_lshlrev_b32 v248, 16, v87\n\t"
    "v_and_b32 v249, 0xffff0000, v87\n\t"
    "v_mul_f32 v248, s65, v248\n\t"
    "ds_read_b64_tr_b16 a[212:213], v237 offset:8584\n\t"
    "ds_read_b64_tr_b16 a[214:215], v237 offset:9096\n\t"
    "v_mfma_f32_16x16x32_bf16 v[196:199], a[132:135], a[20:23], v[196:199]\n\t"
    "v_mul_f32 v249, s65, v249\n\t"
    "v_cvt_pk_bf16_f32 v248, v248, v249\n\t"
    "v_accvgpr_write_b32 a35, v248\n\t"
    "v_cvt_pk_bf16_f32 v210, v184, v185\n\t"
    "v_add_f32 v250, v250, v178\n\t"
    "v_mfma_f32_16x16x32_bf16 v[196:199], a[136:139], a[24:27], v[196:199]\n\t"
    "s_waitcnt vmcnt(14)\n\t"
    "v_lshlrev_b32 v242, 16, v88\n\t"
    "v_and_b32 v243, 0xffff0000, v88\n\t"
    "v_mul_f32 v242, s65, v242\n\t"
    "ds_read_b64_tr_b16 a[184:185], v237 offset:192\n\t"
    "ds_read_b64_tr_b16 a[186:187], v237 offset:704\n\t"
    "v_mfma_f32_16x16x32_bf16 v[196:199], a[140:143], a[28:31], v[196:199]\n\t"
    "v_mul_f32 v243, s65, v243\n\t"
    "v_cvt_pk_bf16_f32 v242, v242, v243\n\t"
    "v_accvgpr_write_b32 a36, v242\n\t"
    "v_exp_f32 v186, v186\n\t"
    "v_mfma_f32_16x16x32_bf16 v[200:203], a[144:147], a[16:19], 0\n\t"
    "v_lshlrev_b32 v244, 16, v89\n\t"
    "v_and_b32 v245, 0xffff0000, v89\n\t"
    "v_mul_f32 v244, s65, v244\n\t"
    "v_mul_f32 v245, s65, v245\n\t"
    "ds_read_b64_tr_b16 a[216:217], v237 offset:8640\n\t"
    "ds_read_b64_tr_b16 a[218:219], v237 offset:9152\n\t"
    "v_mfma_f32_16x16x32_bf16 v[200:203], a[148:151], a[20:23], v[200:203]\n\t"
    "v_cvt_pk_bf16_f32 v244, v244, v245\n\t"
    "v_accvgpr_write_b32 a37, v244\n\t"
    "v_lshlrev_b32 v246, 16, v90\n\t"
    "v_exp_f32 v187, v187\n\t"
    "v_mfma_f32_16x16x32_bf16 v[200:203], a[152:155], a[24:27], v[200:203]\n\t"
    "v_and_b32 v247, 0xffff0000, v90\n\t"
    "v_mul_f32 v246, s65, v246\n\t"
    "v_mul_f32 v247, s65, v247\n\t"
    "ds_read_b64_tr_b16 a[188:189], v237 offset:200\n\t"
    "ds_read_b64_tr_b16 a[190:191], v237 offset:712\n\t"
    "v_mfma_f32_16x16x32_bf16 v[200:203], a[156:159], a[28:31], v[200:203]\n\t"
    "v_cvt_pk_bf16_f32 v246, v246, v247\n\t"
    "v_accvgpr_write_b32 a38, v246\n\t"
    "v_lshlrev_b32 v248, 16, v91\n\t"
    "v_cvt_pk_bf16_f32 v211, v186, v187\n\t"
    "v_add_f32 v250, v250, v179\n\t"
    "v_mfma_f32_16x16x32_bf16 v[220:223], a[112:115], a[80:83], 0\n\t"
    "v_and_b32 v249, 0xffff0000, v91\n\t"
    "v_mul_f32 v248, s65, v248\n\t"
    "v_mul_f32 v249, s65, v249\n\t"
    "ds_read_b64_tr_b16 a[220:221], v237 offset:8648\n\t"
    "ds_read_b64_tr_b16 a[222:223], v237 offset:9160\n\t"
    "v_mfma_f32_16x16x32_bf16 v[220:223], a[116:119], a[84:87], v[220:223]\n\t"
    "v_cvt_pk_bf16_f32 v248, v248, v249\n\t"
    "v_accvgpr_write_b32 a39, v248\n\t"
    "s_waitcnt vmcnt(13)\n\t"
    "v_lshlrev_b32 v242, 16, v92\n\t"
    "v_exp_f32 v188, v188\n\t"
    "v_mfma_f32_16x16x32_bf16 v[220:223], a[120:123], a[88:91], v[220:223]\n\t"
    "v_and_b32 v243, 0xffff0000, v92\n\t"
    "v_mul_f32 v242, s65, v242\n\t"
    "v_mul_f32 v243, s65, v243\n\t"
    "v_mfma_f32_16x16x32_bf16 v[220:223], a[124:127], a[92:95], v[220:223]\n\t"
    "s_waitcnt lgkmcnt(4)\n\t"
    "v_cvt_pk_bf16_f32 v242, v242, v243\n\t"
    "v_accvgpr_write_b32 a40, v242\n\t"
    "v_lshlrev_b32 v244, 16, v93\n\t"
    "v_exp_f32 v189, v189\n\t"
    "v_mfma_f32_16x16x32_bf16 v[4:7], a[160:163], v[204:207], 0\n\t"
    "v_and_b32 v245, 0xffff0000, v93\n\t"
    "v_mul_f32 v244, s65, v244\n\t"
    "v_mul_f32 v245, s65, v245\n\t"
    "v_cvt_pk_bf16_f32 v212, v188, v189\n\t"
    "v_add_f32 v250, v250, v180\n\t"
    "v_mfma_f32_16x16x32_bf16 v[4:7], a[192:195], v[208:211], v[4:7]\n\t"
    "v_cvt_pk_bf16_f32 v244, v244, v245\n\t"
    "v_accvgpr_write_b32 a41, v244\n\t"
    "v_lshlrev_b32 v246, 16, v94\n\t"
    "v_exp_f32 v190, v190\n\t"
    "v_mfma_f32_16x16x32_bf16 v[8:11], a[164:167], v[204:207], 0\n\t"
    "v_and_b32 v247, 0xffff0000, v94\n\t"
    "v_mul_f32 v246, s65, v246\n\t"
    "v_mul_f32 v247, s65, v247\n\t"
    "v_cvt_pk_bf16_f32 v246, v246, v247\n\t"
    "v_exp_f32 v191, v191\n\t"
    "v_mfma_f32_16x16x32_bf16 v[8:11], a[196:199], v[208:211], v[8:11]\n\t"
    "v_accvgpr_write_b32 a42, v246\n\t"
    "v_lshlrev_b32 v248, 16, v95\n\t"
    "v_and_b32 v249, 0xffff0000, v95\n\t"
    "v_cvt_pk_bf16_f32 v213, v190, v191\n\t"
    "v_add_f32 v250, v250, v181\n\t"
    "v_mfma_f32_16x16x32_bf16 v[12:15], a[168:171], v[204:207], 0\n\t"
    "v_mul_f32 v248, s65, v248\n\t"
    "v_mul_f32 v249, s65, v249\n\t"
    "v_cvt_pk_bf16_f32 v248, v248, v249\n\t"
    "v_exp_f32 v192, v192\n\t"
    "v_mfma_f32_16x16x32_bf16 v[12:15], a[200:203], v[208:211], v[12:15]\n\t"
    "v_accvgpr_write_b32 a43, v248\n\t"
    "s_waitcnt vmcnt(12)\n\t"
    "v_lshlrev_b32 v242, 16, v96\n\t"
    "v_and_b32 v243, 0xffff0000, v96\n\t"
    "v_exp_f32 v193, v193\n\t"
    "v_mfma_f32_16x16x32_bf16 v[16:19], a[172:175], v[204:207], 0\n\t"
    "v_mul_f32 v242, s65, v242\n\t"
    "v_mul_f32 v243, s65, v243\n\t"
    "v_cvt_pk_bf16_f32 v242, v242, v243\n\t"
    "v_cvt_pk_bf16_f32 v214, v192, v193\n\t"
    "v_add_f32 v250, v250, v182\n\t"
    "v_mfma_f32_16x16x32_bf16 v[16:19], a[204:207], v[208:211], v[16:19]\n\t"
    "v_accvgpr_write_b32 a44, v242\n\t"
    "v_lshlrev_b32 v244, 16, v97\n\t"
    "v_and_b32 v245, 0xffff0000, v97\n\t"
    "v_exp_f32 v194, v194\n\t"
    "v_mfma_f32_16x16x32_bf16 v[20:23], a[176:179], v[204:207], 0\n\t"
    "v_mul_f32 v244, s65, v244\n\t"
    "v_mul_f32 v245, s65, v245\n\t"
    "v_cvt_pk_bf16_f32 v244, v244, v245\n\t"
    "v_exp_f32 v195, v195\n\t"
    "v_mfma_f32_16x16x32_bf16 v[20:23], a[208:211], v[208:211], v[20:23]\n\t"
    "v_accvgpr_write_b32 a45, v244\n\t"
    "v_lshlrev_b32 v246, 16, v98\n\t"
    "v_and_b32 v247, 0xffff0000, v98\n\t"
    "v_cvt_pk_bf16_f32 v215, v194, v195\n\t"
    "v_add_f32 v250, v250, v183\n\t"
    "v_mfma_f32_16x16x32_bf16 v[24:27], a[180:183], v[204:207], 0\n\t"
    "v_mul_f32 v246, s65, v246\n\t"
    "v_mul_f32 v247, s65, v247\n\t"
    "v_cvt_pk_bf16_f32 v246, v246, v247\n\t"
    "v_mfma_f32_16x16x32_bf16 v[24:27], a[212:215], v[208:211], v[24:27]\n\t"
    "v_accvgpr_write_b32 a46, v246\n\t"
    "v_lshlrev_b32 v248, 16, v99\n\t"
    "v_and_b32 v249, 0xffff0000, v99\n\t"
    "v_exp_f32 v196, v196\n\t"
    "v_mfma_f32_16x16x32_bf16 v[28:31], a[184:187], v[204:207], 0\n\t"
    "v_mul_f32 v248, s65, v248\n\t"
    "v_mul_f32 v249, s65, v249\n\t"
    "v_cvt_pk_bf16_f32 v248, v248, v249\n\t"
    "v_accvgpr_write_b32 a47, v248\n\t"
    "v_exp_f32 v197, v197\n\t"
    "v_mfma_f32_16x16x32_bf16 v[28:31], a[216:219], v[208:211], v[28:31]\n\t"
    "s_waitcnt lgkmcnt(0)\n\t"
    "v_cvt_pk_bf16_f32 v216, v196, v197\n\t"
    "v_add_f32 v250, v250, v184\n\t"
    "v_mfma_f32_16x16x32_bf16 v[32:35], a[188:191], v[204:207], 0\n\t"
    "v_exp_f32 v198, v198\n\t"
    "v_mfma_f32_16x16x32_bf16 v[32:35], a[220:223], v[208:211], v[32:35]\n\t"
    "v_exp_f32 v199, v199\n\t"
    "v_mfma_f32_16x16x32_bf16 v[172:175], a[96:99], a[32:35], 0\n\t"
    "v_cvt_pk_bf16_f32 v217, v198, v199\n\t"
    "v_add_f32 v250, v250, v185\n\t"
    "v_mfma_f32_16x16x32_bf16 v[172:175], a[100:103], a[36:39], v[172:175]\n\t"
    "s_waitcnt vmcnt(11)\n\t"
    "v_lshlrev_b32 v242, 16, v100\n\t"
    "v_and_b32 v243, 0xffff0000, v100\n\t"
    "v_mul_f32 v242, s65, v242\n\t"
    "s_mov_b32 s64, 36864\n\t"
    "buffer_load_dwordx4 v[116:119], v240, s[36:39], s64 offen offset:0\n\t"
    "buffer_load_dwordx4 v[120:123], v240, s[36:39], s64 offen offset:64\n\t"
    "buffer_load_dwordx4 v[124:127], v240, s[36:39], s64 offen offset:128\n\t"
    "buffer_load_dwordx4 v[128:131], v240, s[36:39], s64 offen offset:192\n\t"
    "v_exp_f32 v200, v200\n\t"
    "v_mfma_f32_16x16x32_bf16 v[172:175], a[104:107], a[40:43], v[172:175]\n\t"
    "v_mul_f32 v243, s65, v243\n\t"
    "v_cvt_pk_bf16_f32 v242, v242, v243\n\t"
    "v_accvgpr_write_b32 a48, v242\n\t"
    "v_exp_f32 v220, v220\n\t"
    "v_mfma_f32_16x16x32_bf16 v[172:175], a[108:111], a[44:47], v[172:175]\n\t"
    "v_lshlrev_b32 v244, 16, v101\n\t"
    "v_and_b32 v245, 0xffff0000, v101\n\t"
    "v_mul_f32 v244, s65, v244\n\t"
    "v_exp_f32 v201, v201\n\t"
    "v_mfma_f32_16x16x32_bf16 v[176:179], a[112:115], a[32:35], 0\n\t"
    "v_mul_f32 v245, s65, v245\n\t"
    "v_cvt_pk_bf16_f32 v244, v244, v245\n\t"
    "v_accvgpr_write_b32 a49, v244\n\t"
    "v_exp_f32 v221, v221\n\t"
    "v_mfma_f32_16x16x32_bf16 v[176:179], a[116:119], a[36:39], v[176:179]\n\t"
    "v_lshlrev_b32 v246, 16, v102\n\t"
    "v_and_b32 v247, 0xffff0000, v102\n\t"
    "v_mul_f32 v246, s65, v246\n\t"
    "v_add_f32 v250, v250, v186\n\t"
    "v_add_f32 v250, v250, v187\n\t"
    "v_mfma_f32_16x16x32_bf16 v[176:179], a[120:123], a[40:43], v[176:179]\n\t"
    "v_mul_f32 v247, s65, v247\n\t"
    "v_cvt_pk_bf16_f32 v246, v246, v247\n\t"
    "v_accvgpr_write_b32 a50, v246\n\t"
    "v_cvt_pk_bf16_f32 v218, v200, v201\n\t"
    "v_cvt_pk_bf16_f32 v224, v220, v221\n\t"
    "v_mfma_f32_16x16x32_bf16 v[176:179], a[124:127], a[44:47], v[176:179]\n\t"
    "v_lshlrev_b32 v248, 16, v103\n\t"
    "v_and_b32 v249, 0xffff0000, v103\n\t"
    "v_mul_f32 v248, s65, v248\n\t"
    "v_exp_f32 v202, v202\n\t"
    "v_mfma_f32_16x16x32_bf16 v[180:183], a[128:131], a[32:35], 0\n\t"
    "v_mul_f32 v249, s65, v249\n\t"
    "v_cvt_pk_bf16_f32 v248, v248, v249\n\t"
    "v_accvgpr_write_b32 a51, v248\n\t"
    "v_exp_f32 v222, v222\n\t"
    "v_mfma_f32_16x16x32_bf16 v[180:183], a[132:135], a[36:39], v[180:183]\n\t"
    "s_waitcnt vmcnt(14)\n\t"
    "v_lshlrev_b32 v242, 16, v104\n\t"
    "v_and_b32 v243, 0xffff0000, v104\n\t"
    "v_mul_f32 v242, s65, v242\n\t"
    "v_exp_f32 v203, v203\n\t"
    "v_mfma_f32_16x16x32_bf16 v[180:183], a[136:139], a[40:43], v[180:183]\n\t"
    "v_mul_f32 v243, s65, v243\n\t"
    "v_cvt_pk_bf16_f32 v242, v242, v243\n\t"
    "v_accvgpr_write_b32 a52, v242\n\t"
    "v_mfma_f32_16x16x32_bf16 v[180:183], a[140:143], a[44:47], v[180:183]\n\t"
    "v_lshlrev_b32 v244, 16, v105\n\t"
    "v_and_b32 v245, 0xffff0000, v105\n\t"
    "v_mul_f32 v244, s65, v244\n\t"
    "v_mul_f32 v245, s65, v245\n\t"
    "v_exp_f32 v223, v223\n\t"
    "v_mfma_f32_16x16x32_bf16 v[184:187], a[144:147], a[32:35], 0\n\t"
    "v_cvt_pk_bf16_f32 v244, v244, v245\n\t"
    "v_accvgpr_write_b32 a53, v244\n\t"
    "v_lshlrev_b32 v246, 16, v106\n\t"
    "v_cvt_pk_bf16_f32 v225, v222, v223\n\t"
    "v_cvt_pk_bf16_f32 v219, v202, v203\n\t"
    "v_mfma_f32_16x16x32_bf16 v[184:187], a[148:151], a[36:39], v[184:187]\n\t"
    "v_and_b32 v247, 0xffff0000, v106\n\t"
    "v_mul_f32 v246, s65, v246\n\t"
    "v_mul_f32 v247, s65, v247\n\t"
    "ds_write_b64 v238, v[224:225] offset:8\n\t"
    "v_mfma_f32_16x16x32_bf16 v[184:187], a[152:155], a[40:43], v[184:187]\n\t"
    "v_cvt_pk_bf16_f32 v246, v246, v247\n\t"
    "v_accvgpr_write_b32 a54, v246\n\t"
    "v_lshlrev_b32 v248, 16, v107\n\t"
    "v_exp_f32 v172, v172\n\t"
    "v_mfma_f32_16x16x32_bf16 v[184:187], a[156:159], a[44:47], v[184:187]\n\t"
    "v_and_b32 v249, 0xffff0000, v107\n\t"
    "v_mul_f32 v248, s65, v248\n\t"
    "v_mul_f32 v249, s65, v249\n\t"
    "v_exp_f32 v173, v173\n\t"
    "v_mfma_f32_16x16x16_bf16 a[244:247], a[248:249], v[224:225], 0\n\t"
    "v_cvt_pk_bf16_f32 v248, v248, v249\n\t"
    "v_accvgpr_write_b32 a55, v248\n\t"
    "s_waitcnt vmcnt(13)\n\t"
    "v_lshlrev_b32 v242, 16, v108\n\t"
    "v_cvt_pk_bf16_f32 v204, v172, v173\n\t"
    "v_add_f32 v251, v251, v196\n\t"
    "v_mfma_f32_16x16x32_bf16 v[36:39], a[160:163], v[212:215], 0\n\t"
    "v_and_b32 v243, 0xffff0000, v108\n\t"
    "v_mul_f32 v242, s65, v242\n\t"
    "v_mul_f32 v243, s65, v243\n\t"
    "v_exp_f32 v174, v174\n\t"
    "v_mfma_f32_16x16x32_bf16 v[36:39], a[192:195], v[216:219], v[36:39]\n\t"
    "v_cvt_pk_bf16_f32 v242, v242, v243\n\t"
    "v_accvgpr_write_b32 a56, v242\n\t"
    "v_lshlrev_b32 v244, 16, v109\n\t"
    "v_exp_f32 v175, v175\n\t"
    "v_mfma_f32_16x16x32_bf16 v[40:43], a[164:167], v[212:215], 0\n\t"
    "v_and_b32 v245, 0xffff0000, v109\n\t"
    "v_mul_f32 v244, s65, v244\n\t"
    "v_mul_f32 v245, s65, v245\n\t"
    "v_cvt_pk_bf16_f32 v205, v174, v175\n\t"
    "v_add_f32 v251, v251, v197\n\t"
    "v_mfma_f32_16x16x32_bf16 v[40:43], a[196:199], v[216:219], v[40:43]\n\t"
    "v_cvt_pk_bf16_f32 v244, v244, v245\n\t"
    "v_accvgpr_write_b32 a57, v244\n\t"
    "v_lshlrev_b32 v246, 16, v110\n\t"
    "v_exp_f32 v176, v176\n\t"
    "v_mfma_f32_16x16x32_bf16 v[44:47], a[168:171], v[212:215], 0\n\t"
    "v_and_b32 v247, 0xffff0000, v110\n\t"
    "v_mul_f32 v246, s65, v246\n\t"
    "v_mul_f32 v247, s65, v247\n\t"
    "v_cvt_pk_bf16_f32 v246, v246, v247\n\t"
    "v_exp_f32 v177, v177\n\t"
    "v_mfma_f32_16x16x32_bf16 v[44:47], a[200:203], v[216:219], v[44:47]\n\t"
    "v_accvgpr_write_b32 a58, v246\n\t"
    "v_lshlrev_b32 v248, 16, v111\n\t"
    "v_and_b32 v249, 0xffff0000, v111\n\t"
    "v_cvt_pk_bf16_f32 v206, v176, v177\n\t"
    "v_add_f32 v251, v251, v198\n\t"
    "v_mfma_f32_16x16x32_bf16 v[48:51], a[172:175], v[212:215], 0\n\t"
    "v_mul_f32 v248, s65, v248\n\t"
    "v_mul_f32 v249, s65, v249\n\t"
    "v_cvt_pk_bf16_f32 v248, v248, v249\n\t"
    "v_exp_f32 v178, v178\n\t"
    "v_mfma_f32_16x16x32_bf16 v[48:51], a[204:207], v[216:219], v[48:51]\n\t"
    "v_accvgpr_write_b32 a59, v248\n\t"
    "s_waitcnt vmcnt(12)\n\t"
    "v_lshlrev_b32 v242, 16, v112\n\t"
    "v_and_b32 v243, 0xffff0000, v112\n\t"
    "v_exp_f32 v179, v179\n\t"
    "v_mfma_f32_16x16x32_bf16 v[52:55], a[176:179], v[212:215], 0\n\t"
    "v_mul_f32 v242, s65, v242\n\t"
    "v_mul_f32 v243, s65, v243\n\t"
    "v_cvt_pk_bf16_f32 v242, v242, v243\n\t"
    "v_cvt_pk_bf16_f32 v207, v178, v179\n\t"
    "v_add_f32 v251, v251, v199\n\t"
    "v_mfma_f32_16x16x32_bf16 v[52:55], a[208:211], v[216:219], v[52:55]\n\t"
    "v_accvgpr_write_b32 a60, v242\n\t"
    "v_lshlrev_b32 v244, 16, v113\n\t"
    "v_and_b32 v245, 0xffff0000, v113\n\t"
    "v_mfma_f32_16x16x32_bf16 v[56:59], a[180:183], v[212:215], 0\n\t"
    "v_mul_f32 v244, s65, v244\n\t"
    "v_mul_f32 v245, s65, v245\n\t"
    "v_cvt_pk_bf16_f32 v244, v244, v245\n\t"
    "v_exp_f32 v180, v180\n\t"
    "v_mfma_f32_16x16x32_bf16 v[56:59], a[212:215], v[216:219], v[56:59]\n\t"
    "v_accvgpr_write_b32 a61, v244\n\t"
    "v_lshlrev_b32 v246, 16, v114\n\t"
    "v_and_b32 v247, 0xffff0000, v114\n\t"
    "v_exp_f32 v181, v181\n\t"
    "v_mfma_f32_16x16x32_bf16 v[60:63], a[184:187], v[212:215], 0\n\t"
    "v_mul_f32 v246, s65, v246\n\t"
    "v_mul_f32 v247, s65, v247\n\t"
    "v_cvt_pk_bf16_f32 v246, v246, v247\n\t"
    "v_cvt_pk_bf16_f32 v208, v180, v181\n\t"
    "v_add_f32 v251, v251, v200\n\t"
    "v_mfma_f32_16x16x32_bf16 v[60:63], a[216:219], v[216:219], v[60:63]\n\t"
    "v_accvgpr_write_b32 a62, v246\n\t"
    "v_lshlrev_b32 v248, 16, v115\n\t"
    "v_and_b32 v249, 0xffff0000, v115\n\t"
    "v_exp_f32 v182, v182\n\t"
    "v_mfma_f32_16x16x32_bf16 v[64:67], a[188:191], v[212:215], 0\n\t"
    "v_mul_f32 v248, s65, v248\n\t"
    "v_mul_f32 v249, s65, v249\n\t"
    "v_cvt_pk_bf16_f32 v248, v248, v249\n\t"
    "v_accvgpr_write_b32 a63, v248\n\t"
    "v_exp_f32 v183, v183\n\t"
    "v_mfma_f32_16x16x32_bf16 v[64:67], a[220:223], v[216:219], v[64:67]\n\t"
    "v_cvt_pk_bf16_f32 v209, v182, v183\n\t"
    "v_add_f32 v251, v251, v201\n\t"
    "v_mfma_f32_16x16x32_bf16 a[228:231], a[248:251], v[212:215], 0\n\t"
    "v_exp_f32 v184, v184\n\t"
    "v_mfma_f32_16x16x32_bf16 v[188:191], a[96:99], a[48:51], 0\n\t"
    "v_exp_f32 v185, v185\n\t"
    "v_mfma_f32_16x16x32_bf16 v[188:191], a[100:103], a[52:55], v[188:191]\n\t"
    "s_waitcnt vmcnt(3)\n\t"
    "v_lshlrev_b32 v242, 16, v116\n\t"
    "v_and_b32 v243, 0xffff0000, v116\n\t"
    "v_mul_f32 v242, s65, v242\n\t"
    "v_cvt_pk_bf16_f32 v210, v184, v185\n\t"
    "v_add_f32 v251, v251, v202\n\t"
    "v_mfma_f32_16x16x32_bf16 v[188:191], a[104:107], a[56:59], v[188:191]\n\t"
    "v_mul_f32 v243, s65, v243\n\t"
    "v_cvt_pk_bf16_f32 v242, v242, v243\n\t"
    "v_accvgpr_write_b32 a64, v242\n\t"
    "s_add_u32 m0, s55, 33792\n\t"
    "s_add_u32 s61, s59, 33792\n\t"
    "s_and_b32 s61, s61, 0x7ffff\n\t"
    "buffer_load_dwordx4 v239, s[40:43], s61 offen offset:0 nt lds\n\t"
    "s_add_u32 m0, s55, 38016\n\t"
    "s_nop 0\n\t"
    "buffer_load_dwordx4 v226, s[40:43], s61 offen offset:0 nt lds\n\t"
    "s_add_u32 m0, s55, 42240\n\t"
    "s_nop 0\n\t"
    "buffer_load_dwordx4 v227, s[40:43], s61 offen offset:0 nt lds\n\t"
    "s_add_u32 m0, s55, 46464\n\t"
    "s_nop 0\n\t"
    "buffer_load_dwordx4 v3, s[40:43], s61 offen offset:0 nt lds\n\t"
    "v_exp_f32 v186, v186\n\t"
    "v_mfma_f32_16x16x32_bf16 v[188:191], a[108:111], a[60:63], v[188:191]\n\t"
    "v_lshlrev_b32 v244, 16, v117\n\t"
    "v_and_b32 v245, 0xffff0000, v117\n\t"
    "v_mul_f32 v244, s65, v244\n\t"
    "v_add_f32 v251, v251, v203\n\t"
    "v_mfma_f32_16x16x32_bf16 v[192:195], a[112:115], a[48:51], 0\n\t"
    "v_mul_f32 v245, s65, v245\n\t"
    "v_cvt_pk_bf16_f32 v244, v244, v245\n\t"
    "v_accvgpr_write_b32 a65, v244\n\t"
    "v_exp_f32 v187, v187\n\t"
    "v_mfma_f32_16x16x32_bf16 v[192:195], a[116:119], a[52:55], v[192:195]\n\t"
    "v_lshlrev_b32 v246, 16, v118\n\t"
    "v_and_b32 v247, 0xffff0000, v118\n\t"
    "v_mul_f32 v246, s65, v246\n\t"
    "v_mul_f32 v247, s65, v247\n\t"
    "v_cvt_pk_bf16_f32 v211, v186, v187\n\t"
    "v_mfma_f32_16x16x32_bf16 v[192:195], a[120:123], a[56:59], v[192:195]\n\t"
    "v_cvt_pk_bf16_f32 v246, v246, v247\n\t"
    "v_accvgpr_write_b32 a66, v246\n\t"
    "v_lshlrev_b32 v248, 16, v119\n\t"
    "v_add_f32 v252, v252, v180\n\t"
    "v_add_f32 v252, v252, v181\n\t"
    "v_mfma_f32_16x16x32_bf16 v[192:195], a[124:127], a[60:63], v[192:195]\n\t"
    "v_and_b32 v249, 0xffff0000, v119\n\t"
    "v_mul_f32 v248, s65, v248\n\t"
    "v_mul_f32 v249, s65, v249\n\t"
    "v_add_f32 v252, v252, v182\n\t"
    "v_add_f32 v252, v252, v183\n\t"
    "v_mfma_f32_16x16x32_bf16 v[196:199], a[128:131], a[48:51], 0\n\t"
    "v_cvt_pk_bf16_f32 v248, v248, v249\n\t"
    "v_accvgpr_write_b32 a67, v248\n\t"
    "s_waitcnt vmcnt(6)\n\t"
    "v_lshlrev_b32 v242, 16, v120\n\t"
    "v_mfma_f32_16x16x32_bf16 v[196:199], a[132:135], a[52:55], v[196:199]\n\t"
    "v_and_b32 v243, 0xffff0000, v120\n\t"
    "v_mul_f32 v242, s65, v242\n\t"
    "v_mul_f32 v243, s65, v243\n\t"
    "s_add_u32 m0, s56, 33792\n\t"
    "s_add_u32 s61, s59, 33792\n\t"
    "s_and_b32 s61, s61, 0x7ffff\n\t"
    "buffer_load_dwordx4 v239, s[44:47], s61 offen offset:0 lds\n\t"
    "s_add_u32 m0, s56, 38016\n\t"
    "s_nop 0\n\t"
    "buffer_load_dwordx4 v226, s[44:47], s61 offen offset:0 lds\n\t"
    "s_add_u32 m0, s56, 42240\n\t"
    "s_nop 0\n\t"
    "buffer_load_dwordx4 v227, s[44:47], s61 offen offset:0 lds\n\t"
    "s_add_u32 m0, s56, 46464\n\t"
    "s_nop 0\n\t"
    "buffer_load_dwordx4 v3, s[44:47], s61 offen offset:0 lds\n\t"
    "v_add_f32 v252, v252, v184\n\t"
    "v_add_f32 v252, v252, v185\n\t"
    "v_mfma_f32_16x16x32_bf16 v[196:199], a[136:139], a[56:59], v[196:199]\n\t"
    "v_cvt_pk_bf16_f32 v242, v242, v243\n\t"
    "v_accvgpr_write_b32 a68, v242\n\t"
    "v_lshlrev_b32 v244, 16, v121\n\t"
    "v_and_b32 v245, 0xffff0000, v121\n\t"
    "v_add_f32 v252, v252, v186\n\t"
    "v_add_f32 v252, v252, v187\n\t"
    "v_mfma_f32_16x16x32_bf16 v[196:199], a[140:143], a[60:63], v[196:199]\n\t"
    "v_mul_f32 v244, s65, v244\n\t"
    "v_mul_f32 v245, s65, v245\n\t"
    "v_cvt_pk_bf16_f32 v244, v244, v245\n\t"
    "v_exp_f32 v188, v188\n\t"
    "v_mfma_f32_16x16x32_bf16 v[200:203], a[144:147], a[48:51], 0\n\t"
    "v_accvgpr_write_b32 a69, v244\n\t"
    "v_lshlrev_b32 v246, 16, v122\n\t"
    "v_and_b32 v247, 0xffff0000, v122\n\t"
    "v_exp_f32 v189, v189\n\t"
    "v_mfma_f32_16x16x32_bf16 v[200:203], a[148:151], a[52:55], v[200:203]\n\t"
    "v_mul_f32 v246, s65, v246\n\t"
    "v_mul_f32 v247, s65, v247\n\t"
    "v_cvt_pk_bf16_f32 v246, v246, v247\n\t"
    "v_cvt_pk_bf16_f32 v212, v188, v189\n\t"
    "v_mfma_f32_16x16x32_bf16 v[200:203], a[152:155], a[56:59], v[200:203]\n\t"
    "v_accvgpr_write_b32 a70, v246\n\t"
    "v_lshlrev_b32 v248, 16, v123\n\t"
    "v_and_b32 v249, 0xffff0000, v123\n\t"
    "v_exp_f32 v190, v190\n\t"
    "v_mfma_f32_16x16x32_bf16 v[200:203], a[156:159], a[60:63], v[200:203]\n\t"
    "v_mul_f32 v248, s65, v248\n\t"
    "v_mul_f32 v249, s65, v249\n\t"
    "v_cvt_pk_bf16_f32 v248, v248, v249\n\t"
    "v_accvgpr_write_b32 a71, v248\n\t"
    "v_exp_f32 v191, v191\n\t"
    "v_mfma_f32_16x16x32_bf16 v[68:71], a[160:163], v[204:207], 0\n\t"
    "s_waitcnt vmcnt(9)\n\t"
    "v_lshlrev_b32 v242, 16, v124\n\t"
    "v_and_b32 v243, 0xffff0000, v124\n\t"
    "v_mul_f32 v242, s65, v242\n\t"
    "v_cvt_pk_bf16_f32 v213, v190, v191\n\t"
    "v_mfma_f32_16x16x32_bf16 v[68:71], a[192:195], v[208:211], v[68:71]\n\t"
    "v_mul_f32 v243, s65, v243\n\t"
    "v_cvt_pk_bf16_f32 v242, v242, v243\n\t"
    "v_accvgpr_write_b32 a72, v242\n\t"
    "v_exp_f32 v192, v192\n\t"
    "v_mfma_f32_16x16x32_bf16 v[72:75], a[164:167], v[204:207], 0\n\t"
    "v_lshlrev_b32 v244, 16, v125\n\t"
    "v_and_b32 v245, 0xffff0000, v125\n\t"
    "v_mul_f32 v244, s65, v244\n\t"
    "v_exp_f32 v193, v193\n\t"
    "v_mfma_f32_16x16x32_bf16 v[72:75], a[196:199], v[208:211], v[72:75]\n\t"
    "v_mul_f32 v245, s65, v245\n\t"
    "v_cvt_pk_bf16_f32 v244, v244, v245\n\t"
    "v_accvgpr_write_b32 a73, v244\n\t"
    "v_cvt_pk_bf16_f32 v214, v192, v193\n\t"
    "v_mfma_f32_16x16x32_bf16 v[76:79], a[168:171], v[204:207], 0\n\t"
    "v_lshlrev_b32 v246, 16, v126\n\t"
    "v_and_b32 v247, 0xffff0000, v126\n\t"
    "v_mul_f32 v246, s65, v246\n\t"
    "v_mul_f32 v247, s65, v247\n\t"
    "v_exp_f32 v194, v194\n\t"
    "v_mfma_f32_16x16x32_bf16 v[76:79], a[200:203], v[208:211], v[76:79]\n\t"
    "v_cvt_pk_bf16_f32 v246, v246, v247\n\t"
    "v_accvgpr_write_b32 a74, v246\n\t"
    "v_lshlrev_b32 v248, 16, v127\n\t"
    "v_exp_f32 v195, v195\n\t"
    "v_mfma_f32_16x16x32_bf16 v[80:83], a[172:175], v[204:207], 0\n\t"
    "v_and_b32 v249, 0xffff0000, v127\n\t"
    "v_mul_f32 v248, s65, v248\n\t"
    "v_mul_f32 v249, s65, v249\n\t"
    "v_cvt_pk_bf16_f32 v215, v194, v195\n\t"
    "v_mfma_f32_16x16x32_bf16 v[80:83], a[204:207], v[208:211], v[80:83]\n\t"
    "v_cvt_pk_bf16_f32 v248, v248, v249\n\t"
    "v_accvgpr_write_b32 a75, v248\n\t"
    "s_waitcnt vmcnt(8)\n\t"
    "v_lshlrev_b32 v242, 16, v128\n\t"
    "v_exp_f32 v196, v196\n\t"
    "v_mfma_f32_16x16x32_bf16 v[84:87], a[176:179], v[204:207], 0\n\t"
    "v_and_b32 v243, 0xffff0000, v128\n\t"
    "v_mul_f32 v242, s65, v242\n\t"
    "v_mul_f32 v243, s65, v243\n\t"
    "v_exp_f32 v197, v197\n\t"
    "v_mfma_f32_16x16x32_bf16 v[84:87], a[208:211], v[208:211], v[84:87]\n\t"
    "v_cvt_pk_bf16_f32 v242, v242, v243\n\t"
    "v_accvgpr_write_b32 a76, v242\n\t"
    "v_lshlrev_b32 v244, 16, v129\n\t"
    "v_and_b32 v245, 0xffff0000, v129\n\t"
    "v_cvt_pk_bf16_f32 v216, v196, v197\n\t"
    "v_add_f32 v253, v253, v196\n\t"
    "v_mfma_f32_16x16x32_bf16 v[88:91], a[180:183], v[204:207], 0\n\t"
    "v_mul_f32 v244, s65, v244\n\t"
    "v_mul_f32 v245, s65, v245\n\t"
    "v_cvt_pk_bf16_f32 v244, v244, v245\n\t"
    "v_exp_f32 v198, v198\n\t"
    "v_mfma_f32_16x16x32_bf16 v[88:91], a[212:215], v[208:211], v[88:91]\n\t"
    "v_accvgpr_write_b32 a77, v244\n\t"
    "v_lshlrev_b32 v246, 16, v130\n\t"
    "v_and_b32 v247, 0xffff0000, v130\n\t"
    "v_exp_f32 v199, v199\n\t"
    "v_mfma_f32_16x16x32_bf16 v[92:95], a[184:187], v[204:207], 0\n\t"
    "v_mul_f32 v246, s65, v246\n\t"
    "v_mul_f32 v247, s65, v247\n\t"
    "v_cvt_pk_bf16_f32 v246, v246, v247\n\t"
    "v_cvt_pk_bf16_f32 v217, v198, v199\n\t"
    "v_add_f32 v253, v253, v197\n\t"
    "v_mfma_f32_16x16x32_bf16 v[92:95], a[216:219], v[208:211], v[92:95]\n\t"
    "v_accvgpr_write_b32 a78, v246\n\t"
    "v_lshlrev_b32 v248, 16, v131\n\t"
    "v_and_b32 v249, 0xffff0000, v131\n\t"
    "v_exp_f32 v200, v200\n\t"
    "v_mfma_f32_16x16x32_bf16 v[96:99], a[188:191], v[204:207], 0\n\t"
    "v_mul_f32 v248, s65, v248\n\t"
    "v_mul_f32 v249, s65, v249\n\t"
    "v_cvt_pk_bf16_f32 v248, v248, v249\n\t"
    "v_accvgpr_write_b32 a79, v248\n\t"
    "v_exp_f32 v201, v201\n\t"
    "v_mfma_f32_16x16x32_bf16 v[96:99], a[220:223], v[208:211], v[96:99]\n\t"
    "s_add_u32 s54, s59, 66560\n\t"
    "s_and_b32 s54, s54, 0x7ffff\n\t"
    "s_mov_b32 s57, 33792\n\t"
    "v_cvt_pk_bf16_f32 v218, v200, v201\n\t"
    "v_add_f32 v253, v253, v198\n\t"
    "v_mfma_f32_16x16x32_bf16 a[232:235], a[248:251], v[204:207], 0\n\t"
    "s_waitcnt lgkmcnt(0)\n\t"
    "s_barrier\n\t"
    "v_mfma_f32_16x16x32_bf16 v[172:175], a[96:99], a[64:67], 0\n\t"
    "ds_read_b128 a[96:99], v236 offset:16896\n\t"
    "v_add_f32 v253, v253, v199\n\t"
    "v_mfma_f32_16x16x32_bf16 v[172:175], a[100:103], a[68:71], v[172:175]\n\t"
    "v_exp_f32 v202, v202\n\t"
    "v_mfma_f32_16x16x32_bf16 v[172:175], a[104:107], a[72:75], v[172:175]\n\t"
    "s_add_u32 m0, s55, 50688\n\t"
    "s_add_u32 s61, s59, 50176\n\t"
    "s_and_b32 s61, s61, 0x7ffff\n\t"
    "buffer_load_dwordx4 v239, s[40:43], s61 offen offset:0 nt lds\n\t"
    "s_add_u32 m0, s55, 54912\n\t"
    "s_nop 0\n\t"
    "buffer_load_dwordx4 v226, s[40:43], s61 offen offset:0 nt lds\n\t"
    "s_add_u32 m0, s55, 59136\n\t"
    "s_nop 0\n\t"
    "buffer_load_dwordx4 v227, s[40:43], s61 offen offset:0 nt lds\n\t"
    "s_add_u32 m0, s55, 63360\n\t"
    "s_nop 0\n\t"
    "buffer_load_dwordx4 v3, s[40:43], s61 offen offset:0 nt lds\n\t"
    "ds_read_b128 a[100:103], v236 offset:16960\n\t"
    "ds_read_b128 a[104:107], v236 offset:17024\n\t"
    "v_mfma_f32_16x16x32_bf16 v[172:175], a[108:111], a[76:79], v[172:175]\n\t"
    "v_exp_f32 v203, v203\n\t"
    "v_mfma_f32_16x16x32_bf16 v[176:179], a[112:115], a[64:67], 0\n\t"
    "ds_read_b128 a[108:111], v236 offset:17088\n\t"
    "ds_read_b128 a[112:115], v236 offset:17408\n\t"
    "v_mfma_f32_16x16x32_bf16 v[176:179], a[116:119], a[68:71], v[176:179]\n\t"
    "v_cvt_pk_bf16_f32 v219, v202, v203\n\t"
    "v_add_f32 v253, v253, v200\n\t"
    "v_mfma_f32_16x16x32_bf16 v[176:179], a[120:123], a[72:75], v[176:179]\n\t"
    "ds_read_b128 a[116:119], v236 offset:17472\n\t"
    "ds_read_b128 a[120:123], v236 offset:17536\n\t"
    "v_mfma_f32_16x16x32_bf16 v[176:179], a[124:127], a[76:79], v[176:179]\n\t"
    "v_add_f32 v253, v253, v201\n\t"
    "v_add_f32 v253, v253, v202\n\t"
    "v_mfma_f32_16x16x32_bf16 v[180:183], a[128:131], a[64:67], 0\n\t"
    "ds_read_b128 a[124:127], v236 offset:17600\n\t"
    "v_add_f32 v253, v253, v203\n\t"
    "v_mfma_f32_16x16x32_bf16 v[180:183], a[132:135], a[68:71], v[180:183]\n\t"
    "ds_read_b128 a[128:131], v236 offset:25344\n\t"
    "v_mfma_f32_16x16x32_bf16 v[180:183], a[136:139], a[72:75], v[180:183]\n\t"
    "ds_read_b128 a[132:135], v236 offset:25408\n\t"
    "v_mfma_f32_16x16x32_bf16 v[180:183], a[140:143], a[76:79], v[180:183]\n\t"
    "v_exp_f32 v172, v172\n\t"
    "v_mfma_f32_16x16x32_bf16 v[184:187], a[144:147], a[64:67], 0\n\t"
    "ds_read_b128 a[136:139], v236 offset:25472\n\t"
    "ds_read_b128 a[140:143], v236 offset:25536\n\t"
    "v_mfma_f32_16x16x32_bf16 v[184:187], a[148:151], a[68:71], v[184:187]\n\t"
    "v_exp_f32 v173, v173\n\t"
    "v_mfma_f32_16x16x32_bf16 v[184:187], a[152:155], a[72:75], v[184:187]\n\t"
    "ds_read_b128 a[144:147], v236 offset:25856\n\t"
    "ds_read_b128 a[148:151], v236 offset:25920\n\t"
    "v_mfma_f32_16x16x32_bf16 v[184:187], a[156:159], a[76:79], v[184:187]\n\t"
    "s_add_u32 m0, s56, 50688\n\t"
    "s_add_u32 s61, s59, 50176\n\t"
    "s_and_b32 s61, s61, 0x7ffff\n\t"
    "buffer_load_dwordx4 v239, s[44:47], s61 offen offset:0 lds\n\t"
    "s_add_u32 m0, s56, 54912\n\t"
    "s_nop 0\n\t"
    "buffer_load_dwordx4 v226, s[44:47], s61 offen offset:0 lds\n\t"
    "s_add_u32 m0, s56, 59136\n\t"
    "s_nop 0\n\t"
    "buffer_load_dwordx4 v227, s[44:47], s61 offen offset:0 lds\n\t"
    "s_add_u32 m0, s56, 63360\n\t"
    "s_nop 0\n\t"
    "buffer_load_dwordx4 v3, s[44:47], s61 offen offset:0 lds\n\t"
    "v_cvt_pk_bf16_f32 v204, v172, v173\n\t"
    "v_add_f32 v254, v254, v172\n\t"
    "v_mfma_f32_16x16x32_bf16 v[100:103], a[160:163], v[212:215], 0\n\t"
    "ds_read_b128 a[152:155], v236 offset:25984\n\t"
    "ds_read_b128 a[156:159], v236 offset:26048\n\t"
    "v_mfma_f32_16x16x32_bf16 v[100:103], a[192:195], v[216:219], v[100:103]\n\t"
    "v_exp_f32 v174, v174\n\t"
    "v_mfma_f32_16x16x32_bf16 v[104:107], a[164:167], v[212:215], 0\n\t"
    "ds_read_b128 v[228:231], v238 offset:0\n\t"
    "ds_read_b128 v[232:235], v238 offset:1024\n\t"
    "v_mfma_f32_16x16x32_bf16 v[104:107], a[196:199], v[216:219], v[104:107]\n\t"
    "v_exp_f32 v175, v175\n\t"
    "v_mfma_f32_16x16x32_bf16 v[108:111], a[168:171], v[212:215], 0\n\t"
    "v_cvt_pk_bf16_f32 v205, v174, v175\n\t"
    "v_add_f32 v254, v254, v173\n\t"
    "v_mfma_f32_16x16x32_bf16 v[108:111], a[200:203], v[216:219], v[108:111]\n\t"
    "v_exp_f32 v176, v176\n\t"
    "v_mfma_f32_16x16x32_bf16 v[112:115], a[172:175], v[212:215], 0\n\t"
    "v_exp_f32 v177, v177\n\t"
    "v_mfma_f32_16x16x32_bf16 v[112:115], a[204:207], v[216:219], v[112:115]\n\t"
    "v_cvt_pk_bf16_f32 v206, v176, v177\n\t"
    "v_add_f32 v254, v254, v174\n\t"
    "v_mfma_f32_16x16x32_bf16 v[116:119], a[176:179], v[212:215], 0\n\t"
    "v_exp_f32 v178, v178\n\t"
    "v_mfma_f32_16x16x32_bf16 v[116:119], a[208:211], v[216:219], v[116:119]\n\t"
    "v_exp_f32 v179, v179\n\t"
    "v_mfma_f32_16x16x32_bf16 v[120:123], a[180:183], v[212:215], 0\n\t"
    "v_cvt_pk_bf16_f32 v207, v178, v179\n\t"
    "v_add_f32 v254, v254, v175\n\t"
    "v_mfma_f32_16x16x32_bf16 v[120:123], a[212:215], v[216:219], v[120:123]\n\t"
    "v_exp_f32 v180, v180\n\t"
    "v_mfma_f32_16x16x32_bf16 v[124:127], a[184:187], v[212:215], 0\n\t"
    "v_exp_f32 v181, v181\n\t"
    "v_mfma_f32_16x16x32_bf16 v[124:127], a[216:219], v[216:219], v[124:127]\n\t"
    "v_cvt_pk_bf16_f32 v208, v180, v181\n\t"
    "v_add_f32 v254, v254, v176\n\t"
    "v_mfma_f32_16x16x32_bf16 v[128:131], a[188:191], v[212:215], 0\n\t"
    "v_exp_f32 v182, v182\n\t"
    "v_mfma_f32_16x16x32_bf16 v[128:131], a[220:223], v[216:219], v[128:131]\n\t"
    "v_exp_f32 v183, v183\n\t"
    "v_mfma_f32_16x16x32_bf16 a[236:239], a[248:251], v[212:215], 0\n\t"
    "s_waitcnt lgkmcnt(0)\n\t"
    "v_cvt_pk_bf16_f32 v209, v182, v183\n\t"
    "v_add_f32 v254, v254, v177\n\t"
    "LOOP1_%=:\n\t"
    "s_cmp_lg_u32 s60, 0\n\t"
    "s_cbranch_scc1 NOPF1_%=\n\t"
    "s_mov_b32 s42, 0\n\t"
    "s_mov_b32 s46, 0\n\t"
    "s_mov_b32 s61, TAIL1_%=-CPF1_%=\n\t"
    "v_lshlrev_b32 v2, 7, v1\n\t"
    "buffer_load_dword v255, v2, s[96:99], s61 offen\n\t"
    "NOPF1_%=:\n\t"
    "v_mfma_f32_16x16x32_bf16 v[188:191], a[96:99], a[0:3], 0\n\t"
    "v_exp_f32 v184, v184\n\t"
    "v_mfma_f32_16x16x32_bf16 v[188:191], a[100:103], a[4:7], v[188:191]\n\t"
    "s_add_u32 m0, s55, 0\n\t"
    "v_exp_f32 v185, v185\n\t"
    "v_mfma_f32_16x16x32_bf16 v[188:191], a[104:107], a[8:11], v[188:191]\n\t"
    "buffer_load_dwordx4 v239, s[40:43], s54 offen offset:0 nt lds\n\t"
    "v_mfma_f32_16x16x32_bf16 v[188:191], a[108:111], a[12:15], v[188:191]\n\t"
    "v_cvt_pk_bf16_f32 v210, v184, v185\n\t"
    "v_add_f32 v254, v254, v178\n\t"
    "v_mfma_f32_16x16x32_bf16 v[192:195], a[112:115], a[0:3], 0\n\t"
    "v_exp_f32 v186, v186\n\t"
    "v_mfma_f32_16x16x32_bf16 v[192:195], a[116:119], a[4:7], v[192:195]\n\t"
    "v_exp_f32 v187, v187\n\t"
    "v_mfma_f32_16x16x32_bf16 v[192:195], a[120:123], a[8:11], v[192:195]\n\t"
    "v_cvt_pk_bf16_f32 v211, v186, v187\n\t"
    "v_add_f32 v254, v254, v179\n\t"
    "v_mfma_f32_16x16x32_bf16 v[192:195], a[124:127], a[12:15], v[192:195]\n\t"
    "v_add_f32 v254, v254, v180\n\t"
    "v_add_f32 v254, v254, v181\n\t"
    "v_mfma_f32_16x16x32_bf16 v[196:199], a[128:131], a[0:3], 0\n\t"
    "v_add_f32 v254, v254, v182\n\t"
    "v_add_f32 v254, v254, v183\n\t"
    "v_mfma_f32_16x16x32_bf16 v[196:199], a[132:135], a[4:7], v[196:199]\n\t"
    "v_add_f32 v254, v254, v184\n\t"
    "v_add_f32 v254, v254, v185\n\t"
    "v_mfma_f32_16x16x32_bf16 v[196:199], a[136:139], a[8:11], v[196:199]\n\t"
    "v_add_f32 v254, v254, v186\n\t"
    "v_add_f32 v254, v254, v187\n\t"
    "v_mfma_f32_16x16x32_bf16 v[196:199], a[140:143], a[12:15], v[196:199]\n\t"
    "v_exp_f32 v188, v188\n\t"
    "v_mfma_f32_16x16x32_bf16 v[200:203], a[144:147], a[0:3], 0\n\t"
    "v_exp_f32 v189, v189\n\t"
    "v_mfma_f32_16x16x32_bf16 v[200:203], a[148:151], a[4:7], v[200:203]\n\t"
    "v_cvt_pk_bf16_f32 v212, v188, v189\n\t"
    "v_add_f32 v250, v250, v188\n\t"
    "v_mfma_f32_16x16x32_bf16 v[200:203], a[152:155], a[8:11], v[200:203]\n\t"
    "v_exp_f32 v190, v190\n\t"
    "v_mfma_f32_16x16x32_bf16 v[200:203], a[156:159], a[12:15], v[200:203]\n\t"
    "v_exp_f32 v191, v191\n\t"
    "v_mfma_f32_16x16x32_bf16 v[164:167], a[168:171], v[228:231], v[164:167]\n\t"
    "v_cvt_pk_bf16_f32 v213, v190, v191\n\t"
    "v_add_f32 v250, v250, v189\n\t"
    "v_mfma_f32_16x16x32_bf16 v[164:167], a[200:203], v[232:235], v[164:167]\n\t"
    "s_add_u32 m0, s56, 0\n\t"
    "v_exp_f32 v192, v192\n\t"
    "v_mfma_f32_16x16x32_bf16 v[168:171], a[172:175], v[228:231], v[168:171]\n\t"
    "buffer_load_dwordx4 v239, s[44:47], s54 offen offset:0 lds\n\t"
    "v_mfma_f32_16x16x32_bf16 v[168:171], a[204:207], v[232:235], v[168:171]\n\t"
    "v_exp_f32 v193, v193\n\t"
    "v_mfma_f32_16x16x32_bf16 v[132:135], a[160:163], v[204:207], v[132:135]\n\t"
    "ds_read_b64_tr_b16 a[160:161], v237 offset:16896\n\t"
    "ds_read_b64_tr_b16 a[162:163], v237 offset:17408\n\t"
    "v_mfma_f32_16x16x32_bf16 v[132:135], a[192:195], v[208:211], v[132:135]\n\t"
    "v_cvt_pk_bf16_f32 v214, v192, v193\n\t"
    "v_add_f32 v250, v250, v190\n\t"
    "v_mfma_f32_16x16x32_bf16 v[136:139], a[164:167], v[204:207], v[136:139]\n\t"
    "ds_read_b64_tr_b16 a[192:193], v237 offset:25344\n\t"
    "ds_read_b64_tr_b16 a[194:195], v237 offset:25856\n\t"
    "v_mfma_f32_16x16x32_bf16 v[136:139], a[196:199], v[208:211], v[136:139]\n\t"
    "v_exp_f32 v194, v194\n\t"
    "v_mfma_f32_16x16x32_bf16 v[140:143], a[168:171], v[204:207], v[140:143]\n\t"
    "ds_read_b64_tr_b16 a[164:165], v237 offset:16904\n\t"
    "ds_read_b64_tr_b16 a[166:167], v237 offset:17416\n\t"
    "v_mfma_f32_16x16x32_bf16 v[140:143], a[200:203], v[208:211], v[140:143]\n\t"
    "v_exp_f32 v195, v195\n\t"
    "v_mfma_f32_16x16x32_bf16 v[144:147], a[172:175], v[204:207], v[144:147]\n\t"
    "ds_read_b64_tr_b16 a[196:197], v237 offset:25352\n\t"
    "ds_read_b64_tr_b16 a[198:199], v237 offset:25864\n\t"
    "v_mfma_f32_16x16x32_bf16 v[144:147], a[204:207], v[208:211], v[144:147]\n\t"
    "v_cvt_pk_bf16_f32 v215, v194, v195\n\t"
    "v_add_f32 v250, v250, v191\n\t"
    "v_mfma_f32_16x16x32_bf16 v[148:151], a[176:179], v[204:207], v[148:151]\n\t"
    "ds_read_b64_tr_b16 a[168:169], v237 offset:16960\n\t"
    "ds_read_b64_tr_b16 a[170:171], v237 offset:17472\n\t"
    "v_mfma_f32_16x16x32_bf16 v[148:151], a[208:211], v[208:211], v[148:151]\n\t"
    "v_exp_f32 v196, v196\n\t"
    "v_mfma_f32_16x16x32_bf16 v[152:155], a[180:183], v[204:207], v[152:155]\n\t"
    "ds_read_b64_tr_b16 a[200:201], v237 offset:25408\n\t"
    "ds_read_b64_tr_b16 a[202:203], v237 offset:25920\n\t"
    "v_mfma_f32_16x16x32_bf16 v[152:155], a[212:215], v[208:211], v[152:155]\n\t"
    "v_exp_f32 v197, v197\n\t"
    "v_mfma_f32_16x16x32_bf16 v[156:159], a[184:187], v[204:207], v[156:159]\n\t"
    "ds_read_b64_tr_b16 a[172:173], v237 offset:16968\n\t"
    "ds_read_b64_tr_b16 a[174:175], v237 offset:17480\n\t"
    "v_mfma_f32_16x16x32_bf16 v[156:159], a[216:219], v[208:211], v[156:159]\n\t"
    "s_add_u32 m0, s55, 4224\n\t"
    "v_cvt_pk_bf16_f32 v216, v196, v197\n\t"
    "v_add_f32 v250, v250, v192\n\t"
    "v_mfma_f32_16x16x32_bf16 v[160:163], a[188:191], v[204:207], v[160:163]\n\t"
    "buffer_load_dwordx4 v226, s[40:43], s54 offen offset:0 nt lds\n\t"
    "v_mfma_f32_16x16x32_bf16 v[160:163], a[220:223], v[208:211], v[160:163]\n\t"
    "v_exp_f32 v198, v198\n\t"
    "v_mfma_f32_16x16x32_bf16 v[172:175], a[96:99], a[16:19], 0\n\t"
    "ds_read_b64_tr_b16 a[204:205], v237 offset:25416\n\t"
    "ds_read_b64_tr_b16 a[206:207], v237 offset:25928\n\t"
    "v_mfma_f32_16x16x32_bf16 v[172:175], a[100:103], a[20:23], v[172:175]\n\t"
    "v_exp_f32 v199, v199\n\t"
    "v_mfma_f32_16x16x32_bf16 v[172:175], a[104:107], a[24:27], v[172:175]\n\t"
    "ds_read_b64_tr_b16 a[176:177], v237 offset:17024\n\t"
    "ds_read_b64_tr_b16 a[178:179], v237 offset:17536\n\t"
    "v_mfma_f32_16x16x32_bf16 v[172:175], a[108:111], a[28:31], v[172:175]\n\t"
    "v_cvt_pk_bf16_f32 v217, v198, v199\n\t"
    "v_add_f32 v250, v250, v193\n\t"
    "v_mfma_f32_16x16x32_bf16 v[176:179], a[112:115], a[16:19], 0\n\t"
    "ds_read_b64_tr_b16 a[208:209], v237 offset:25472\n\t"
    "ds_read_b64_tr_b16 a[210:211], v237 offset:25984\n\t"
    "v_mfma_f32_16x16x32_bf16 v[176:179], a[116:119], a[20:23], v[176:179]\n\t"
    "v_exp_f32 v200, v200\n\t"
    "v_mfma_f32_16x16x32_bf16 v[176:179], a[120:123], a[24:27], v[176:179]\n\t"
    "ds_read_b64_tr_b16 a[180:181], v237 offset:17032\n\t"
    "ds_read_b64_tr_b16 a[182:183], v237 offset:17544\n\t"
    "v_mfma_f32_16x16x32_bf16 v[176:179], a[124:127], a[28:31], v[176:179]\n\t"
    "v_exp_f32 v201, v201\n\t"
    "v_mfma_f32_16x16x32_bf16 v[180:183], a[128:131], a[16:19], 0\n\t"
    "ds_read_b64_tr_b16 a[212:213], v237 offset:25480\n\t"
    "ds_read_b64_tr_b16 a[214:215], v237 offset:25992\n\t"
    "v_mfma_f32_16x16x32_bf16 v[180:183], a[132:135], a[20:23], v[180:183]\n\t"
    "v_cvt_pk_bf16_f32 v218, v200, v201\n\t"
    "v_add_f32 v250, v250, v194\n\t"
    "v_mfma_f32_16x16x32_bf16 v[180:183], a[136:139], a[24:27], v[180:183]\n\t"
    "ds_read_b64_tr_b16 a[184:185], v237 offset:17088\n\t"
    "ds_read_b64_tr_b16 a[186:187], v237 offset:17600\n\t"
    "v_mfma_f32_16x16x32_bf16 v[180:183], a[140:143], a[28:31], v[180:183]\n\t"
    "v_exp_f32 v202, v202\n\t"
    "v_mfma_f32_16x16x32_bf16 v[184:187], a[144:147], a[16:19], 0\n\t"
    "ds_read_b64_tr_b16 a[216:217], v237 offset:25536\n\t"
    "ds_read_b64_tr_b16 a[218:219], v237 offset:26048\n\t"
    "v_mfma_f32_16x16x32_bf16 v[184:187], a[148:151], a[20:23], v[184:187]\n\t"
    "v_exp_f32 v203, v203\n\t"
    "v_mfma_f32_16x16x32_bf16 v[184:187], a[152:155], a[24:27], v[184:187]\n\t"
    "ds_read_b64_tr_b16 a[188:189], v237 offset:17096\n\t"
    "ds_read_b64_tr_b16 a[190:191], v237 offset:17608\n\t"
    "v_mfma_f32_16x16x32_bf16 v[184:187], a[156:159], a[28:31], v[184:187]\n\t"
    "v_cvt_pk_bf16_f32 v219, v202, v203\n\t"
    "v_add_f32 v250, v250, v195\n\t"
    "v_mfma_f32_16x16x32_bf16 v[220:223], a[112:115], a[80:83], 0\n\t"
    "ds_read_b64_tr_b16 a[220:221], v237 offset:25544\n\t"
    "ds_read_b64_tr_b16 a[222:223], v237 offset:26056\n\t"
    "v_mfma_f32_16x16x32_bf16 v[220:223], a[116:119], a[84:87], v[220:223]\n\t"
    "s_add_u32 m0, s56, 4224\n\t"
    "v_exp_f32 v172, v172\n\t"
    "v_mfma_f32_16x16x32_bf16 v[220:223], a[120:123], a[88:91], v[220:223]\n\t"
    "buffer_load_dwordx4 v226, s[44:47], s54 offen offset:0 lds\n\t"
    "v_mfma_f32_16x16x32_bf16 v[220:223], a[124:127], a[92:95], v[220:223]\n\t"
    "s_waitcnt lgkmcnt(4)\n\t"
    "v_exp_f32 v173, v173\n\t"
    "v_mfma_f32_16x16x32_bf16 v[4:7], a[160:163], v[212:215], v[4:7]\n\t"
    "v_cvt_pk_bf16_f32 v204, v172, v173\n\t"
    "v_add_f32 v250, v250, v196\n\t"
    "v_mfma_f32_16x16x32_bf16 v[4:7], a[192:195], v[216:219], v[4:7]\n\t"
    "v_exp_f32 v174, v174\n\t"
    "v_mfma_f32_16x16x32_bf16 v[8:11], a[164:167], v[212:215], v[8:11]\n\t"
    "v_exp_f32 v175, v175\n\t"
    "v_mfma_f32_16x16x32_bf16 v[8:11], a[196:199], v[216:219], v[8:11]\n\t"
    "v_cvt_pk_bf16_f32 v205, v174, v175\n\t"
    "v_add_f32 v250, v250, v197\n\t"
    "v_mfma_f32_16x16x32_bf16 v[12:15], a[168:171], v[212:215], v[12:15]\n\t"
    "v_exp_f32 v176, v176\n\t"
    "v_mfma_f32_16x16x32_bf16 v[12:15], a[200:203], v[216:219], v[12:15]\n\t"
    "v_exp_f32 v177, v177\n\t"
    "v_mfma_f32_16x16x32_bf16 v[16:19], a[172:175], v[212:215], v[16:19]\n\t"
    "v_cvt_pk_bf16_f32 v206, v176, v177\n\t"
    "v_add_f32 v250, v250, v198\n\t"
    "v_mfma_f32_16x16x32_bf16 v[16:19], a[204:207], v[216:219], v[16:19]\n\t"
    "v_exp_f32 v178, v178\n\t"
    "v_mfma_f32_16x16x32_bf16 v[20:23], a[176:179], v[212:215], v[20:23]\n\t"
    "v_exp_f32 v179, v179\n\t"
    "v_mfma_f32_16x16x32_bf16 v[20:23], a[208:211], v[216:219], v[20:23]\n\t"
    "s_add_u32 m0, s55, 8448\n\t"
    "v_cvt_pk_bf16_f32 v207, v178, v179\n\t"
    "v_add_f32 v250, v250, v199\n\t"
    "v_mfma_f32_16x16x32_bf16 v[24:27], a[180:183], v[212:215], v[24:27]\n\t"
    "buffer_load_dwordx4 v227, s[40:43], s54 offen offset:0 nt lds\n\t"
    "v_mfma_f32_16x16x32_bf16 v[24:27], a[212:215], v[216:219], v[24:27]\n\t"
    "v_exp_f32 v180, v180\n\t"
    "v_mfma_f32_16x16x32_bf16 v[28:31], a[184:187], v[212:215], v[28:31]\n\t"
    "v_exp_f32 v181, v181\n\t"
    "v_mfma_f32_16x16x32_bf16 v[28:31], a[216:219], v[216:219], v[28:31]\n\t"
    "s_waitcnt lgkmcnt(0)\n\t"
    "v_cvt_pk_bf16_f32 v208, v180, v181\n\t"
    "v_add_f32 v250, v250, v200\n\t"
    "v_mfma_f32_16x16x32_bf16 v[32:35], a[188:191], v[212:215], v[32:35]\n\t"
    "v_exp_f32 v182, v182\n\t"
    "v_mfma_f32_16x16x32_bf16 v[32:35], a[220:223], v[216:219], v[32:35]\n\t"
    "v_exp_f32 v183, v183\n\t"
    "v_mfma_f32_16x16x32_bf16 v[188:191], a[96:99], a[32:35], 0\n\t"
    "v_cvt_pk_bf16_f32 v209, v182, v183\n\t"
    "v_add_f32 v250, v250, v201\n\t"
    "v_mfma_f32_16x16x32_bf16 v[188:191], a[100:103], a[36:39], v[188:191]\n\t"
    "v_exp_f32 v184, v184\n\t"
    "v_mfma_f32_16x16x32_bf16 v[188:191], a[104:107], a[40:43], v[188:191]\n\t"
    "v_exp_f32 v220, v220\n\t"
    "v_mfma_f32_16x16x32_bf16 v[188:191], a[108:111], a[44:47], v[188:191]\n\t"
    "v_exp_f32 v185, v185\n\t"
    "v_mfma_f32_16x16x32_bf16 v[192:195], a[112:115], a[32:35], 0\n\t"
    "v_exp_f32 v221, v221\n\t"
    "v_mfma_f32_16x16x32_bf16 v[192:195], a[116:119], a[36:39], v[192:195]\n\t"
    "v_add_f32 v250, v250, v202\n\t"
    "v_add_f32 v250, v250, v203\n\t"
    "v_mfma_f32_16x16x32_bf16 v[192:195], a[120:123], a[40:43], v[192:195]\n\t"
    "v_cvt_pk_bf16_f32 v210, v184, v185\n\t"
    "v_cvt_pk_bf16_f32 v224, v220, v221\n\t"
    "v_mfma_f32_16x16x32_bf16 v[192:195], a[124:127], a[44:47], v[192:195]\n\t"
    "v_exp_f32 v186, v186\n\t"
    "v_mfma_f32_16x16x32_bf16 v[196:199], a[128:131], a[32:35], 0\n\t"
    "v_exp_f32 v222, v222\n\t"
    "v_mfma_f32_16x16x32_bf16 v[196:199], a[132:135], a[36:39], v[196:199]\n\t"
    "s_add_u32 m0, s56, 8448\n\t"
    "v_exp_f32 v187, v187\n\t"
    "v_mfma_f32_16x16x32_bf16 v[196:199], a[136:139], a[40:43], v[196:199]\n\t"
    "buffer_load_dwordx4 v227, s[44:47], s54 offen offset:0 lds\n\t"
    "v_mfma_f32_16x16x32_bf16 v[196:199], a[140:143], a[44:47], v[196:199]\n\t"
    "v_exp_f32 v223, v223\n\t"
    "v_mfma_f32_16x16x32_bf16 v[200:203], a[144:147], a[32:35], 0\n\t"
    "v_cvt_pk_bf16_f32 v225, v222, v223\n\t"
    "v_cvt_pk_bf16_f32 v211, v186, v187\n\t"
    "v_mfma_f32_16x16x32_bf16 v[200:203], a[148:151], a[36:39], v[200:203]\n\t"
    "ds_write_b64 v238, v[224:225] offset:2056\n\t"
    "v_mfma_f32_16x16x32_bf16 v[200:203], a[152:155], a[40:43], v[200:203]\n\t"
    "v_exp_f32 v188, v188\n\t"
    "v_mfma_f32_16x16x32_bf16 v[200:203], a[156:159], a[44:47], v[200:203]\n\t"
    "v_exp_f32 v189, v189\n\t"
    "v_mfma_f32_16x16x16_bf16 a[244:247], a[248:249], v[224:225], a[244:247]\n\t"
    "v_cvt_pk_bf16_f32 v212, v188, v189\n\t"
    "v_add_f32 v251, v251, v180\n\t"
    "v_mfma_f32_16x16x32_bf16 v[36:39], a[160:163], v[204:207], v[36:39]\n\t"
    "v_exp_f32 v190, v190\n\t"
    "v_mfma_f32_16x16x32_bf16 v[36:39], a[192:195], v[208:211], v[36:39]\n\t"
    "v_exp_f32 v191, v191\n\t"
    "v_mfma_f32_16x16x32_bf16 v[40:43], a[164:167], v[204:207], v[40:43]\n\t"
    "v_cvt_pk_bf16_f32 v213, v190, v191\n\t"
    "v_add_f32 v251, v251, v181\n\t"
    "v_mfma_f32_16x16x32_bf16 v[40:43], a[196:199], v[208:211], v[40:43]\n\t"
    "v_exp_f32 v192, v192\n\t"
    "v_mfma_f32_16x16x32_bf16 v[44:47], a[168:171], v[204:207], v[44:47]\n\t"
    "v_exp_f32 v193, v193\n\t"
    "v_mfma_f32_16x16x32_bf16 v[44:47], a[200:203], v[208:211], v[44:47]\n\t"
    "v_cvt_pk_bf16_f32 v214, v192, v193\n\t"
    "v_add_f32 v251, v251, v182\n\t"
    "v_mfma_f32_16x16x32_bf16 v[48:51], a[172:175], v[204:207], v[48:51]\n\t"
    "v_exp_f32 v194, v194\n\t"
    "v_mfma_f32_16x16x32_bf16 v[48:51], a[204:207], v[208:211], v[48:51]\n\t"
    "v_exp_f32 v195, v195\n\t"
    "v_mfma_f32_16x16x32_bf16 v[52:55], a[176:179], v[204:207], v[52:55]\n\t"
    "s_add_u32 m0, s55, 12672\n\t"
    "v_cvt_pk_bf16_f32 v215, v194, v195\n\t"
    "v_add_f32 v251, v251, v183\n\t"
    "v_mfma_f32_16x16x32_bf16 v[52:55], a[208:211], v[208:211], v[52:55]\n\t"
    "buffer_load_dwordx4 v3, s[40:43], s54 offen offset:0 nt lds\n\t"
    "v_mfma_f32_16x16x32_bf16 v[56:59], a[180:183], v[204:207], v[56:59]\n\t"
    "v_exp_f32 v196, v196\n\t"
    "v_mfma_f32_16x16x32_bf16 v[56:59], a[212:215], v[208:211], v[56:59]\n\t"
    "v_exp_f32 v197, v197\n\t"
    "v_mfma_f32_16x16x32_bf16 v[60:63], a[184:187], v[204:207], v[60:63]\n\t"
    "v_cvt_pk_bf16_f32 v216, v196, v197\n\t"
    "v_add_f32 v251, v251, v184\n\t"
    "v_mfma_f32_16x16x32_bf16 v[60:63], a[216:219], v[208:211], v[60:63]\n\t"
    "v_exp_f32 v198, v198\n\t"
    "v_mfma_f32_16x16x32_bf16 v[64:67], a[188:191], v[204:207], v[64:67]\n\t"
    "v_exp_f32 v199, v199\n\t"
    "v_mfma_f32_16x16x32_bf16 v[64:67], a[220:223], v[208:211], v[64:67]\n\t"
    "v_cvt_pk_bf16_f32 v217, v198, v199\n\t"
    "v_add_f32 v251, v251, v185\n\t"
    "v_mfma_f32_16x16x32_bf16 a[228:231], a[248:251], v[204:207], a[228:231]\n\t"
    "v_exp_f32 v200, v200\n\t"
    "v_mfma_f32_16x16x32_bf16 v[172:175], a[96:99], a[48:51], 0\n\t"
    "v_exp_f32 v201, v201\n\t"
    "v_mfma_f32_16x16x32_bf16 v[172:175], a[100:103], a[52:55], v[172:175]\n\t"
    "v_cvt_pk_bf16_f32 v218, v200, v201\n\t"
    "v_add_f32 v251, v251, v186\n\t"
    "v_mfma_f32_16x16x32_bf16 v[172:175], a[104:107], a[56:59], v[172:175]\n\t"
    "v_exp_f32 v202, v202\n\t"
    "v_mfma_f32_16x16x32_bf16 v[172:175], a[108:111], a[60:63], v[172:175]\n\t"
    "v_add_f32 v251, v251, v187\n\t"
    "v_add_u32 v236, s58, v236\n\t"
    "v_mfma_f32_16x16x32_bf16 v[176:179], a[112:115], a[48:51], 0\n\t"
    "v_exp_f32 v203, v203\n\t"
    "v_mfma_f32_16x16x32_bf16 v[176:179], a[116:119], a[52:55], v[176:179]\n\t"
    "v_cvt_pk_bf16_f32 v219, v202, v203\n\t"
    "v_add_u32 v237, s58, v237\n\t"
    "v_mfma_f32_16x16x32_bf16 v[176:179], a[120:123], a[56:59], v[176:179]\n\t"
    "v_add_f32 v252, v252, v196\n\t"
    "v_add_f32 v252, v252, v197\n\t"
    "v_mfma_f32_16x16x32_bf16 v[176:179], a[124:127], a[60:63], v[176:179]\n\t"
    "s_add_u32 m0, s56, 12672\n\t"
    "v_add_f32 v252, v252, v198\n\t"
    "v_add_f32 v252, v252, v199\n\t"
    "v_mfma_f32_16x16x32_bf16 v[180:183], a[128:131], a[48:51], 0\n\t"
    "buffer_load_dwordx4 v3, s[44:47], s54 offen offset:0 lds\n\t"
    "v_mfma_f32_16x16x32_bf16 v[180:183], a[132:135], a[52:55], v[180:183]\n\t"
    "v_add_f32 v252, v252, v200\n\t"
    "v_add_f32 v252, v252, v201\n\t"
    "v_mfma_f32_16x16x32_bf16 v[180:183], a[136:139], a[56:59], v[180:183]\n\t"
    "v_add_f32 v252, v252, v202\n\t"
    "v_add_f32 v252, v252, v203\n\t"
    "v_mfma_f32_16x16x32_bf16 v[180:183], a[140:143], a[60:63], v[180:183]\n\t"
    "v_exp_f32 v172, v172\n\t"
    "v_mfma_f32_16x16x32_bf16 v[184:187], a[144:147], a[48:51], 0\n\t"
    "v_exp_f32 v173, v173\n\t"
    "v_mfma_f32_16x16x32_bf16 v[184:187], a[148:151], a[52:55], v[184:187]\n\t"
    "v_cvt_pk_bf16_f32 v204, v172, v173\n\t"
    "v_mfma_f32_16x16x32_bf16 v[184:187], a[152:155], a[56:59], v[184:187]\n\t"
    "v_exp_f32 v174, v174\n\t"
    "v_mfma_f32_16x16x32_bf16 v[184:187], a[156:159], a[60:63], v[184:187]\n\t"
    "v_exp_f32 v175, v175\n\t"
    "v_mfma_f32_16x16x32_bf16 v[68:71], a[160:163], v[212:215], v[68:71]\n\t"
    "v_cvt_pk_bf16_f32 v205, v174, v175\n\t"
    "v_mfma_f32_16x16x32_bf16 v[68:71], a[192:195], v[216:219], v[68:71]\n\t"
    "v_exp_f32 v176, v176\n\t"
    "v_mfma_f32_16x16x32_bf16 v[72:75], a[164:167], v[212:215], v[72:75]\n\t"
    "v_exp_f32 v177, v177\n\t"
    "v_mfma_f32_16x16x32_bf16 v[72:75], a[196:199], v[216:219], v[72:75]\n\t"
    "v_cvt_pk_bf16_f32 v206, v176, v177\n\t"
    "v_mfma_f32_16x16x32_bf16 v[76:79], a[168:171], v[212:215], v[76:79]\n\t"
    "v_exp_f32 v178, v178\n\t"
    "v_mfma_f32_16x16x32_bf16 v[76:79], a[200:203], v[216:219], v[76:79]\n\t"
    "v_exp_f32 v179, v179\n\t"
    "v_mfma_f32_16x16x32_bf16 v[80:83], a[172:175], v[212:215], v[80:83]\n\t"
    "v_cvt_pk_bf16_f32 v207, v178, v179\n\t"
    "v_mfma_f32_16x16x32_bf16 v[80:83], a[204:207], v[216:219], v[80:83]\n\t"
    "v_exp_f32 v180, v180\n\t"
    "v_mfma_f32_16x16x32_bf16 v[84:87], a[176:179], v[212:215], v[84:87]\n\t"
    "v_exp_f32 v181, v181\n\t"
    "v_mfma_f32_16x16x32_bf16 v[84:87], a[208:211], v[216:219], v[84:87]\n\t"
    "v_cvt_pk_bf16_f32 v208, v180, v181\n\t"
    "v_add_f32 v253, v253, v180\n\t"
    "v_mfma_f32_16x16x32_bf16 v[88:91], a[180:183], v[212:215], v[88:91]\n\t"
    "v_exp_f32 v182, v182\n\t"
    "v_mfma_f32_16x16x32_bf16 v[88:91], a[212:215], v[216:219], v[88:91]\n\t"
    "v_exp_f32 v183, v183\n\t"
    "v_mfma_f32_16x16x32_bf16 v[92:95], a[184:187], v[212:215], v[92:95]\n\t"
    "v_cvt_pk_bf16_f32 v209, v182, v183\n\t"
    "v_add_f32 v253, v253, v181\n\t"
    "v_mfma_f32_16x16x32_bf16 v[92:95], a[216:219], v[216:219], v[92:95]\n\t"
    "v_exp_f32 v184, v184\n\t"
    "v_mfma_f32_16x16x32_bf16 v[96:99], a[188:191], v[212:215], v[96:99]\n\t"
    "v_exp_f32 v185, v185\n\t"
    "v_mfma_f32_16x16x32_bf16 v[96:99], a[220:223], v[216:219], v[96:99]\n\t"
    "s_add_u32 s54, s54, 16384\n\t"
    "v_cvt_pk_bf16_f32 v210, v184, v185\n\t"
    "v_add_f32 v253, v253, v182\n\t"
    "v_mfma_f32_16x16x32_bf16 a[232:235], a[248:251], v[212:215], a[232:235]\n\t"
    "s_waitcnt vmcnt(16) lgkmcnt(0)\n\t"
    "s_barrier\n\t"
    "v_mfma_f32_16x16x32_bf16 v[188:191], a[96:99], a[64:67], 0\n\t"
    "ds_read_b128 a[96:99], v236 offset:0\n\t"
    "v_add_f32 v253, v253, v183\n\t"
    "v_mfma_f32_16x16x32_bf16 v[188:191], a[100:103], a[68:71], v[188:191]\n\t"
    "s_sub_u32 s58, 0, s58\n\t"
    "v_exp_f32 v186, v186\n\t"
    "v_mfma_f32_16x16x32_bf16 v[188:191], a[104:107], a[72:75], v[188:191]\n\t"
    "ds_read_b128 a[100:103], v236 offset:64\n\t"
    "ds_read_b128 a[104:107], v236 offset:128\n\t"
    "v_mfma_f32_16x16x32_bf16 v[188:191], a[108:111], a[76:79], v[188:191]\n\t"
    "s_and_b32 s54, s54, 0x7ffff\n\t"
    "v_exp_f32 v187, v187\n\t"
    "v_mfma_f32_16x16x32_bf16 v[192:195], a[112:115], a[64:67], 0\n\t"
    "ds_read_b128 a[108:111], v236 offset:192\n\t"
    "ds_read_b128 a[112:115], v236 offset:512\n\t"
    "v_mfma_f32_16x16x32_bf16 v[192:195], a[116:119], a[68:71], v[192:195]\n\t"
    "v_cvt_pk_bf16_f32 v211, v186, v187\n\t"
    "v_add_f32 v253, v253, v184\n\t"
    "v_mfma_f32_16x16x32_bf16 v[192:195], a[120:123], a[72:75], v[192:195]\n\t"
    "ds_read_b128 a[116:119], v236 offset:576\n\t"
    "ds_read_b128 a[120:123], v236 offset:640\n\t"
    "v_mfma_f32_16x16x32_bf16 v[192:195], a[124:127], a[76:79], v[192:195]\n\t"
    "v_add_f32 v253, v253, v185\n\t"
    "v_add_f32 v253, v253, v186\n\t"
    "v_mfma_f32_16x16x32_bf16 v[196:199], a[128:131], a[64:67], 0\n\t"
    "ds_read_b128 a[124:127], v236 offset:704\n\t"
    "v_add_f32 v253, v253, v187\n\t"
    "v_mfma_f32_16x16x32_bf16 v[196:199], a[132:135], a[68:71], v[196:199]\n\t"
    "ds_read_b128 a[128:131], v236 offset:8448\n\t"
    "v_mfma_f32_16x16x32_bf16 v[196:199], a[136:139], a[72:75], v[196:199]\n\t"
    "ds_read_b128 a[132:135], v236 offset:8512\n\t"
    "v_mfma_f32_16x16x32_bf16 v[196:199], a[140:143], a[76:79], v[196:199]\n\t"
    "v_exp_f32 v188, v188\n\t"
    "v_mfma_f32_16x16x32_bf16 v[200:203], a[144:147], a[64:67], 0\n\t"
    "ds_read_b128 a[136:139], v236 offset:8576\n\t"
    "ds_read_b128 a[140:143], v236 offset:8640\n\t"
    "v_mfma_f32_16x16x32_bf16 v[200:203], a[148:151], a[68:71], v[200:203]\n\t"
    "v_exp_f32 v189, v189\n\t"
    "v_mfma_f32_16x16x32_bf16 v[200:203], a[152:155], a[72:75], v[200:203]\n\t"
    "ds_read_b128 a[144:147], v236 offset:8960\n\t"
    "ds_read_b128 a[148:151], v236 offset:9024\n\t"
    "v_mfma_f32_16x16x32_bf16 v[200:203], a[156:159], a[76:79], v[200:203]\n\t"
    "v_cvt_pk_bf16_f32 v212, v188, v189\n\t"
    "v_add_f32 v254, v254, v188\n\t"
    "v_mfma_f32_16x16x32_bf16 v[100:103], a[160:163], v[204:207], v[100:103]\n\t"
    "ds_read_b128 a[152:155], v236 offset:9088\n\t"
    "ds_read_b128 a[156:159], v236 offset:9152\n\t"
    "v_mfma_f32_16x16x32_bf16 v[100:103], a[192:195], v[208:211], v[100:103]\n\t"
    "v_exp_f32 v190, v190\n\t"
    "v_mfma_f32_16x16x32_bf16 v[104:107], a[164:167], v[204:207], v[104:107]\n\t"
    "ds_read_b128 v[228:231], v238 offset:2048\n\t"
    "ds_read_b128 v[232:235], v238 offset:3072\n\t"
    "v_mfma_f32_16x16x32_bf16 v[104:107], a[196:199], v[208:211], v[104:107]\n\t"
    "v_exp_f32 v191, v191\n\t"
    "v_mfma_f32_16x16x32_bf16 v[108:111], a[168:171], v[204:207], v[108:111]\n\t"
    "v_cvt_pk_bf16_f32 v213, v190, v191\n\t"
    "v_add_f32 v254, v254, v189\n\t"
    "v_mfma_f32_16x16x32_bf16 v[108:111], a[200:203], v[208:211], v[108:111]\n\t"
    "v_exp_f32 v192, v192\n\t"
    "v_mfma_f32_16x16x32_bf16 v[112:115], a[172:175], v[204:207], v[112:115]\n\t"
    "v_exp_f32 v193, v193\n\t"
    "v_mfma_f32_16x16x32_bf16 v[112:115], a[204:207], v[208:211], v[112:115]\n\t"
    "v_cvt_pk_bf16_f32 v214, v192, v193\n\t"
    "v_add_f32 v254, v254, v190\n\t"
    "v_mfma_f32_16x16x32_bf16 v[116:119], a[176:179], v[204:207], v[116:119]\n\t"
    "v_exp_f32 v194, v194\n\t"
    "v_mfma_f32_16x16x32_bf16 v[116:119], a[208:211], v[208:211], v[116:119]\n\t"
    "v_exp_f32 v195, v195\n\t"
    "v_mfma_f32_16x16x32_bf16 v[120:123], a[180:183], v[204:207], v[120:123]\n\t"
    "v_cvt_pk_bf16_f32 v215, v194, v195\n\t"
    "v_add_f32 v254, v254, v191\n\t"
    "v_mfma_f32_16x16x32_bf16 v[120:123], a[212:215], v[208:211], v[120:123]\n\t"
    "v_exp_f32 v196, v196\n\t"
    "v_mfma_f32_16x16x32_bf16 v[124:127], a[184:187], v[204:207], v[124:127]\n\t"
    "v_exp_f32 v197, v197\n\t"
    "v_mfma_f32_16x16x32_bf16 v[124:127], a[216:219], v[208:211], v[124:127]\n\t"
    "v_cvt_pk_bf16_f32 v216, v196, v197\n\t"
    "v_add_f32 v254, v254, v192\n\t"
    "v_mfma_f32_16x16x32_bf16 v[128:131], a[188:191], v[204:207], v[128:131]\n\t"
    "v_exp_f32 v198, v198\n\t"
    "v_mfma_f32_16x16x32_bf16 v[128:131], a[220:223], v[208:211], v[128:131]\n\t"
    "v_exp_f32 v199, v199\n\t"
    "v_mfma_f32_16x16x32_bf16 a[236:239], a[248:251], v[204:207], a[236:239]\n\t"
    "s_waitcnt lgkmcnt(0)\n\t"
    "v_cvt_pk_bf16_f32 v217, v198, v199\n\t"
    "v_add_f32 v254, v254, v193\n\t"
    "v_mfma_f32_16x16x32_bf16 v[172:175], a[96:99], a[0:3], 0\n\t"
    "v_exp_f32 v200, v200\n\t"
    "v_mfma_f32_16x16x32_bf16 v[172:175], a[100:103], a[4:7], v[172:175]\n\t"
    "s_add_u32 m0, s55, 16896\n\t"
    "v_exp_f32 v201, v201\n\t"
    "v_mfma_f32_16x16x32_bf16 v[172:175], a[104:107], a[8:11], v[172:175]\n\t"
    "buffer_load_dwordx4 v239, s[40:43], s54 offen offset:0 nt lds\n\t"
    "v_mfma_f32_16x16x32_bf16 v[172:175], a[108:111], a[12:15], v[172:175]\n\t"
    "v_cvt_pk_bf16_f32 v218, v200, v201\n\t"
    "v_add_f32 v254, v254, v194\n\t"
    "v_mfma_f32_16x16x32_bf16 v[176:179], a[112:115], a[0:3], 0\n\t"
    "v_exp_f32 v202, v202\n\t"
    "v_mfma_f32_16x16x32_bf16 v[176:179], a[116:119], a[4:7], v[176:179]\n\t"
    "v_exp_f32 v203, v203\n\t"
    "v_mfma_f32_16x16x32_bf16 v[176:179], a[120:123], a[8:11], v[176:179]\n\t"
    "v_cvt_pk_bf16_f32 v219, v202, v203\n\t"
    "v_add_f32 v254, v254, v195\n\t"
    "v_mfma_f32_16x16x32_bf16 v[176:179], a[124:127], a[12:15], v[176:179]\n\t"
    "v_add_f32 v254, v254, v196\n\t"
    "v_add_f32 v254, v254, v197\n\t"
    "v_mfma_f32_16x16x32_bf16 v[180:183], a[128:131], a[0:3], 0\n\t"
    "v_add_f32 v254, v254, v198\n\t"
    "v_add_f32 v254, v254, v199\n\t"
    "v_mfma_f32_16x16x32_bf16 v[180:183], a[132:135], a[4:7], v[180:183]\n\t"
    "v_add_f32 v254, v254, v200\n\t"
    "v_add_f32 v254, v254, v201\n\t"
    "v_mfma_f32_16x16x32_bf16 v[180:183], a[136:139], a[8:11], v[180:183]\n\t"
    "v_add_f32 v254, v254, v202\n\t"
    "v_add_f32 v254, v254, v203\n\t"
    "v_mfma_f32_16x16x32_bf16 v[180:183], a[140:143], a[12:15], v[180:183]\n\t"
    "v_exp_f32 v172, v172\n\t"
    "v_mfma_f32_16x16x32_bf16 v[184:187], a[144:147], a[0:3], 0\n\t"
    "v_exp_f32 v173, v173\n\t"
    "v_mfma_f32_16x16x32_bf16 v[184:187], a[148:151], a[4:7], v[184:187]\n\t"
    "v_cvt_pk_bf16_f32 v204, v172, v173\n\t"
    "v_add_f32 v250, v250, v172\n\t"
    "v_mfma_f32_16x16x32_bf16 v[184:187], a[152:155], a[8:11], v[184:187]\n\t"
    "v_exp_f32 v174, v174\n\t"
    "v_mfma_f32_16x16x32_bf16 v[184:187], a[156:159], a[12:15], v[184:187]\n\t"
    "v_exp_f32 v175, v175\n\t"
    "v_mfma_f32_16x16x32_bf16 v[164:167], a[168:171], v[228:231], v[164:167]\n\t"
    "v_cvt_pk_bf16_f32 v205, v174, v175\n\t"
    "v_add_f32 v250, v250, v173\n\t"
    "v_mfma_f32_16x16x32_bf16 v[164:167], a[200:203], v[232:235], v[164:167]\n\t"
    "s_add_u32 m0, s56, 16896\n\t"
    "v_exp_f32 v176, v176\n\t"
    "v_mfma_f32_16x16x32_bf16 v[168:171], a[172:175], v[228:231], v[168:171]\n\t"
    "buffer_load_dwordx4 v239, s[44:47], s54 offen offset:0 lds\n\t"
    "v_mfma_f32_16x16x32_bf16 v[168:171], a[204:207], v[232:235], v[168:171]\n\t"
    "v_exp_f32 v177, v177\n\t"
    "v_mfma_f32_16x16x32_bf16 v[132:135], a[160:163], v[212:215], v[132:135]\n\t"
    "ds_read_b64_tr_b16 a[160:161], v237 offset:0\n\t"
    "ds_read_b64_tr_b16 a[162:163], v237 offset:512\n\t"
    "v_mfma_f32_16x16x32_bf16 v[132:135], a[192:195], v[216:219], v[132:135]\n\t"
    "v_cvt_pk_bf16_f32 v206, v176, v177\n\t"
    "v_add_f32 v250, v250, v174\n\t"
    "v_mfma_f32_16x16x32_bf16 v[136:139], a[164:167], v[212:215], v[136:139]\n\t"
    "ds_read_b64_tr_b16 a[192:193], v237 offset:8448\n\t"
    "ds_read_b64_tr_b16 a[194:195], v237 offset:8960\n\t"
    "v_mfma_f32_16x16x32_bf16 v[136:139], a[196:199], v[216:219], v[136:139]\n\t"
    "v_exp_f32 v178, v178\n\t"
    "v_mfma_f32_16x16x32_bf16 v[140:143], a[168:171], v[212:215], v[140:143]\n\t"
    "ds_read_b64_tr_b16 a[164:165], v237 offset:8\n\t"
    "ds_read_b64_tr_b16 a[166:167], v237 offset:520\n\t"
    "v_mfma_f32_16x16x32_bf16 v[140:143], a[200:203], v[216:219], v[140:143]\n\t"
    "v_exp_f32 v179, v179\n\t"
    "v_mfma_f32_16x16x32_bf16 v[144:147], a[172:175], v[212:215], v[144:147]\n\t"
    "ds_read_b64_tr_b16 a[196:197], v237 offset:8456\n\t"
    "ds_read_b64_tr_b16 a[198:199], v237 offset:8968\n\t"
    "v_mfma_f32_16x16x32_bf16 v[144:147], a[204:207], v[216:219], v[144:147]\n\t"
    "v_cvt_pk_bf16_f32 v207, v178, v179\n\t"
    "v_add_f32 v250, v250, v175\n\t"
    "v_mfma_f32_16x16x32_bf16 v[148:151], a[176:179], v[212:215], v[148:151]\n\t"
    "ds_read_b64_tr_b16 a[168:169], v237 offset:64\n\t"
    "ds_read_b64_tr_b16 a[170:171], v237 offset:576\n\t"
    "v_mfma_f32_16x16x32_bf16 v[148:151], a[208:211], v[216:219], v[148:151]\n\t"
    "v_exp_f32 v180, v180\n\t"
    "v_mfma_f32_16x16x32_bf16 v[152:155], a[180:183], v[212:215], v[152:155]\n\t"
    "ds_read_b64_tr_b16 a[200:201], v237 offset:8512\n\t"
    "ds_read_b64_tr_b16 a[202:203], v237 offset:9024\n\t"
    "v_mfma_f32_16x16x32_bf16 v[152:155], a[212:215], v[216:219], v[152:155]\n\t"
    "v_exp_f32 v181, v181\n\t"
    "v_mfma_f32_16x16x32_bf16 v[156:159], a[184:187], v[212:215], v[156:159]\n\t"
    "ds_read_b64_tr_b16 a[172:173], v237 offset:72\n\t"
    "ds_read_b64_tr_b16 a[174:175], v237 offset:584\n\t"
    "v_mfma_f32_16x16x32_bf16 v[156:159], a[216:219], v[216:219], v[156:159]\n\t"
    "s_add_u32 m0, s55, 21120\n\t"
    "v_cvt_pk_bf16_f32 v208, v180, v181\n\t"
    "v_add_f32 v250, v250, v176\n\t"
    "v_mfma_f32_16x16x32_bf16 v[160:163], a[188:191], v[212:215], v[160:163]\n\t"
    "buffer_load_dwordx4 v226, s[40:43], s54 offen offset:0 nt lds\n\t"
    "v_mfma_f32_16x16x32_bf16 v[160:163], a[220:223], v[216:219], v[160:163]\n\t"
    "v_exp_f32 v182, v182\n\t"
    "v_mfma_f32_16x16x32_bf16 v[188:191], a[96:99], a[16:19], 0\n\t"
    "ds_read_b64_tr_b16 a[204:205], v237 offset:8520\n\t"
    "ds_read_b64_tr_b16 a[206:207], v237 offset:9032\n\t"
    "v_mfma_f32_16x16x32_bf16 v[188:191], a[100:103], a[20:23], v[188:191]\n\t"
    "v_exp_f32 v183, v183\n\t"
    "v_mfma_f32_16x16x32_bf16 v[188:191], a[104:107], a[24:27], v[188:191]\n\t"
    "ds_read_b64_tr_b16 a[176:177], v237 offset:128\n\t"
    "ds_read_b64_tr_b16 a[178:179], v237 offset:640\n\t"
    "v_mfma_f32_16x16x32_bf16 v[188:191], a[108:111], a[28:31], v[188:191]\n\t"
    "v_cvt_pk_bf16_f32 v209, v182, v183\n\t"
    "v_add_f32 v250, v250, v177\n\t"
    "v_mfma_f32_16x16x32_bf16 v[192:195], a[112:115], a[16:19], 0\n\t"
    "ds_read_b64_tr_b16 a[208:209], v237 offset:8576\n\t"
    "ds_read_b64_tr_b16 a[210:211], v237 offset:9088\n\t"
    "v_mfma_f32_16x16x32_bf16 v[192:195], a[116:119], a[20:23], v[192:195]\n\t"
    "v_exp_f32 v184, v184\n\t"
    "v_mfma_f32_16x16x32_bf16 v[192:195], a[120:123], a[24:27], v[192:195]\n\t"
    "ds_read_b64_tr_b16 a[180:181], v237 offset:136\n\t"
    "ds_read_b64_tr_b16 a[182:183], v237 offset:648\n\t"
    "v_mfma_f32_16x16x32_bf16 v[192:195], a[124:127], a[28:31], v[192:195]\n\t"
    "v_exp_f32 v185, v185\n\t"
    "v_mfma_f32_16x16x32_bf16 v[196:199], a[128:131], a[16:19], 0\n\t"
    "ds_read_b64_tr_b16 a[212:213], v237 offset:8584\n\t"
    "ds_read_b64_tr_b16 a[214:215], v237 offset:9096\n\t"
    "v_mfma_f32_16x16x32_bf16 v[196:199], a[132:135], a[20:23], v[196:199]\n\t"
    "v_cvt_pk_bf16_f32 v210, v184, v185\n\t"
    "v_add_f32 v250, v250, v178\n\t"
    "v_mfma_f32_16x16x32_bf16 v[196:199], a[136:139], a[24:27], v[196:199]\n\t"
    "ds_read_b64_tr_b16 a[184:185], v237 offset:192\n\t"
    "ds_read_b64_tr_b16 a[186:187], v237 offset:704\n\t"
    "v_mfma_f32_16x16x32_bf16 v[196:199], a[140:143], a[28:31], v[196:199]\n\t"
    "v_exp_f32 v186, v186\n\t"
    "v_mfma_f32_16x16x32_bf16 v[200:203], a[144:147], a[16:19], 0\n\t"
    "ds_read_b64_tr_b16 a[216:217], v237 offset:8640\n\t"
    "ds_read_b64_tr_b16 a[218:219], v237 offset:9152\n\t"
    "v_mfma_f32_16x16x32_bf16 v[200:203], a[148:151], a[20:23], v[200:203]\n\t"
    "v_exp_f32 v187, v187\n\t"
    "v_mfma_f32_16x16x32_bf16 v[200:203], a[152:155], a[24:27], v[200:203]\n\t"
    "ds_read_b64_tr_b16 a[188:189], v237 offset:200\n\t"
    "ds_read_b64_tr_b16 a[190:191], v237 offset:712\n\t"
    "v_mfma_f32_16x16x32_bf16 v[200:203], a[156:159], a[28:31], v[200:203]\n\t"
    "v_cvt_pk_bf16_f32 v211, v186, v187\n\t"
    "v_add_f32 v250, v250, v179\n\t"
    "v_mfma_f32_16x16x32_bf16 v[220:223], a[112:115], a[80:83], 0\n\t"
    "ds_read_b64_tr_b16 a[220:221], v237 offset:8648\n\t"
    "ds_read_b64_tr_b16 a[222:223], v237 offset:9160\n\t"
    "v_mfma_f32_16x16x32_bf16 v[220:223], a[116:119], a[84:87], v[220:223]\n\t"
    "s_add_u32 m0, s56, 21120\n\t"
    "v_exp_f32 v188, v188\n\t"
    "v_mfma_f32_16x16x32_bf16 v[220:223], a[120:123], a[88:91], v[220:223]\n\t"
    "buffer_load_dwordx4 v226, s[44:47], s54 offen offset:0 lds\n\t"
    "v_mfma_f32_16x16x32_bf16 v[220:223], a[124:127], a[92:95], v[220:223]\n\t"
    "s_waitcnt lgkmcnt(4)\n\t"
    "v_exp_f32 v189, v189\n\t"
    "v_mfma_f32_16x16x32_bf16 v[4:7], a[160:163], v[204:207], v[4:7]\n\t"
    "v_cvt_pk_bf16_f32 v212, v188, v189\n\t"
    "v_add_f32 v250, v250, v180\n\t"
    "v_mfma_f32_16x16x32_bf16 v[4:7], a[192:195], v[208:211], v[4:7]\n\t"
    "v_exp_f32 v190, v190\n\t"
    "v_mfma_f32_16x16x32_bf16 v[8:11], a[164:167], v[204:207], v[8:11]\n\t"
    "v_exp_f32 v191, v191\n\t"
    "v_mfma_f32_16x16x32_bf16 v[8:11], a[196:199], v[208:211], v[8:11]\n\t"
    "v_cvt_pk_bf16_f32 v213, v190, v191\n\t"
    "v_add_f32 v250, v250, v181\n\t"
    "v_mfma_f32_16x16x32_bf16 v[12:15], a[168:171], v[204:207], v[12:15]\n\t"
    "v_exp_f32 v192, v192\n\t"
    "v_mfma_f32_16x16x32_bf16 v[12:15], a[200:203], v[208:211], v[12:15]\n\t"
    "v_exp_f32 v193, v193\n\t"
    "v_mfma_f32_16x16x32_bf16 v[16:19], a[172:175], v[204:207], v[16:19]\n\t"
    "v_cvt_pk_bf16_f32 v214, v192, v193\n\t"
    "v_add_f32 v250, v250, v182\n\t"
    "v_mfma_f32_16x16x32_bf16 v[16:19], a[204:207], v[208:211], v[16:19]\n\t"
    "v_exp_f32 v194, v194\n\t"
    "v_mfma_f32_16x16x32_bf16 v[20:23], a[176:179], v[204:207], v[20:23]\n\t"
    "v_exp_f32 v195, v195\n\t"
    "v_mfma_f32_16x16x32_bf16 v[20:23], a[208:211], v[208:211], v[20:23]\n\t"
    "s_add_u32 m0, s55, 25344\n\t"
    "v_cvt_pk_bf16_f32 v215, v194, v195\n\t"
    "v_add_f32 v250, v250, v183\n\t"
    "v_mfma_f32_16x16x32_bf16 v[24:27], a[180:183], v[204:207], v[24:27]\n\t"
    "buffer_load_dwordx4 v227, s[40:43], s54 offen offset:0 nt lds\n\t"
    "v_mfma_f32_16x16x32_bf16 v[24:27], a[212:215], v[208:211], v[24:27]\n\t"
    "v_exp_f32 v196, v196\n\t"
    "v_mfma_f32_16x16x32_bf16 v[28:31], a[184:187], v[204:207], v[28:31]\n\t"
    "v_exp_f32 v197, v197\n\t"
    "v_mfma_f32_16x16x32_bf16 v[28:31], a[216:219], v[208:211], v[28:31]\n\t"
    "s_waitcnt lgkmcnt(0)\n\t"
    "v_cvt_pk_bf16_f32 v216, v196, v197\n\t"
    "v_add_f32 v250, v250, v184\n\t"
    "v_mfma_f32_16x16x32_bf16 v[32:35], a[188:191], v[204:207], v[32:35]\n\t"
    "v_exp_f32 v198, v198\n\t"
    "v_mfma_f32_16x16x32_bf16 v[32:35], a[220:223], v[208:211], v[32:35]\n\t"
    "v_exp_f32 v199, v199\n\t"
    "v_mfma_f32_16x16x32_bf16 v[172:175], a[96:99], a[32:35], 0\n\t"
    "v_cvt_pk_bf16_f32 v217, v198, v199\n\t"
    "v_add_f32 v250, v250, v185\n\t"
    "v_mfma_f32_16x16x32_bf16 v[172:175], a[100:103], a[36:39], v[172:175]\n\t"
    "v_exp_f32 v200, v200\n\t"
    "v_mfma_f32_16x16x32_bf16 v[172:175], a[104:107], a[40:43], v[172:175]\n\t"
    "v_exp_f32 v220, v220\n\t"
    "v_mfma_f32_16x16x32_bf16 v[172:175], a[108:111], a[44:47], v[172:175]\n\t"
    "v_exp_f32 v201, v201\n\t"
    "v_mfma_f32_16x16x32_bf16 v[176:179], a[112:115], a[32:35], 0\n\t"
    "v_exp_f32 v221, v221\n\t"
    "v_mfma_f32_16x16x32_bf16 v[176:179], a[116:119], a[36:39], v[176:179]\n\t"
    "v_add_f32 v250, v250, v186\n\t"
    "v_add_f32 v250, v250, v187\n\t"
    "v_mfma_f32_16x16x32_bf16 v[176:179], a[120:123], a[40:43], v[176:179]\n\t"
    "v_cvt_pk_bf16_f32 v218, v200, v201\n\t"
    "v_cvt_pk_bf16_f32 v224, v220, v221\n\t"
    "v_mfma_f32_16x16x32_bf16 v[176:179], a[124:127], a[44:47], v[176:179]\n\t"
    "v_exp_f32 v202, v202\n\t"
    "v_mfma_f32_16x16x32_bf16 v[180:183], a[128:131], a[32:35], 0\n\t"
    "v_exp_f32 v222, v222\n\t"
    "v_mfma_f32_16x16x32_bf16 v[180:183], a[132:135], a[36:39], v[180:183]\n\t"
    "s_add_u32 m0, s56, 25344\n\t"
    "v_exp_f32 v203, v203\n\t"
    "v_mfma_f32_16x16x32_bf16 v[180:183], a[136:139], a[40:43], v[180:183]\n\t"
    "buffer_load_dwordx4 v227, s[44:47], s54 offen offset:0 lds\n\t"
    "v_mfma_f32_16x16x32_bf16 v[180:183], a[140:143], a[44:47], v[180:183]\n\t"
    "v_exp_f32 v223, v223\n\t"
    "v_mfma_f32_16x16x32_bf16 v[184:187], a[144:147], a[32:35], 0\n\t"
    "v_cvt_pk_bf16_f32 v225, v222, v223\n\t"
    "v_cvt_pk_bf16_f32 v219, v202, v203\n\t"
    "v_mfma_f32_16x16x32_bf16 v[184:187], a[148:151], a[36:39], v[184:187]\n\t"
    "ds_write_b64 v238, v[224:225] offset:8\n\t"
    "v_mfma_f32_16x16x32_bf16 v[184:187], a[152:155], a[40:43], v[184:187]\n\t"
    "v_exp_f32 v172, v172\n\t"
    "v_mfma_f32_16x16x32_bf16 v[184:187], a[156:159], a[44:47], v[184:187]\n\t"
    "v_exp_f32 v173, v173\n\t"
    "v_mfma_f32_16x16x16_bf16 a[244:247], a[248:249], v[224:225], a[244:247]\n\t"
    "v_cvt_pk_bf16_f32 v204, v172, v173\n\t"
    "v_add_f32 v251, v251, v196\n\t"
    "v_mfma_f32_16x16x32_bf16 v[36:39], a[160:163], v[212:215], v[36:39]\n\t"
    "v_exp_f32 v174, v174\n\t"
    "v_mfma_f32_16x16x32_bf16 v[36:39], a[192:195], v[216:219], v[36:39]\n\t"
    "v_exp_f32 v175, v175\n\t"
    "v_mfma_f32_16x16x32_bf16 v[40:43], a[164:167], v[212:215], v[40:43]\n\t"
    "v_cvt_pk_bf16_f32 v205, v174, v175\n\t"
    "v_add_f32 v251, v251, v197\n\t"
    "v_mfma_f32_16x16x32_bf16 v[40:43], a[196:199], v[216:219], v[40:43]\n\t"
    "v_exp_f32 v176, v176\n\t"
    "v_mfma_f32_16x16x32_bf16 v[44:47], a[168:171], v[212:215], v[44:47]\n\t"
    "v_exp_f32 v177, v177\n\t"
    "v_mfma_f32_16x16x32_bf16 v[44:47], a[200:203], v[216:219], v[44:47]\n\t"
    "v_cvt_pk_bf16_f32 v206, v176, v177\n\t"
    "v_add_f32 v251, v251, v198\n\t"
    "v_mfma_f32_16x16x32_bf16 v[48:51], a[172:175], v[212:215], v[48:51]\n\t"
    "v_exp_f32 v178, v178\n\t"
    "v_mfma_f32_16x16x32_bf16 v[48:51], a[204:207], v[216:219], v[48:51]\n\t"
    "v_exp_f32 v179, v179\n\t"
    "v_mfma_f32_16x16x32_bf16 v[52:55], a[176:179], v[212:215], v[52:55]\n\t"
    "s_add_u32 m0, s55, 29568\n\t"
    "v_cvt_pk_bf16_f32 v207, v178, v179\n\t"
    "v_add_f32 v251, v251, v199\n\t"
    "v_mfma_f32_16x16x32_bf16 v[52:55], a[208:211], v[216:219], v[52:55]\n\t"
    "buffer_load_dwordx4 v3, s[40:43], s54 offen offset:0 nt lds\n\t"
    "v_mfma_f32_16x16x32_bf16 v[56:59], a[180:183], v[212:215], v[56:59]\n\t"
    "v_exp_f32 v180, v180\n\t"
    "v_mfma_f32_16x16x32_bf16 v[56:59], a[212:215], v[216:219], v[56:59]\n\t"
    "v_exp_f32 v181, v181\n\t"
    "v_mfma_f32_16x16x32_bf16 v[60:63], a[184:187], v[212:215], v[60:63]\n\t"
    "v_cvt_pk_bf16_f32 v208, v180, v181\n\t"
    "v_add_f32 v251, v251, v200\n\t"
    "v_mfma_f32_16x16x32_bf16 v[60:63], a[216:219], v[216:219], v[60:63]\n\t"
    "v_exp_f32 v182, v182\n\t"
    "v_mfma_f32_16x16x32_bf16 v[64:67], a[188:191], v[212:215], v[64:67]\n\t"
    "v_exp_f32 v183, v183\n\t"
    "v_mfma_f32_16x16x32_bf16 v[64:67], a[220:223], v[216:219], v[64:67]\n\t"
    "v_cvt_pk_bf16_f32 v209, v182, v183\n\t"
    "v_add_f32 v251, v251, v201\n\t"
    "v_mfma_f32_16x16x32_bf16 a[228:231], a[248:251], v[212:215], a[228:231]\n\t"
    "v_exp_f32 v184, v184\n\t"
    "v_mfma_f32_16x16x32_bf16 v[188:191], a[96:99], a[48:51], 0\n\t"
    "v_exp_f32 v185, v185\n\t"
    "v_mfma_f32_16x16x32_bf16 v[188:191], a[100:103], a[52:55], v[188:191]\n\t"
    "v_cvt_pk_bf16_f32 v210, v184, v185\n\t"
    "v_add_f32 v251, v251, v202\n\t"
    "v_mfma_f32_16x16x32_bf16 v[188:191], a[104:107], a[56:59], v[188:191]\n\t"
    "v_exp_f32 v186, v186\n\t"
    "v_mfma_f32_16x16x32_bf16 v[188:191], a[108:111], a[60:63], v[188:191]\n\t"
    "v_add_f32 v251, v251, v203\n\t"
    "v_mfma_f32_16x16x32_bf16 v[192:195], a[112:115], a[48:51], 0\n\t"
    "v_exp_f32 v187, v187\n\t"
    "v_mfma_f32_16x16x32_bf16 v[192:195], a[116:119], a[52:55], v[192:195]\n\t"
    "v_cvt_pk_bf16_f32 v211, v186, v187\n\t"
    "v_mfma_f32_16x16x32_bf16 v[192:195], a[120:123], a[56:59], v[192:195]\n\t"
    "v_add_f32 v252, v252, v180\n\t"
    "v_add_f32 v252, v252, v181\n\t"
    "v_mfma_f32_16x16x32_bf16 v[192:195], a[124:127], a[60:63], v[192:195]\n\t"
    "s_add_u32 m0, s56, 29568\n\t"
    "v_add_f32 v252, v252, v182\n\t"
    "v_add_f32 v252, v252, v183\n\t"
    "v_mfma_f32_16x16x32_bf16 v[196:199], a[128:131], a[48:51], 0\n\t"
    "buffer_load_dwordx4 v3, s[44:47], s54 offen offset:0 lds\n\t"
    "v_mfma_f32_16x16x32_bf16 v[196:199], a[132:135], a[52:55], v[196:199]\n\t"
    "v_add_f32 v252, v252, v184\n\t"
    "v_add_f32 v252, v252, v185\n\t"
    "v_mfma_f32_16x16x32_bf16 v[196:199], a[136:139], a[56:59], v[196:199]\n\t"
    "v_add_f32 v252, v252, v186\n\t"
    "v_add_f32 v252, v252, v187\n\t"
    "v_mfma_f32_16x16x32_bf16 v[196:199], a[140:143], a[60:63], v[196:199]\n\t"
    "v_exp_f32 v188, v188\n\t"
    "v_mfma_f32_16x16x32_bf16 v[200:203], a[144:147], a[48:51], 0\n\t"
    "v_exp_f32 v189, v189\n\t"
    "v_mfma_f32_16x16x32_bf16 v[200:203], a[148:151], a[52:55], v[200:203]\n\t"
    "v_cvt_pk_bf16_f32 v212, v188, v189\n\t"
    "v_mfma_f32_16x16x32_bf16 v[200:203], a[152:155], a[56:59], v[200:203]\n\t"
    "v_exp_f32 v190, v190\n\t"
    "v_mfma_f32_16x16x32_bf16 v[200:203], a[156:159], a[60:63], v[200:203]\n\t"
    "v_exp_f32 v191, v191\n\t"
    "v_mfma_f32_16x16x32_bf16 v[68:71], a[160:163], v[204:207], v[68:71]\n\t"
    "v_cvt_pk_bf16_f32 v213, v190, v191\n\t"
    "v_mfma_f32_16x16x32_bf16 v[68:71], a[192:195], v[208:211], v[68:71]\n\t"
    "v_exp_f32 v192, v192\n\t"
    "v_mfma_f32_16x16x32_bf16 v[72:75], a[164:167], v[204:207], v[72:75]\n\t"
    "v_exp_f32 v193, v193\n\t"
    "v_mfma_f32_16x16x32_bf16 v[72:75], a[196:199], v[208:211], v[72:75]\n\t"
    "v_cvt_pk_bf16_f32 v214, v192, v193\n\t"
    "v_mfma_f32_16x16x32_bf16 v[76:79], a[168:171], v[204:207], v[76:79]\n\t"
    "v_exp_f32 v194, v194\n\t"
    "v_mfma_f32_16x16x32_bf16 v[76:79], a[200:203], v[208:211], v[76:79]\n\t"
    "v_exp_f32 v195, v195\n\t"
    "v_mfma_f32_16x16x32_bf16 v[80:83], a[172:175], v[204:207], v[80:83]\n\t"
    "v_cvt_pk_bf16_f32 v215, v194, v195\n\t"
    "v_mfma_f32_16x16x32_bf16 v[80:83], a[204:207], v[208:211], v[80:83]\n\t"
    "v_exp_f32 v196, v196\n\t"
    "v_mfma_f32_16x16x32_bf16 v[84:87], a[176:179], v[204:207], v[84:87]\n\t"
    "v_exp_f32 v197, v197\n\t"
    "v_mfma_f32_16x16x32_bf16 v[84:87], a[208:211], v[208:211], v[84:87]\n\t"
    "v_cvt_pk_bf16_f32 v216, v196, v197\n\t"
    "v_add_f32 v253, v253, v196\n\t"
    "v_mfma_f32_16x16x32_bf16 v[88:91], a[180:183], v[204:207], v[88:91]\n\t"
    "v_exp_f32 v198, v198\n\t"
    "v_mfma_f32_16x16x32_bf16 v[88:91], a[212:215], v[208:211], v[88:91]\n\t"
    "v_exp_f32 v199, v199\n\t"
    "v_mfma_f32_16x16x32_bf16 v[92:95], a[184:187], v[204:207], v[92:95]\n\t"
    "v_cvt_pk_bf16_f32 v217, v198, v199\n\t"
    "v_add_f32 v253, v253, v197\n\t"
    "v_mfma_f32_16x16x32_bf16 v[92:95], a[216:219], v[208:211], v[92:95]\n\t"
    "v_exp_f32 v200, v200\n\t"
    "v_mfma_f32_16x16x32_bf16 v[96:99], a[188:191], v[204:207], v[96:99]\n\t"
    "v_exp_f32 v201, v201\n\t"
    "v_mfma_f32_16x16x32_bf16 v[96:99], a[220:223], v[208:211], v[96:99]\n\t"
    "s_add_u32 s54, s54, 16384\n\t"
    "v_cvt_pk_bf16_f32 v218, v200, v201\n\t"
    "v_add_f32 v253, v253, v198\n\t"
    "v_mfma_f32_16x16x32_bf16 a[232:235], a[248:251], v[204:207], a[232:235]\n\t"
    "s_waitcnt vmcnt(16) lgkmcnt(0)\n\t"
    "s_barrier\n\t"
    "v_mfma_f32_16x16x32_bf16 v[172:175], a[96:99], a[64:67], 0\n\t"
    "ds_read_b128 a[96:99], v236 offset:16896\n\t"
    "v_add_f32 v253, v253, v199\n\t"
    "v_mfma_f32_16x16x32_bf16 v[172:175], a[100:103], a[68:71], v[172:175]\n\t"
    "v_exp_f32 v202, v202\n\t"
    "v_mfma_f32_16x16x32_bf16 v[172:175], a[104:107], a[72:75], v[172:175]\n\t"
    "ds_read_b128 a[100:103], v236 offset:16960\n\t"
    "ds_read_b128 a[104:107], v236 offset:17024\n\t"
    "v_mfma_f32_16x16x32_bf16 v[172:175], a[108:111], a[76:79], v[172:175]\n\t"
    "s_and_b32 s54, s54, 0x7ffff\n\t"
    "v_exp_f32 v203, v203\n\t"
    "v_mfma_f32_16x16x32_bf16 v[176:179], a[112:115], a[64:67], 0\n\t"
    "ds_read_b128 a[108:111], v236 offset:17088\n\t"
    "ds_read_b128 a[112:115], v236 offset:17408\n\t"
    "v_mfma_f32_16x16x32_bf16 v[176:179], a[116:119], a[68:71], v[176:179]\n\t"
    "v_cvt_pk_bf16_f32 v219, v202, v203\n\t"
    "v_add_f32 v253, v253, v200\n\t"
    "v_mfma_f32_16x16x32_bf16 v[176:179], a[120:123], a[72:75], v[176:179]\n\t"
    "ds_read_b128 a[116:119], v236 offset:17472\n\t"
    "ds_read_b128 a[120:123], v236 offset:17536\n\t"
    "v_mfma_f32_16x16x32_bf16 v[176:179], a[124:127], a[76:79], v[176:179]\n\t"
    "v_add_f32 v253, v253, v201\n\t"
    "v_add_f32 v253, v253, v202\n\t"
    "v_mfma_f32_16x16x32_bf16 v[180:183], a[128:131], a[64:67], 0\n\t"
    "ds_read_b128 a[124:127], v236 offset:17600\n\t"
    "v_add_f32 v253, v253, v203\n\t"
    "v_mfma_f32_16x16x32_bf16 v[180:183], a[132:135], a[68:71], v[180:183]\n\t"
    "ds_read_b128 a[128:131], v236 offset:25344\n\t"
    "v_mfma_f32_16x16x32_bf16 v[180:183], a[136:139], a[72:75], v[180:183]\n\t"
    "ds_read_b128 a[132:135], v236 offset:25408\n\t"
    "v_mfma_f32_16x16x32_bf16 v[180:183], a[140:143], a[76:79], v[180:183]\n\t"
    "s_add_u32 s55, s55, s57\n\t"
    "v_exp_f32 v172, v172\n\t"
    "v_mfma_f32_16x16x32_bf16 v[184:187], a[144:147], a[64:67], 0\n\t"
    "ds_read_b128 a[136:139], v236 offset:25472\n\t"
    "ds_read_b128 a[140:143], v236 offset:25536\n\t"
    "v_mfma_f32_16x16x32_bf16 v[184:187], a[148:151], a[68:71], v[184:187]\n\t"
    "s_add_u32 s56, s56, s57\n\t"
    "v_exp_f32 v173, v173\n\t"
    "v_mfma_f32_16x16x32_bf16 v[184:187], a[152:155], a[72:75], v[184:187]\n\t"
    "ds_read_b128 a[144:147], v236 offset:25856\n\t"
    "ds_read_b128 a[148:151], v236 offset:25920\n\t"
    "v_mfma_f32_16x16x32_bf16 v[184:187], a[156:159], a[76:79], v[184:187]\n\t"
    "s_sub_u32 s57, 0, s57\n\t"
    "v_cvt_pk_bf16_f32 v204, v172, v173\n\t"
    "v_add_f32 v254, v254, v172\n\t"
    "v_mfma_f32_16x16x32_bf16 v[100:103], a[160:163], v[212:215], v[100:103]\n\t"
    "ds_read_b128 a[152:155], v236 offset:25984\n\t"
    "ds_read_b128 a[156:159], v236 offset:26048\n\t"
    "v_mfma_f32_16x16x32_bf16 v[100:103], a[192:195], v[216:219], v[100:103]\n\t"
    "v_exp_f32 v174, v174\n\t"
    "v_mfma_f32_16x16x32_bf16 v[104:107], a[164:167], v[212:215], v[104:107]\n\t"
    "ds_read_b128 v[228:231], v238 offset:0\n\t"
    "ds_read_b128 v[232:235], v238 offset:1024\n\t"
    "v_mfma_f32_16x16x32_bf16 v[104:107], a[196:199], v[216:219], v[104:107]\n\t"
    "v_exp_f32 v175, v175\n\t"
    "v_mfma_f32_16x16x32_bf16 v[108:111], a[168:171], v[212:215], v[108:111]\n\t"
    "v_cvt_pk_bf16_f32 v205, v174, v175\n\t"
    "v_add_f32 v254, v254, v173\n\t"
    "v_mfma_f32_16x16x32_bf16 v[108:111], a[200:203], v[216:219], v[108:111]\n\t"
    "v_exp_f32 v176, v176\n\t"
    "v_mfma_f32_16x16x32_bf16 v[112:115], a[172:175], v[212:215], v[112:115]\n\t"
    "v_exp_f32 v177, v177\n\t"
    "v_mfma_f32_16x16x32_bf16 v[112:115], a[204:207], v[216:219], v[112:115]\n\t"
    "v_cvt_pk_bf16_f32 v206, v176, v177\n\t"
    "v_add_f32 v254, v254, v174\n\t"
    "v_mfma_f32_16x16x32_bf16 v[116:119], a[176:179], v[212:215], v[116:119]\n\t"
    "v_exp_f32 v178, v178\n\t"
    "v_mfma_f32_16x16x32_bf16 v[116:119], a[208:211], v[216:219], v[116:119]\n\t"
    "v_exp_f32 v179, v179\n\t"
    "v_mfma_f32_16x16x32_bf16 v[120:123], a[180:183], v[212:215], v[120:123]\n\t"
    "v_cvt_pk_bf16_f32 v207, v178, v179\n\t"
    "v_add_f32 v254, v254, v175\n\t"
    "v_mfma_f32_16x16x32_bf16 v[120:123], a[212:215], v[216:219], v[120:123]\n\t"
    "v_exp_f32 v180, v180\n\t"
    "v_mfma_f32_16x16x32_bf16 v[124:127], a[184:187], v[212:215], v[124:127]\n\t"
    "v_exp_f32 v181, v181\n\t"
    "v_mfma_f32_16x16x32_bf16 v[124:127], a[216:219], v[216:219], v[124:127]\n\t"
    "v_cvt_pk_bf16_f32 v208, v180, v181\n\t"
    "v_add_f32 v254, v254, v176\n\t"
    "v_mfma_f32_16x16x32_bf16 v[128:131], a[188:191], v[212:215], v[128:131]\n\t"
    "v_exp_f32 v182, v182\n\t"
    "v_mfma_f32_16x16x32_bf16 v[128:131], a[220:223], v[216:219], v[128:131]\n\t"
    "v_exp_f32 v183, v183\n\t"
    "v_mfma_f32_16x16x32_bf16 a[236:239], a[248:251], v[212:215], a[236:239]\n\t"
    "s_waitcnt lgkmcnt(0)\n\t"
    "v_cvt_pk_bf16_f32 v209, v182, v183\n\t"
    "v_add_f32 v254, v254, v177\n\t"
    "s_sub_u32 s60, s60, 1\n\t"
    "s_cbranch_scc0 LOOP1_%=\n\t"
    "TAIL1_%=:\n\t"
    "v_mfma_f32_16x16x32_bf16 v[188:191], a[96:99], a[0:3], 0\n\t"
    "v_exp_f32 v184, v184\n\t"
    "v_mfma_f32_16x16x32_bf16 v[188:191], a[100:103], a[4:7], v[188:191]\n\t"
    "v_exp_f32 v185, v185\n\t"
    "v_mfma_f32_16x16x32_bf16 v[188:191], a[104:107], a[8:11], v[188:191]\n\t"
    "v_mfma_f32_16x16x32_bf16 v[188:191], a[108:111], a[12:15], v[188:191]\n\t"
    "v_cvt_pk_bf16_f32 v210, v184, v185\n\t"
    "v_add_f32 v254, v254, v178\n\t"
    "v_mfma_f32_16x16x32_bf16 v[192:195], a[112:115], a[0:3], 0\n\t"
    "v_exp_f32 v186, v186\n\t"
    "v_mfma_f32_16x16x32_bf16 v[192:195], a[116:119], a[4:7], v[192:195]\n\t"
    "v_exp_f32 v187, v187\n\t"
    "v_mfma_f32_16x16x32_bf16 v[192:195], a[120:123], a[8:11], v[192:195]\n\t"
    "v_cvt_pk_bf16_f32 v211, v186, v187\n\t"
    "v_add_f32 v254, v254, v179\n\t"
    "v_mfma_f32_16x16x32_bf16 v[192:195], a[124:127], a[12:15], v[192:195]\n\t"
    "v_add_f32 v254, v254, v180\n\t"
    "v_add_f32 v254, v254, v181\n\t"
    "v_mfma_f32_16x16x32_bf16 v[196:199], a[128:131], a[0:3], 0\n\t"
    "v_add_f32 v254, v254, v182\n\t"
    "v_add_f32 v254, v254, v183\n\t"
    "v_mfma_f32_16x16x32_bf16 v[196:199], a[132:135], a[4:7], v[196:199]\n\t"
    "v_add_f32 v254, v254, v184\n\t"
    "v_add_f32 v254, v254, v185\n\t"
    "v_mfma_f32_16x16x32_bf16 v[196:199], a[136:139], a[8:11], v[196:199]\n\t"
    "v_add_f32 v254, v254, v186\n\t"
    "v_add_f32 v254, v254, v187\n\t"
    "v_mfma_f32_16x16x32_bf16 v[196:199], a[140:143], a[12:15], v[196:199]\n\t"
    "v_exp_f32 v188, v188\n\t"
    "v_mfma_f32_16x16x32_bf16 v[200:203], a[144:147], a[0:3], 0\n\t"
    "v_exp_f32 v189, v189\n\t"
    "v_mfma_f32_16x16x32_bf16 v[200:203], a[148:151], a[4:7], v[200:203]\n\t"
    "v_cvt_pk_bf16_f32 v212, v188, v189\n\t"
    "v_add_f32 v250, v250, v188\n\t"
    "v_mfma_f32_16x16x32_bf16 v[200:203], a[152:155], a[8:11], v[200:203]\n\t"
    "v_exp_f32 v190, v190\n\t"
    "v_mfma_f32_16x16x32_bf16 v[200:203], a[156:159], a[12:15], v[200:203]\n\t"
    "v_exp_f32 v191, v191\n\t"
    "v_mfma_f32_16x16x32_bf16 v[164:167], a[168:171], v[228:231], v[164:167]\n\t"
    "v_cvt_pk_bf16_f32 v213, v190, v191\n\t"
    "v_add_f32 v250, v250, v189\n\t"
    "v_mfma_f32_16x16x32_bf16 v[164:167], a[200:203], v[232:235], v[164:167]\n\t"
    "v_exp_f32 v192, v192\n\t"
    "v_mfma_f32_16x16x32_bf16 v[168:171], a[172:175], v[228:231], v[168:171]\n\t"
    "v_mfma_f32_16x16x32_bf16 v[168:171], a[204:207], v[232:235], v[168:171]\n\t"
    "v_exp_f32 v193, v193\n\t"
    "v_mfma_f32_16x16x32_bf16 v[132:135], a[160:163], v[204:207], v[132:135]\n\t"
    "ds_read_b64_tr_b16 a[160:161], v237 offset:16896\n\t"
    "ds_read_b64_tr_b16 a[162:163], v237 offset:17408\n\t"
    "v_accvgpr_write_b32 a0, 0\n\t"
    "v_mfma_f32_16x16x32_bf16 v[132:135], a[192:195], v[208:211], v[132:135]\n\t"
    "v_cvt_pk_bf16_f32 v214, v192, v193\n\t"
    "v_add_f32 v250, v250, v190\n\t"
    "v_accvgpr_write_b32 a1, 0\n\t"
    "v_mfma_f32_16x16x32_bf16 v[136:139], a[164:167], v[204:207], v[136:139]\n\t"
    "ds_read_b64_tr_b16 a[192:193], v237 offset:25344\n\t"
    "ds_read_b64_tr_b16 a[194:195], v237 offset:25856\n\t"
    "v_accvgpr_write_b32 a2, 0\n\t"
    "v_mfma_f32_16x16x32_bf16 v[136:139], a[196:199], v[208:211], v[136:139]\n\t"
    "v_exp_f32 v194, v194\n\t"
    "v_accvgpr_write_b32 a3, 0\n\t"
    "v_mfma_f32_16x16x32_bf16 v[140:143], a[168:171], v[204:207], v[140:143]\n\t"
    "ds_read_b64_tr_b16 a[164:165], v237 offset:16904\n\t"
    "ds_read_b64_tr_b16 a[166:167], v237 offset:17416\n\t"
    "v_accvgpr_write_b32 a4, 0\n\t"
    "v_mfma_f32_16x16x32_bf16 v[140:143], a[200:203], v[208:211], v[140:143]\n\t"
    "v_exp_f32 v195, v195\n\t"
    "v_mfma_f32_16x16x32_bf16 v[144:147], a[172:175], v[204:207], v[144:147]\n\t"
    "ds_read_b64_tr_b16 a[196:197], v237 offset:25352\n\t"
    "ds_read_b64_tr_b16 a[198:199], v237 offset:25864\n\t"
    "v_mfma_f32_16x16x32_bf16 v[144:147], a[204:207], v[208:211], v[144:147]\n\t"
    "v_cvt_pk_bf16_f32 v215, v194, v195\n\t"
    "v_add_f32 v250, v250, v191\n\t"
    "v_mfma_f32_16x16x32_bf16 v[148:151], a[176:179], v[204:207], v[148:151]\n\t"
    "ds_read_b64_tr_b16 a[168:169], v237 offset:16960\n\t"
    "ds_read_b64_tr_b16 a[170:171], v237 offset:17472\n\t"
    "v_mfma_f32_16x16x32_bf16 v[148:151], a[208:211], v[208:211], v[148:151]\n\t"
    "v_exp_f32 v196, v196\n\t"
    "v_mfma_f32_16x16x32_bf16 v[152:155], a[180:183], v[204:207], v[152:155]\n\t"
    "ds_read_b64_tr_b16 a[200:201], v237 offset:25408\n\t"
    "ds_read_b64_tr_b16 a[202:203], v237 offset:25920\n\t"
    "v_mfma_f32_16x16x32_bf16 v[152:155], a[212:215], v[208:211], v[152:155]\n\t"
    "v_exp_f32 v197, v197\n\t"
    "v_mfma_f32_16x16x32_bf16 v[156:159], a[184:187], v[204:207], v[156:159]\n\t"
    "ds_read_b64_tr_b16 a[172:173], v237 offset:16968\n\t"
    "ds_read_b64_tr_b16 a[174:175], v237 offset:17480\n\t"
    "v_mfma_f32_16x16x32_bf16 v[156:159], a[216:219], v[208:211], v[156:159]\n\t"
    "v_cvt_pk_bf16_f32 v216, v196, v197\n\t"
    "v_add_f32 v250, v250, v192\n\t"
    "v_mfma_f32_16x16x32_bf16 v[160:163], a[188:191], v[204:207], v[160:163]\n\t"
    "v_mfma_f32_16x16x32_bf16 v[160:163], a[220:223], v[208:211], v[160:163]\n\t"
    "v_exp_f32 v198, v198\n\t"
    "v_mfma_f32_16x16x32_bf16 v[172:175], a[96:99], a[16:19], 0\n\t"
    "ds_read_b64_tr_b16 a[204:205], v237 offset:25416\n\t"
    "ds_read_b64_tr_b16 a[206:207], v237 offset:25928\n\t"
    "v_mfma_f32_16x16x32_bf16 v[172:175], a[100:103], a[20:23], v[172:175]\n\t"
    "v_exp_f32 v199, v199\n\t"
    "v_mfma_f32_16x16x32_bf16 v[172:175], a[104:107], a[24:27], v[172:175]\n\t"
    "ds_read_b64_tr_b16 a[176:177], v237 offset:17024\n\t"
    "ds_read_b64_tr_b16 a[178:179], v237 offset:17536\n\t"
    "v_mfma_f32_16x16x32_bf16 v[172:175], a[108:111], a[28:31], v[172:175]\n\t"
    "v_cvt_pk_bf16_f32 v217, v198, v199\n\t"
    "v_add_f32 v250, v250, v193\n\t"
    "v_mfma_f32_16x16x32_bf16 v[176:179], a[112:115], a[16:19], 0\n\t"
    "ds_read_b64_tr_b16 a[208:209], v237 offset:25472\n\t"
    "ds_read_b64_tr_b16 a[210:211], v237 offset:25984\n\t"
    "v_mfma_f32_16x16x32_bf16 v[176:179], a[116:119], a[20:23], v[176:179]\n\t"
    "v_exp_f32 v200, v200\n\t"
    "v_mfma_f32_16x16x32_bf16 v[176:179], a[120:123], a[24:27], v[176:179]\n\t"
    "ds_read_b64_tr_b16 a[180:181], v237 offset:17032\n\t"
    "ds_read_b64_tr_b16 a[182:183], v237 offset:17544\n\t"
    "v_mfma_f32_16x16x32_bf16 v[176:179], a[124:127], a[28:31], v[176:179]\n\t"
    "v_exp_f32 v201, v201\n\t"
    "v_mfma_f32_16x16x32_bf16 v[180:183], a[128:131], a[16:19], 0\n\t"
    "ds_read_b64_tr_b16 a[212:213], v237 offset:25480\n\t"
    "ds_read_b64_tr_b16 a[214:215], v237 offset:25992\n\t"
    "v_mfma_f32_16x16x32_bf16 v[180:183], a[132:135], a[20:23], v[180:183]\n\t"
    "v_cvt_pk_bf16_f32 v218, v200, v201\n\t"
    "v_add_f32 v250, v250, v194\n\t"
    "v_mfma_f32_16x16x32_bf16 v[180:183], a[136:139], a[24:27], v[180:183]\n\t"
    "ds_read_b64_tr_b16 a[184:185], v237 offset:17088\n\t"
    "ds_read_b64_tr_b16 a[186:187], v237 offset:17600\n\t"
    "v_mfma_f32_16x16x32_bf16 v[180:183], a[140:143], a[28:31], v[180:183]\n\t"
    "v_exp_f32 v202, v202\n\t"
    "v_mfma_f32_16x16x32_bf16 v[184:187], a[144:147], a[16:19], 0\n\t"
    "ds_read_b64_tr_b16 a[216:217], v237 offset:25536\n\t"
    "ds_read_b64_tr_b16 a[218:219], v237 offset:26048\n\t"
    "v_mfma_f32_16x16x32_bf16 v[184:187], a[148:151], a[20:23], v[184:187]\n\t"
    "v_exp_f32 v203, v203\n\t"
    "v_mfma_f32_16x16x32_bf16 v[184:187], a[152:155], a[24:27], v[184:187]\n\t"
    "ds_read_b64_tr_b16 a[188:189], v237 offset:17096\n\t"
    "ds_read_b64_tr_b16 a[190:191], v237 offset:17608\n\t"
    "v_mfma_f32_16x16x32_bf16 v[184:187], a[156:159], a[28:31], v[184:187]\n\t"
    "v_cvt_pk_bf16_f32 v219, v202, v203\n\t"
    "v_add_f32 v250, v250, v195\n\t"
    "v_mfma_f32_16x16x32_bf16 v[220:223], a[112:115], a[80:83], 0\n\t"
    "ds_read_b64_tr_b16 a[220:221], v237 offset:25544\n\t"
    "ds_read_b64_tr_b16 a[222:223], v237 offset:26056\n\t"
    "v_mfma_f32_16x16x32_bf16 v[220:223], a[116:119], a[84:87], v[220:223]\n\t"
    "v_exp_f32 v172, v172\n\t"
    "v_mfma_f32_16x16x32_bf16 v[220:223], a[120:123], a[88:91], v[220:223]\n\t"
    "v_mfma_f32_16x16x32_bf16 v[220:223], a[124:127], a[92:95], v[220:223]\n\t"
    "s_waitcnt lgkmcnt(4)\n\t"
    "v_exp_f32 v173, v173\n\t"
    "v_mfma_f32_16x16x32_bf16 v[4:7], a[160:163], v[212:215], v[4:7]\n\t"
    "v_cvt_pk_bf16_f32 v204, v172, v173\n\t"
    "v_add_f32 v250, v250, v196\n\t"
    "v_mfma_f32_16x16x32_bf16 v[4:7], a[192:195], v[216:219], v[4:7]\n\t"
    "v_exp_f32 v174, v174\n\t"
    "v_mfma_f32_16x16x32_bf16 v[8:11], a[164:167], v[212:215], v[8:11]\n\t"
    "v_exp_f32 v175, v175\n\t"
    "v_mfma_f32_16x16x32_bf16 v[8:11], a[196:199], v[216:219], v[8:11]\n\t"
    "v_cvt_pk_bf16_f32 v205, v174, v175\n\t"
    "v_add_f32 v250, v250, v197\n\t"
    "v_mfma_f32_16x16x32_bf16 v[12:15], a[168:171], v[212:215], v[12:15]\n\t"
    "v_exp_f32 v176, v176\n\t"
    "v_mfma_f32_16x16x32_bf16 v[12:15], a[200:203], v[216:219], v[12:15]\n\t"
    "v_exp_f32 v177, v177\n\t"
    "v_mfma_f32_16x16x32_bf16 v[16:19], a[172:175], v[212:215], v[16:19]\n\t"
    "v_cvt_pk_bf16_f32 v206, v176, v177\n\t"
    "v_add_f32 v250, v250, v198\n\t"
    "v_mfma_f32_16x16x32_bf16 v[16:19], a[204:207], v[216:219], v[16:19]\n\t"
    "v_exp_f32 v178, v178\n\t"
    "v_mfma_f32_16x16x32_bf16 v[20:23], a[176:179], v[212:215], v[20:23]\n\t"
    "v_exp_f32 v179, v179\n\t"
    "v_mfma_f32_16x16x32_bf16 v[20:23], a[208:211], v[216:219], v[20:23]\n\t"
    "v_cvt_pk_bf16_f32 v207, v178, v179\n\t"
    "v_add_f32 v250, v250, v199\n\t"
    "v_mfma_f32_16x16x32_bf16 v[24:27], a[180:183], v[212:215], v[24:27]\n\t"
    "v_mfma_f32_16x16x32_bf16 v[24:27], a[212:215], v[216:219], v[24:27]\n\t"
    "v_exp_f32 v180, v180\n\t"
    "v_mfma_f32_16x16x32_bf16 v[28:31], a[184:187], v[212:215], v[28:31]\n\t"
    "v_exp_f32 v181, v181\n\t"
    "v_mfma_f32_16x16x32_bf16 v[28:31], a[216:219], v[216:219], v[28:31]\n\t"
    "s_waitcnt lgkmcnt(0)\n\t"
    "v_cvt_pk_bf16_f32 v208, v180, v181\n\t"
    "v_add_f32 v250, v250, v200\n\t"
    "v_mfma_f32_16x16x32_bf16 v[32:35], a[188:191], v[212:215], v[32:35]\n\t"
    "v_exp_f32 v182, v182\n\t"
    "v_mfma_f32_16x16x32_bf16 v[32:35], a[220:223], v[216:219], v[32:35]\n\t"
    "v_exp_f32 v183, v183\n\t"
    "v_mfma_f32_16x16x32_bf16 v[188:191], a[96:99], a[32:35], 0\n\t"
    "v_cvt_pk_bf16_f32 v209, v182, v183\n\t"
    "v_add_f32 v250, v250, v201\n\t"
    "v_mfma_f32_16x16x32_bf16 v[188:191], a[100:103], a[36:39], v[188:191]\n\t"
    "v_exp_f32 v184, v184\n\t"
    "v_mfma_f32_16x16x32_bf16 v[188:191], a[104:107], a[40:43], v[188:191]\n\t"
    "v_exp_f32 v220, v220\n\t"
    "v_mfma_f32_16x16x32_bf16 v[188:191], a[108:111], a[44:47], v[188:191]\n\t"
    "v_exp_f32 v185, v185\n\t"
    "v_mfma_f32_16x16x32_bf16 v[192:195], a[112:115], a[32:35], 0\n\t"
    "v_exp_f32 v221, v221\n\t"
    "v_mfma_f32_16x16x32_bf16 v[192:195], a[116:119], a[36:39], v[192:195]\n\t"
    "v_add_f32 v250, v250, v202\n\t"
    "v_add_f32 v250, v250, v203\n\t"
    "v_mfma_f32_16x16x32_bf16 v[192:195], a[120:123], a[40:43], v[192:195]\n\t"
    "v_cvt_pk_bf16_f32 v210, v184, v185\n\t"
    "v_cvt_pk_bf16_f32 v224, v220, v221\n\t"
    "v_mfma_f32_16x16x4_f32 a[224:227], a252, v250, 0\n\t"
    "v_mfma_f32_16x16x32_bf16 v[192:195], a[124:127], a[44:47], v[192:195]\n\t"
    "v_exp_f32 v186, v186\n\t"
    "v_mfma_f32_16x16x32_bf16 v[196:199], a[128:131], a[32:35], 0\n\t"
    "v_exp_f32 v222, v222\n\t"
    "v_mfma_f32_16x16x32_bf16 v[196:199], a[132:135], a[36:39], v[196:199]\n\t"
    "v_exp_f32 v187, v187\n\t"
    "v_mfma_f32_16x16x32_bf16 v[196:199], a[136:139], a[40:43], v[196:199]\n\t"
    "s_nop 2\n\t"
    "v_accvgpr_read_b32 v242, a224\n\t"
    "v_subrev_u32 v246, 0x2b800000, v242\n\t"
    "v_cmp_lt_u32 vcc, 0x28000000, v246\n\t"
    "s_nop 1\n\t"
    "s_or_b64 s[62:63], s[62:63], vcc\n\t"
    "v_rcp_f32 v243, v242\n\t"
    "s_mov_b32 s64, 20480\n\t"
    "v_mul_f32 v4, v4, v243\n\t"
    "v_mfma_f32_16x16x32_bf16 v[196:199], a[140:143], a[44:47], v[196:199]\n\t"
    "v_exp_f32 v223, v223\n\t"
    "v_mul_f32 v5, v5, v243\n\t"
    "v_mul_f32 v6, v6, v243\n\t"
    "v_mul_f32 v7, v7, v243\n\t"
    "v_mul_f32 v8, v8, v243\n\t"
    "v_mfma_f32_16x16x32_bf16 v[200:203], a[144:147], a[32:35], 0\n\t"
    "v_cvt_pk_bf16_f32 v225, v222, v223\n\t"
    "v_cvt_pk_bf16_f32 v211, v186, v187\n\t"
    "v_mul_f32 v9, v9, v243\n\t"
    "v_mul_f32 v10, v10, v243\n\t"
    "v_mul_f32 v11, v11, v243\n\t"
    "v_cvt_pk_bf16_f32 v4, v4, v5\n\t"
    "v_mfma_f32_16x16x32_bf16 v[200:203], a[148:151], a[36:39], v[200:203]\n\t"
    "ds_write_b64 v238, v[224:225] offset:2056\n\t"
    "v_cvt_pk_bf16_f32 v5, v6, v7\n\t"
    "v_cvt_pk_bf16_f32 v6, v8, v9\n\t"
    "v_cvt_pk_bf16_f32 v7, v10, v11\n\t"
    "s_nop 1\n\t"
    "v_mfma_f32_16x16x32_bf16 a[4:7], a[0:3], v[4:7], a[4:7]\n\t"
    "v_mfma_f32_16x16x32_bf16 v[200:203], a[152:155], a[40:43], v[200:203]\n\t"
    "v_exp_f32 v188, v188\n\t"
    "v_mul_f32 v12, v12, v243\n\t"
    "v_mul_f32 v13, v13, v243\n\t"
    "v_mul_f32 v14, v14, v243\n\t"
    "v_mul_f32 v15, v15, v243\n\t"
    "v_mfma_f32_16x16x32_bf16 v[200:203], a[156:159], a[44:47], v[200:203]\n\t"
    "buffer_store_dwordx4 v[4:7], v241, s[48:51], s64 offen offset:0 sc0 sc1\n\t"
    "v_exp_f32 v189, v189\n\t"
    "v_mul_f32 v16, v16, v243\n\t"
    "v_mul_f32 v17, v17, v243\n\t"
    "v_mul_f32 v18, v18, v243\n\t"
    "v_mul_f32 v19, v19, v243\n\t"
    "v_mfma_f32_16x16x16_bf16 a[244:247], a[248:249], v[224:225], a[244:247]\n\t"
    "v_cvt_pk_bf16_f32 v212, v188, v189\n\t"
    "v_add_f32 v251, v251, v180\n\t"
    "v_cvt_pk_bf16_f32 v12, v12, v13\n\t"
    "v_cvt_pk_bf16_f32 v13, v14, v15\n\t"
    "v_cvt_pk_bf16_f32 v14, v16, v17\n\t"
    "v_cvt_pk_bf16_f32 v15, v18, v19\n\t"
    "v_mfma_f32_16x16x32_bf16 v[36:39], a[160:163], v[204:207], v[36:39]\n\t"
    "v_exp_f32 v190, v190\n\t"
    "v_mfma_f32_16x16x32_bf16 a[4:7], a[0:3], v[12:15], a[4:7]\n\t"
    "v_mul_f32 v20, v20, v243\n\t"
    "v_mul_f32 v21, v21, v243\n\t"
    "v_mul_f32 v22, v22, v243\n\t"
    "v_mfma_f32_16x16x32_bf16 v[36:39], a[192:195], v[208:211], v[36:39]\n\t"
    "v_exp_f32 v191, v191\n\t"
    "v_mul_f32 v23, v23, v243\n\t"
    "v_mul_f32 v24, v24, v243\n\t"
    "v_mul_f32 v25, v25, v243\n\t"
    "v_mul_f32 v26, v26, v243\n\t"
    "v_mfma_f32_16x16x32_bf16 v[40:43], a[164:167], v[204:207], v[40:43]\n\t"
    "buffer_store_dwordx4 v[12:15], v241, s[48:51], s64 offen offset:64 sc0 sc1\n\t"
    "v_cvt_pk_bf16_f32 v213, v190, v191\n\t"
    "v_add_f32 v251, v251, v181\n\t"
    "v_mul_f32 v27, v27, v243\n\t"
    "v_cvt_pk_bf16_f32 v20, v20, v21\n\t"
    "v_cvt_pk_bf16_f32 v21, v22, v23\n\t"
    "v_mfma_f32_16x16x32_bf16 v[40:43], a[196:199], v[208:211], v[40:43]\n\t"
    "v_exp_f32 v192, v192\n\t"
    "v_cvt_pk_bf16_f32 v22, v24, v25\n\t"
    "v_cvt_pk_bf16_f32 v23, v26, v27\n\t"
    "s_nop 1\n\t"
    "v_mfma_f32_16x16x32_bf16 a[4:7], a[0:3], v[20:23], a[4:7]\n\t"
    "v_mul_f32 v28, v28, v243\n\t"
    "v_mfma_f32_16x16x32_bf16 v[44:47], a[168:171], v[204:207], v[44:47]\n\t"
    "v_exp_f32 v193, v193\n\t"
    "v_mul_f32 v29, v29, v243\n\t"
    "v_mul_f32 v30, v30, v243\n\t"
    "v_mul_f32 v31, v31, v243\n\t"
    "v_mul_f32 v32, v32, v243\n\t"
    "v_mfma_f32_16x16x32_bf16 v[44:47], a[200:203], v[208:211], v[44:47]\n\t"
    "buffer_store_dwordx4 v[20:23], v241, s[48:51], s64 offen offset:128 sc0 sc1\n\t"
    "v_cvt_pk_bf16_f32 v214, v192, v193\n\t"
    "v_add_f32 v251, v251, v182\n\t"
    "v_mul_f32 v33, v33, v243\n\t"
    "v_mul_f32 v34, v34, v243\n\t"
    "v_mul_f32 v35, v35, v243\n\t"
    "v_mfma_f32_16x16x32_bf16 v[48:51], a[172:175], v[204:207], v[48:51]\n\t"
    "v_exp_f32 v194, v194\n\t"
    "v_cvt_pk_bf16_f32 v28, v28, v29\n\t"
    "v_cvt_pk_bf16_f32 v29, v30, v31\n\t"
    "v_cvt_pk_bf16_f32 v30, v32, v33\n\t"
    "v_cvt_pk_bf16_f32 v31, v34, v35\n\t"
    "v_mfma_f32_16x16x32_bf16 v[48:51], a[204:207], v[208:211], v[48:51]\n\t"
    "v_exp_f32 v195, v195\n\t"
    "v_mfma_f32_16x16x32_bf16 a[4:7], a[0:3], v[28:31], a[4:7]\n\t"
    "v_mfma_f32_16x16x32_bf16 v[52:55], a[176:179], v[204:207], v[52:55]\n\t"
    "v_cvt_pk_bf16_f32 v215, v194, v195\n\t"
    "v_add_f32 v251, v251, v183\n\t"
    "v_and_b32 v220, 15, v1\n\t"
    "v_mfma_f32_16x16x32_bf16 v[52:55], a[208:211], v[208:211], v[52:55]\n\t"
    "buffer_store_dwordx4 v[28:31], v241, s[48:51], s64 offen offset:192 sc0 sc1\n\t"
    "v_lshlrev_b32 v220, 2, v220\n\t"
    "v_mfma_f32_16x16x32_bf16 v[56:59], a[180:183], v[204:207], v[56:59]\n\t"
    "s_add_u32 s61, s53, 139264\n\t"
    "v_exp_f32 v196, v196\n\t"
    "v_add_u32 v220, s61, v220\n\t"
    "v_mfma_f32_16x16x32_bf16 v[56:59], a[212:215], v[208:211], v[56:59]\n\t"
    "v_exp_f32 v197, v197\n\t"
    "v_accvgpr_read_b32 v221, a244\n\t"
    "v_mfma_f32_16x16x32_bf16 v[60:63], a[184:187], v[204:207], v[60:63]\n\t"
    "ds_write_b32 v220, v221 offset:64\n\t"
    "v_cvt_pk_bf16_f32 v216, v196, v197\n\t"
    "v_add_f32 v251, v251, v184\n\t"
    "v_mfma_f32_16x16x32_bf16 v[60:63], a[216:219], v[208:211], v[60:63]\n\t"
    "v_exp_f32 v198, v198\n\t"
    "v_mfma_f32_16x16x32_bf16 v[64:67], a[188:191], v[204:207], v[64:67]\n\t"
    "v_exp_f32 v199, v199\n\t"
    "v_mfma_f32_16x16x32_bf16 v[64:67], a[220:223], v[208:211], v[64:67]\n\t"
    "v_cvt_pk_bf16_f32 v217, v198, v199\n\t"
    "v_add_f32 v251, v251, v185\n\t"
    "v_mfma_f32_16x16x32_bf16 a[228:231], a[248:251], v[204:207], a[228:231]\n\t"
    "v_exp_f32 v200, v200\n\t"
    "v_mfma_f32_16x16x32_bf16 v[172:175], a[96:99], a[48:51], 0\n\t"
    "v_exp_f32 v201, v201\n\t"
    "v_mfma_f32_16x16x32_bf16 v[172:175], a[100:103], a[52:55], v[172:175]\n\t"
    "v_cvt_pk_bf16_f32 v218, v200, v201\n\t"
    "v_add_f32 v251, v251, v186\n\t"
    "v_mfma_f32_16x16x32_bf16 v[172:175], a[104:107], a[56:59], v[172:175]\n\t"
    "v_exp_f32 v202, v202\n\t"
    "v_mfma_f32_16x16x32_bf16 v[172:175], a[108:111], a[60:63], v[172:175]\n\t"
    "v_add_f32 v251, v251, v187\n\t"
    "v_mfma_f32_16x16x32_bf16 v[176:179], a[112:115], a[48:51], 0\n\t"
    "v_exp_f32 v203, v203\n\t"
    "v_mfma_f32_16x16x32_bf16 v[176:179], a[116:119], a[52:55], v[176:179]\n\t"
    "v_cvt_pk_bf16_f32 v219, v202, v203\n\t"
    "v_mfma_f32_16x16x32_bf16 v[176:179], a[120:123], a[56:59], v[176:179]\n\t"
    "v_add_f32 v252, v252, v196\n\t"
    "v_add_f32 v252, v252, v197\n\t"
    "v_mfma_f32_16x16x4_f32 a[228:231], a252, v251, a[228:231]\n\t"
    "v_mfma_f32_16x16x32_bf16 v[176:179], a[124:127], a[60:63], v[176:179]\n\t"
    "v_add_f32 v252, v252, v198\n\t"
    "v_add_f32 v252, v252, v199\n\t"
    "v_mfma_f32_16x16x32_bf16 v[180:183], a[128:131], a[48:51], 0\n\t"
    "v_mfma_f32_16x16x32_bf16 v[180:183], a[132:135], a[52:55], v[180:183]\n\t"
    "v_add_f32 v252, v252, v200\n\t"
    "v_add_f32 v252, v252, v201\n\t"
    "v_mfma_f32_16x16x32_bf16 v[180:183], a[136:139], a[56:59], v[180:183]\n\t"
    "v_add_f32 v252, v252, v202\n\t"
    "v_add_f32 v252, v252, v203\n\t"
    "v_accvgpr_read_b32 v244, a228\n\t"
    "v_subrev_u32 v246, 0x2b800000, v244\n\t"
    "v_cmp_lt_u32 vcc, 0x28000000, v246\n\t"
    "s_nop 1\n\t"
    "s_or_b64 s[62:63], s[62:63], vcc\n\t"
    "v_mfma_f32_16x16x32_bf16 v[180:183], a[140:143], a[60:63], v[180:183]\n\t"
    "v_exp_f32 v172, v172\n\t"
    "v_rcp_f32 v245, v244\n\t"
    "s_mov_b32 s64, 24576\n\t"
    "v_mul_f32 v36, v36, v245\n\t"
    "v_mul_f32 v37, v37, v245\n\t"
    "v_mfma_f32_16x16x32_bf16 v[184:187], a[144:147], a[48:51], 0\n\t"
    "v_exp_f32 v173, v173\n\t"
    "v_mul_f32 v38, v38, v245\n\t"
    "v_mul_f32 v39, v39, v245\n\t"
    "v_mul_f32 v40, v40, v245\n\t"
    "v_mul_f32 v41, v41, v245\n\t"
    "v_mfma_f32_16x16x32_bf16 v[184:187], a[148:151], a[52:55], v[184:187]\n\t"
    "v_cvt_pk_bf16_f32 v204, v172, v173\n\t"
    "v_mul_f32 v42, v42, v245\n\t"
    "v_mul_f32 v43, v43, v245\n\t"
    "v_cvt_pk_bf16_f32 v36, v36, v37\n\t"
    "v_cvt_pk_bf16_f32 v37, v38, v39\n\t"
    "v_cvt_pk_bf16_f32 v38, v40, v41\n\t"
    "v_mfma_f32_16x16x32_bf16 v[184:187], a[152:155], a[56:59], v[184:187]\n\t"
    "v_exp_f32 v174, v174\n\t"
    "v_cvt_pk_bf16_f32 v39, v42, v43\n\t"
    "s_nop 1\n\t"
    "v_mfma_f32_16x16x32_bf16 a[4:7], a[0:3], v[36:39], a[4:7]\n\t"
    "v_mul_f32 v44, v44, v245\n\t"
    "v_mul_f32 v45, v45, v245\n\t"
    "v_mfma_f32_16x16x32_bf16 v[184:187], a[156:159], a[60:63], v[184:187]\n\t"
    "v_exp_f32 v175, v175\n\t"
    "v_mul_f32 v46, v46, v245\n\t"
    "v_mul_f32 v47, v47, v245\n\t"
    "v_mul_f32 v48, v48, v245\n\t"
    "v_mul_f32 v49, v49, v245\n\t"
    "v_mfma_f32_16x16x32_bf16 v[68:71], a[160:163], v[212:215], v[68:71]\n\t"
    "buffer_store_dwordx4 v[36:39], v241, s[48:51], s64 offen offset:0 sc0 sc1\n\t"
    "v_cvt_pk_bf16_f32 v205, v174, v175\n\t"
    "v_mul_f32 v50, v50, v245\n\t"
    "v_mul_f32 v51, v51, v245\n\t"
    "v_cvt_pk_bf16_f32 v44, v44, v45\n\t"
    "v_cvt_pk_bf16_f32 v45, v46, v47\n\t"
    "v_mfma_f32_16x16x32_bf16 v[68:71], a[192:195], v[216:219], v[68:71]\n\t"
    "v_exp_f32 v176, v176\n\t"
    "v_cvt_pk_bf16_f32 v46, v48, v49\n\t"
    "v_cvt_pk_bf16_f32 v47, v50, v51\n\t"
    "s_nop 1\n\t"
    "v_mfma_f32_16x16x32_bf16 a[4:7], a[0:3], v[44:47], a[4:7]\n\t"
    "v_mul_f32 v52, v52, v245\n\t"
    "v_mfma_f32_16x16x32_bf16 v[72:75], a[164:167], v[212:215], v[72:75]\n\t"
    "v_exp_f32 v177, v177\n\t"
    "v_mul_f32 v53, v53, v245\n\t"
    "v_mul_f32 v54, v54, v245\n\t"
    "v_mul_f32 v55, v55, v245\n\t"
    "v_mul_f32 v56, v56, v245\n\t"
    "v_mfma_f32_16x16x32_bf16 v[72:75], a[196:199], v[216:219], v[72:75]\n\t"
    "buffer_store_dwordx4 v[44:47], v241, s[48:51], s64 offen offset:64 sc0 sc1\n\t"
    "v_cvt_pk_bf16_f32 v206, v176, v177\n\t"
    "v_mul_f32 v57, v57, v245\n\t"
    "v_mul_f32 v58, v58, v245\n\t"
    "v_mul_f32 v59, v59, v245\n\t"
    "v_cvt_pk_bf16_f32 v52, v52, v53\n\t"
    "v_mfma_f32_16x16x32_bf16 v[76:79], a[168:171], v[212:215], v[76:79]\n\t"
    "v_exp_f32 v178, v178\n\t"
    "v_cvt_pk_bf16_f32 v53, v54, v55\n\t"
    "v_cvt_pk_bf16_f32 v54, v56, v57\n\t"
    "v_cvt_pk_bf16_f32 v55, v58, v59\n\t"
    "s_nop 1\n\t"
    "v_mfma_f32_16x16x32_bf16 a[4:7], a[0:3], v[52:55], a[4:7]\n\t"
    "v_mfma_f32_16x16x32_bf16 v[76:79], a[200:203], v[216:219], v[76:79]\n\t"
    "v_exp_f32 v179, v179\n\t"
    "v_mul_f32 v60, v60, v245\n\t"
    "v_mul_f32 v61, v61, v245\n\t"
    "v_mul_f32 v62, v62, v245\n\t"
    "v_mul_f32 v63, v63, v245\n\t"
    "v_mfma_f32_16x16x32_bf16 v[80:83], a[172:175], v[212:215], v[80:83]\n\t"
    "buffer_store_dwordx4 v[52:55], v241, s[48:51], s64 offen offset:128 sc0 sc1\n\t"
    "v_cvt_pk_bf16_f32 v207, v178, v179\n\t"
    "v_mul_f32 v64, v64, v245\n\t"
    "v_mul_f32 v65, v65, v245\n\t"
    "v_mul_f32 v66, v66, v245\n\t"
    "v_mul_f32 v67, v67, v245\n\t"
    "v_mfma_f32_16x16x32_bf16 v[80:83], a[204:207], v[216:219], v[80:83]\n\t"
    "v_exp_f32 v180, v180\n\t"
    "v_cvt_pk_bf16_f32 v60, v60, v61\n\t"
    "v_cvt_pk_bf16_f32 v61, v62, v63\n\t"
    "v_cvt_pk_bf16_f32 v62, v64, v65\n\t"
    "v_cvt_pk_bf16_f32 v63, v66, v67\n\t"
    "v_mfma_f32_16x16x32_bf16 v[84:87], a[176:179], v[212:215], v[84:87]\n\t"
    "v_exp_f32 v181, v181\n\t"
    "v_mfma_f32_16x16x32_bf16 a[4:7], a[0:3], v[60:63], a[4:7]\n\t"
    "v_mfma_f32_16x16x32_bf16 v[84:87], a[208:211], v[216:219], v[84:87]\n\t"
    "v_cvt_pk_bf16_f32 v208, v180, v181\n\t"
    "v_add_f32 v253, v253, v180\n\t"
    "v_mfma_f32_16x16x32_bf16 v[88:91], a[180:183], v[212:215], v[88:91]\n\t"
    "buffer_store_dwordx4 v[60:63], v241, s[48:51], s64 offen offset:192 sc0 sc1\n\t"
    "v_exp_f32 v182, v182\n\t"
    "v_mfma_f32_16x16x32_bf16 v[88:91], a[212:215], v[216:219], v[88:91]\n\t"
    "v_exp_f32 v183, v183\n\t"
    "v_mfma_f32_16x16x32_bf16 v[92:95], a[184:187], v[212:215], v[92:95]\n\t"
    "v_cvt_pk_bf16_f32 v209, v182, v183\n\t"
    "v_add_f32 v253, v253, v181\n\t"
    "v_mfma_f32_16x16x32_bf16 v[92:95], a[216:219], v[216:219], v[92:95]\n\t"
    "v_exp_f32 v184, v184\n\t"
    "v_mfma_f32_16x16x32_bf16 v[96:99], a[188:191], v[212:215], v[96:99]\n\t"
    "v_exp_f32 v185, v185\n\t"
    "v_mfma_f32_16x16x32_bf16 v[96:99], a[220:223], v[216:219], v[96:99]\n\t"
    "v_cvt_pk_bf16_f32 v210, v184, v185\n\t"
    "v_add_f32 v253, v253, v182\n\t"
    "v_mfma_f32_16x16x32_bf16 a[232:235], a[248:251], v[212:215], a[232:235]\n\t"
    "s_waitcnt vmcnt(8) lgkmcnt(0)\n\t"
    "s_barrier\n\t"
    "v_mfma_f32_16x16x32_bf16 v[188:191], a[96:99], a[64:67], 0\n\t"
    "v_add_f32 v253, v253, v183\n\t"
    "v_mfma_f32_16x16x32_bf16 v[188:191], a[100:103], a[68:71], v[188:191]\n\t"
    "v_exp_f32 v186, v186\n\t"
    "v_mfma_f32_16x16x32_bf16 v[188:191], a[104:107], a[72:75], v[188:191]\n\t"
    "v_mfma_f32_16x16x32_bf16 v[188:191], a[108:111], a[76:79], v[188:191]\n\t"
    "v_exp_f32 v187, v187\n\t"
    "v_mfma_f32_16x16x32_bf16 v[192:195], a[112:115], a[64:67], 0\n\t"
    "v_mfma_f32_16x16x32_bf16 v[192:195], a[116:119], a[68:71], v[192:195]\n\t"
    "v_cvt_pk_bf16_f32 v211, v186, v187\n\t"
    "v_add_f32 v253, v253, v184\n\t"
    "v_mfma_f32_16x16x32_bf16 v[192:195], a[120:123], a[72:75], v[192:195]\n\t"
    "v_mfma_f32_16x16x4_f32 a[232:235], a252, v252, a[232:235]\n\t"
    "v_mfma_f32_16x16x32_bf16 v[192:195], a[124:127], a[76:79], v[192:195]\n\t"
    "v_add_f32 v253, v253, v185\n\t"
    "v_add_f32 v253, v253, v186\n\t"
    "v_mfma_f32_16x16x32_bf16 v[196:199], a[128:131], a[64:67], 0\n\t"
    "v_add_f32 v253, v253, v187\n\t"
    "v_mfma_f32_16x16x32_bf16 v[196:199], a[132:135], a[68:71], v[196:199]\n\t"
    "v_mfma_f32_16x16x32_bf16 v[196:199], a[136:139], a[72:75], v[196:199]\n\t"
    "s_nop 2\n\t"
    "v_accvgpr_read_b32 v242, a232\n\t"
    "v_subrev_u32 v246, 0x2b800000, v242\n\t"
    "v_cmp_lt_u32 vcc, 0x28000000, v246\n\t"
    "s_nop 1\n\t"
    "s_or_b64 s[62:63], s[62:63], vcc\n\t"
    "v_rcp_f32 v243, v242\n\t"
    "s_mov_b32 s64, 28672\n\t"
    "v_mul_f32 v68, v68, v243\n\t"
    "v_mfma_f32_16x16x32_bf16 v[196:199], a[140:143], a[76:79], v[196:199]\n\t"
    "v_exp_f32 v188, v188\n\t"
    "v_mul_f32 v69, v69, v243\n\t"
    "v_mul_f32 v70, v70, v243\n\t"
    "v_mul_f32 v71, v71, v243\n\t"
    "v_mul_f32 v72, v72, v243\n\t"
    "v_mfma_f32_16x16x32_bf16 v[200:203], a[144:147], a[64:67], 0\n\t"
    "v_mul_f32 v73, v73, v243\n\t"
    "v_mul_f32 v74, v74, v243\n\t"
    "v_mul_f32 v75, v75, v243\n\t"
    "v_cvt_pk_bf16_f32 v68, v68, v69\n\t"
    "v_cvt_pk_bf16_f32 v69, v70, v71\n\t"
    "v_cvt_pk_bf16_f32 v70, v72, v73\n\t"
    "v_mfma_f32_16x16x32_bf16 v[200:203], a[148:151], a[68:71], v[200:203]\n\t"
    "v_exp_f32 v189, v189\n\t"
    "v_cvt_pk_bf16_f32 v71, v74, v75\n\t"
    "s_nop 1\n\t"
    "v_mfma_f32_16x16x32_bf16 a[4:7], a[0:3], v[68:71], a[4:7]\n\t"
    "v_mul_f32 v76, v76, v243\n\t"
    "v_mul_f32 v77, v77, v243\n\t"
    "v_mfma_f32_16x16x32_bf16 v[200:203], a[152:155], a[72:75], v[200:203]\n\t"
    "v_mul_f32 v78, v78, v243\n\t"
    "v_mul_f32 v79, v79, v243\n\t"
    "v_mul_f32 v80, v80, v243\n\t"
    "v_mul_f32 v81, v81, v243\n\t"
    "v_mul_f32 v82, v82, v243\n\t"
    "v_mul_f32 v83, v83, v243\n\t"
    "v_mfma_f32_16x16x32_bf16 v[200:203], a[156:159], a[76:79], v[200:203]\n\t"
    "buffer_store_dwordx4 v[68:71], v241, s[48:51], s64 offen offset:0 sc0 sc1\n\t"
    "v_cvt_pk_bf16_f32 v212, v188, v189\n\t"
    "v_add_f32 v254, v254, v188\n\t"
    "v_cvt_pk_bf16_f32 v76, v76, v77\n\t"
    "v_cvt_pk_bf16_f32 v77, v78, v79\n\t"
    "v_cvt_pk_bf16_f32 v78, v80, v81\n\t"
    "v_mfma_f32_16x16x32_bf16 v[100:103], a[160:163], v[204:207], v[100:103]\n\t"
    "v_cvt_pk_bf16_f32 v79, v82, v83\n\t"
    "s_nop 1\n\t"
    "v_mfma_f32_16x16x32_bf16 a[4:7], a[0:3], v[76:79], a[4:7]\n\t"
    "v_mul_f32 v84, v84, v243\n\t"
    "v_mul_f32 v85, v85, v243\n\t"
    "v_mul_f32 v86, v86, v243\n\t"
    "v_mul_f32 v87, v87, v243\n\t"
    "v_mfma_f32_16x16x32_bf16 v[100:103], a[192:195], v[208:211], v[100:103]\n\t"
    "v_exp_f32 v190, v190\n\t"
    "v_mul_f32 v88, v88, v243\n\t"
    "v_mul_f32 v89, v89, v243\n\t"
    "v_mul_f32 v90, v90, v243\n\t"
    "v_mul_f32 v91, v91, v243\n\t"
    "v_mfma_f32_16x16x32_bf16 v[104:107], a[164:167], v[204:207], v[104:107]\n\t"
    "ds_read_b128 v[228:231], v238 offset:2048\n\t"
    "ds_read_b128 v[232:235], v238 offset:3072\n\t"
    "v_cvt_pk_bf16_f32 v84, v84, v85\n\t"
    "v_cvt_pk_bf16_f32 v85, v86, v87\n\t"
    "v_cvt_pk_bf16_f32 v86, v88, v89\n\t"
    "v_cvt_pk_bf16_f32 v87, v90, v91\n\t"
    "v_mfma_f32_16x16x32_bf16 v[104:107], a[196:199], v[208:211], v[104:107]\n\t"
    "buffer_store_dwordx4 v[76:79], v241, s[48:51], s64 offen offset:64 sc0 sc1\n\t"
    "v_exp_f32 v191, v191\n\t"
    "v_mfma_f32_16x16x32_bf16 a[4:7], a[0:3], v[84:87], a[4:7]\n\t"
    "v_mul_f32 v92, v92, v243\n\t"
    "v_mul_f32 v93, v93, v243\n\t"
    "v_mul_f32 v94, v94, v243\n\t"
    "v_mfma_f32_16x16x32_bf16 v[108:111], a[168:171], v[204:207], v[108:111]\n\t"
    "v_cvt_pk_bf16_f32 v213, v190, v191\n\t"
    "v_add_f32 v254, v254, v189\n\t"
    "v_mul_f32 v95, v95, v243\n\t"
    "v_mul_f32 v96, v96, v243\n\t"
    "v_mul_f32 v97, v97, v243\n\t"
    "v_mul_f32 v98, v98, v243\n\t"
    "v_mfma_f32_16x16x32_bf16 v[108:111], a[200:203], v[208:211], v[108:111]\n\t"
    "buffer_store_dwordx4 v[84:87], v241, s[48:51], s64 offen offset:128 sc0 sc1\n\t"
    "v_exp_f32 v192, v192\n\t"
    "v_mul_f32 v99, v99, v243\n\t"
    "v_cvt_pk_bf16_f32 v92, v92, v93\n\t"
    "v_cvt_pk_bf16_f32 v93, v94, v95\n\t"
    "v_cvt_pk_bf16_f32 v94, v96, v97\n\t"
    "v_mfma_f32_16x16x32_bf16 v[112:115], a[172:175], v[204:207], v[112:115]\n\t"
    "v_exp_f32 v193, v193\n\t"
    "v_cvt_pk_bf16_f32 v95, v98, v99\n\t"
    "s_nop 1\n\t"
    "v_mfma_f32_16x16x32_bf16 a[4:7], a[0:3], v[92:95], a[4:7]\n\t"
    "ds_read_b32 v222, v220 offset:0\n\t"
    "ds_read_b32 v223, v220 offset:64\n\t"
    "v_mfma_f32_16x16x32_bf16 v[112:115], a[204:207], v[208:211], v[112:115]\n\t"
    "v_cvt_pk_bf16_f32 v214, v192, v193\n\t"
    "v_add_f32 v254, v254, v190\n\t"
    "ds_read_b32 v224, v220 offset:128\n\t"
    "ds_read_b32 v225, v220 offset:192\n\t"
    "s_waitcnt lgkmcnt(2)\n\t"
    "v_add_f32 v222, v222, v223\n\t"
    "s_waitcnt lgkmcnt(0)\n\t"
    "v_add_f32 v224, v224, v225\n\t"
    "v_mfma_f32_16x16x32_bf16 v[116:119], a[176:179], v[204:207], v[116:119]\n\t"
    "buffer_store_dwordx4 v[92:95], v241, s[48:51], s64 offen offset:192 sc0 sc1\n\t"
    "v_exp_f32 v194, v194\n\t"
    "v_add_f32 v248, v222, v224\n\t"
    "v_subrev_u32 v223, 0x2b800000, v248\n\t"
    "v_cmp_lt_u32 vcc, 0x28000000, v223\n\t"
    "s_nop 1\n\t"
    "s_or_b64 s[62:63], s[62:63], vcc\n\t"
    "v_mfma_f32_16x16x32_bf16 v[116:119], a[208:211], v[208:211], v[116:119]\n\t"
    "v_exp_f32 v195, v195\n\t"
    "v_rcp_f32 v249, v248\n\t"
    "v_mfma_f32_16x16x32_bf16 v[120:123], a[180:183], v[204:207], v[120:123]\n\t"
    "v_cvt_pk_bf16_f32 v215, v194, v195\n\t"
    "v_add_f32 v254, v254, v191\n\t"
    "v_mfma_f32_16x16x32_bf16 v[120:123], a[212:215], v[208:211], v[120:123]\n\t"
    "v_exp_f32 v196, v196\n\t"
    "v_mfma_f32_16x16x32_bf16 v[124:127], a[184:187], v[204:207], v[124:127]\n\t"
    "v_exp_f32 v197, v197\n\t"
    "v_mfma_f32_16x16x32_bf16 v[124:127], a[216:219], v[208:211], v[124:127]\n\t"
    "v_cvt_pk_bf16_f32 v216, v196, v197\n\t"
    "v_add_f32 v254, v254, v192\n\t"
    "v_mfma_f32_16x16x32_bf16 v[128:131], a[188:191], v[204:207], v[128:131]\n\t"
    "v_exp_f32 v198, v198\n\t"
    "v_mfma_f32_16x16x32_bf16 v[128:131], a[220:223], v[208:211], v[128:131]\n\t"
    "v_exp_f32 v199, v199\n\t"
    "v_mfma_f32_16x16x32_bf16 a[236:239], a[248:251], v[204:207], a[236:239]\n\t"
    "v_cvt_pk_bf16_f32 v217, v198, v199\n\t"
    "v_add_f32 v254, v254, v193\n\t"
    "v_exp_f32 v200, v200\n\t"
    "v_exp_f32 v201, v201\n\t"
    "s_nop 0\n\t"
    "v_cvt_pk_bf16_f32 v218, v200, v201\n\t"
    "v_add_f32 v254, v254, v194\n\t"
    "v_exp_f32 v202, v202\n\t"
    "v_exp_f32 v203, v203\n\t"
    "s_nop 0\n\t"
    "v_cvt_pk_bf16_f32 v219, v202, v203\n\t"
    "v_add_f32 v254, v254, v195\n\t"
    "v_mfma_f32_16x16x4_f32 a[236:239], a252, v253, a[236:239]\n\t"
    "v_add_f32 v254, v254, v196\n\t"
    "v_add_f32 v254, v254, v197\n\t"
    "v_add_f32 v254, v254, v198\n\t"
    "v_add_f32 v254, v254, v199\n\t"
    "v_add_f32 v254, v254, v200\n\t"
    "v_add_f32 v254, v254, v201\n\t"
    "v_add_f32 v254, v254, v202\n\t"
    "v_add_f32 v254, v254, v203\n\t"
    "s_nop 1\n\t"
    "v_accvgpr_read_b32 v244, a236\n\t"
    "v_subrev_u32 v246, 0x2b800000, v244\n\t"
    "v_cmp_lt_u32 vcc, 0x28000000, v246\n\t"
    "s_nop 1\n\t"
    "s_or_b64 s[62:63], s[62:63], vcc\n\t"
    "v_rcp_f32 v245, v244\n\t"
    "s_mov_b32 s64, 32768\n\t"
    "v_mul_f32 v100, v100, v245\n\t"
    "v_mul_f32 v101, v101, v245\n\t"
    "v_mul_f32 v102, v102, v245\n\t"
    "v_mul_f32 v103, v103, v245\n\t"
    "v_mul_f32 v104, v104, v245\n\t"
    "v_mul_f32 v105, v105, v245\n\t"
    "v_mul_f32 v106, v106, v245\n\t"
    "v_mul_f32 v107, v107, v245\n\t"
    "v_cvt_pk_bf16_f32 v100, v100, v101\n\t"
    "v_cvt_pk_bf16_f32 v101, v102, v103\n\t"
    "v_cvt_pk_bf16_f32 v102, v104, v105\n\t"
    "v_cvt_pk_bf16_f32 v103, v106, v107\n\t"
    "buffer_store_dwordx4 v[100:103], v241, s[48:51], s64 offen offset:0 sc0 sc1\n\t"
    "s_nop 0\n\t"
    "v_mfma_f32_16x16x32_bf16 a[4:7], a[0:3], v[100:103], a[4:7]\n\t"
    "v_mul_f32 v108, v108, v245\n\t"
    "v_mul_f32 v109, v109, v245\n\t"
    "v_mul_f32 v110, v110, v245\n\t"
    "v_mul_f32 v111, v111, v245\n\t"
    "v_mul_f32 v112, v112, v245\n\t"
    "v_mul_f32 v113, v113, v245\n\t"
    "v_mul_f32 v114, v114, v245\n\t"
    "v_mul_f32 v115, v115, v245\n\t"
    "v_cvt_pk_bf16_f32 v108, v108, v109\n\t"
    "v_cvt_pk_bf16_f32 v109, v110, v111\n\t"
    "v_cvt_pk_bf16_f32 v110, v112, v113\n\t"
    "v_cvt_pk_bf16_f32 v111, v114, v115\n\t"
    "buffer_store_dwordx4 v[108:111], v241, s[48:51], s64 offen offset:64 sc0 sc1\n\t"
    "s_nop 0\n\t"
    "v_mfma_f32_16x16x32_bf16 a[4:7], a[0:3], v[108:111], a[4:7]\n\t"
    "v_mul_f32 v116, v116, v245\n\t"
    "v_mul_f32 v117, v117, v245\n\t"
    "v_mul_f32 v118, v118, v245\n\t"
    "v_mfma_f32_16x16x32_bf16 v[164:167], a[168:171], v[228:231], v[164:167]\n\t"
    "v_mul_f32 v119, v119, v245\n\t"
    "v_mul_f32 v120, v120, v245\n\t"
    "v_mul_f32 v121, v121, v245\n\t"
    "v_mul_f32 v122, v122, v245\n\t"
    "v_mul_f32 v123, v123, v245\n\t"
    "v_cvt_pk_bf16_f32 v116, v116, v117\n\t"
    "v_mfma_f32_16x16x32_bf16 v[164:167], a[200:203], v[232:235], v[164:167]\n\t"
    "v_cvt_pk_bf16_f32 v117, v118, v119\n\t"
    "v_cvt_pk_bf16_f32 v118, v120, v121\n\t"
    "v_cvt_pk_bf16_f32 v119, v122, v123\n\t"
    "s_nop 1\n\t"
    "v_mfma_f32_16x16x32_bf16 a[4:7], a[0:3], v[116:119], a[4:7]\n\t"
    "v_mul_f32 v124, v124, v245\n\t"
    "v_mul_f32 v125, v125, v245\n\t"
    "v_mfma_f32_16x16x32_bf16 v[168:171], a[172:175], v[228:231], v[168:171]\n\t"
    "v_mul_f32 v126, v126, v245\n\t"
    "v_mul_f32 v127, v127, v245\n\t"
    "v_mul_f32 v128, v128, v245\n\t"
    "v_mul_f32 v129, v129, v245\n\t"
    "v_mul_f32 v130, v130, v245\n\t"
    "v_mul_f32 v131, v131, v245\n\t"
    "v_mfma_f32_16x16x32_bf16 v[168:171], a[204:207], v[232:235], v[168:171]\n\t"
    "buffer_store_dwordx4 v[116:119], v241, s[48:51], s64 offen offset:128 sc0 sc1\n\t"
    "v_cvt_pk_bf16_f32 v124, v124, v125\n\t"
    "v_cvt_pk_bf16_f32 v125, v126, v127\n\t"
    "v_cvt_pk_bf16_f32 v126, v128, v129\n\t"
    "v_cvt_pk_bf16_f32 v127, v130, v131\n\t"
    "s_nop 1\n\t"
    "v_mfma_f32_16x16x32_bf16 a[4:7], a[0:3], v[124:127], a[4:7]\n\t"
    "v_mfma_f32_16x16x32_bf16 v[132:135], a[160:163], v[212:215], v[132:135]\n\t"
    "v_mfma_f32_16x16x4_f32 a[240:243], a252, v254, 0\n\t"
    "v_mfma_f32_16x16x32_bf16 v[132:135], a[192:195], v[216:219], v[132:135]\n\t"
    "buffer_store_dwordx4 v[124:127], v241, s[48:51], s64 offen offset:192 sc0 sc1\n\t"
    "v_mfma_f32_16x16x32_bf16 v[136:139], a[164:167], v[212:215], v[136:139]\n\t"
    "v_mfma_f32_16x16x32_bf16 v[136:139], a[196:199], v[216:219], v[136:139]\n\t"
    "v_mfma_f32_16x16x32_bf16 v[140:143], a[168:171], v[212:215], v[140:143]\n\t"
    "v_mfma_f32_16x16x32_bf16 v[140:143], a[200:203], v[216:219], v[140:143]\n\t"
    "v_mfma_f32_16x16x32_bf16 v[144:147], a[172:175], v[212:215], v[144:147]\n\t"
    "v_mfma_f32_16x16x32_bf16 v[144:147], a[204:207], v[216:219], v[144:147]\n\t"
    "v_mfma_f32_16x16x32_bf16 v[148:151], a[176:179], v[212:215], v[148:151]\n\t"
    "s_mov_b32 s61, 81920\n\t"
    "v_mul_f32 v164, v164, v249\n\t"
    "v_mul_f32 v165, v165, v249\n\t"
    "v_mul_f32 v166, v166, v249\n\t"
    "v_mul_f32 v167, v167, v249\n\t"
    "v_mul_f32 v168, v168, v249\n\t"
    "v_mul_f32 v169, v169, v249\n\t"
    "v_mfma_f32_16x16x32_bf16 v[148:151], a[208:211], v[216:219], v[148:151]\n\t"
    "v_mul_f32 v170, v170, v249\n\t"
    "v_mul_f32 v171, v171, v249\n\t"
    "v_cvt_pk_bf16_f32 v164, v164, v165\n\t"
    "v_cvt_pk_bf16_f32 v165, v166, v167\n\t"
    "v_cvt_pk_bf16_f32 v166, v168, v169\n\t"
    "v_cvt_pk_bf16_f32 v167, v170, v171\n\t"
    "v_mfma_f32_16x16x32_bf16 v[152:155], a[180:183], v[212:215], v[152:155]\n\t"
    "s_nop 0\n\t"
    "v_mfma_f32_16x16x32_bf16 a[4:7], a[0:3], v[164:167], a[4:7]\n\t"
    "v_accvgpr_read_b32 v242, a240\n\t"
    "v_subrev_u32 v246, 0x2b800000, v242\n\t"
    "v_cmp_lt_u32 vcc, 0x28000000, v246\n\t"
    "s_nop 1\n\t"
    "s_or_b64 s[62:63], s[62:63], vcc\n\t"
    "v_rcp_f32 v243, v242\n\t"
    "v_mfma_f32_16x16x32_bf16 v[152:155], a[212:215], v[216:219], v[152:155]\n\t"
    "v_mfma_f32_16x16x32_bf16 v[156:159], a[184:187], v[212:215], v[156:159]\n\t"
    "buffer_store_dwordx4 v[164:167], v241, s[48:51], s61 offen offset:64 sc0 sc1\n\t"
    "s_mov_b32 s64, 36864\n\t"
    "v_mfma_f32_16x16x32_bf16 v[156:159], a[216:219], v[216:219], v[156:159]\n\t"
    "v_mfma_f32_16x16x32_bf16 v[160:163], a[188:191], v[212:215], v[160:163]\n\t"
    "v_mfma_f32_16x16x32_bf16 v[160:163], a[220:223], v[216:219], v[160:163]\n\t"
    "v_mul_f32 v132, v132, v243\n\t"
    "v_mul_f32 v133, v133, v243\n\t"
    "v_mul_f32 v134, v134, v243\n\t"
    "v_mul_f32 v135, v135, v243\n\t"
    "v_mul_f32 v136, v136, v243\n\t"
    "v_mul_f32 v137, v137, v243\n\t"
    "v_mul_f32 v138, v138, v243\n\t"
    "v_mul_f32 v139, v139, v243\n\t"
    "v_cvt_pk_bf16_f32 v132, v132, v133\n\t"
    "v_cvt_pk_bf16_f32 v133, v134, v135\n\t"
    "v_cvt_pk_bf16_f32 v134, v136, v137\n\t"
    "v_cvt_pk_bf16_f32 v135, v138, v139\n\t"
    "v_mul_f32 v140, v140, v243\n\t"
    "v_mul_f32 v141, v141, v243\n\t"
    "v_mul_f32 v142, v142, v243\n\t"
    "v_mul_f32 v143, v143, v243\n\t"
    "v_mul_f32 v144, v144, v243\n\t"
    "v_mul_f32 v145, v145, v243\n\t"
    "v_mul_f32 v146, v146, v243\n\t"
    "v_mul_f32 v147, v147, v243\n\t"
    "v_cvt_pk_bf16_f32 v140, v140, v141\n\t"
    "v_cvt_pk_bf16_f32 v141, v142, v143\n\t"
    "v_cvt_pk_bf16_f32 v142, v144, v145\n\t"
    "v_cvt_pk_bf16_f32 v143, v146, v147\n\t"
    "buffer_store_dwordx4 v[132:135], v241, s[48:51], s64 offen offset:0 sc0 sc1\n\t"
    "v_mfma_f32_16x16x32_bf16 a[4:7], a[0:3], v[132:135], a[4:7]\n\t"
    "v_mul_f32 v148, v148, v243\n\t"
    "v_mul_f32 v149, v149, v243\n\t"
    "v_mul_f32 v150, v150, v243\n\t"
    "v_mul_f32 v151, v151, v243\n\t"
    "v_mul_f32 v152, v152, v243\n\t"
    "v_mul_f32 v153, v153, v243\n\t"
    "v_mul_f32 v154, v154, v243\n\t"
    "v_mul_f32 v155, v155, v243\n\t"
    "v_cvt_pk_bf16_f32 v148, v148, v149\n\t"
    "v_cvt_pk_bf16_f32 v149, v150, v151\n\t"
    "v_cvt_pk_bf16_f32 v150, v152, v153\n\t"
    "v_cvt_pk_bf16_f32 v151, v154, v155\n\t"
    "buffer_store_dwordx4 v[140:143], v241, s[48:51], s64 offen offset:64 sc0 sc1\n\t"
    "v_mfma_f32_16x16x32_bf16 a[4:7], a[0:3], v[140:143], a[4:7]\n\t"
    "v_mul_f32 v156, v156, v243\n\t"
    "v_mul_f32 v157, v157, v243\n\t"
    "v_mul_f32 v158, v158, v243\n\t"
    "v_mul_f32 v159, v159, v243\n\t"
    "v_mul_f32 v160, v160, v243\n\t"
    "v_mul_f32 v161, v161, v243\n\t"
    "v_mul_f32 v162, v162, v243\n\t"
    "v_mul_f32 v163, v163, v243\n\t"
    "v_cvt_pk_bf16_f32 v156, v156, v157\n\t"
    "v_cvt_pk_bf16_f32 v157, v158, v159\n\t"
    "v_cvt_pk_bf16_f32 v158, v160, v161\n\t"
    "v_cvt_pk_bf16_f32 v159, v162, v163\n\t"
    "buffer_store_dwordx4 v[148:151], v241, s[48:51], s64 offen offset:128 sc0 sc1\n\t"
    "v_mfma_f32_16x16x32_bf16 a[4:7], a[0:3], v[148:151], a[4:7]\n\t"
    "buffer_store_dwordx4 v[156:159], v241, s[48:51], s64 offen offset:192 sc0 sc1\n\t"
    "v_mfma_f32_16x16x32_bf16 a[4:7], a[0:3], v[156:159], a[4:7]\n\t"
    "s_nop 7\n\t"
    "v_accvgpr_read_b32 v246, a4\n\t"
    "v_cmp_u_f32 vcc, v246, v246\n\t"
    "s_nop 1\n\t"
    "s_or_b64 s[62:63], s[62:63], vcc\n\t"
    "s_cmp_lg_u64 s[62:63], 0\n\t"
    "s_cselect_b32 %0, 1, 0\n\t"
    "s_branch END_%=\n\t"
    "WEND1_%=:\n\t"
    "W2_%=:\n\t"
    "WS2_%=:\n\t"
    "CPF2_%=:\n\t"
    "s_add_u32 s56, s53, 69696\n\t"
    "v_add_u32 v226, 4096, v239\n\t"
    "v_add_u32 v227, 8192, v239\n\t"
    "v_add_u32 v3, 12288, v239\n\t"
    "s_mov_b32 s58, 33792\n\t"
    "s_mov_b32 s60, 14\n\t"
    "s_mov_b32 s65, 0x3e0293ee\n\t"
    "s_mov_b64 s[62:63], 0\n\t"
    "v_bfe_u32 v242, v2, 2, 1\n\t"
    "v_and_b32 v243, 3, v2\n\t"
    "v_lshl_add_u32 v242, v242, 2, v243\n\t"
    "v_mul_u32_u24 v242, 0x420, v242\n\t"
    "v_lshrrev_b32 v243, 3, v2\n\t"
    "v_lshl_add_u32 v242, v243, 8, v242\n\t"
    "v_lshl_add_u32 v242, v255, 4, v242\n\t"
    "s_add_u32 s61, s53, 0\n\t"
    "v_add_u32 v236, s61, v242\n\t"
    "v_and_b32 v242, 1, v255\n\t"
    "v_bfe_u32 v243, v1, 2, 2\n\t"
    "v_lshl_add_u32 v242, v242, 2, v243\n\t"
    "v_mul_u32_u24 v242, 0x420, v242\n\t"
    "v_lshrrev_b32 v243, 1, v255\n\t"
    "v_lshl_add_u32 v242, v243, 8, v242\n\t"
    "v_and_b32 v243, 3, v1\n\t"
    "v_lshl_add_u32 v242, v243, 4, v242\n\t"
    "v_bfe_u32 v243, v1, 2, 1\n\t"
    "v_mul_u32_u24 v243, 24, v243\n\t"
    "v_sub_u32 v242, v242, v243\n\t"
    "s_add_u32 s61, s53, 67584\n\t"
    "v_add_u32 v237, s61, v242\n\t"
    "s_add_u32 s61, s53, 135168\n\t"
    "v_add_u32 v238, s61, v239\n\t"
    "v_lshlrev_b32 v241, 8, v2\n\t"
    "v_lshl_add_u32 v241, v255, 4, v241\n\t"
    "v_mov_b32 v242, 0x3f803f80\n\t"
    "v_accvgpr_write_b32 a248, v242\n\t"
    "v_accvgpr_write_b32 a249, v242\n\t"
    "v_accvgpr_write_b32 a250, v242\n\t"
    "v_accvgpr_write_b32 a251, v242\n\t"
    "v_mov_b32 v132, 0\n\t"
    "v_mov_b32 v133, 0\n\t"
    "v_mov_b32 v134, 0\n\t"
    "v_mov_b32 v135, 0\n\t"
    "v_mov_b32 v136, 0\n\t"
    "v_mov_b32 v137, 0\n\t"
    "v_mov_b32 v138, 0\n\t"
    "v_mov_b32 v139, 0\n\t"
    "v_mov_b32 v140, 0\n\t"
    "v_mov_b32 v141, 0\n\t"
    "v_mov_b32 v142, 0\n\t"
    "v_mov_b32 v143, 0\n\t"
    "v_mov_b32 v144, 0\n\t"
    "v_mov_b32 v145, 0\n\t"
    "v_mov_b32 v146, 0\n\t"
    "v_mov_b32 v147, 0\n\t"
    "v_mov_b32 v148, 0\n\t"
    "v_mov_b32 v149, 0\n\t"
    "v_mov_b32 v150, 0\n\t"
    "v_mov_b32 v151, 0\n\t"
    "v_mov_b32 v152, 0\n\t"
    "v_mov_b32 v153, 0\n\t"
    "v_mov_b32 v154, 0\n\t"
    "v_mov_b32 v155, 0\n\t"
    "v_mov_b32 v156, 0\n\t"
    "v_mov_b32 v157, 0\n\t"
    "v_mov_b32 v158, 0\n\t"
    "v_mov_b32 v159, 0\n\t"
    "v_mov_b32 v160, 0\n\t"
    "v_mov_b32 v161, 0\n\t"
    "v_mov_b32 v162, 0\n\t"
    "v_mov_b32 v163, 0\n\t"
    "v_mov_b32 v164, 0\n\t"
    "v_mov_b32 v165, 0\n\t"
    "v_mov_b32 v166, 0\n\t"
    "v_mov_b32 v167, 0\n\t"
    "v_mov_b32 v168, 0\n\t"
    "v_mov_b32 v169, 0\n\t"
    "v_mov_b32 v170, 0\n\t"
    "v_mov_b32 v171, 0\n\t"
    "v_mov_b32 v250, 0\n\t"
    "v_mov_b32 v251, 0\n\t"
    "v_mov_b32 v252, 0\n\t"
    "v_mov_b32 v253, 0\n\t"
    "v_mov_b32 v254, 0\n\t"
    "v_mov_b32 v242, 1.0\n\t"
    "v_accvgpr_write_b32 a252, v242\n\t"
    "s_mov_b32 s64, 45056\n\t"
    "buffer_load_dwordx4 v[116:119], v240, s[36:39], s64 offen offset:0\n\t"
    "buffer_load_dwordx4 v[120:123], v240, s[36:39], s64 offen offset:64\n\t"
    "buffer_load_dwordx4 v[124:127], v240, s[36:39], s64 offen offset:128\n\t"
    "buffer_load_dwordx4 v[128:131], v240, s[36:39], s64 offen offset:192\n\t"
    "s_waitcnt vmcnt(9)\n\t"
    "s_barrier\n\t"
    "ds_read_b128 a[96:99], v236 offset:0\n\t"
    "ds_read_b128 a[100:103], v236 offset:64\n\t"
    "ds_read_b128 a[104:107], v236 offset:128\n\t"
    "ds_read_b128 a[108:111], v236 offset:192\n\t"
    "ds_read_b128 a[112:115], v236 offset:512\n\t"
    "ds_read_b128 a[116:119], v236 offset:576\n\t"
    "ds_read_b128 a[120:123], v236 offset:640\n\t"
    "ds_read_b128 a[124:127], v236 offset:704\n\t"
    "ds_read_b128 a[128:131], v236 offset:8448\n\t"
    "ds_read_b128 a[132:135], v236 offset:8512\n\t"
    "ds_read_b128 a[136:139], v236 offset:8576\n\t"
    "ds_read_b128 a[140:143], v236 offset:8640\n\t"
    "ds_read_b128 a[144:147], v236 offset:8960\n\t"
    "ds_read_b128 a[148:151], v236 offset:9024\n\t"
    "ds_read_b128 a[152:155], v236 offset:9088\n\t"
    "ds_read_b128 a[156:159], v236 offset:9152\n\t"
    "s_add_u32 m0, s56, 0\n\t"
    "s_add_u32 s61, s59, 2048\n\t"
    "s_and_b32 s61, s61, 0x7ffff\n\t"
    "buffer_load_dwordx4 v239, s[44:47], s61 offen offset:0 lds\n\t"
    "s_add_u32 m0, s56, 4224\n\t"
    "s_nop 0\n\t"
    "buffer_load_dwordx4 v226, s[44:47], s61 offen offset:0 lds\n\t"
    "s_add_u32 m0, s56, 8448\n\t"
    "s_nop 0\n\t"
    "buffer_load_dwordx4 v227, s[44:47], s61 offen offset:0 lds\n\t"
    "s_add_u32 m0, s56, 12672\n\t"
    "s_nop 0\n\t"
    "buffer_load_dwordx4 v3, s[44:47], s61 offen offset:0 lds\n\t"
    "s_waitcnt vmcnt(12)\n\t"
    "v_lshlrev_b32 v242, 16, v100\n\t"
    "v_and_b32 v243, 0xffff0000, v100\n\t"
    "v_mul_f32 v242, s65, v242\n\t"
    "v_mul_f32 v243, s65, v243\n\t"
    "v_cvt_pk_bf16_f32 v242, v242, v243\n\t"
    "v_accvgpr_write_b32 a0, v242\n\t"
    "s_mov_b32 s64, 81920\n\t"
    "buffer_load_dwordx4 v[68:71], v240, s[36:39], s64 offen offset:0\n\t"
    "buffer_load_dwordx4 v[72:75], v240, s[36:39], s64 offen offset:64\n\t"
    "buffer_load_dwordx4 v[76:79], v240, s[36:39], s64 offen offset:128\n\t"
    "buffer_load_dwordx4 v[80:83], v240, s[36:39], s64 offen offset:192\n\t"
    "v_lshlrev_b32 v244, 16, v101\n\t"
    "v_and_b32 v245, 0xffff0000, v101\n\t"
    "v_mul_f32 v244, s65, v244\n\t"
    "v_mul_f32 v245, s65, v245\n\t"
    "v_cvt_pk_bf16_f32 v244, v244, v245\n\t"
    "v_accvgpr_write_b32 a1, v244\n\t"
    "v_lshlrev_b32 v246, 16, v102\n\t"
    "v_and_b32 v247, 0xffff0000, v102\n\t"
    "v_mul_f32 v246, s65, v246\n\t"
    "v_mul_f32 v247, s65, v247\n\t"
    "v_cvt_pk_bf16_f32 v246, v246, v247\n\t"
    "v_accvgpr_write_b32 a2, v246\n\t"
    "v_lshlrev_b32 v248, 16, v103\n\t"
    "v_and_b32 v249, 0xffff0000, v103\n\t"
    "v_mul_f32 v248, s65, v248\n\t"
    "v_mul_f32 v249, s65, v249\n\t"
    "v_cvt_pk_bf16_f32 v248, v248, v249\n\t"
    "v_accvgpr_write_b32 a3, v248\n\t"
    "s_waitcnt vmcnt(15)\n\t"
    "v_lshlrev_b32 v242, 16, v104\n\t"
    "v_and_b32 v243, 0xffff0000, v104\n\t"
    "v_mul_f32 v242, s65, v242\n\t"
    "v_mul_f32 v243, s65, v243\n\t"
    "v_cvt_pk_bf16_f32 v242, v242, v243\n\t"
    "v_accvgpr_write_b32 a4, v242\n\t"
    "v_lshlrev_b32 v244, 16, v105\n\t"
    "v_and_b32 v245, 0xffff0000, v105\n\t"
    "v_mul_f32 v244, s65, v244\n\t"
    "v_mul_f32 v245, s65, v245\n\t"
    "v_cvt_pk_bf16_f32 v244, v244, v245\n\t"
    "v_accvgpr_write_b32 a5, v244\n\t"
    "v_lshlrev_b32 v246, 16, v106\n\t"
    "v_and_b32 v247, 0xffff0000, v106\n\t"
    "v_mul_f32 v246, s65, v246\n\t"
    "v_mul_f32 v247, s65, v247\n\t"
    "v_cvt_pk_bf16_f32 v246, v246, v247\n\t"
    "v_accvgpr_write_b32 a6, v246\n\t"
    "v_lshlrev_b32 v248, 16, v107\n\t"
    "v_and_b32 v249, 0xffff0000, v107\n\t"
    "v_mul_f32 v248, s65, v248\n\t"
    "v_mul_f32 v249, s65, v249\n\t"
    "v_cvt_pk_bf16_f32 v248, v248, v249\n\t"
    "v_accvgpr_write_b32 a7, v248\n\t"
    "s_waitcnt vmcnt(14)\n\t"
    "v_lshlrev_b32 v242, 16, v108\n\t"
    "v_and_b32 v243, 0xffff0000, v108\n\t"
    "v_mul_f32 v242, s65, v242\n\t"
    "v_mul_f32 v243, s65, v243\n\t"
    "v_cvt_pk_bf16_f32 v242, v242, v243\n\t"
    "v_accvgpr_write_b32 a8, v242\n\t"
    "v_lshlrev_b32 v244, 16, v109\n\t"
    "v_and_b32 v245, 0xffff0000, v109\n\t"
    "v_mul_f32 v244, s65, v244\n\t"
    "v_mul_f32 v245, s65, v245\n\t"
    "v_cvt_pk_bf16_f32 v244, v244, v245\n\t"
    "v_accvgpr_write_b32 a9, v244\n\t"
    "v_lshlrev_b32 v246, 16, v110\n\t"
    "v_and_b32 v247, 0xffff0000, v110\n\t"
    "v_mul_f32 v246, s65, v246\n\t"
    "v_mul_f32 v247, s65, v247\n\t"
    "v_cvt_pk_bf16_f32 v246, v246, v247\n\t"
    "v_accvgpr_write_b32 a10, v246\n\t"
    "v_lshlrev_b32 v248, 16, v111\n\t"
    "v_and_b32 v249, 0xffff0000, v111\n\t"
    "v_mul_f32 v248, s65, v248\n\t"
    "v_mul_f32 v249, s65, v249\n\t"
    "v_cvt_pk_bf16_f32 v248, v248, v249\n\t"
    "v_accvgpr_write_b32 a11, v248\n\t"
    "s_waitcnt vmcnt(13)\n\t"
    "v_lshlrev_b32 v242, 16, v112\n\t"
    "v_and_b32 v243, 0xffff0000, v112\n\t"
    "v_mul_f32 v242, s65, v242\n\t"
    "v_mul_f32 v243, s65, v243\n\t"
    "v_cvt_pk_bf16_f32 v242, v242, v243\n\t"
    "v_accvgpr_write_b32 a12, v242\n\t"
    "v_lshlrev_b32 v244, 16, v113\n\t"
    "v_and_b32 v245, 0xffff0000, v113\n\t"
    "v_mul_f32 v244, s65, v244\n\t"
    "v_mul_f32 v245, s65, v245\n\t"
    "v_cvt_pk_bf16_f32 v244, v244, v245\n\t"
    "v_accvgpr_write_b32 a13, v244\n\t"
    "v_lshlrev_b32 v246, 16, v114\n\t"
    "v_and_b32 v247, 0xffff0000, v114\n\t"
    "v_mul_f32 v246, s65, v246\n\t"
    "v_mul_f32 v247, s65, v247\n\t"
    "v_cvt_pk_bf16_f32 v246, v246, v247\n\t"
    "v_accvgpr_write_b32 a14, v246\n\t"
    "v_lshlrev_b32 v248, 16, v115\n\t"
    "v_and_b32 v249, 0xffff0000, v115\n\t"
    "v_mul_f32 v248, s65, v248\n\t"
    "v_mul_f32 v249, s65, v249\n\t"
    "v_cvt_pk_bf16_f32 v248, v248, v249\n\t"
    "v_accvgpr_write_b32 a15, v248\n\t"
    "s_waitcnt lgkmcnt(15)\n\t"
    "v_mfma_f32_16x16x32_bf16 v[172:175], a[96:99], a[0:3], 0\n\t"
    "s_waitcnt lgkmcnt(14)\n\t"
    "v_mfma_f32_16x16x32_bf16 v[172:175], a[100:103], a[4:7], v[172:175]\n\t"
    "s_waitcnt lgkmcnt(13)\n\t"
    "s_waitcnt vmcnt(11)\n\t"
    "v_lshlrev_b32 v242, 16, v116\n\t"
    "v_and_b32 v243, 0xffff0000, v116\n\t"
    "v_mul_f32 v242, s65, v242\n\t"
    "v_mul_f32 v243, s65, v243\n\t"
    "v_cvt_pk_bf16_f32 v242, v242, v243\n\t"
    "v_accvgpr_write_b32 a16, v242\n\t"
    "s_mov_b32 s64, 49152\n\t"
    "buffer_load_dwordx4 v[84:87], v240, s[36:39], s64 offen offset:0\n\t"
    "buffer_load_dwordx4 v[88:91], v240, s[36:39], s64 offen offset:64\n\t"
    "buffer_load_dwordx4 v[92:95], v240, s[36:39], s64 offen offset:128\n\t"
    "buffer_load_dwordx4 v[96:99], v240, s[36:39], s64 offen offset:192\n\t"
    "v_mfma_f32_16x16x32_bf16 v[172:175], a[104:107], a[8:11], v[172:175]\n\t"
    "s_waitcnt lgkmcnt(12)\n\t"
    "v_lshlrev_b32 v244, 16, v117\n\t"
    "v_and_b32 v245, 0xffff0000, v117\n\t"
    "v_mul_f32 v244, s65, v244\n\t"
    "v_mul_f32 v245, s65, v245\n\t"
    "v_cvt_pk_bf16_f32 v244, v244, v245\n\t"
    "v_accvgpr_write_b32 a17, v244\n\t"
    "v_mfma_f32_16x16x32_bf16 v[172:175], a[108:111], a[12:15], v[172:175]\n\t"
    "s_waitcnt lgkmcnt(11)\n\t"
    "v_lshlrev_b32 v246, 16, v118\n\t"
    "v_and_b32 v247, 0xffff0000, v118\n\t"
    "v_mul_f32 v246, s65, v246\n\t"
    "v_mul_f32 v247, s65, v247\n\t"
    "v_cvt_pk_bf16_f32 v246, v246, v247\n\t"
    "v_accvgpr_write_b32 a18, v246\n\t"
    "v_lshlrev_b32 v248, 16, v119\n\t"
    "v_mfma_f32_16x16x32_bf16 v[176:179], a[112:115], a[0:3], 0\n\t"
    "s_waitcnt lgkmcnt(10)\n\t"
    "v_and_b32 v249, 0xffff0000, v119\n\t"
    "v_mul_f32 v248, s65, v248\n\t"
    "v_mul_f32 v249, s65, v249\n\t"
    "v_cvt_pk_bf16_f32 v248, v248, v249\n\t"
    "v_accvgpr_write_b32 a19, v248\n\t"
    "s_waitcnt vmcnt(14)\n\t"
    "v_lshlrev_b32 v242, 16, v120\n\t"
    "v_mfma_f32_16x16x32_bf16 v[176:179], a[116:119], a[4:7], v[176:179]\n\t"
    "s_waitcnt lgkmcnt(9)\n\t"
    "v_and_b32 v243, 0xffff0000, v120\n\t"
    "v_mul_f32 v242, s65, v242\n\t"
    "v_mul_f32 v243, s65, v243\n\t"
    "v_cvt_pk_bf16_f32 v242, v242, v243\n\t"
    "v_accvgpr_write_b32 a20, v242\n\t"
    "v_lshlrev_b32 v244, 16, v121\n\t"
    "v_and_b32 v245, 0xffff0000, v121\n\t"
    "v_mfma_f32_16x16x32_bf16 v[176:179], a[120:123], a[8:11], v[176:179]\n\t"
    "s_waitcnt lgkmcnt(0)\n\t"
    "v_mul_f32 v244, s65, v244\n\t"
    "v_mul_f32 v245, s65, v245\n\t"
    "v_cvt_pk_bf16_f32 v244, v244, v245\n\t"
    "v_accvgpr_write_b32 a21, v244\n\t"
    "v_lshlrev_b32 v246, 16, v122\n\t"
    "v_and_b32 v247, 0xffff0000, v122\n\t"
    "v_mfma_f32_16x16x32_bf16 v[176:179], a[124:127], a[12:15], v[176:179]\n\t"
    "v_mul_f32 v246, s65, v246\n\t"
    "v_mul_f32 v247, s65, v247\n\t"
    "v_cvt_pk_bf16_f32 v246, v246, v247\n\t"
    "v_accvgpr_write_b32 a22, v246\n\t"
    "v_lshlrev_b32 v248, 16, v123\n\t"
    "v_and_b32 v249, 0xffff0000, v123\n\t"
    "v_mfma_f32_16x16x32_bf16 v[180:183], a[128:131], a[0:3], 0\n\t"
    "v_mul_f32 v248, s65, v248\n\t"
    "v_mul_f32 v249, s65, v249\n\t"
    "v_cvt_pk_bf16_f32 v248, v248, v249\n\t"
    "v_accvgpr_write_b32 a23, v248\n\t"
    "s_waitcnt vmcnt(13)\n\t"
    "v_lshlrev_b32 v242, 16, v124\n\t"
    "v_and_b32 v243, 0xffff0000, v124\n\t"
    "v_mul_f32 v242, s65, v242\n\t"
    "v_mfma_f32_16x16x32_bf16 v[180:183], a[132:135], a[4:7], v[180:183]\n\t"
    "v_mul_f32 v243, s65, v243\n\t"
    "v_cvt_pk_bf16_f32 v242, v242, v243\n\t"
    "v_accvgpr_write_b32 a24, v242\n\t"
    "v_lshlrev_b32 v244, 16, v125\n\t"
    "v_and_b32 v245, 0xffff0000, v125\n\t"
    "v_mul_f32 v244, s65, v244\n\t"
    "v_mfma_f32_16x16x32_bf16 v[180:183], a[136:139], a[8:11], v[180:183]\n\t"
    "v_mul_f32 v245, s65, v245\n\t"
    "v_cvt_pk_bf16_f32 v244, v244, v245\n\t"
    "v_accvgpr_write_b32 a25, v244\n\t"
    "v_lshlrev_b32 v246, 16, v126\n\t"
    "v_and_b32 v247, 0xffff0000, v126\n\t"
    "v_mul_f32 v246, s65, v246\n\t"
    "v_mul_f32 v247, s65, v247\n\t"
    "v_mfma_f32_16x16x32_bf16 v[180:183], a[140:143], a[12:15], v[180:183]\n\t"
    "v_cvt_pk_bf16_f32 v246, v246, v247\n\t"
    "v_accvgpr_write_b32 a26, v246\n\t"
    "v_lshlrev_b32 v248, 16, v127\n\t"
    "v_and_b32 v249, 0xffff0000, v127\n\t"
    "v_mul_f32 v248, s65, v248\n\t"
    "v_mul_f32 v249, s65, v249\n\t"
    "v_exp_f32 v172, v172\n\t"
    "v_mfma_f32_16x16x32_bf16 v[184:187], a[144:147], a[0:3], 0\n\t"
    "v_cvt_pk_bf16_f32 v248, v248, v249\n\t"
    "v_accvgpr_write_b32 a27, v248\n\t"
    "s_waitcnt vmcnt(12)\n\t"
    "v_lshlrev_b32 v242, 16, v128\n\t"
    "v_and_b32 v243, 0xffff0000, v128\n\t"
    "v_mul_f32 v242, s65, v242\n\t"
    "v_mul_f32 v243, s65, v243\n\t"
    "v_exp_f32 v173, v173\n\t"
    "v_mfma_f32_16x16x32_bf16 v[184:187], a[148:151], a[4:7], v[184:187]\n\t"
    "v_cvt_pk_bf16_f32 v242, v242, v243\n\t"
    "v_accvgpr_write_b32 a28, v242\n\t"
    "v_lshlrev_b32 v244, 16, v129\n\t"
    "v_and_b32 v245, 0xffff0000, v129\n\t"
    "v_mul_f32 v244, s65, v244\n\t"
    "v_mul_f32 v245, s65, v245\n\t"
    "v_cvt_pk_bf16_f32 v244, v244, v245\n\t"
    "v_cvt_pk_bf16_f32 v204, v172, v173\n\t"
    "v_add_f32 v250, v250, v172\n\t"
    "v_mfma_f32_16x16x32_bf16 v[184:187], a[152:155], a[8:11], v[184:187]\n\t"
    "v_accvgpr_write_b32 a29, v244\n\t"
    "v_lshlrev_b32 v246, 16, v130\n\t"
    "v_and_b32 v247, 0xffff0000, v130\n\t"
    "v_mul_f32 v246, s65, v246\n\t"
    "v_mul_f32 v247, s65, v247\n\t"
    "v_cvt_pk_bf16_f32 v246, v246, v247\n\t"
    "v_exp_f32 v174, v174\n\t"
    "v_mfma_f32_16x16x32_bf16 v[184:187], a[156:159], a[12:15], v[184:187]\n\t"
    "v_accvgpr_write_b32 a30, v246\n\t"
    "v_lshlrev_b32 v248, 16, v131\n\t"
    "v_and_b32 v249, 0xffff0000, v131\n\t"
    "v_mul_f32 v248, s65, v248\n\t"
    "v_mul_f32 v249, s65, v249\n\t"
    "v_cvt_pk_bf16_f32 v248, v248, v249\n\t"
    "v_accvgpr_write_b32 a31, v248\n\t"
    "v_exp_f32 v175, v175\n\t"
    "s_waitcnt vmcnt(8)\n\t"
    "s_barrier\n\t"
    "v_cvt_pk_bf16_f32 v205, v174, v175\n\t"
    "v_add_f32 v250, v250, v173\n\t"
    "s_mov_b32 s64, 53248\n\t"
    "buffer_load_dwordx4 v[100:103], v240, s[36:39], s64 offen offset:0\n\t"
    "buffer_load_dwordx4 v[104:107], v240, s[36:39], s64 offen offset:64\n\t"
    "buffer_load_dwordx4 v[108:111], v240, s[36:39], s64 offen offset:128\n\t"
    "buffer_load_dwordx4 v[112:115], v240, s[36:39], s64 offen offset:192\n\t"
    "v_exp_f32 v176, v176\n\t"
    "v_exp_f32 v177, v177\n\t"
    "ds_read_b64_tr_b16 a[160:161], v237 offset:0\n\t"
    "ds_read_b64_tr_b16 a[162:163], v237 offset:512\n\t"
    "v_cvt_pk_bf16_f32 v206, v176, v177\n\t"
    "v_add_f32 v250, v250, v174\n\t"
    "s_waitcnt vmcnt(11)\n\t"
    "v_lshlrev_b32 v242, 16, v68\n\t"
    "v_and_b32 v243, 0xffff0000, v68\n\t"
    "v_mul_f32 v242, s65, v242\n\t"
    "v_mul_f32 v243, s65, v243\n\t"
    "v_cvt_pk_bf16_f32 v242, v242, v243\n\t"
    "v_accvgpr_write_b32 a80, v242\n\t"
    "s_add_u32 m0, s55, 16896\n\t"
    "s_add_u32 s61, s59, 18432\n\t"
    "s_and_b32 s61, s61, 0x7ffff\n\t"
    "buffer_load_dwordx4 v239, s[40:43], s61 offen offset:0 nt lds\n\t"
    "s_add_u32 m0, s55, 21120\n\t"
    "s_nop 0\n\t"
    "buffer_load_dwordx4 v226, s[40:43], s61 offen offset:0 nt lds\n\t"
    "s_add_u32 m0, s55, 25344\n\t"
    "s_nop 0\n\t"
    "buffer_load_dwordx4 v227, s[40:43], s61 offen offset:0 nt lds\n\t"
    "s_add_u32 m0, s55, 29568\n\t"
    "s_nop 0\n\t"
    "buffer_load_dwordx4 v3, s[40:43], s61 offen offset:0 nt lds\n\t"
    "ds_read_b64_tr_b16 a[192:193], v237 offset:8448\n\t"
    "ds_read_b64_tr_b16 a[194:195], v237 offset:8960\n\t"
    "v_lshlrev_b32 v244, 16, v69\n\t"
    "v_and_b32 v245, 0xffff0000, v69\n\t"
    "v_mul_f32 v244, s65, v244\n\t"
    "v_mul_f32 v245, s65, v245\n\t"
    "v_cvt_pk_bf16_f32 v244, v244, v245\n\t"
    "v_accvgpr_write_b32 a81, v244\n\t"
    "v_lshlrev_b32 v246, 16, v70\n\t"
    "v_exp_f32 v178, v178\n\t"
    "v_and_b32 v247, 0xffff0000, v70\n\t"
    "v_mul_f32 v246, s65, v246\n\t"
    "v_mul_f32 v247, s65, v247\n\t"
    "v_cvt_pk_bf16_f32 v246, v246, v247\n\t"
    "v_accvgpr_write_b32 a82, v246\n\t"
    "v_lshlrev_b32 v248, 16, v71\n\t"
    "v_and_b32 v249, 0xffff0000, v71\n\t"
    "ds_read_b64_tr_b16 a[164:165], v237 offset:8\n\t"
    "ds_read_b64_tr_b16 a[166:167], v237 offset:520\n\t"
    "v_mul_f32 v248, s65, v248\n\t"
    "v_mul_f32 v249, s65, v249\n\t"
    "v_cvt_pk_bf16_f32 v248, v248, v249\n\t"
    "v_accvgpr_write_b32 a83, v248\n\t"
    "s_waitcnt vmcnt(14)\n\t"
    "v_lshlrev_b32 v242, 16, v72\n\t"
    "v_and_b32 v243, 0xffff0000, v72\n\t"
    "v_mul_f32 v242, s65, v242\n\t"
    "v_exp_f32 v179, v179\n\t"
    "v_mul_f32 v243, s65, v243\n\t"
    "v_cvt_pk_bf16_f32 v242, v242, v243\n\t"
    "v_accvgpr_write_b32 a84, v242\n\t"
    "v_lshlrev_b32 v244, 16, v73\n\t"
    "v_and_b32 v245, 0xffff0000, v73\n\t"
    "v_mul_f32 v244, s65, v244\n\t"
    "v_mul_f32 v245, s65, v245\n\t"
    "ds_read_b64_tr_b16 a[196:197], v237 offset:8456\n\t"
    "ds_read_b64_tr_b16 a[198:199], v237 offset:8968\n\t"
    "v_cvt_pk_bf16_f32 v244, v244, v245\n\t"
    "v_accvgpr_write_b32 a85, v244\n\t"
    "v_lshlrev_b32 v246, 16, v74\n\t"
    "v_and_b32 v247, 0xffff0000, v74\n\t"
    "v_mul_f32 v246, s65, v246\n\t"
    "v_mul_f32 v247, s65, v247\n\t"
    "v_cvt_pk_bf16_f32 v246, v246, v247\n\t"
    "v_cvt_pk_bf16_f32 v207, v178, v179\n\t"
    "v_add_f32 v250, v250, v175\n\t"
    "v_accvgpr_write_b32 a86, v246\n\t"
    "v_lshlrev_b32 v248, 16, v75\n\t"
    "v_and_b32 v249, 0xffff0000, v75\n\t"
    "v_mul_f32 v248, s65, v248\n\t"
    "v_mul_f32 v249, s65, v249\n\t"
    "v_cvt_pk_bf16_f32 v248, v248, v249\n\t"
    "v_accvgpr_write_b32 a87, v248\n\t"
    "ds_read_b64_tr_b16 a[168:169], v237 offset:64\n\t"
    "ds_read_b64_tr_b16 a[170:171], v237 offset:576\n\t"
    "s_waitcnt vmcnt(13)\n\t"
    "v_lshlrev_b32 v242, 16, v76\n\t"
    "v_and_b32 v243, 0xffff0000, v76\n\t"
    "v_mul_f32 v242, s65, v242\n\t"
    "v_mul_f32 v243, s65, v243\n\t"
    "v_cvt_pk_bf16_f32 v242, v242, v243\n\t"
    "v_accvgpr_write_b32 a88, v242\n\t"
    "v_exp_f32 v180, v180\n\t"
    "v_lshlrev_b32 v244, 16, v77\n\t"
    "v_and_b32 v245, 0xffff0000, v77\n\t"
    "v_mul_f32 v244, s65, v244\n\t"
    "v_mul_f32 v245, s65, v245\n\t"
    "v_cvt_pk_bf16_f32 v244, v244, v245\n\t"
    "v_accvgpr_write_b32 a89, v244\n\t"
    "v_lshlrev_b32 v246, 16, v78\n\t"
    "ds_read_b64_tr_b16 a[200:201], v237 offset:8512\n\t"
    "ds_read_b64_tr_b16 a[202:203], v237 offset:9024\n\t"
    "v_and_b32 v247, 0xffff0000, v78\n\t"
    "v_mul_f32 v246, s65, v246\n\t"
    "v_mul_f32 v247, s65, v247\n\t"
    "v_cvt_pk_bf16_f32 v246, v246, v247\n\t"
    "v_accvgpr_write_b32 a90, v246\n\t"
    "v_lshlrev_b32 v248, 16, v79\n\t"
    "v_and_b32 v249, 0xffff0000, v79\n\t"
    "v_exp_f32 v181, v181\n\t"
    "v_mul_f32 v248, s65, v248\n\t"
    "v_mul_f32 v249, s65, v249\n\t"
    "v_cvt_pk_bf16_f32 v248, v248, v249\n\t"
    "v_accvgpr_write_b32 a91, v248\n\t"
    "s_waitcnt vmcnt(12)\n\t"
    "v_lshlrev_b32 v242, 16, v80\n\t"
    "v_and_b32 v243, 0xffff0000, v80\n\t"
    "v_mul_f32 v242, s65, v242\n\t"
    "ds_read_b64_tr_b16 a[172:173], v237 offset:72\n\t"
    "ds_read_b64_tr_b16 a[174:175], v237 offset:584\n\t"
    "v_mul_f32 v243, s65, v243\n\t"
    "v_cvt_pk_bf16_f32 v242, v242, v243\n\t"
    "v_accvgpr_write_b32 a92, v242\n\t"
    "v_lshlrev_b32 v244, 16, v81\n\t"
    "v_and_b32 v245, 0xffff0000, v81\n\t"
    "v_mul_f32 v244, s65, v244\n\t"
    "v_mul_f32 v245, s65, v245\n\t"
    "v_cvt_pk_bf16_f32 v208, v180, v181\n\t"
    "v_add_f32 v250, v250, v176\n\t"
    "v_cvt_pk_bf16_f32 v244, v244, v245\n\t"
    "v_accvgpr_write_b32 a93, v244\n\t"
    "v_lshlrev_b32 v246, 16, v82\n\t"
    "v_and_b32 v247, 0xffff0000, v82\n\t"
    "v_mul_f32 v246, s65, v246\n\t"
    "v_mul_f32 v247, s65, v247\n\t"
    "v_cvt_pk_bf16_f32 v246, v246, v247\n\t"
    "v_accvgpr_write_b32 a94, v246\n\t"
    "v_lshlrev_b32 v248, 16, v83\n\t"
    "v_and_b32 v249, 0xffff0000, v83\n\t"
    "v_mul_f32 v248, s65, v248\n\t"
    "v_mul_f32 v249, s65, v249\n\t"
    "v_cvt_pk_bf16_f32 v248, v248, v249\n\t"
    "v_accvgpr_write_b32 a95, v248\n\t"
    "v_exp_f32 v182, v182\n\t"
    "v_mfma_f32_16x16x32_bf16 v[188:191], a[96:99], a[16:19], 0\n\t"
    "ds_read_b64_tr_b16 a[204:205], v237 offset:8520\n\t"
    "ds_read_b64_tr_b16 a[206:207], v237 offset:9032\n\t"
    "v_mfma_f32_16x16x32_bf16 v[188:191], a[100:103], a[20:23], v[188:191]\n\t"
    "v_exp_f32 v183, v183\n\t"
    "v_mfma_f32_16x16x32_bf16 v[188:191], a[104:107], a[24:27], v[188:191]\n\t"
    "s_waitcnt vmcnt(11)\n\t"
    "v_lshlrev_b32 v242, 16, v84\n\t"
    "v_and_b32 v243, 0xffff0000, v84\n\t"
    "v_mul_f32 v242, s65, v242\n\t"
    "s_add_u32 m0, s56, 16896\n\t"
    "s_add_u32 s61, s59, 18432\n\t"
    "s_and_b32 s61, s61, 0x7ffff\n\t"
    "buffer_load_dwordx4 v239, s[44:47], s61 offen offset:0 lds\n\t"
    "s_add_u32 m0, s56, 21120\n\t"
    "s_nop 0\n\t"
    "buffer_load_dwordx4 v226, s[44:47], s61 offen offset:0 lds\n\t"
    "s_add_u32 m0, s56, 25344\n\t"
    "s_nop 0\n\t"
    "buffer_load_dwordx4 v227, s[44:47], s61 offen offset:0 lds\n\t"
    "s_add_u32 m0, s56, 29568\n\t"
    "s_nop 0\n\t"
    "buffer_load_dwordx4 v3, s[44:47], s61 offen offset:0 lds\n\t"
    "ds_read_b64_tr_b16 a[176:177], v237 offset:128\n\t"
    "ds_read_b64_tr_b16 a[178:179], v237 offset:640\n\t"
    "v_mfma_f32_16x16x32_bf16 v[188:191], a[108:111], a[28:31], v[188:191]\n\t"
    "v_mul_f32 v243, s65, v243\n\t"
    "v_cvt_pk_bf16_f32 v242, v242, v243\n\t"
    "v_accvgpr_write_b32 a32, v242\n\t"
    "v_cvt_pk_bf16_f32 v209, v182, v183\n\t"
    "v_add_f32 v250, v250, v177\n\t"
    "v_mfma_f32_16x16x32_bf16 v[192:195], a[112:115], a[16:19], 0\n\t"
    "v_lshlrev_b32 v244, 16, v85\n\t"
    "v_and_b32 v245, 0xffff0000, v85\n\t"
    "v_mul_f32 v244, s65, v244\n\t"
    "ds_read_b64_tr_b16 a[208:209], v237 offset:8576\n\t"
    "ds_read_b64_tr_b16 a[210:211], v237 offset:9088\n\t"
    "v_mfma_f32_16x16x32_bf16 v[192:195], a[116:119], a[20:23], v[192:195]\n\t"
    "v_mul_f32 v245, s65, v245\n\t"
    "v_cvt_pk_bf16_f32 v244, v244, v245\n\t"
    "v_accvgpr_write_b32 a33, v244\n\t"
    "v_exp_f32 v184, v184\n\t"
    "v_mfma_f32_16x16x32_bf16 v[192:195], a[120:123], a[24:27], v[192:195]\n\t"
    "v_lshlrev_b32 v246, 16, v86\n\t"
    "v_and_b32 v247, 0xffff0000, v86\n\t"
    "v_mul_f32 v246, s65, v246\n\t"
    "ds_read_b64_tr_b16 a[180:181], v237 offset:136\n\t"
    "ds_read_b64_tr_b16 a[182:183], v237 offset:648\n\t"
    "v_mfma_f32_16x16x32_bf16 v[192:195], a[124:127], a[28:31], v[192:195]\n\t"
    "v_mul_f32 v247, s65, v247\n\t"
    "v_cvt_pk_bf16_f32 v246, v246, v247\n\t"
    "v_accvgpr_write_b32 a34, v246\n\t"
    "v_exp_f32 v185, v185\n\t"
    "v_mfma_f32_16x16x32_bf16 v[196:199], a[128:131], a[16:19], 0\n\t"
    "v_lshlrev_b32 v248, 16, v87\n\t"
    "v_and_b32 v249, 0xffff0000, v87\n\t"
    "v_mul_f32 v248, s65, v248\n\t"
    "ds_read_b64_tr_b16 a[212:213], v237 offset:8584\n\t"
    "ds_read_b64_tr_b16 a[214:215], v237 offset:9096\n\t"
    "v_mfma_f32_16x16x32_bf16 v[196:199], a[132:135], a[20:23], v[196:199]\n\t"
    "v_mul_f32 v249, s65, v249\n\t"
    "v_cvt_pk_bf16_f32 v248, v248, v249\n\t"
    "v_accvgpr_write_b32 a35, v248\n\t"
    "v_cvt_pk_bf16_f32 v210, v184, v185\n\t"
    "v_add_f32 v250, v250, v178\n\t"
    "v_mfma_f32_16x16x32_bf16 v[196:199], a[136:139], a[24:27], v[196:199]\n\t"
    "s_waitcnt vmcnt(14)\n\t"
    "v_lshlrev_b32 v242, 16, v88\n\t"
    "v_and_b32 v243, 0xffff0000, v88\n\t"
    "v_mul_f32 v242, s65, v242\n\t"
    "ds_read_b64_tr_b16 a[184:185], v237 offset:192\n\t"
    "ds_read_b64_tr_b16 a[186:187], v237 offset:704\n\t"
    "v_mfma_f32_16x16x32_bf16 v[196:199], a[140:143], a[28:31], v[196:199]\n\t"
    "v_mul_f32 v243, s65, v243\n\t"
    "v_cvt_pk_bf16_f32 v242, v242, v243\n\t"
    "v_accvgpr_write_b32 a36, v242\n\t"
    "v_exp_f32 v186, v186\n\t"
    "v_mfma_f32_16x16x32_bf16 v[200:203], a[144:147], a[16:19], 0\n\t"
    "v_lshlrev_b32 v244, 16, v89\n\t"
    "v_and_b32 v245, 0xffff0000, v89\n\t"
    "v_mul_f32 v244, s65, v244\n\t"
    "v_mul_f32 v245, s65, v245\n\t"
    "ds_read_b64_tr_b16 a[216:217], v237 offset:8640\n\t"
    "ds_read_b64_tr_b16 a[218:219], v237 offset:9152\n\t"
    "v_mfma_f32_16x16x32_bf16 v[200:203], a[148:151], a[20:23], v[200:203]\n\t"
    "v_cvt_pk_bf16_f32 v244, v244, v245\n\t"
    "v_accvgpr_write_b32 a37, v244\n\t"
    "v_lshlrev_b32 v246, 16, v90\n\t"
    "v_exp_f32 v187, v187\n\t"
    "v_mfma_f32_16x16x32_bf16 v[200:203], a[152:155], a[24:27], v[200:203]\n\t"
    "v_and_b32 v247, 0xffff0000, v90\n\t"
    "v_mul_f32 v246, s65, v246\n\t"
    "v_mul_f32 v247, s65, v247\n\t"
    "ds_read_b64_tr_b16 a[188:189], v237 offset:200\n\t"
    "ds_read_b64_tr_b16 a[190:191], v237 offset:712\n\t"
    "v_mfma_f32_16x16x32_bf16 v[200:203], a[156:159], a[28:31], v[200:203]\n\t"
    "v_cvt_pk_bf16_f32 v246, v246, v247\n\t"
    "v_accvgpr_write_b32 a38, v246\n\t"
    "v_lshlrev_b32 v248, 16, v91\n\t"
    "v_cvt_pk_bf16_f32 v211, v186, v187\n\t"
    "v_add_f32 v250, v250, v179\n\t"
    "v_mfma_f32_16x16x32_bf16 v[220:223], a[128:131], a[80:83], 0\n\t"
    "v_and_b32 v249, 0xffff0000, v91\n\t"
    "v_mul_f32 v248, s65, v248\n\t"
    "v_mul_f32 v249, s65, v249\n\t"
    "ds_read_b64_tr_b16 a[220:221], v237 offset:8648\n\t"
    "ds_read_b64_tr_b16 a[222:223], v237 offset:9160\n\t"
    "v_mfma_f32_16x16x32_bf16 v[220:223], a[132:135], a[84:87], v[220:223]\n\t"
    "v_cvt_pk_bf16_f32 v248, v248, v249\n\t"
    "v_accvgpr_write_b32 a39, v248\n\t"
    "s_waitcnt vmcnt(13)\n\t"
    "v_lshlrev_b32 v242, 16, v92\n\t"
    "v_exp_f32 v188, v188\n\t"
    "v_mfma_f32_16x16x32_bf16 v[220:223], a[136:139], a[88:91], v[220:223]\n\t"
    "v_and_b32 v243, 0xffff0000, v92\n\t"
    "v_mul_f32 v242, s65, v242\n\t"
    "v_mul_f32 v243, s65, v243\n\t"
    "v_mfma_f32_16x16x32_bf16 v[220:223], a[140:143], a[92:95], v[220:223]\n\t"
    "s_waitcnt lgkmcnt(4)\n\t"
    "v_cvt_pk_bf16_f32 v242, v242, v243\n\t"
    "v_accvgpr_write_b32 a40, v242\n\t"
    "v_lshlrev_b32 v244, 16, v93\n\t"
    "v_exp_f32 v189, v189\n\t"
    "v_mfma_f32_16x16x32_bf16 v[4:7], a[160:163], v[204:207], 0\n\t"
    "v_and_b32 v245, 0xffff0000, v93\n\t"
    "v_mul_f32 v244, s65, v244\n\t"
    "v_mul_f32 v245, s65, v245\n\t"
    "v_cvt_pk_bf16_f32 v212, v188, v189\n\t"
    "v_add_f32 v250, v250, v180\n\t"
    "v_mfma_f32_16x16x32_bf16 v[4:7], a[192:195], v[208:211], v[4:7]\n\t"
    "v_cvt_pk_bf16_f32 v244, v244, v245\n\t"
    "v_accvgpr_write_b32 a41, v244\n\t"
    "v_lshlrev_b32 v246, 16, v94\n\t"
    "v_exp_f32 v190, v190\n\t"
    "v_mfma_f32_16x16x32_bf16 v[8:11], a[164:167], v[204:207], 0\n\t"
    "v_and_b32 v247, 0xffff0000, v94\n\t"
    "v_mul_f32 v246, s65, v246\n\t"
    "v_mul_f32 v247, s65, v247\n\t"
    "v_cvt_pk_bf16_f32 v246, v246, v247\n\t"
    "v_exp_f32 v191, v191\n\t"
    "v_mfma_f32_16x16x32_bf16 v[8:11], a[196:199], v[208:211], v[8:11]\n\t"
    "v_accvgpr_write_b32 a42, v246\n\t"
    "v_lshlrev_b32 v248, 16, v95\n\t"
    "v_and_b32 v249, 0xffff0000, v95\n\t"
    "v_cvt_pk_bf16_f32 v213, v190, v191\n\t"
    "v_add_f32 v250, v250, v181\n\t"
    "v_mfma_f32_16x16x32_bf16 v[12:15], a[168:171], v[204:207], 0\n\t"
    "v_mul_f32 v248, s65, v248\n\t"
    "v_mul_f32 v249, s65, v249\n\t"
    "v_cvt_pk_bf16_f32 v248, v248, v249\n\t"
    "v_exp_f32 v192, v192\n\t"
    "v_mfma_f32_16x16x32_bf16 v[12:15], a[200:203], v[208:211], v[12:15]\n\t"
    "v_accvgpr_write_b32 a43, v248\n\t"
    "s_waitcnt vmcnt(12)\n\t"
    "v_lshlrev_b32 v242, 16, v96\n\t"
    "v_and_b32 v243, 0xffff0000, v96\n\t"
    "v_exp_f32 v193, v193\n\t"
    "v_mfma_f32_16x16x32_bf16 v[16:19], a[172:175], v[204:207], 0\n\t"
    "v_mul_f32 v242, s65, v242\n\t"
    "v_mul_f32 v243, s65, v243\n\t"
    "v_cvt_pk_bf16_f32 v242, v242, v243\n\t"
    "v_cvt_pk_bf16_f32 v214, v192, v193\n\t"
    "v_add_f32 v250, v250, v182\n\t"
    "v_mfma_f32_16x16x32_bf16 v[16:19], a[204:207], v[208:211], v[16:19]\n\t"
    "v_accvgpr_write_b32 a44, v242\n\t"
    "v_lshlrev_b32 v244, 16, v97\n\t"
    "v_and_b32 v245, 0xffff0000, v97\n\t"
    "v_exp_f32 v194, v194\n\t"
    "v_mfma_f32_16x16x32_bf16 v[20:23], a[176:179], v[204:207], 0\n\t"
    "v_mul_f32 v244, s65, v244\n\t"
    "v_mul_f32 v245, s65, v245\n\t"
    "v_cvt_pk_bf16_f32 v244, v244, v245\n\t"
    "v_exp_f32 v195, v195\n\t"
    "v_mfma_f32_16x16x32_bf16 v[20:23], a[208:211], v[208:211], v[20:23]\n\t"
    "v_accvgpr_write_b32 a45, v244\n\t"
    "v_lshlrev_b32 v246, 16, v98\n\t"
    "v_and_b32 v247, 0xffff0000, v98\n\t"
    "v_cvt_pk_bf16_f32 v215, v194, v195\n\t"
    "v_add_f32 v250, v250, v183\n\t"
    "v_mfma_f32_16x16x32_bf16 v[24:27], a[180:183], v[204:207], 0\n\t"
    "v_mul_f32 v246, s65, v246\n\t"
    "v_mul_f32 v247, s65, v247\n\t"
    "v_cvt_pk_bf16_f32 v246, v246, v247\n\t"
    "v_mfma_f32_16x16x32_bf16 v[24:27], a[212:215], v[208:211], v[24:27]\n\t"
    "v_accvgpr_write_b32 a46, v246\n\t"
    "v_lshlrev_b32 v248, 16, v99\n\t"
    "v_and_b32 v249, 0xffff0000, v99\n\t"
    "v_exp_f32 v196, v196\n\t"
    "v_mfma_f32_16x16x32_bf16 v[28:31], a[184:187], v[204:207], 0\n\t"
    "v_mul_f32 v248, s65, v248\n\t"
    "v_mul_f32 v249, s65, v249\n\t"
    "v_cvt_pk_bf16_f32 v248, v248, v249\n\t"
    "v_accvgpr_write_b32 a47, v248\n\t"
    "v_exp_f32 v197, v197\n\t"
    "v_mfma_f32_16x16x32_bf16 v[28:31], a[216:219], v[208:211], v[28:31]\n\t"
    "s_waitcnt lgkmcnt(0)\n\t"
    "v_cvt_pk_bf16_f32 v216, v196, v197\n\t"
    "v_add_f32 v250, v250, v184\n\t"
    "v_mfma_f32_16x16x32_bf16 v[32:35], a[188:191], v[204:207], 0\n\t"
    "v_exp_f32 v198, v198\n\t"
    "v_mfma_f32_16x16x32_bf16 v[32:35], a[220:223], v[208:211], v[32:35]\n\t"
    "v_exp_f32 v199, v199\n\t"
    "v_mfma_f32_16x16x32_bf16 v[172:175], a[96:99], a[32:35], 0\n\t"
    "v_cvt_pk_bf16_f32 v217, v198, v199\n\t"
    "v_add_f32 v250, v250, v185\n\t"
    "v_mfma_f32_16x16x32_bf16 v[172:175], a[100:103], a[36:39], v[172:175]\n\t"
    "s_waitcnt vmcnt(11)\n\t"
    "v_lshlrev_b32 v242, 16, v100\n\t"
    "v_and_b32 v243, 0xffff0000, v100\n\t"
    "v_mul_f32 v242, s65, v242\n\t"
    "s_mov_b32 s64, 57344\n\t"
    "buffer_load_dwordx4 v[116:119], v240, s[36:39], s64 offen offset:0\n\t"
    "buffer_load_dwordx4 v[120:123], v240, s[36:39], s64 offen offset:64\n\t"
    "buffer_load_dwordx4 v[124:127], v240, s[36:39], s64 offen offset:128\n\t"
    "buffer_load_dwordx4 v[128:131], v240, s[36:39], s64 offen offset:192\n\t"
    "v_exp_f32 v200, v200\n\t"
    "v_mfma_f32_16x16x32_bf16 v[172:175], a[104:107], a[40:43], v[172:175]\n\t"
    "v_mul_f32 v243, s65, v243\n\t"
    "v_cvt_pk_bf16_f32 v242, v242, v243\n\t"
    "v_accvgpr_write_b32 a48, v242\n\t"
    "v_exp_f32 v220, v220\n\t"
    "v_mfma_f32_16x16x32_bf16 v[172:175], a[108:111], a[44:47], v[172:175]\n\t"
    "v_lshlrev_b32 v244, 16, v101\n\t"
    "v_and_b32 v245, 0xffff0000, v101\n\t"
    "v_mul_f32 v244, s65, v244\n\t"
    "v_exp_f32 v201, v201\n\t"
    "v_mfma_f32_16x16x32_bf16 v[176:179], a[112:115], a[32:35], 0\n\t"
    "v_mul_f32 v245, s65, v245\n\t"
    "v_cvt_pk_bf16_f32 v244, v244, v245\n\t"
    "v_accvgpr_write_b32 a49, v244\n\t"
    "v_exp_f32 v221, v221\n\t"
    "v_mfma_f32_16x16x32_bf16 v[176:179], a[116:119], a[36:39], v[176:179]\n\t"
    "v_lshlrev_b32 v246, 16, v102\n\t"
    "v_and_b32 v247, 0xffff0000, v102\n\t"
    "v_mul_f32 v246, s65, v246\n\t"
    "v_add_f32 v250, v250, v186\n\t"
    "v_add_f32 v250, v250, v187\n\t"
    "v_mfma_f32_16x16x32_bf16 v[176:179], a[120:123], a[40:43], v[176:179]\n\t"
    "v_mul_f32 v247, s65, v247\n\t"
    "v_cvt_pk_bf16_f32 v246, v246, v247\n\t"
    "v_accvgpr_write_b32 a50, v246\n\t"
    "v_cvt_pk_bf16_f32 v218, v200, v201\n\t"
    "v_cvt_pk_bf16_f32 v224, v220, v221\n\t"
    "v_mfma_f32_16x16x32_bf16 v[176:179], a[124:127], a[44:47], v[176:179]\n\t"
    "v_lshlrev_b32 v248, 16, v103\n\t"
    "v_and_b32 v249, 0xffff0000, v103\n\t"
    "v_mul_f32 v248, s65, v248\n\t"
    "v_exp_f32 v202, v202\n\t"
    "v_mfma_f32_16x16x32_bf16 v[180:183], a[128:131], a[32:35], 0\n\t"
    "v_mul_f32 v249, s65, v249\n\t"
    "v_cvt_pk_bf16_f32 v248, v248, v249\n\t"
    "v_accvgpr_write_b32 a51, v248\n\t"
    "v_exp_f32 v222, v222\n\t"
    "v_mfma_f32_16x16x32_bf16 v[180:183], a[132:135], a[36:39], v[180:183]\n\t"
    "s_waitcnt vmcnt(14)\n\t"
    "v_lshlrev_b32 v242, 16, v104\n\t"
    "v_and_b32 v243, 0xffff0000, v104\n\t"
    "v_mul_f32 v242, s65, v242\n\t"
    "v_exp_f32 v203, v203\n\t"
    "v_mfma_f32_16x16x32_bf16 v[180:183], a[136:139], a[40:43], v[180:183]\n\t"
    "v_mul_f32 v243, s65, v243\n\t"
    "v_cvt_pk_bf16_f32 v242, v242, v243\n\t"
    "v_accvgpr_write_b32 a52, v242\n\t"
    "v_mfma_f32_16x16x32_bf16 v[180:183], a[140:143], a[44:47], v[180:183]\n\t"
    "v_lshlrev_b32 v244, 16, v105\n\t"
    "v_and_b32 v245, 0xffff0000, v105\n\t"
    "v_mul_f32 v244, s65, v244\n\t"
    "v_mul_f32 v245, s65, v245\n\t"
    "v_exp_f32 v223, v223\n\t"
    "v_mfma_f32_16x16x32_bf16 v[184:187], a[144:147], a[32:35], 0\n\t"
    "v_cvt_pk_bf16_f32 v244, v244, v245\n\t"
    "v_accvgpr_write_b32 a53, v244\n\t"
    "v_lshlrev_b32 v246, 16, v106\n\t"
    "v_cvt_pk_bf16_f32 v225, v222, v223\n\t"
    "v_cvt_pk_bf16_f32 v219, v202, v203\n\t"
    "v_mfma_f32_16x16x32_bf16 v[184:187], a[148:151], a[36:39], v[184:187]\n\t"
    "v_and_b32 v247, 0xffff0000, v106\n\t"
    "v_mul_f32 v246, s65, v246\n\t"
    "v_mul_f32 v247, s65, v247\n\t"
    "ds_write_b64 v238, v[224:225] offset:1024\n\t"
    "v_mfma_f32_16x16x32_bf16 v[184:187], a[152:155], a[40:43], v[184:187]\n\t"
    "v_cvt_pk_bf16_f32 v246, v246, v247\n\t"
    "v_accvgpr_write_b32 a54, v246\n\t"
    "v_lshlrev_b32 v248, 16, v107\n\t"
    "v_exp_f32 v172, v172\n\t"
    "v_mfma_f32_16x16x32_bf16 v[184:187], a[156:159], a[44:47], v[184:187]\n\t"
    "v_and_b32 v249, 0xffff0000, v107\n\t"
    "v_mul_f32 v248, s65, v248\n\t"
    "v_mul_f32 v249, s65, v249\n\t"
    "v_exp_f32 v173, v173\n\t"
    "v_mfma_f32_16x16x16_bf16 a[244:247], a[248:249], v[224:225], 0\n\t"
    "v_cvt_pk_bf16_f32 v248, v248, v249\n\t"
    "v_accvgpr_write_b32 a55, v248\n\t"
    "s_waitcnt vmcnt(13)\n\t"
    "v_lshlrev_b32 v242, 16, v108\n\t"
    "v_cvt_pk_bf16_f32 v204, v172, v173\n\t"
    "v_add_f32 v251, v251, v196\n\t"
    "v_mfma_f32_16x16x32_bf16 v[36:39], a[160:163], v[212:215], 0\n\t"
    "v_and_b32 v243, 0xffff0000, v108\n\t"
    "v_mul_f32 v242, s65, v242\n\t"
    "v_mul_f32 v243, s65, v243\n\t"
    "v_exp_f32 v174, v174\n\t"
    "v_mfma_f32_16x16x32_bf16 v[36:39], a[192:195], v[216:219], v[36:39]\n\t"
    "v_cvt_pk_bf16_f32 v242, v242, v243\n\t"
    "v_accvgpr_write_b32 a56, v242\n\t"
    "v_lshlrev_b32 v244, 16, v109\n\t"
    "v_exp_f32 v175, v175\n\t"
    "v_mfma_f32_16x16x32_bf16 v[40:43], a[164:167], v[212:215], 0\n\t"
    "v_and_b32 v245, 0xffff0000, v109\n\t"
    "v_mul_f32 v244, s65, v244\n\t"
    "v_mul_f32 v245, s65, v245\n\t"
    "v_cvt_pk_bf16_f32 v205, v174, v175\n\t"
    "v_add_f32 v251, v251, v197\n\t"
    "v_mfma_f32_16x16x32_bf16 v[40:43], a[196:199], v[216:219], v[40:43]\n\t"
    "v_cvt_pk_bf16_f32 v244, v244, v245\n\t"
    "v_accvgpr_write_b32 a57, v244\n\t"
    "v_lshlrev_b32 v246, 16, v110\n\t"
    "v_exp_f32 v176, v176\n\t"
    "v_mfma_f32_16x16x32_bf16 v[44:47], a[168:171], v[212:215], 0\n\t"
    "v_and_b32 v247, 0xffff0000, v110\n\t"
    "v_mul_f32 v246, s65, v246\n\t"
    "v_mul_f32 v247, s65, v247\n\t"
    "v_cvt_pk_bf16_f32 v246, v246, v247\n\t"
    "v_exp_f32 v177, v177\n\t"
    "v_mfma_f32_16x16x32_bf16 v[44:47], a[200:203], v[216:219], v[44:47]\n\t"
    "v_accvgpr_write_b32 a58, v246\n\t"
    "v_lshlrev_b32 v248, 16, v111\n\t"
    "v_and_b32 v249, 0xffff0000, v111\n\t"
    "v_cvt_pk_bf16_f32 v206, v176, v177\n\t"
    "v_add_f32 v251, v251, v198\n\t"
    "v_mfma_f32_16x16x32_bf16 v[48:51], a[172:175], v[212:215], 0\n\t"
    "v_mul_f32 v248, s65, v248\n\t"
    "v_mul_f32 v249, s65, v249\n\t"
    "v_cvt_pk_bf16_f32 v248, v248, v249\n\t"
    "v_exp_f32 v178, v178\n\t"
    "v_mfma_f32_16x16x32_bf16 v[48:51], a[204:207], v[216:219], v[48:51]\n\t"
    "v_accvgpr_write_b32 a59, v248\n\t"
    "s_waitcnt vmcnt(12)\n\t"
    "v_lshlrev_b32 v242, 16, v112\n\t"
    "v_and_b32 v243, 0xffff0000, v112\n\t"
    "v_exp_f32 v179, v179\n\t"
    "v_mfma_f32_16x16x32_bf16 v[52:55], a[176:179], v[212:215], 0\n\t"
    "v_mul_f32 v242, s65, v242\n\t"
    "v_mul_f32 v243, s65, v243\n\t"
    "v_cvt_pk_bf16_f32 v242, v242, v243\n\t"
    "v_cvt_pk_bf16_f32 v207, v178, v179\n\t"
    "v_add_f32 v251, v251, v199\n\t"
    "v_mfma_f32_16x16x32_bf16 v[52:55], a[208:211], v[216:219], v[52:55]\n\t"
    "v_accvgpr_write_b32 a60, v242\n\t"
    "v_lshlrev_b32 v244, 16, v113\n\t"
    "v_and_b32 v245, 0xffff0000, v113\n\t"
    "v_mfma_f32_16x16x32_bf16 v[56:59], a[180:183], v[212:215], 0\n\t"
    "v_mul_f32 v244, s65, v244\n\t"
    "v_mul_f32 v245, s65, v245\n\t"
    "v_cvt_pk_bf16_f32 v244, v244, v245\n\t"
    "v_exp_f32 v180, v180\n\t"
    "v_mfma_f32_16x16x32_bf16 v[56:59], a[212:215], v[216:219], v[56:59]\n\t"
    "v_accvgpr_write_b32 a61, v244\n\t"
    "v_lshlrev_b32 v246, 16, v114\n\t"
    "v_and_b32 v247, 0xffff0000, v114\n\t"
    "v_exp_f32 v181, v181\n\t"
    "v_mfma_f32_16x16x32_bf16 v[60:63], a[184:187], v[212:215], 0\n\t"
    "v_mul_f32 v246, s65, v246\n\t"
    "v_mul_f32 v247, s65, v247\n\t"
    "v_cvt_pk_bf16_f32 v246, v246, v247\n\t"
    "v_cvt_pk_bf16_f32 v208, v180, v181\n\t"
    "v_add_f32 v251, v251, v200\n\t"
    "v_mfma_f32_16x16x32_bf16 v[60:63], a[216:219], v[216:219], v[60:63]\n\t"
    "v_accvgpr_write_b32 a62, v246\n\t"
    "v_lshlrev_b32 v248, 16, v115\n\t"
    "v_and_b32 v249, 0xffff0000, v115\n\t"
    "v_exp_f32 v182, v182\n\t"
    "v_mfma_f32_16x16x32_bf16 v[64:67], a[188:191], v[212:215], 0\n\t"
    "v_mul_f32 v248, s65, v248\n\t"
    "v_mul_f32 v249, s65, v249\n\t"
    "v_cvt_pk_bf16_f32 v248, v248, v249\n\t"
    "v_accvgpr_write_b32 a63, v248\n\t"
    "v_exp_f32 v183, v183\n\t"
    "v_mfma_f32_16x16x32_bf16 v[64:67], a[220:223], v[216:219], v[64:67]\n\t"
    "v_cvt_pk_bf16_f32 v209, v182, v183\n\t"
    "v_add_f32 v251, v251, v201\n\t"
    "v_mfma_f32_16x16x32_bf16 a[228:231], a[248:251], v[212:215], 0\n\t"
    "v_exp_f32 v184, v184\n\t"
    "v_mfma_f32_16x16x32_bf16 v[188:191], a[96:99], a[48:51], 0\n\t"
    "v_exp_f32 v185, v185\n\t"
    "v_mfma_f32_16x16x32_bf16 v[188:191], a[100:103], a[52:55], v[188:191]\n\t"
    "s_waitcnt vmcnt(3)\n\t"
    "v_lshlrev_b32 v242, 16, v116\n\t"
    "v_and_b32 v243, 0xffff0000, v116\n\t"
    "v_mul_f32 v242, s65, v242\n\t"
    "v_cvt_pk_bf16_f32 v210, v184, v185\n\t"
    "v_add_f32 v251, v251, v202\n\t"
    "v_mfma_f32_16x16x32_bf16 v[188:191], a[104:107], a[56:59], v[188:191]\n\t"
    "v_mul_f32 v243, s65, v243\n\t"
    "v_cvt_pk_bf16_f32 v242, v242, v243\n\t"
    "v_accvgpr_write_b32 a64, v242\n\t"
    "s_add_u32 m0, s55, 33792\n\t"
    "s_add_u32 s61, s59, 34816\n\t"
    "s_and_b32 s61, s61, 0x7ffff\n\t"
    "buffer_load_dwordx4 v239, s[40:43], s61 offen offset:0 nt lds\n\t"
    "s_add_u32 m0, s55, 38016\n\t"
    "s_nop 0\n\t"
    "buffer_load_dwordx4 v226, s[40:43], s61 offen offset:0 nt lds\n\t"
    "s_add_u32 m0, s55, 42240\n\t"
    "s_nop 0\n\t"
    "buffer_load_dwordx4 v227, s[40:43], s61 offen offset:0 nt lds\n\t"
    "s_add_u32 m0, s55, 46464\n\t"
    "s_nop 0\n\t"
    "buffer_load_dwordx4 v3, s[40:43], s61 offen offset:0 nt lds\n\t"
    "v_exp_f32 v186, v186\n\t"
    "v_mfma_f32_16x16x32_bf16 v[188:191], a[108:111], a[60:63], v[188:191]\n\t"
    "v_lshlrev_b32 v244, 16, v117\n\t"
    "v_and_b32 v245, 0xffff0000, v117\n\t"
    "v_mul_f32 v244, s65, v244\n\t"
    "v_add_f32 v251, v251, v203\n\t"
    "v_mfma_f32_16x16x32_bf16 v[192:195], a[112:115], a[48:51], 0\n\t"
    "v_mul_f32 v245, s65, v245\n\t"
    "v_cvt_pk_bf16_f32 v244, v244, v245\n\t"
    "v_accvgpr_write_b32 a65, v244\n\t"
    "v_exp_f32 v187, v187\n\t"
    "v_mfma_f32_16x16x32_bf16 v[192:195], a[116:119], a[52:55], v[192:195]\n\t"
    "v_lshlrev_b32 v246, 16, v118\n\t"
    "v_and_b32 v247, 0xffff0000, v118\n\t"
    "v_mul_f32 v246, s65, v246\n\t"
    "v_mul_f32 v247, s65, v247\n\t"
    "v_cvt_pk_bf16_f32 v211, v186, v187\n\t"
    "v_mfma_f32_16x16x32_bf16 v[192:195], a[120:123], a[56:59], v[192:195]\n\t"
    "v_cvt_pk_bf16_f32 v246, v246, v247\n\t"
    "v_accvgpr_write_b32 a66, v246\n\t"
    "v_lshlrev_b32 v248, 16, v119\n\t"
    "v_add_f32 v252, v252, v180\n\t"
    "v_add_f32 v252, v252, v181\n\t"
    "v_mfma_f32_16x16x32_bf16 v[192:195], a[124:127], a[60:63], v[192:195]\n\t"
    "v_and_b32 v249, 0xffff0000, v119\n\t"
    "v_mul_f32 v248, s65, v248\n\t"
    "v_mul_f32 v249, s65, v249\n\t"
    "v_add_f32 v252, v252, v182\n\t"
    "v_add_f32 v252, v252, v183\n\t"
    "v_mfma_f32_16x16x32_bf16 v[196:199], a[128:131], a[48:51], 0\n\t"
    "v_cvt_pk_bf16_f32 v248, v248, v249\n\t"
    "v_accvgpr_write_b32 a67, v248\n\t"
    "s_waitcnt vmcnt(6)\n\t"
    "v_lshlrev_b32 v242, 16, v120\n\t"
    "v_mfma_f32_16x16x32_bf16 v[196:199], a[132:135], a[52:55], v[196:199]\n\t"
    "v_and_b32 v243, 0xffff0000, v120\n\t"
    "v_mul_f32 v242, s65, v242\n\t"
    "v_mul_f32 v243, s65, v243\n\t"
    "s_add_u32 m0, s56, 33792\n\t"
    "s_add_u32 s61, s59, 34816\n\t"
    "s_and_b32 s61, s61, 0x7ffff\n\t"
    "buffer_load_dwordx4 v239, s[44:47], s61 offen offset:0 lds\n\t"
    "s_add_u32 m0, s56, 38016\n\t"
    "s_nop 0\n\t"
    "buffer_load_dwordx4 v226, s[44:47], s61 offen offset:0 lds\n\t"
    "s_add_u32 m0, s56, 42240\n\t"
    "s_nop 0\n\t"
    "buffer_load_dwordx4 v227, s[44:47], s61 offen offset:0 lds\n\t"
    "s_add_u32 m0, s56, 46464\n\t"
    "s_nop 0\n\t"
    "buffer_load_dwordx4 v3, s[44:47], s61 offen offset:0 lds\n\t"
    "v_add_f32 v252, v252, v184\n\t"
    "v_add_f32 v252, v252, v185\n\t"
    "v_mfma_f32_16x16x32_bf16 v[196:199], a[136:139], a[56:59], v[196:199]\n\t"
    "v_cvt_pk_bf16_f32 v242, v242, v243\n\t"
    "v_accvgpr_write_b32 a68, v242\n\t"
    "v_lshlrev_b32 v244, 16, v121\n\t"
    "v_and_b32 v245, 0xffff0000, v121\n\t"
    "v_add_f32 v252, v252, v186\n\t"
    "v_add_f32 v252, v252, v187\n\t"
    "v_mfma_f32_16x16x32_bf16 v[196:199], a[140:143], a[60:63], v[196:199]\n\t"
    "v_mul_f32 v244, s65, v244\n\t"
    "v_mul_f32 v245, s65, v245\n\t"
    "v_cvt_pk_bf16_f32 v244, v244, v245\n\t"
    "v_exp_f32 v188, v188\n\t"
    "v_mfma_f32_16x16x32_bf16 v[200:203], a[144:147], a[48:51], 0\n\t"
    "v_accvgpr_write_b32 a69, v244\n\t"
    "v_lshlrev_b32 v246, 16, v122\n\t"
    "v_and_b32 v247, 0xffff0000, v122\n\t"
    "v_exp_f32 v189, v189\n\t"
    "v_mfma_f32_16x16x32_bf16 v[200:203], a[148:151], a[52:55], v[200:203]\n\t"
    "v_mul_f32 v246, s65, v246\n\t"
    "v_mul_f32 v247, s65, v247\n\t"
    "v_cvt_pk_bf16_f32 v246, v246, v247\n\t"
    "v_cvt_pk_bf16_f32 v212, v188, v189\n\t"
    "v_mfma_f32_16x16x32_bf16 v[200:203], a[152:155], a[56:59], v[200:203]\n\t"
    "v_accvgpr_write_b32 a70, v246\n\t"
    "v_lshlrev_b32 v248, 16, v123\n\t"
    "v_and_b32 v249, 0xffff0000, v123\n\t"
    "v_exp_f32 v190, v190\n\t"
    "v_mfma_f32_16x16x32_bf16 v[200:203], a[156:159], a[60:63], v[200:203]\n\t"
    "v_mul_f32 v248, s65, v248\n\t"
    "v_mul_f32 v249, s65, v249\n\t"
    "v_cvt_pk_bf16_f32 v248, v248, v249\n\t"
    "v_accvgpr_write_b32 a71, v248\n\t"
    "v_exp_f32 v191, v191\n\t"
    "v_mfma_f32_16x16x32_bf16 v[68:71], a[160:163], v[204:207], 0\n\t"
    "s_waitcnt vmcnt(9)\n\t"
    "v_lshlrev_b32 v242, 16, v124\n\t"
    "v_and_b32 v243, 0xffff0000, v124\n\t"
    "v_mul_f32 v242, s65, v242\n\t"
    "v_cvt_pk_bf16_f32 v213, v190, v191\n\t"
    "v_mfma_f32_16x16x32_bf16 v[68:71], a[192:195], v[208:211], v[68:71]\n\t"
    "v_mul_f32 v243, s65, v243\n\t"
    "v_cvt_pk_bf16_f32 v242, v242, v243\n\t"
    "v_accvgpr_write_b32 a72, v242\n\t"
    "v_exp_f32 v192, v192\n\t"
    "v_mfma_f32_16x16x32_bf16 v[72:75], a[164:167], v[204:207], 0\n\t"
    "v_lshlrev_b32 v244, 16, v125\n\t"
    "v_and_b32 v245, 0xffff0000, v125\n\t"
    "v_mul_f32 v244, s65, v244\n\t"
    "v_exp_f32 v193, v193\n\t"
    "v_mfma_f32_16x16x32_bf16 v[72:75], a[196:199], v[208:211], v[72:75]\n\t"
    "v_mul_f32 v245, s65, v245\n\t"
    "v_cvt_pk_bf16_f32 v244, v244, v245\n\t"
    "v_accvgpr_write_b32 a73, v244\n\t"
    "v_cvt_pk_bf16_f32 v214, v192, v193\n\t"
    "v_mfma_f32_16x16x32_bf16 v[76:79], a[168:171], v[204:207], 0\n\t"
    "v_lshlrev_b32 v246, 16, v126\n\t"
    "v_and_b32 v247, 0xffff0000, v126\n\t"
    "v_mul_f32 v246, s65, v246\n\t"
    "v_mul_f32 v247, s65, v247\n\t"
    "v_exp_f32 v194, v194\n\t"
    "v_mfma_f32_16x16x32_bf16 v[76:79], a[200:203], v[208:211], v[76:79]\n\t"
    "v_cvt_pk_bf16_f32 v246, v246, v247\n\t"
    "v_accvgpr_write_b32 a74, v246\n\t"
    "v_lshlrev_b32 v248, 16, v127\n\t"
    "v_exp_f32 v195, v195\n\t"
    "v_mfma_f32_16x16x32_bf16 v[80:83], a[172:175], v[204:207], 0\n\t"
    "v_and_b32 v249, 0xffff0000, v127\n\t"
    "v_mul_f32 v248, s65, v248\n\t"
    "v_mul_f32 v249, s65, v249\n\t"
    "v_cvt_pk_bf16_f32 v215, v194, v195\n\t"
    "v_mfma_f32_16x16x32_bf16 v[80:83], a[204:207], v[208:211], v[80:83]\n\t"
    "v_cvt_pk_bf16_f32 v248, v248, v249\n\t"
    "v_accvgpr_write_b32 a75, v248\n\t"
    "s_waitcnt vmcnt(8)\n\t"
    "v_lshlrev_b32 v242, 16, v128\n\t"
    "v_exp_f32 v196, v196\n\t"
    "v_mfma_f32_16x16x32_bf16 v[84:87], a[176:179], v[204:207], 0\n\t"
    "v_and_b32 v243, 0xffff0000, v128\n\t"
    "v_mul_f32 v242, s65, v242\n\t"
    "v_mul_f32 v243, s65, v243\n\t"
    "v_exp_f32 v197, v197\n\t"
    "v_mfma_f32_16x16x32_bf16 v[84:87], a[208:211], v[208:211], v[84:87]\n\t"
    "v_cvt_pk_bf16_f32 v242, v242, v243\n\t"
    "v_accvgpr_write_b32 a76, v242\n\t"
    "v_lshlrev_b32 v244, 16, v129\n\t"
    "v_and_b32 v245, 0xffff0000, v129\n\t"
    "v_cvt_pk_bf16_f32 v216, v196, v197\n\t"
    "v_add_f32 v253, v253, v196\n\t"
    "v_mfma_f32_16x16x32_bf16 v[88:91], a[180:183], v[204:207], 0\n\t"
    "v_mul_f32 v244, s65, v244\n\t"
    "v_mul_f32 v245, s65, v245\n\t"
    "v_cvt_pk_bf16_f32 v244, v244, v245\n\t"
    "v_exp_f32 v198, v198\n\t"
    "v_mfma_f32_16x16x32_bf16 v[88:91], a[212:215], v[208:211], v[88:91]\n\t"
    "v_accvgpr_write_b32 a77, v244\n\t"
    "v_lshlrev_b32 v246, 16, v130\n\t"
    "v_and_b32 v247, 0xffff0000, v130\n\t"
    "v_exp_f32 v199, v199\n\t"
    "v_mfma_f32_16x16x32_bf16 v[92:95], a[184:187], v[204:207], 0\n\t"
    "v_mul_f32 v246, s65, v246\n\t"
    "v_mul_f32 v247, s65, v247\n\t"
    "v_cvt_pk_bf16_f32 v246, v246, v247\n\t"
    "v_cvt_pk_bf16_f32 v217, v198, v199\n\t"
    "v_add_f32 v253, v253, v197\n\t"
    "v_mfma_f32_16x16x32_bf16 v[92:95], a[216:219], v[208:211], v[92:95]\n\t"
    "v_accvgpr_write_b32 a78, v246\n\t"
    "v_lshlrev_b32 v248, 16, v131\n\t"
    "v_and_b32 v249, 0xffff0000, v131\n\t"
    "v_exp_f32 v200, v200\n\t"
    "v_mfma_f32_16x16x32_bf16 v[96:99], a[188:191], v[204:207], 0\n\t"
    "v_mul_f32 v248, s65, v248\n\t"
    "v_mul_f32 v249, s65, v249\n\t"
    "v_cvt_pk_bf16_f32 v248, v248, v249\n\t"
    "v_accvgpr_write_b32 a79, v248\n\t"
    "v_exp_f32 v201, v201\n\t"
    "v_mfma_f32_16x16x32_bf16 v[96:99], a[220:223], v[208:211], v[96:99]\n\t"
    "s_add_u32 s54, s59, 67584\n\t"
    "s_and_b32 s54, s54, 0x7ffff\n\t"
    "s_mov_b32 s57, 33792\n\t"
    "v_cvt_pk_bf16_f32 v218, v200, v201\n\t"
    "v_add_f32 v253, v253, v198\n\t"
    "v_mfma_f32_16x16x32_bf16 a[232:235], a[248:251], v[204:207], 0\n\t"
    "s_waitcnt lgkmcnt(0)\n\t"
    "s_barrier\n\t"
    "v_mfma_f32_16x16x32_bf16 v[172:175], a[96:99], a[64:67], 0\n\t"
    "ds_read_b128 a[96:99], v236 offset:16896\n\t"
    "v_add_f32 v253, v253, v199\n\t"
    "v_mfma_f32_16x16x32_bf16 v[172:175], a[100:103], a[68:71], v[172:175]\n\t"
    "v_exp_f32 v202, v202\n\t"
    "v_mfma_f32_16x16x32_bf16 v[172:175], a[104:107], a[72:75], v[172:175]\n\t"
    "s_add_u32 m0, s55, 50688\n\t"
    "s_add_u32 s61, s59, 51200\n\t"
    "s_and_b32 s61, s61, 0x7ffff\n\t"
    "buffer_load_dwordx4 v239, s[40:43], s61 offen offset:0 nt lds\n\t"
    "s_add_u32 m0, s55, 54912\n\t"
    "s_nop 0\n\t"
    "buffer_load_dwordx4 v226, s[40:43], s61 offen offset:0 nt lds\n\t"
    "s_add_u32 m0, s55, 59136\n\t"
    "s_nop 0\n\t"
    "buffer_load_dwordx4 v227, s[40:43], s61 offen offset:0 nt lds\n\t"
    "s_add_u32 m0, s55, 63360\n\t"
    "s_nop 0\n\t"
    "buffer_load_dwordx4 v3, s[40:43], s61 offen offset:0 nt lds\n\t"
    "ds_read_b128 a[100:103], v236 offset:16960\n\t"
    "ds_read_b128 a[104:107], v236 offset:17024\n\t"
    "v_mfma_f32_16x16x32_bf16 v[172:175], a[108:111], a[76:79], v[172:175]\n\t"
    "v_exp_f32 v203, v203\n\t"
    "v_mfma_f32_16x16x32_bf16 v[176:179], a[112:115], a[64:67], 0\n\t"
    "ds_read_b128 a[108:111], v236 offset:17088\n\t"
    "ds_read_b128 a[112:115], v236 offset:17408\n\t"
    "v_mfma_f32_16x16x32_bf16 v[176:179], a[116:119], a[68:71], v[176:179]\n\t"
    "v_cvt_pk_bf16_f32 v219, v202, v203\n\t"
    "v_add_f32 v253, v253, v200\n\t"
    "v_mfma_f32_16x16x32_bf16 v[176:179], a[120:123], a[72:75], v[176:179]\n\t"
    "ds_read_b128 a[116:119], v236 offset:17472\n\t"
    "ds_read_b128 a[120:123], v236 offset:17536\n\t"
    "v_mfma_f32_16x16x32_bf16 v[176:179], a[124:127], a[76:79], v[176:179]\n\t"
    "v_add_f32 v253, v253, v201\n\t"
    "v_add_f32 v253, v253, v202\n\t"
    "v_mfma_f32_16x16x32_bf16 v[180:183], a[128:131], a[64:67], 0\n\t"
    "ds_read_b128 a[124:127], v236 offset:17600\n\t"
    "v_add_f32 v253, v253, v203\n\t"
    "v_mfma_f32_16x16x32_bf16 v[180:183], a[132:135], a[68:71], v[180:183]\n\t"
    "ds_read_b128 a[128:131], v236 offset:25344\n\t"
    "v_mfma_f32_16x16x32_bf16 v[180:183], a[136:139], a[72:75], v[180:183]\n\t"
    "ds_read_b128 a[132:135], v236 offset:25408\n\t"
    "v_mfma_f32_16x16x32_bf16 v[180:183], a[140:143], a[76:79], v[180:183]\n\t"
    "v_exp_f32 v172, v172\n\t"
    "v_mfma_f32_16x16x32_bf16 v[184:187], a[144:147], a[64:67], 0\n\t"
    "ds_read_b128 a[136:139], v236 offset:25472\n\t"
    "ds_read_b128 a[140:143], v236 offset:25536\n\t"
    "v_mfma_f32_16x16x32_bf16 v[184:187], a[148:151], a[68:71], v[184:187]\n\t"
    "v_exp_f32 v173, v173\n\t"
    "v_mfma_f32_16x16x32_bf16 v[184:187], a[152:155], a[72:75], v[184:187]\n\t"
    "ds_read_b128 a[144:147], v236 offset:25856\n\t"
    "ds_read_b128 a[148:151], v236 offset:25920\n\t"
    "v_mfma_f32_16x16x32_bf16 v[184:187], a[156:159], a[76:79], v[184:187]\n\t"
    "s_add_u32 m0, s56, 50688\n\t"
    "s_add_u32 s61, s59, 51200\n\t"
    "s_and_b32 s61, s61, 0x7ffff\n\t"
    "buffer_load_dwordx4 v239, s[44:47], s61 offen offset:0 lds\n\t"
    "s_add_u32 m0, s56, 54912\n\t"
    "s_nop 0\n\t"
    "buffer_load_dwordx4 v226, s[44:47], s61 offen offset:0 lds\n\t"
    "s_add_u32 m0, s56, 59136\n\t"
    "s_nop 0\n\t"
    "buffer_load_dwordx4 v227, s[44:47], s61 offen offset:0 lds\n\t"
    "s_add_u32 m0, s56, 63360\n\t"
    "s_nop 0\n\t"
    "buffer_load_dwordx4 v3, s[44:47], s61 offen offset:0 lds\n\t"
    "v_cvt_pk_bf16_f32 v204, v172, v173\n\t"
    "v_add_f32 v254, v254, v172\n\t"
    "v_mfma_f32_16x16x32_bf16 v[100:103], a[160:163], v[212:215], 0\n\t"
    "ds_read_b128 a[152:155], v236 offset:25984\n\t"
    "ds_read_b128 a[156:159], v236 offset:26048\n\t"
    "v_mfma_f32_16x16x32_bf16 v[100:103], a[192:195], v[216:219], v[100:103]\n\t"
    "v_exp_f32 v174, v174\n\t"
    "v_mfma_f32_16x16x32_bf16 v[104:107], a[164:167], v[212:215], 0\n\t"
    "ds_read_b128 v[228:231], v238 offset:0\n\t"
    "ds_read_b128 v[232:235], v238 offset:1024\n\t"
    "v_mfma_f32_16x16x32_bf16 v[104:107], a[196:199], v[216:219], v[104:107]\n\t"
    "v_exp_f32 v175, v175\n\t"
    "v_mfma_f32_16x16x32_bf16 v[108:111], a[168:171], v[212:215], 0\n\t"
    "v_cvt_pk_bf16_f32 v205, v174, v175\n\t"
    "v_add_f32 v254, v254, v173\n\t"
    "v_mfma_f32_16x16x32_bf16 v[108:111], a[200:203], v[216:219], v[108:111]\n\t"
    "v_exp_f32 v176, v176\n\t"
    "v_mfma_f32_16x16x32_bf16 v[112:115], a[172:175], v[212:215], 0\n\t"
    "v_exp_f32 v177, v177\n\t"
    "v_mfma_f32_16x16x32_bf16 v[112:115], a[204:207], v[216:219], v[112:115]\n\t"
    "v_cvt_pk_bf16_f32 v206, v176, v177\n\t"
    "v_add_f32 v254, v254, v174\n\t"
    "v_mfma_f32_16x16x32_bf16 v[116:119], a[176:179], v[212:215], 0\n\t"
    "v_exp_f32 v178, v178\n\t"
    "v_mfma_f32_16x16x32_bf16 v[116:119], a[208:211], v[216:219], v[116:119]\n\t"
    "v_exp_f32 v179, v179\n\t"
    "v_mfma_f32_16x16x32_bf16 v[120:123], a[180:183], v[212:215], 0\n\t"
    "v_cvt_pk_bf16_f32 v207, v178, v179\n\t"
    "v_add_f32 v254, v254, v175\n\t"
    "v_mfma_f32_16x16x32_bf16 v[120:123], a[212:215], v[216:219], v[120:123]\n\t"
    "v_exp_f32 v180, v180\n\t"
    "v_mfma_f32_16x16x32_bf16 v[124:127], a[184:187], v[212:215], 0\n\t"
    "v_exp_f32 v181, v181\n\t"
    "v_mfma_f32_16x16x32_bf16 v[124:127], a[216:219], v[216:219], v[124:127]\n\t"
    "v_cvt_pk_bf16_f32 v208, v180, v181\n\t"
    "v_add_f32 v254, v254, v176\n\t"
    "v_mfma_f32_16x16x32_bf16 v[128:131], a[188:191], v[212:215], 0\n\t"
    "v_exp_f32 v182, v182\n\t"
    "v_mfma_f32_16x16x32_bf16 v[128:131], a[220:223], v[216:219], v[128:131]\n\t"
    "v_exp_f32 v183, v183\n\t"
    "v_mfma_f32_16x16x32_bf16 a[236:239], a[248:251], v[212:215], 0\n\t"
    "s_waitcnt lgkmcnt(0)\n\t"
    "v_cvt_pk_bf16_f32 v209, v182, v183\n\t"
    "v_add_f32 v254, v254, v177\n\t"
    "LOOP2_%=:\n\t"
    "s_cmp_lg_u32 s60, 0\n\t"
    "s_cbranch_scc1 NOPF2_%=\n\t"
    "s_mov_b32 s42, 0\n\t"
    "s_mov_b32 s46, 0\n\t"
    "s_mov_b32 s61, TAIL2_%=-CPF2_%=\n\t"
    "v_lshlrev_b32 v2, 7, v1\n\t"
    "buffer_load_dword v255, v2, s[96:99], s61 offen\n\t"
    "NOPF2_%=:\n\t"
    "v_mfma_f32_16x16x32_bf16 v[188:191], a[96:99], a[0:3], 0\n\t"
    "v_exp_f32 v184, v184\n\t"
    "v_mfma_f32_16x16x32_bf16 v[188:191], a[100:103], a[4:7], v[188:191]\n\t"
    "s_add_u32 m0, s55, 0\n\t"
    "v_exp_f32 v185, v185\n\t"
    "v_mfma_f32_16x16x32_bf16 v[188:191], a[104:107], a[8:11], v[188:191]\n\t"
    "buffer_load_dwordx4 v239, s[40:43], s54 offen offset:0 nt lds\n\t"
    "v_mfma_f32_16x16x32_bf16 v[188:191], a[108:111], a[12:15], v[188:191]\n\t"
    "v_cvt_pk_bf16_f32 v210, v184, v185\n\t"
    "v_add_f32 v254, v254, v178\n\t"
    "v_mfma_f32_16x16x32_bf16 v[192:195], a[112:115], a[0:3], 0\n\t"
    "v_exp_f32 v186, v186\n\t"
    "v_mfma_f32_16x16x32_bf16 v[192:195], a[116:119], a[4:7], v[192:195]\n\t"
    "v_exp_f32 v187, v187\n\t"
    "v_mfma_f32_16x16x32_bf16 v[192:195], a[120:123], a[8:11], v[192:195]\n\t"
    "v_cvt_pk_bf16_f32 v211, v186, v187\n\t"
    "v_add_f32 v254, v254, v179\n\t"
    "v_mfma_f32_16x16x32_bf16 v[192:195], a[124:127], a[12:15], v[192:195]\n\t"
    "v_add_f32 v254, v254, v180\n\t"
    "v_add_f32 v254, v254, v181\n\t"
    "v_mfma_f32_16x16x32_bf16 v[196:199], a[128:131], a[0:3], 0\n\t"
    "v_add_f32 v254, v254, v182\n\t"
    "v_add_f32 v254, v254, v183\n\t"
    "v_mfma_f32_16x16x32_bf16 v[196:199], a[132:135], a[4:7], v[196:199]\n\t"
    "v_add_f32 v254, v254, v184\n\t"
    "v_add_f32 v254, v254, v185\n\t"
    "v_mfma_f32_16x16x32_bf16 v[196:199], a[136:139], a[8:11], v[196:199]\n\t"
    "v_add_f32 v254, v254, v186\n\t"
    "v_add_f32 v254, v254, v187\n\t"
    "v_mfma_f32_16x16x32_bf16 v[196:199], a[140:143], a[12:15], v[196:199]\n\t"
    "v_exp_f32 v188, v188\n\t"
    "v_mfma_f32_16x16x32_bf16 v[200:203], a[144:147], a[0:3], 0\n\t"
    "v_exp_f32 v189, v189\n\t"
    "v_mfma_f32_16x16x32_bf16 v[200:203], a[148:151], a[4:7], v[200:203]\n\t"
    "v_cvt_pk_bf16_f32 v212, v188, v189\n\t"
    "v_add_f32 v250, v250, v188\n\t"
    "v_mfma_f32_16x16x32_bf16 v[200:203], a[152:155], a[8:11], v[200:203]\n\t"
    "v_exp_f32 v190, v190\n\t"
    "v_mfma_f32_16x16x32_bf16 v[200:203], a[156:159], a[12:15], v[200:203]\n\t"
    "v_exp_f32 v191, v191\n\t"
    "v_mfma_f32_16x16x32_bf16 v[164:167], a[176:179], v[228:231], v[164:167]\n\t"
    "v_cvt_pk_bf16_f32 v213, v190, v191\n\t"
    "v_add_f32 v250, v250, v189\n\t"
    "v_mfma_f32_16x16x32_bf16 v[164:167], a[208:211], v[232:235], v[164:167]\n\t"
    "s_add_u32 m0, s56, 0\n\t"
    "v_exp_f32 v192, v192\n\t"
    "v_mfma_f32_16x16x32_bf16 v[168:171], a[180:183], v[228:231], v[168:171]\n\t"
    "buffer_load_dwordx4 v239, s[44:47], s54 offen offset:0 lds\n\t"
    "v_mfma_f32_16x16x32_bf16 v[168:171], a[212:215], v[232:235], v[168:171]\n\t"
    "v_exp_f32 v193, v193\n\t"
    "v_mfma_f32_16x16x32_bf16 v[132:135], a[160:163], v[204:207], v[132:135]\n\t"
    "ds_read_b64_tr_b16 a[160:161], v237 offset:16896\n\t"
    "ds_read_b64_tr_b16 a[162:163], v237 offset:17408\n\t"
    "v_mfma_f32_16x16x32_bf16 v[132:135], a[192:195], v[208:211], v[132:135]\n\t"
    "v_cvt_pk_bf16_f32 v214, v192, v193\n\t"
    "v_add_f32 v250, v250, v190\n\t"
    "v_mfma_f32_16x16x32_bf16 v[136:139], a[164:167], v[204:207], v[136:139]\n\t"
    "ds_read_b64_tr_b16 a[192:193], v237 offset:25344\n\t"
    "ds_read_b64_tr_b16 a[194:195], v237 offset:25856\n\t"
    "v_mfma_f32_16x16x32_bf16 v[136:139], a[196:199], v[208:211], v[136:139]\n\t"
    "v_exp_f32 v194, v194\n\t"
    "v_mfma_f32_16x16x32_bf16 v[140:143], a[168:171], v[204:207], v[140:143]\n\t"
    "ds_read_b64_tr_b16 a[164:165], v237 offset:16904\n\t"
    "ds_read_b64_tr_b16 a[166:167], v237 offset:17416\n\t"
    "v_mfma_f32_16x16x32_bf16 v[140:143], a[200:203], v[208:211], v[140:143]\n\t"
    "v_exp_f32 v195, v195\n\t"
    "v_mfma_f32_16x16x32_bf16 v[144:147], a[172:175], v[204:207], v[144:147]\n\t"
    "ds_read_b64_tr_b16 a[196:197], v237 offset:25352\n\t"
    "ds_read_b64_tr_b16 a[198:199], v237 offset:25864\n\t"
    "v_mfma_f32_16x16x32_bf16 v[144:147], a[204:207], v[208:211], v[144:147]\n\t"
    "v_cvt_pk_bf16_f32 v215, v194, v195\n\t"
    "v_add_f32 v250, v250, v191\n\t"
    "v_mfma_f32_16x16x32_bf16 v[148:151], a[176:179], v[204:207], v[148:151]\n\t"
    "ds_read_b64_tr_b16 a[168:169], v237 offset:16960\n\t"
    "ds_read_b64_tr_b16 a[170:171], v237 offset:17472\n\t"
    "v_mfma_f32_16x16x32_bf16 v[148:151], a[208:211], v[208:211], v[148:151]\n\t"
    "v_exp_f32 v196, v196\n\t"
    "v_mfma_f32_16x16x32_bf16 v[152:155], a[180:183], v[204:207], v[152:155]\n\t"
    "ds_read_b64_tr_b16 a[200:201], v237 offset:25408\n\t"
    "ds_read_b64_tr_b16 a[202:203], v237 offset:25920\n\t"
    "v_mfma_f32_16x16x32_bf16 v[152:155], a[212:215], v[208:211], v[152:155]\n\t"
    "v_exp_f32 v197, v197\n\t"
    "v_mfma_f32_16x16x32_bf16 v[156:159], a[184:187], v[204:207], v[156:159]\n\t"
    "ds_read_b64_tr_b16 a[172:173], v237 offset:16968\n\t"
    "ds_read_b64_tr_b16 a[174:175], v237 offset:17480\n\t"
    "v_mfma_f32_16x16x32_bf16 v[156:159], a[216:219], v[208:211], v[156:159]\n\t"
    "s_add_u32 m0, s55, 4224\n\t"
    "v_cvt_pk_bf16_f32 v216, v196, v197\n\t"
    "v_add_f32 v250, v250, v192\n\t"
    "v_mfma_f32_16x16x32_bf16 v[160:163], a[188:191], v[204:207], v[160:163]\n\t"
    "buffer_load_dwordx4 v226, s[40:43], s54 offen offset:0 nt lds\n\t"
    "v_mfma_f32_16x16x32_bf16 v[160:163], a[220:223], v[208:211], v[160:163]\n\t"
    "v_exp_f32 v198, v198\n\t"
    "v_mfma_f32_16x16x32_bf16 v[172:175], a[96:99], a[16:19], 0\n\t"
    "ds_read_b64_tr_b16 a[204:205], v237 offset:25416\n\t"
    "ds_read_b64_tr_b16 a[206:207], v237 offset:25928\n\t"
    "v_mfma_f32_16x16x32_bf16 v[172:175], a[100:103], a[20:23], v[172:175]\n\t"
    "v_exp_f32 v199, v199\n\t"
    "v_mfma_f32_16x16x32_bf16 v[172:175], a[104:107], a[24:27], v[172:175]\n\t"
    "ds_read_b64_tr_b16 a[176:177], v237 offset:17024\n\t"
    "ds_read_b64_tr_b16 a[178:179], v237 offset:17536\n\t"
    "v_mfma_f32_16x16x32_bf16 v[172:175], a[108:111], a[28:31], v[172:175]\n\t"
    "v_cvt_pk_bf16_f32 v217, v198, v199\n\t"
    "v_add_f32 v250, v250, v193\n\t"
    "v_mfma_f32_16x16x32_bf16 v[176:179], a[112:115], a[16:19], 0\n\t"
    "ds_read_b64_tr_b16 a[208:209], v237 offset:25472\n\t"
    "ds_read_b64_tr_b16 a[210:211], v237 offset:25984\n\t"
    "v_mfma_f32_16x16x32_bf16 v[176:179], a[116:119], a[20:23], v[176:179]\n\t"
    "v_exp_f32 v200, v200\n\t"
    "v_mfma_f32_16x16x32_bf16 v[176:179], a[120:123], a[24:27], v[176:179]\n\t"
    "ds_read_b64_tr_b16 a[180:181], v237 offset:17032\n\t"
    "ds_read_b64_tr_b16 a[182:183], v237 offset:17544\n\t"
    "v_mfma_f32_16x16x32_bf16 v[176:179], a[124:127], a[28:31], v[176:179]\n\t"
    "v_exp_f32 v201, v201\n\t"
    "v_mfma_f32_16x16x32_bf16 v[180:183], a[128:131], a[16:19], 0\n\t"
    "ds_read_b64_tr_b16 a[212:213], v237 offset:25480\n\t"
    "ds_read_b64_tr_b16 a[214:215], v237 offset:25992\n\t"
    "v_mfma_f32_16x16x32_bf16 v[180:183], a[132:135], a[20:23], v[180:183]\n\t"
    "v_cvt_pk_bf16_f32 v218, v200, v201\n\t"
    "v_add_f32 v250, v250, v194\n\t"
    "v_mfma_f32_16x16x32_bf16 v[180:183], a[136:139], a[24:27], v[180:183]\n\t"
    "ds_read_b64_tr_b16 a[184:185], v237 offset:17088\n\t"
    "ds_read_b64_tr_b16 a[186:187], v237 offset:17600\n\t"
    "v_mfma_f32_16x16x32_bf16 v[180:183], a[140:143], a[28:31], v[180:183]\n\t"
    "v_exp_f32 v202, v202\n\t"
    "v_mfma_f32_16x16x32_bf16 v[184:187], a[144:147], a[16:19], 0\n\t"
    "ds_read_b64_tr_b16 a[216:217], v237 offset:25536\n\t"
    "ds_read_b64_tr_b16 a[218:219], v237 offset:26048\n\t"
    "v_mfma_f32_16x16x32_bf16 v[184:187], a[148:151], a[20:23], v[184:187]\n\t"
    "v_exp_f32 v203, v203\n\t"
    "v_mfma_f32_16x16x32_bf16 v[184:187], a[152:155], a[24:27], v[184:187]\n\t"
    "ds_read_b64_tr_b16 a[188:189], v237 offset:17096\n\t"
    "ds_read_b64_tr_b16 a[190:191], v237 offset:17608\n\t"
    "v_mfma_f32_16x16x32_bf16 v[184:187], a[156:159], a[28:31], v[184:187]\n\t"
    "v_cvt_pk_bf16_f32 v219, v202, v203\n\t"
    "v_add_f32 v250, v250, v195\n\t"
    "v_mfma_f32_16x16x32_bf16 v[220:223], a[128:131], a[80:83], 0\n\t"
    "ds_read_b64_tr_b16 a[220:221], v237 offset:25544\n\t"
    "ds_read_b64_tr_b16 a[222:223], v237 offset:26056\n\t"
    "v_mfma_f32_16x16x32_bf16 v[220:223], a[132:135], a[84:87], v[220:223]\n\t"
    "s_add_u32 m0, s56, 4224\n\t"
    "v_exp_f32 v172, v172\n\t"
    "v_mfma_f32_16x16x32_bf16 v[220:223], a[136:139], a[88:91], v[220:223]\n\t"
    "buffer_load_dwordx4 v226, s[44:47], s54 offen offset:0 lds\n\t"
    "v_mfma_f32_16x16x32_bf16 v[220:223], a[140:143], a[92:95], v[220:223]\n\t"
    "s_waitcnt lgkmcnt(4)\n\t"
    "v_exp_f32 v173, v173\n\t"
    "v_mfma_f32_16x16x32_bf16 v[4:7], a[160:163], v[212:215], v[4:7]\n\t"
    "v_cvt_pk_bf16_f32 v204, v172, v173\n\t"
    "v_add_f32 v250, v250, v196\n\t"
    "v_mfma_f32_16x16x32_bf16 v[4:7], a[192:195], v[216:219], v[4:7]\n\t"
    "v_exp_f32 v174, v174\n\t"
    "v_mfma_f32_16x16x32_bf16 v[8:11], a[164:167], v[212:215], v[8:11]\n\t"
    "v_exp_f32 v175, v175\n\t"
    "v_mfma_f32_16x16x32_bf16 v[8:11], a[196:199], v[216:219], v[8:11]\n\t"
    "v_cvt_pk_bf16_f32 v205, v174, v175\n\t"
    "v_add_f32 v250, v250, v197\n\t"
    "v_mfma_f32_16x16x32_bf16 v[12:15], a[168:171], v[212:215], v[12:15]\n\t"
    "v_exp_f32 v176, v176\n\t"
    "v_mfma_f32_16x16x32_bf16 v[12:15], a[200:203], v[216:219], v[12:15]\n\t"
    "v_exp_f32 v177, v177\n\t"
    "v_mfma_f32_16x16x32_bf16 v[16:19], a[172:175], v[212:215], v[16:19]\n\t"
    "v_cvt_pk_bf16_f32 v206, v176, v177\n\t"
    "v_add_f32 v250, v250, v198\n\t"
    "v_mfma_f32_16x16x32_bf16 v[16:19], a[204:207], v[216:219], v[16:19]\n\t"
    "v_exp_f32 v178, v178\n\t"
    "v_mfma_f32_16x16x32_bf16 v[20:23], a[176:179], v[212:215], v[20:23]\n\t"
    "v_exp_f32 v179, v179\n\t"
    "v_mfma_f32_16x16x32_bf16 v[20:23], a[208:211], v[216:219], v[20:23]\n\t"
    "s_add_u32 m0, s55, 8448\n\t"
    "v_cvt_pk_bf16_f32 v207, v178, v179\n\t"
    "v_add_f32 v250, v250, v199\n\t"
    "v_mfma_f32_16x16x32_bf16 v[24:27], a[180:183], v[212:215], v[24:27]\n\t"
    "buffer_load_dwordx4 v227, s[40:43], s54 offen offset:0 nt lds\n\t"
    "v_mfma_f32_16x16x32_bf16 v[24:27], a[212:215], v[216:219], v[24:27]\n\t"
    "v_exp_f32 v180, v180\n\t"
    "v_mfma_f32_16x16x32_bf16 v[28:31], a[184:187], v[212:215], v[28:31]\n\t"
    "v_exp_f32 v181, v181\n\t"
    "v_mfma_f32_16x16x32_bf16 v[28:31], a[216:219], v[216:219], v[28:31]\n\t"
    "s_waitcnt lgkmcnt(0)\n\t"
    "v_cvt_pk_bf16_f32 v208, v180, v181\n\t"
    "v_add_f32 v250, v250, v200\n\t"
    "v_mfma_f32_16x16x32_bf16 v[32:35], a[188:191], v[212:215], v[32:35]\n\t"
    "v_exp_f32 v182, v182\n\t"
    "v_mfma_f32_16x16x32_bf16 v[32:35], a[220:223], v[216:219], v[32:35]\n\t"
    "v_exp_f32 v183, v183\n\t"
    "v_mfma_f32_16x16x32_bf16 v[188:191], a[96:99], a[32:35], 0\n\t"
    "v_cvt_pk_bf16_f32 v209, v182, v183\n\t"
    "v_add_f32 v250, v250, v201\n\t"
    "v_mfma_f32_16x16x32_bf16 v[188:191], a[100:103], a[36:39], v[188:191]\n\t"
    "v_exp_f32 v184, v184\n\t"
    "v_mfma_f32_16x16x32_bf16 v[188:191], a[104:107], a[40:43], v[188:191]\n\t"
    "v_exp_f32 v220, v220\n\t"
    "v_mfma_f32_16x16x32_bf16 v[188:191], a[108:111], a[44:47], v[188:191]\n\t"
    "v_exp_f32 v185, v185\n\t"
    "v_mfma_f32_16x16x32_bf16 v[192:195], a[112:115], a[32:35], 0\n\t"
    "v_exp_f32 v221, v221\n\t"
    "v_mfma_f32_16x16x32_bf16 v[192:195], a[116:119], a[36:39], v[192:195]\n\t"
    "v_add_f32 v250, v250, v202\n\t"
    "v_add_f32 v250, v250, v203\n\t"
    "v_mfma_f32_16x16x32_bf16 v[192:195], a[120:123], a[40:43], v[192:195]\n\t"
    "v_cvt_pk_bf16_f32 v210, v184, v185\n\t"
    "v_cvt_pk_bf16_f32 v224, v220, v221\n\t"
    "v_mfma_f32_16x16x32_bf16 v[192:195], a[124:127], a[44:47], v[192:195]\n\t"
    "v_exp_f32 v186, v186\n\t"
    "v_mfma_f32_16x16x32_bf16 v[196:199], a[128:131], a[32:35], 0\n\t"
    "v_exp_f32 v222, v222\n\t"
    "v_mfma_f32_16x16x32_bf16 v[196:199], a[132:135], a[36:39], v[196:199]\n\t"
    "s_add_u32 m0, s56, 8448\n\t"
    "v_exp_f32 v187, v187\n\t"
    "v_mfma_f32_16x16x32_bf16 v[196:199], a[136:139], a[40:43], v[196:199]\n\t"
    "buffer_load_dwordx4 v227, s[44:47], s54 offen offset:0 lds\n\t"
    "v_mfma_f32_16x16x32_bf16 v[196:199], a[140:143], a[44:47], v[196:199]\n\t"
    "v_exp_f32 v223, v223\n\t"
    "v_mfma_f32_16x16x32_bf16 v[200:203], a[144:147], a[32:35], 0\n\t"
    "v_cvt_pk_bf16_f32 v225, v222, v223\n\t"
    "v_cvt_pk_bf16_f32 v211, v186, v187\n\t"
    "v_mfma_f32_16x16x32_bf16 v[200:203], a[148:151], a[36:39], v[200:203]\n\t"
    "ds_write_b64 v238, v[224:225] offset:3072\n\t"
    "v_mfma_f32_16x16x32_bf16 v[200:203], a[152:155], a[40:43], v[200:203]\n\t"
    "v_exp_f32 v188, v188\n\t"
    "v_mfma_f32_16x16x32_bf16 v[200:203], a[156:159], a[44:47], v[200:203]\n\t"
    "v_exp_f32 v189, v189\n\t"
    "v_mfma_f32_16x16x16_bf16 a[244:247], a[248:249], v[224:225], a[244:247]\n\t"
    "v_cvt_pk_bf16_f32 v212, v188, v189\n\t"
    "v_add_f32 v251, v251, v180\n\t"
    "v_mfma_f32_16x16x32_bf16 v[36:39], a[160:163], v[204:207], v[36:39]\n\t"
    "v_exp_f32 v190, v190\n\t"
    "v_mfma_f32_16x16x32_bf16 v[36:39], a[192:195], v[208:211], v[36:39]\n\t"
    "v_exp_f32 v191, v191\n\t"
    "v_mfma_f32_16x16x32_bf16 v[40:43], a[164:167], v[204:207], v[40:43]\n\t"
    "v_cvt_pk_bf16_f32 v213, v190, v191\n\t"
    "v_add_f32 v251, v251, v181\n\t"
    "v_mfma_f32_16x16x32_bf16 v[40:43], a[196:199], v[208:211], v[40:43]\n\t"
    "v_exp_f32 v192, v192\n\t"
    "v_mfma_f32_16x16x32_bf16 v[44:47], a[168:171], v[204:207], v[44:47]\n\t"
    "v_exp_f32 v193, v193\n\t"
    "v_mfma_f32_16x16x32_bf16 v[44:47], a[200:203], v[208:211], v[44:47]\n\t"
    "v_cvt_pk_bf16_f32 v214, v192, v193\n\t"
    "v_add_f32 v251, v251, v182\n\t"
    "v_mfma_f32_16x16x32_bf16 v[48:51], a[172:175], v[204:207], v[48:51]\n\t"
    "v_exp_f32 v194, v194\n\t"
    "v_mfma_f32_16x16x32_bf16 v[48:51], a[204:207], v[208:211], v[48:51]\n\t"
    "v_exp_f32 v195, v195\n\t"
    "v_mfma_f32_16x16x32_bf16 v[52:55], a[176:179], v[204:207], v[52:55]\n\t"
    "s_add_u32 m0, s55, 12672\n\t"
    "v_cvt_pk_bf16_f32 v215, v194, v195\n\t"
    "v_add_f32 v251, v251, v183\n\t"
    "v_mfma_f32_16x16x32_bf16 v[52:55], a[208:211], v[208:211], v[52:55]\n\t"
    "buffer_load_dwordx4 v3, s[40:43], s54 offen offset:0 nt lds\n\t"
    "v_mfma_f32_16x16x32_bf16 v[56:59], a[180:183], v[204:207], v[56:59]\n\t"
    "v_exp_f32 v196, v196\n\t"
    "v_mfma_f32_16x16x32_bf16 v[56:59], a[212:215], v[208:211], v[56:59]\n\t"
    "v_exp_f32 v197, v197\n\t"
    "v_mfma_f32_16x16x32_bf16 v[60:63], a[184:187], v[204:207], v[60:63]\n\t"
    "v_cvt_pk_bf16_f32 v216, v196, v197\n\t"
    "v_add_f32 v251, v251, v184\n\t"
    "v_mfma_f32_16x16x32_bf16 v[60:63], a[216:219], v[208:211], v[60:63]\n\t"
    "v_exp_f32 v198, v198\n\t"
    "v_mfma_f32_16x16x32_bf16 v[64:67], a[188:191], v[204:207], v[64:67]\n\t"
    "v_exp_f32 v199, v199\n\t"
    "v_mfma_f32_16x16x32_bf16 v[64:67], a[220:223], v[208:211], v[64:67]\n\t"
    "v_cvt_pk_bf16_f32 v217, v198, v199\n\t"
    "v_add_f32 v251, v251, v185\n\t"
    "v_mfma_f32_16x16x32_bf16 a[228:231], a[248:251], v[204:207], a[228:231]\n\t"
    "v_exp_f32 v200, v200\n\t"
    "v_mfma_f32_16x16x32_bf16 v[172:175], a[96:99], a[48:51], 0\n\t"
    "v_exp_f32 v201, v201\n\t"
    "v_mfma_f32_16x16x32_bf16 v[172:175], a[100:103], a[52:55], v[172:175]\n\t"
    "v_cvt_pk_bf16_f32 v218, v200, v201\n\t"
    "v_add_f32 v251, v251, v186\n\t"
    "v_mfma_f32_16x16x32_bf16 v[172:175], a[104:107], a[56:59], v[172:175]\n\t"
    "v_exp_f32 v202, v202\n\t"
    "v_mfma_f32_16x16x32_bf16 v[172:175], a[108:111], a[60:63], v[172:175]\n\t"
    "v_add_f32 v251, v251, v187\n\t"
    "v_add_u32 v236, s58, v236\n\t"
    "v_mfma_f32_16x16x32_bf16 v[176:179], a[112:115], a[48:51], 0\n\t"
    "v_exp_f32 v203, v203\n\t"
    "v_mfma_f32_16x16x32_bf16 v[176:179], a[116:119], a[52:55], v[176:179]\n\t"
    "v_cvt_pk_bf16_f32 v219, v202, v203\n\t"
    "v_add_u32 v237, s58, v237\n\t"
    "v_mfma_f32_16x16x32_bf16 v[176:179], a[120:123], a[56:59], v[176:179]\n\t"
    "v_add_f32 v252, v252, v196\n\t"
    "v_add_f32 v252, v252, v197\n\t"
    "v_mfma_f32_16x16x32_bf16 v[176:179], a[124:127], a[60:63], v[176:179]\n\t"
    "s_add_u32 m0, s56, 12672\n\t"
    "v_add_f32 v252, v252, v198\n\t"
    "v_add_f32 v252, v252, v199\n\t"
    "v_mfma_f32_16x16x32_bf16 v[180:183], a[128:131], a[48:51], 0\n\t"
    "buffer_load_dwordx4 v3, s[44:47], s54 offen offset:0 lds\n\t"
    "v_mfma_f32_16x16x32_bf16 v[180:183], a[132:135], a[52:55], v[180:183]\n\t"
    "v_add_f32 v252, v252, v200\n\t"
    "v_add_f32 v252, v252, v201\n\t"
    "v_mfma_f32_16x16x32_bf16 v[180:183], a[136:139], a[56:59], v[180:183]\n\t"
    "v_add_f32 v252, v252, v202\n\t"
    "v_add_f32 v252, v252, v203\n\t"
    "v_mfma_f32_16x16x32_bf16 v[180:183], a[140:143], a[60:63], v[180:183]\n\t"
    "v_exp_f32 v172, v172\n\t"
    "v_mfma_f32_16x16x32_bf16 v[184:187], a[144:147], a[48:51], 0\n\t"
    "v_exp_f32 v173, v173\n\t"
    "v_mfma_f32_16x16x32_bf16 v[184:187], a[148:151], a[52:55], v[184:187]\n\t"
    "v_cvt_pk_bf16_f32 v204, v172, v173\n\t"
    "v_mfma_f32_16x16x32_bf16 v[184:187], a[152:155], a[56:59], v[184:187]\n\t"
    "v_exp_f32 v174, v174\n\t"
    "v_mfma_f32_16x16x32_bf16 v[184:187], a[156:159], a[60:63], v[184:187]\n\t"
    "v_exp_f32 v175, v175\n\t"
    "v_mfma_f32_16x16x32_bf16 v[68:71], a[160:163], v[212:215], v[68:71]\n\t"
    "v_cvt_pk_bf16_f32 v205, v174, v175\n\t"
    "v_mfma_f32_16x16x32_bf16 v[68:71], a[192:195], v[216:219], v[68:71]\n\t"
    "v_exp_f32 v176, v176\n\t"
    "v_mfma_f32_16x16x32_bf16 v[72:75], a[164:167], v[212:215], v[72:75]\n\t"
    "v_exp_f32 v177, v177\n\t"
    "v_mfma_f32_16x16x32_bf16 v[72:75], a[196:199], v[216:219], v[72:75]\n\t"
    "v_cvt_pk_bf16_f32 v206, v176, v177\n\t"
    "v_mfma_f32_16x16x32_bf16 v[76:79], a[168:171], v[212:215], v[76:79]\n\t"
    "v_exp_f32 v178, v178\n\t"
    "v_mfma_f32_16x16x32_bf16 v[76:79], a[200:203], v[216:219], v[76:79]\n\t"
    "v_exp_f32 v179, v179\n\t"
    "v_mfma_f32_16x16x32_bf16 v[80:83], a[172:175], v[212:215], v[80:83]\n\t"
    "v_cvt_pk_bf16_f32 v207, v178, v179\n\t"
    "v_mfma_f32_16x16x32_bf16 v[80:83], a[204:207], v[216:219], v[80:83]\n\t"
    "v_exp_f32 v180, v180\n\t"
    "v_mfma_f32_16x16x32_bf16 v[84:87], a[176:179], v[212:215], v[84:87]\n\t"
    "v_exp_f32 v181, v181\n\t"
    "v_mfma_f32_16x16x32_bf16 v[84:87], a[208:211], v[216:219], v[84:87]\n\t"
    "v_cvt_pk_bf16_f32 v208, v180, v181\n\t"
    "v_add_f32 v253, v253, v180\n\t"
    "v_mfma_f32_16x16x32_bf16 v[88:91], a[180:183], v[212:215], v[88:91]\n\t"
    "v_exp_f32 v182, v182\n\t"
    "v_mfma_f32_16x16x32_bf16 v[88:91], a[212:215], v[216:219], v[88:91]\n\t"
    "v_exp_f32 v183, v183\n\t"
    "v_mfma_f32_16x16x32_bf16 v[92:95], a[184:187], v[212:215], v[92:95]\n\t"
    "v_cvt_pk_bf16_f32 v209, v182, v183\n\t"
    "v_add_f32 v253, v253, v181\n\t"
    "v_mfma_f32_16x16x32_bf16 v[92:95], a[216:219], v[216:219], v[92:95]\n\t"
    "v_exp_f32 v184, v184\n\t"
    "v_mfma_f32_16x16x32_bf16 v[96:99], a[188:191], v[212:215], v[96:99]\n\t"
    "v_exp_f32 v185, v185\n\t"
    "v_mfma_f32_16x16x32_bf16 v[96:99], a[220:223], v[216:219], v[96:99]\n\t"
    "s_add_u32 s54, s54, 16384\n\t"
    "v_cvt_pk_bf16_f32 v210, v184, v185\n\t"
    "v_add_f32 v253, v253, v182\n\t"
    "v_mfma_f32_16x16x32_bf16 a[232:235], a[248:251], v[212:215], a[232:235]\n\t"
    "s_waitcnt vmcnt(16) lgkmcnt(0)\n\t"
    "s_barrier\n\t"
    "v_mfma_f32_16x16x32_bf16 v[188:191], a[96:99], a[64:67], 0\n\t"
    "ds_read_b128 a[96:99], v236 offset:0\n\t"
    "v_add_f32 v253, v253, v183\n\t"
    "v_mfma_f32_16x16x32_bf16 v[188:191], a[100:103], a[68:71], v[188:191]\n\t"
    "s_sub_u32 s58, 0, s58\n\t"
    "v_exp_f32 v186, v186\n\t"
    "v_mfma_f32_16x16x32_bf16 v[188:191], a[104:107], a[72:75], v[188:191]\n\t"
    "ds_read_b128 a[100:103], v236 offset:64\n\t"
    "ds_read_b128 a[104:107], v236 offset:128\n\t"
    "v_mfma_f32_16x16x32_bf16 v[188:191], a[108:111], a[76:79], v[188:191]\n\t"
    "s_and_b32 s54, s54, 0x7ffff\n\t"
    "v_exp_f32 v187, v187\n\t"
    "v_mfma_f32_16x16x32_bf16 v[192:195], a[112:115], a[64:67], 0\n\t"
    "ds_read_b128 a[108:111], v236 offset:192\n\t"
    "ds_read_b128 a[112:115], v236 offset:512\n\t"
    "v_mfma_f32_16x16x32_bf16 v[192:195], a[116:119], a[68:71], v[192:195]\n\t"
    "v_cvt_pk_bf16_f32 v211, v186, v187\n\t"
    "v_add_f32 v253, v253, v184\n\t"
    "v_mfma_f32_16x16x32_bf16 v[192:195], a[120:123], a[72:75], v[192:195]\n\t"
    "ds_read_b128 a[116:119], v236 offset:576\n\t"
    "ds_read_b128 a[120:123], v236 offset:640\n\t"
    "v_mfma_f32_16x16x32_bf16 v[192:195], a[124:127], a[76:79], v[192:195]\n\t"
    "v_add_f32 v253, v253, v185\n\t"
    "v_add_f32 v253, v253, v186\n\t"
    "v_mfma_f32_16x16x32_bf16 v[196:199], a[128:131], a[64:67], 0\n\t"
    "ds_read_b128 a[124:127], v236 offset:704\n\t"
    "v_add_f32 v253, v253, v187\n\t"
    "v_mfma_f32_16x16x32_bf16 v[196:199], a[132:135], a[68:71], v[196:199]\n\t"
    "ds_read_b128 a[128:131], v236 offset:8448\n\t"
    "v_mfma_f32_16x16x32_bf16 v[196:199], a[136:139], a[72:75], v[196:199]\n\t"
    "ds_read_b128 a[132:135], v236 offset:8512\n\t"
    "v_mfma_f32_16x16x32_bf16 v[196:199], a[140:143], a[76:79], v[196:199]\n\t"
    "v_exp_f32 v188, v188\n\t"
    "v_mfma_f32_16x16x32_bf16 v[200:203], a[144:147], a[64:67], 0\n\t"
    "ds_read_b128 a[136:139], v236 offset:8576\n\t"
    "ds_read_b128 a[140:143], v236 offset:8640\n\t"
    "v_mfma_f32_16x16x32_bf16 v[200:203], a[148:151], a[68:71], v[200:203]\n\t"
    "v_exp_f32 v189, v189\n\t"
    "v_mfma_f32_16x16x32_bf16 v[200:203], a[152:155], a[72:75], v[200:203]\n\t"
    "ds_read_b128 a[144:147], v236 offset:8960\n\t"
    "ds_read_b128 a[148:151], v236 offset:9024\n\t"
    "v_mfma_f32_16x16x32_bf16 v[200:203], a[156:159], a[76:79], v[200:203]\n\t"
    "v_cvt_pk_bf16_f32 v212, v188, v189\n\t"
    "v_add_f32 v254, v254, v188\n\t"
    "v_mfma_f32_16x16x32_bf16 v[100:103], a[160:163], v[204:207], v[100:103]\n\t"
    "ds_read_b128 a[152:155], v236 offset:9088\n\t"
    "ds_read_b128 a[156:159], v236 offset:9152\n\t"
    "v_mfma_f32_16x16x32_bf16 v[100:103], a[192:195], v[208:211], v[100:103]\n\t"
    "v_exp_f32 v190, v190\n\t"
    "v_mfma_f32_16x16x32_bf16 v[104:107], a[164:167], v[204:207], v[104:107]\n\t"
    "ds_read_b128 v[228:231], v238 offset:2048\n\t"
    "ds_read_b128 v[232:235], v238 offset:3072\n\t"
    "v_mfma_f32_16x16x32_bf16 v[104:107], a[196:199], v[208:211], v[104:107]\n\t"
    "v_exp_f32 v191, v191\n\t"
    "v_mfma_f32_16x16x32_bf16 v[108:111], a[168:171], v[204:207], v[108:111]\n\t"
    "v_cvt_pk_bf16_f32 v213, v190, v191\n\t"
    "v_add_f32 v254, v254, v189\n\t"
    "v_mfma_f32_16x16x32_bf16 v[108:111], a[200:203], v[208:211], v[108:111]\n\t"
    "v_exp_f32 v192, v192\n\t"
    "v_mfma_f32_16x16x32_bf16 v[112:115], a[172:175], v[204:207], v[112:115]\n\t"
    "v_exp_f32 v193, v193\n\t"
    "v_mfma_f32_16x16x32_bf16 v[112:115], a[204:207], v[208:211], v[112:115]\n\t"
    "v_cvt_pk_bf16_f32 v214, v192, v193\n\t"
    "v_add_f32 v254, v254, v190\n\t"
    "v_mfma_f32_16x16x32_bf16 v[116:119], a[176:179], v[204:207], v[116:119]\n\t"
    "v_exp_f32 v194, v194\n\t"
    "v_mfma_f32_16x16x32_bf16 v[116:119], a[208:211], v[208:211], v[116:119]\n\t"
    "v_exp_f32 v195, v195\n\t"
    "v_mfma_f32_16x16x32_bf16 v[120:123], a[180:183], v[204:207], v[120:123]\n\t"
    "v_cvt_pk_bf16_f32 v215, v194, v195\n\t"
    "v_add_f32 v254, v254, v191\n\t"
    "v_mfma_f32_16x16x32_bf16 v[120:123], a[212:215], v[208:211], v[120:123]\n\t"
    "v_exp_f32 v196, v196\n\t"
    "v_mfma_f32_16x16x32_bf16 v[124:127], a[184:187], v[204:207], v[124:127]\n\t"
    "v_exp_f32 v197, v197\n\t"
    "v_mfma_f32_16x16x32_bf16 v[124:127], a[216:219], v[208:211], v[124:127]\n\t"
    "v_cvt_pk_bf16_f32 v216, v196, v197\n\t"
    "v_add_f32 v254, v254, v192\n\t"
    "v_mfma_f32_16x16x32_bf16 v[128:131], a[188:191], v[204:207], v[128:131]\n\t"
    "v_exp_f32 v198, v198\n\t"
    "v_mfma_f32_16x16x32_bf16 v[128:131], a[220:223], v[208:211], v[128:131]\n\t"
    "v_exp_f32 v199, v199\n\t"
    "v_mfma_f32_16x16x32_bf16 a[236:239], a[248:251], v[204:207], a[236:239]\n\t"
    "s_waitcnt lgkmcnt(0)\n\t"
    "v_cvt_pk_bf16_f32 v217, v198, v199\n\t"
    "v_add_f32 v254, v254, v193\n\t"
    "v_mfma_f32_16x16x32_bf16 v[172:175], a[96:99], a[0:3], 0\n\t"
    "v_exp_f32 v200, v200\n\t"
    "v_mfma_f32_16x16x32_bf16 v[172:175], a[100:103], a[4:7], v[172:175]\n\t"
    "s_add_u32 m0, s55, 16896\n\t"
    "v_exp_f32 v201, v201\n\t"
    "v_mfma_f32_16x16x32_bf16 v[172:175], a[104:107], a[8:11], v[172:175]\n\t"
    "buffer_load_dwordx4 v239, s[40:43], s54 offen offset:0 nt lds\n\t"
    "v_mfma_f32_16x16x32_bf16 v[172:175], a[108:111], a[12:15], v[172:175]\n\t"
    "v_cvt_pk_bf16_f32 v218, v200, v201\n\t"
    "v_add_f32 v254, v254, v194\n\t"
    "v_mfma_f32_16x16x32_bf16 v[176:179], a[112:115], a[0:3], 0\n\t"
    "v_exp_f32 v202, v202\n\t"
    "v_mfma_f32_16x16x32_bf16 v[176:179], a[116:119], a[4:7], v[176:179]\n\t"
    "v_exp_f32 v203, v203\n\t"
    "v_mfma_f32_16x16x32_bf16 v[176:179], a[120:123], a[8:11], v[176:179]\n\t"
    "v_cvt_pk_bf16_f32 v219, v202, v203\n\t"
    "v_add_f32 v254, v254, v195\n\t"
    "v_mfma_f32_16x16x32_bf16 v[176:179], a[124:127], a[12:15], v[176:179]\n\t"
    "v_add_f32 v254, v254, v196\n\t"
    "v_add_f32 v254, v254, v197\n\t"
    "v_mfma_f32_16x16x32_bf16 v[180:183], a[128:131], a[0:3], 0\n\t"
    "v_add_f32 v254, v254, v198\n\t"
    "v_add_f32 v254, v254, v199\n\t"
    "v_mfma_f32_16x16x32_bf16 v[180:183], a[132:135], a[4:7], v[180:183]\n\t"
    "v_add_f32 v254, v254, v200\n\t"
    "v_add_f32 v254, v254, v201\n\t"
    "v_mfma_f32_16x16x32_bf16 v[180:183], a[136:139], a[8:11], v[180:183]\n\t"
    "v_add_f32 v254, v254, v202\n\t"
    "v_add_f32 v254, v254, v203\n\t"
    "v_mfma_f32_16x16x32_bf16 v[180:183], a[140:143], a[12:15], v[180:183]\n\t"
    "v_exp_f32 v172, v172\n\t"
    "v_mfma_f32_16x16x32_bf16 v[184:187], a[144:147], a[0:3], 0\n\t"
    "v_exp_f32 v173, v173\n\t"
    "v_mfma_f32_16x16x32_bf16 v[184:187], a[148:151], a[4:7], v[184:187]\n\t"
    "v_cvt_pk_bf16_f32 v204, v172, v173\n\t"
    "v_add_f32 v250, v250, v172\n\t"
    "v_mfma_f32_16x16x32_bf16 v[184:187], a[152:155], a[8:11], v[184:187]\n\t"
    "v_exp_f32 v174, v174\n\t"
    "v_mfma_f32_16x16x32_bf16 v[184:187], a[156:159], a[12:15], v[184:187]\n\t"
    "v_exp_f32 v175, v175\n\t"
    "v_mfma_f32_16x16x32_bf16 v[164:167], a[176:179], v[228:231], v[164:167]\n\t"
    "v_cvt_pk_bf16_f32 v205, v174, v175\n\t"
    "v_add_f32 v250, v250, v173\n\t"
    "v_mfma_f32_16x16x32_bf16 v[164:167], a[208:211], v[232:235], v[164:167]\n\t"
    "s_add_u32 m0, s56, 16896\n\t"
    "v_exp_f32 v176, v176\n\t"
    "v_mfma_f32_16x16x32_bf16 v[168:171], a[180:183], v[228:231], v[168:171]\n\t"
    "buffer_load_dwordx4 v239, s[44:47], s54 offen offset:0 lds\n\t"
    "v_mfma_f32_16x16x32_bf16 v[168:171], a[212:215], v[232:235], v[168:171]\n\t"
    "v_exp_f32 v177, v177\n\t"
    "v_mfma_f32_16x16x32_bf16 v[132:135], a[160:163], v[212:215], v[132:135]\n\t"
    "ds_read_b64_tr_b16 a[160:161], v237 offset:0\n\t"
    "ds_read_b64_tr_b16 a[162:163], v237 offset:512\n\t"
    "v_mfma_f32_16x16x32_bf16 v[132:135], a[192:195], v[216:219], v[132:135]\n\t"
    "v_cvt_pk_bf16_f32 v206, v176, v177\n\t"
    "v_add_f32 v250, v250, v174\n\t"
    "v_mfma_f32_16x16x32_bf16 v[136:139], a[164:167], v[212:215], v[136:139]\n\t"
    "ds_read_b64_tr_b16 a[192:193], v237 offset:8448\n\t"
    "ds_read_b64_tr_b16 a[194:195], v237 offset:8960\n\t"
    "v_mfma_f32_16x16x32_bf16 v[136:139], a[196:199], v[216:219], v[136:139]\n\t"
    "v_exp_f32 v178, v178\n\t"
    "v_mfma_f32_16x16x32_bf16 v[140:143], a[168:171], v[212:215], v[140:143]\n\t"
    "ds_read_b64_tr_b16 a[164:165], v237 offset:8\n\t"
    "ds_read_b64_tr_b16 a[166:167], v237 offset:520\n\t"
    "v_mfma_f32_16x16x32_bf16 v[140:143], a[200:203], v[216:219], v[140:143]\n\t"
    "v_exp_f32 v179, v179\n\t"
    "v_mfma_f32_16x16x32_bf16 v[144:147], a[172:175], v[212:215], v[144:147]\n\t"
    "ds_read_b64_tr_b16 a[196:197], v237 offset:8456\n\t"
    "ds_read_b64_tr_b16 a[198:199], v237 offset:8968\n\t"
    "v_mfma_f32_16x16x32_bf16 v[144:147], a[204:207], v[216:219], v[144:147]\n\t"
    "v_cvt_pk_bf16_f32 v207, v178, v179\n\t"
    "v_add_f32 v250, v250, v175\n\t"
    "v_mfma_f32_16x16x32_bf16 v[148:151], a[176:179], v[212:215], v[148:151]\n\t"
    "ds_read_b64_tr_b16 a[168:169], v237 offset:64\n\t"
    "ds_read_b64_tr_b16 a[170:171], v237 offset:576\n\t"
    "v_mfma_f32_16x16x32_bf16 v[148:151], a[208:211], v[216:219], v[148:151]\n\t"
    "v_exp_f32 v180, v180\n\t"
    "v_mfma_f32_16x16x32_bf16 v[152:155], a[180:183], v[212:215], v[152:155]\n\t"
    "ds_read_b64_tr_b16 a[200:201], v237 offset:8512\n\t"
    "ds_read_b64_tr_b16 a[202:203], v237 offset:9024\n\t"
    "v_mfma_f32_16x16x32_bf16 v[152:155], a[212:215], v[216:219], v[152:155]\n\t"
    "v_exp_f32 v181, v181\n\t"
    "v_mfma_f32_16x16x32_bf16 v[156:159], a[184:187], v[212:215], v[156:159]\n\t"
    "ds_read_b64_tr_b16 a[172:173], v237 offset:72\n\t"
    "ds_read_b64_tr_b16 a[174:175], v237 offset:584\n\t"
    "v_mfma_f32_16x16x32_bf16 v[156:159], a[216:219], v[216:219], v[156:159]\n\t"
    "s_add_u32 m0, s55, 21120\n\t"
    "v_cvt_pk_bf16_f32 v208, v180, v181\n\t"
    "v_add_f32 v250, v250, v176\n\t"
    "v_mfma_f32_16x16x32_bf16 v[160:163], a[188:191], v[212:215], v[160:163]\n\t"
    "buffer_load_dwordx4 v226, s[40:43], s54 offen offset:0 nt lds\n\t"
    "v_mfma_f32_16x16x32_bf16 v[160:163], a[220:223], v[216:219], v[160:163]\n\t"
    "v_exp_f32 v182, v182\n\t"
    "v_mfma_f32_16x16x32_bf16 v[188:191], a[96:99], a[16:19], 0\n\t"
    "ds_read_b64_tr_b16 a[204:205], v237 offset:8520\n\t"
    "ds_read_b64_tr_b16 a[206:207], v237 offset:9032\n\t"
    "v_mfma_f32_16x16x32_bf16 v[188:191], a[100:103], a[20:23], v[188:191]\n\t"
    "v_exp_f32 v183, v183\n\t"
    "v_mfma_f32_16x16x32_bf16 v[188:191], a[104:107], a[24:27], v[188:191]\n\t"
    "ds_read_b64_tr_b16 a[176:177], v237 offset:128\n\t"
    "ds_read_b64_tr_b16 a[178:179], v237 offset:640\n\t"
    "v_mfma_f32_16x16x32_bf16 v[188:191], a[108:111], a[28:31], v[188:191]\n\t"
    "v_cvt_pk_bf16_f32 v209, v182, v183\n\t"
    "v_add_f32 v250, v250, v177\n\t"
    "v_mfma_f32_16x16x32_bf16 v[192:195], a[112:115], a[16:19], 0\n\t"
    "ds_read_b64_tr_b16 a[208:209], v237 offset:8576\n\t"
    "ds_read_b64_tr_b16 a[210:211], v237 offset:9088\n\t"
    "v_mfma_f32_16x16x32_bf16 v[192:195], a[116:119], a[20:23], v[192:195]\n\t"
    "v_exp_f32 v184, v184\n\t"
    "v_mfma_f32_16x16x32_bf16 v[192:195], a[120:123], a[24:27], v[192:195]\n\t"
    "ds_read_b64_tr_b16 a[180:181], v237 offset:136\n\t"
    "ds_read_b64_tr_b16 a[182:183], v237 offset:648\n\t"
    "v_mfma_f32_16x16x32_bf16 v[192:195], a[124:127], a[28:31], v[192:195]\n\t"
    "v_exp_f32 v185, v185\n\t"
    "v_mfma_f32_16x16x32_bf16 v[196:199], a[128:131], a[16:19], 0\n\t"
    "ds_read_b64_tr_b16 a[212:213], v237 offset:8584\n\t"
    "ds_read_b64_tr_b16 a[214:215], v237 offset:9096\n\t"
    "v_mfma_f32_16x16x32_bf16 v[196:199], a[132:135], a[20:23], v[196:199]\n\t"
    "v_cvt_pk_bf16_f32 v210, v184, v185\n\t"
    "v_add_f32 v250, v250, v178\n\t"
    "v_mfma_f32_16x16x32_bf16 v[196:199], a[136:139], a[24:27], v[196:199]\n\t"
    "ds_read_b64_tr_b16 a[184:185], v237 offset:192\n\t"
    "ds_read_b64_tr_b16 a[186:187], v237 offset:704\n\t"
    "v_mfma_f32_16x16x32_bf16 v[196:199], a[140:143], a[28:31], v[196:199]\n\t"
    "v_exp_f32 v186, v186\n\t"
    "v_mfma_f32_16x16x32_bf16 v[200:203], a[144:147], a[16:19], 0\n\t"
    "ds_read_b64_tr_b16 a[216:217], v237 offset:8640\n\t"
    "ds_read_b64_tr_b16 a[218:219], v237 offset:9152\n\t"
    "v_mfma_f32_16x16x32_bf16 v[200:203], a[148:151], a[20:23], v[200:203]\n\t"
    "v_exp_f32 v187, v187\n\t"
    "v_mfma_f32_16x16x32_bf16 v[200:203], a[152:155], a[24:27], v[200:203]\n\t"
    "ds_read_b64_tr_b16 a[188:189], v237 offset:200\n\t"
    "ds_read_b64_tr_b16 a[190:191], v237 offset:712\n\t"
    "v_mfma_f32_16x16x32_bf16 v[200:203], a[156:159], a[28:31], v[200:203]\n\t"
    "v_cvt_pk_bf16_f32 v211, v186, v187\n\t"
    "v_add_f32 v250, v250, v179\n\t"
    "v_mfma_f32_16x16x32_bf16 v[220:223], a[128:131], a[80:83], 0\n\t"
    "ds_read_b64_tr_b16 a[220:221], v237 offset:8648\n\t"
    "ds_read_b64_tr_b16 a[222:223], v237 offset:9160\n\t"
    "v_mfma_f32_16x16x32_bf16 v[220:223], a[132:135], a[84:87], v[220:223]\n\t"
    "s_add_u32 m0, s56, 21120\n\t"
    "v_exp_f32 v188, v188\n\t"
    "v_mfma_f32_16x16x32_bf16 v[220:223], a[136:139], a[88:91], v[220:223]\n\t"
    "buffer_load_dwordx4 v226, s[44:47], s54 offen offset:0 lds\n\t"
    "v_mfma_f32_16x16x32_bf16 v[220:223], a[140:143], a[92:95], v[220:223]\n\t"
    "s_waitcnt lgkmcnt(4)\n\t"
    "v_exp_f32 v189, v189\n\t"
    "v_mfma_f32_16x16x32_bf16 v[4:7], a[160:163], v[204:207], v[4:7]\n\t"
    "v_cvt_pk_bf16_f32 v212, v188, v189\n\t"
    "v_add_f32 v250, v250, v180\n\t"
    "v_mfma_f32_16x16x32_bf16 v[4:7], a[192:195], v[208:211], v[4:7]\n\t"
    "v_exp_f32 v190, v190\n\t"
    "v_mfma_f32_16x16x32_bf16 v[8:11], a[164:167], v[204:207], v[8:11]\n\t"
    "v_exp_f32 v191, v191\n\t"
    "v_mfma_f32_16x16x32_bf16 v[8:11], a[196:199], v[208:211], v[8:11]\n\t"
    "v_cvt_pk_bf16_f32 v213, v190, v191\n\t"
    "v_add_f32 v250, v250, v181\n\t"
    "v_mfma_f32_16x16x32_bf16 v[12:15], a[168:171], v[204:207], v[12:15]\n\t"
    "v_exp_f32 v192, v192\n\t"
    "v_mfma_f32_16x16x32_bf16 v[12:15], a[200:203], v[208:211], v[12:15]\n\t"
    "v_exp_f32 v193, v193\n\t"
    "v_mfma_f32_16x16x32_bf16 v[16:19], a[172:175], v[204:207], v[16:19]\n\t"
    "v_cvt_pk_bf16_f32 v214, v192, v193\n\t"
    "v_add_f32 v250, v250, v182\n\t"
    "v_mfma_f32_16x16x32_bf16 v[16:19], a[204:207], v[208:211], v[16:19]\n\t"
    "v_exp_f32 v194, v194\n\t"
    "v_mfma_f32_16x16x32_bf16 v[20:23], a[176:179], v[204:207], v[20:23]\n\t"
    "v_exp_f32 v195, v195\n\t"
    "v_mfma_f32_16x16x32_bf16 v[20:23], a[208:211], v[208:211], v[20:23]\n\t"
    "s_add_u32 m0, s55, 25344\n\t"
    "v_cvt_pk_bf16_f32 v215, v194, v195\n\t"
    "v_add_f32 v250, v250, v183\n\t"
    "v_mfma_f32_16x16x32_bf16 v[24:27], a[180:183], v[204:207], v[24:27]\n\t"
    "buffer_load_dwordx4 v227, s[40:43], s54 offen offset:0 nt lds\n\t"
    "v_mfma_f32_16x16x32_bf16 v[24:27], a[212:215], v[208:211], v[24:27]\n\t"
    "v_exp_f32 v196, v196\n\t"
    "v_mfma_f32_16x16x32_bf16 v[28:31], a[184:187], v[204:207], v[28:31]\n\t"
    "v_exp_f32 v197, v197\n\t"
    "v_mfma_f32_16x16x32_bf16 v[28:31], a[216:219], v[208:211], v[28:31]\n\t"
    "s_waitcnt lgkmcnt(0)\n\t"
    "v_cvt_pk_bf16_f32 v216, v196, v197\n\t"
    "v_add_f32 v250, v250, v184\n\t"
    "v_mfma_f32_16x16x32_bf16 v[32:35], a[188:191], v[204:207], v[32:35]\n\t"
    "v_exp_f32 v198, v198\n\t"
    "v_mfma_f32_16x16x32_bf16 v[32:35], a[220:223], v[208:211], v[32:35]\n\t"
    "v_exp_f32 v199, v199\n\t"
    "v_mfma_f32_16x16x32_bf16 v[172:175], a[96:99], a[32:35], 0\n\t"
    "v_cvt_pk_bf16_f32 v217, v198, v199\n\t"
    "v_add_f32 v250, v250, v185\n\t"
    "v_mfma_f32_16x16x32_bf16 v[172:175], a[100:103], a[36:39], v[172:175]\n\t"
    "v_exp_f32 v200, v200\n\t"
    "v_mfma_f32_16x16x32_bf16 v[172:175], a[104:107], a[40:43], v[172:175]\n\t"
    "v_exp_f32 v220, v220\n\t"
    "v_mfma_f32_16x16x32_bf16 v[172:175], a[108:111], a[44:47], v[172:175]\n\t"
    "v_exp_f32 v201, v201\n\t"
    "v_mfma_f32_16x16x32_bf16 v[176:179], a[112:115], a[32:35], 0\n\t"
    "v_exp_f32 v221, v221\n\t"
    "v_mfma_f32_16x16x32_bf16 v[176:179], a[116:119], a[36:39], v[176:179]\n\t"
    "v_add_f32 v250, v250, v186\n\t"
    "v_add_f32 v250, v250, v187\n\t"
    "v_mfma_f32_16x16x32_bf16 v[176:179], a[120:123], a[40:43], v[176:179]\n\t"
    "v_cvt_pk_bf16_f32 v218, v200, v201\n\t"
    "v_cvt_pk_bf16_f32 v224, v220, v221\n\t"
    "v_mfma_f32_16x16x32_bf16 v[176:179], a[124:127], a[44:47], v[176:179]\n\t"
    "v_exp_f32 v202, v202\n\t"
    "v_mfma_f32_16x16x32_bf16 v[180:183], a[128:131], a[32:35], 0\n\t"
    "v_exp_f32 v222, v222\n\t"
    "v_mfma_f32_16x16x32_bf16 v[180:183], a[132:135], a[36:39], v[180:183]\n\t"
    "s_add_u32 m0, s56, 25344\n\t"
    "v_exp_f32 v203, v203\n\t"
    "v_mfma_f32_16x16x32_bf16 v[180:183], a[136:139], a[40:43], v[180:183]\n\t"
    "buffer_load_dwordx4 v227, s[44:47], s54 offen offset:0 lds\n\t"
    "v_mfma_f32_16x16x32_bf16 v[180:183], a[140:143], a[44:47], v[180:183]\n\t"
    "v_exp_f32 v223, v223\n\t"
    "v_mfma_f32_16x16x32_bf16 v[184:187], a[144:147], a[32:35], 0\n\t"
    "v_cvt_pk_bf16_f32 v225, v222, v223\n\t"
    "v_cvt_pk_bf16_f32 v219, v202, v203\n\t"
    "v_mfma_f32_16x16x32_bf16 v[184:187], a[148:151], a[36:39], v[184:187]\n\t"
    "ds_write_b64 v238, v[224:225] offset:1024\n\t"
    "v_mfma_f32_16x16x32_bf16 v[184:187], a[152:155], a[40:43], v[184:187]\n\t"
    "v_exp_f32 v172, v172\n\t"
    "v_mfma_f32_16x16x32_bf16 v[184:187], a[156:159], a[44:47], v[184:187]\n\t"
    "v_exp_f32 v173, v173\n\t"
    "v_mfma_f32_16x16x16_bf16 a[244:247], a[248:249], v[224:225], a[244:247]\n\t"
    "v_cvt_pk_bf16_f32 v204, v172, v173\n\t"
    "v_add_f32 v251, v251, v196\n\t"
    "v_mfma_f32_16x16x32_bf16 v[36:39], a[160:163], v[212:215], v[36:39]\n\t"
    "v_exp_f32 v174, v174\n\t"
    "v_mfma_f32_16x16x32_bf16 v[36:39], a[192:195], v[216:219], v[36:39]\n\t"
    "v_exp_f32 v175, v175\n\t"
    "v_mfma_f32_16x16x32_bf16 v[40:43], a[164:167], v[212:215], v[40:43]\n\t"
    "v_cvt_pk_bf16_f32 v205, v174, v175\n\t"
    "v_add_f32 v251, v251, v197\n\t"
    "v_mfma_f32_16x16x32_bf16 v[40:43], a[196:199], v[216:219], v[40:43]\n\t"
    "v_exp_f32 v176, v176\n\t"
    "v_mfma_f32_16x16x32_bf16 v[44:47], a[168:171], v[212:215], v[44:47]\n\t"
    "v_exp_f32 v177, v177\n\t"
    "v_mfma_f32_16x16x32_bf16 v[44:47], a[200:203], v[216:219], v[44:47]\n\t"
    "v_cvt_pk_bf16_f32 v206, v176, v177\n\t"
    "v_add_f32 v251, v251, v198\n\t"
    "v_mfma_f32_16x16x32_bf16 v[48:51], a[172:175], v[212:215], v[48:51]\n\t"
    "v_exp_f32 v178, v178\n\t"
    "v_mfma_f32_16x16x32_bf16 v[48:51], a[204:207], v[216:219], v[48:51]\n\t"
    "v_exp_f32 v179, v179\n\t"
    "v_mfma_f32_16x16x32_bf16 v[52:55], a[176:179], v[212:215], v[52:55]\n\t"
    "s_add_u32 m0, s55, 29568\n\t"
    "v_cvt_pk_bf16_f32 v207, v178, v179\n\t"
    "v_add_f32 v251, v251, v199\n\t"
    "v_mfma_f32_16x16x32_bf16 v[52:55], a[208:211], v[216:219], v[52:55]\n\t"
    "buffer_load_dwordx4 v3, s[40:43], s54 offen offset:0 nt lds\n\t"
    "v_mfma_f32_16x16x32_bf16 v[56:59], a[180:183], v[212:215], v[56:59]\n\t"
    "v_exp_f32 v180, v180\n\t"
    "v_mfma_f32_16x16x32_bf16 v[56:59], a[212:215], v[216:219], v[56:59]\n\t"
    "v_exp_f32 v181, v181\n\t"
    "v_mfma_f32_16x16x32_bf16 v[60:63], a[184:187], v[212:215], v[60:63]\n\t"
    "v_cvt_pk_bf16_f32 v208, v180, v181\n\t"
    "v_add_f32 v251, v251, v200\n\t"
    "v_mfma_f32_16x16x32_bf16 v[60:63], a[216:219], v[216:219], v[60:63]\n\t"
    "v_exp_f32 v182, v182\n\t"
    "v_mfma_f32_16x16x32_bf16 v[64:67], a[188:191], v[212:215], v[64:67]\n\t"
    "v_exp_f32 v183, v183\n\t"
    "v_mfma_f32_16x16x32_bf16 v[64:67], a[220:223], v[216:219], v[64:67]\n\t"
    "v_cvt_pk_bf16_f32 v209, v182, v183\n\t"
    "v_add_f32 v251, v251, v201\n\t"
    "v_mfma_f32_16x16x32_bf16 a[228:231], a[248:251], v[212:215], a[228:231]\n\t"
    "v_exp_f32 v184, v184\n\t"
    "v_mfma_f32_16x16x32_bf16 v[188:191], a[96:99], a[48:51], 0\n\t"
    "v_exp_f32 v185, v185\n\t"
    "v_mfma_f32_16x16x32_bf16 v[188:191], a[100:103], a[52:55], v[188:191]\n\t"
    "v_cvt_pk_bf16_f32 v210, v184, v185\n\t"
    "v_add_f32 v251, v251, v202\n\t"
    "v_mfma_f32_16x16x32_bf16 v[188:191], a[104:107], a[56:59], v[188:191]\n\t"
    "v_exp_f32 v186, v186\n\t"
    "v_mfma_f32_16x16x32_bf16 v[188:191], a[108:111], a[60:63], v[188:191]\n\t"
    "v_add_f32 v251, v251, v203\n\t"
    "v_mfma_f32_16x16x32_bf16 v[192:195], a[112:115], a[48:51], 0\n\t"
    "v_exp_f32 v187, v187\n\t"
    "v_mfma_f32_16x16x32_bf16 v[192:195], a[116:119], a[52:55], v[192:195]\n\t"
    "v_cvt_pk_bf16_f32 v211, v186, v187\n\t"
    "v_mfma_f32_16x16x32_bf16 v[192:195], a[120:123], a[56:59], v[192:195]\n\t"
    "v_add_f32 v252, v252, v180\n\t"
    "v_add_f32 v252, v252, v181\n\t"
    "v_mfma_f32_16x16x32_bf16 v[192:195], a[124:127], a[60:63], v[192:195]\n\t"
    "s_add_u32 m0, s56, 29568\n\t"
    "v_add_f32 v252, v252, v182\n\t"
    "v_add_f32 v252, v252, v183\n\t"
    "v_mfma_f32_16x16x32_bf16 v[196:199], a[128:131], a[48:51], 0\n\t"
    "buffer_load_dwordx4 v3, s[44:47], s54 offen offset:0 lds\n\t"
    "v_mfma_f32_16x16x32_bf16 v[196:199], a[132:135], a[52:55], v[196:199]\n\t"
    "v_add_f32 v252, v252, v184\n\t"
    "v_add_f32 v252, v252, v185\n\t"
    "v_mfma_f32_16x16x32_bf16 v[196:199], a[136:139], a[56:59], v[196:199]\n\t"
    "v_add_f32 v252, v252, v186\n\t"
    "v_add_f32 v252, v252, v187\n\t"
    "v_mfma_f32_16x16x32_bf16 v[196:199], a[140:143], a[60:63], v[196:199]\n\t"
    "v_exp_f32 v188, v188\n\t"
    "v_mfma_f32_16x16x32_bf16 v[200:203], a[144:147], a[48:51], 0\n\t"
    "v_exp_f32 v189, v189\n\t"
    "v_mfma_f32_16x16x32_bf16 v[200:203], a[148:151], a[52:55], v[200:203]\n\t"
    "v_cvt_pk_bf16_f32 v212, v188, v189\n\t"
    "v_mfma_f32_16x16x32_bf16 v[200:203], a[152:155], a[56:59], v[200:203]\n\t"
    "v_exp_f32 v190, v190\n\t"
    "v_mfma_f32_16x16x32_bf16 v[200:203], a[156:159], a[60:63], v[200:203]\n\t"
    "v_exp_f32 v191, v191\n\t"
    "v_mfma_f32_16x16x32_bf16 v[68:71], a[160:163], v[204:207], v[68:71]\n\t"
    "v_cvt_pk_bf16_f32 v213, v190, v191\n\t"
    "v_mfma_f32_16x16x32_bf16 v[68:71], a[192:195], v[208:211], v[68:71]\n\t"
    "v_exp_f32 v192, v192\n\t"
    "v_mfma_f32_16x16x32_bf16 v[72:75], a[164:167], v[204:207], v[72:75]\n\t"
    "v_exp_f32 v193, v193\n\t"
    "v_mfma_f32_16x16x32_bf16 v[72:75], a[196:199], v[208:211], v[72:75]\n\t"
    "v_cvt_pk_bf16_f32 v214, v192, v193\n\t"
    "v_mfma_f32_16x16x32_bf16 v[76:79], a[168:171], v[204:207], v[76:79]\n\t"
    "v_exp_f32 v194, v194\n\t"
    "v_mfma_f32_16x16x32_bf16 v[76:79], a[200:203], v[208:211], v[76:79]\n\t"
    "v_exp_f32 v195, v195\n\t"
    "v_mfma_f32_16x16x32_bf16 v[80:83], a[172:175], v[204:207], v[80:83]\n\t"
    "v_cvt_pk_bf16_f32 v215, v194, v195\n\t"
    "v_mfma_f32_16x16x32_bf16 v[80:83], a[204:207], v[208:211], v[80:83]\n\t"
    "v_exp_f32 v196, v196\n\t"
    "v_mfma_f32_16x16x32_bf16 v[84:87], a[176:179], v[204:207], v[84:87]\n\t"
    "v_exp_f32 v197, v197\n\t"
    "v_mfma_f32_16x16x32_bf16 v[84:87], a[208:211], v[208:211], v[84:87]\n\t"
    "v_cvt_pk_bf16_f32 v216, v196, v197\n\t"
    "v_add_f32 v253, v253, v196\n\t"
    "v_mfma_f32_16x16x32_bf16 v[88:91], a[180:183], v[204:207], v[88:91]\n\t"
    "v_exp_f32 v198, v198\n\t"
    "v_mfma_f32_16x16x32_bf16 v[88:91], a[212:215], v[208:211], v[88:91]\n\t"
    "v_exp_f32 v199, v199\n\t"
    "v_mfma_f32_16x16x32_bf16 v[92:95], a[184:187], v[204:207], v[92:95]\n\t"
    "v_cvt_pk_bf16_f32 v217, v198, v199\n\t"
    "v_add_f32 v253, v253, v197\n\t"
    "v_mfma_f32_16x16x32_bf16 v[92:95], a[216:219], v[208:211], v[92:95]\n\t"
    "v_exp_f32 v200, v200\n\t"
    "v_mfma_f32_16x16x32_bf16 v[96:99], a[188:191], v[204:207], v[96:99]\n\t"
    "v_exp_f32 v201, v201\n\t"
    "v_mfma_f32_16x16x32_bf16 v[96:99], a[220:223], v[208:211], v[96:99]\n\t"
    "s_add_u32 s54, s54, 16384\n\t"
    "v_cvt_pk_bf16_f32 v218, v200, v201\n\t"
    "v_add_f32 v253, v253, v198\n\t"
    "v_mfma_f32_16x16x32_bf16 a[232:235], a[248:251], v[204:207], a[232:235]\n\t"
    "s_waitcnt vmcnt(16) lgkmcnt(0)\n\t"
    "s_barrier\n\t"
    "v_mfma_f32_16x16x32_bf16 v[172:175], a[96:99], a[64:67], 0\n\t"
    "ds_read_b128 a[96:99], v236 offset:16896\n\t"
    "v_add_f32 v253, v253, v199\n\t"
    "v_mfma_f32_16x16x32_bf16 v[172:175], a[100:103], a[68:71], v[172:175]\n\t"
    "v_exp_f32 v202, v202\n\t"
    "v_mfma_f32_16x16x32_bf16 v[172:175], a[104:107], a[72:75], v[172:175]\n\t"
    "ds_read_b128 a[100:103], v236 offset:16960\n\t"
    "ds_read_b128 a[104:107], v236 offset:17024\n\t"
    "v_mfma_f32_16x16x32_bf16 v[172:175], a[108:111], a[76:79], v[172:175]\n\t"
    "s_and_b32 s54, s54, 0x7ffff\n\t"
    "v_exp_f32 v203, v203\n\t"
    "v_mfma_f32_16x16x32_bf16 v[176:179], a[112:115], a[64:67], 0\n\t"
    "ds_read_b128 a[108:111], v236 offset:17088\n\t"
    "ds_read_b128 a[112:115], v236 offset:17408\n\t"
    "v_mfma_f32_16x16x32_bf16 v[176:179], a[116:119], a[68:71], v[176:179]\n\t"
    "v_cvt_pk_bf16_f32 v219, v202, v203\n\t"
    "v_add_f32 v253, v253, v200\n\t"
    "v_mfma_f32_16x16x32_bf16 v[176:179], a[120:123], a[72:75], v[176:179]\n\t"
    "ds_read_b128 a[116:119], v236 offset:17472\n\t"
    "ds_read_b128 a[120:123], v236 offset:17536\n\t"
    "v_mfma_f32_16x16x32_bf16 v[176:179], a[124:127], a[76:79], v[176:179]\n\t"
    "v_add_f32 v253, v253, v201\n\t"
    "v_add_f32 v253, v253, v202\n\t"
    "v_mfma_f32_16x16x32_bf16 v[180:183], a[128:131], a[64:67], 0\n\t"
    "ds_read_b128 a[124:127], v236 offset:17600\n\t"
    "v_add_f32 v253, v253, v203\n\t"
    "v_mfma_f32_16x16x32_bf16 v[180:183], a[132:135], a[68:71], v[180:183]\n\t"
    "ds_read_b128 a[128:131], v236 offset:25344\n\t"
    "v_mfma_f32_16x16x32_bf16 v[180:183], a[136:139], a[72:75], v[180:183]\n\t"
    "ds_read_b128 a[132:135], v236 offset:25408\n\t"
    "v_mfma_f32_16x16x32_bf16 v[180:183], a[140:143], a[76:79], v[180:183]\n\t"
    "s_add_u32 s55, s55, s57\n\t"
    "v_exp_f32 v172, v172\n\t"
    "v_mfma_f32_16x16x32_bf16 v[184:187], a[144:147], a[64:67], 0\n\t"
    "ds_read_b128 a[136:139], v236 offset:25472\n\t"
    "ds_read_b128 a[140:143], v236 offset:25536\n\t"
    "v_mfma_f32_16x16x32_bf16 v[184:187], a[148:151], a[68:71], v[184:187]\n\t"
    "s_add_u32 s56, s56, s57\n\t"
    "v_exp_f32 v173, v173\n\t"
    "v_mfma_f32_16x16x32_bf16 v[184:187], a[152:155], a[72:75], v[184:187]\n\t"
    "ds_read_b128 a[144:147], v236 offset:25856\n\t"
    "ds_read_b128 a[148:151], v236 offset:25920\n\t"
    "v_mfma_f32_16x16x32_bf16 v[184:187], a[156:159], a[76:79], v[184:187]\n\t"
    "s_sub_u32 s57, 0, s57\n\t"
    "v_cvt_pk_bf16_f32 v204, v172, v173\n\t"
    "v_add_f32 v254, v254, v172\n\t"
    "v_mfma_f32_16x16x32_bf16 v[100:103], a[160:163], v[212:215], v[100:103]\n\t"
    "ds_read_b128 a[152:155], v236 offset:25984\n\t"
    "ds_read_b128 a[156:159], v236 offset:26048\n\t"
    "v_mfma_f32_16x16x32_bf16 v[100:103], a[192:195], v[216:219], v[100:103]\n\t"
    "v_exp_f32 v174, v174\n\t"
    "v_mfma_f32_16x16x32_bf16 v[104:107], a[164:167], v[212:215], v[104:107]\n\t"
    "ds_read_b128 v[228:231], v238 offset:0\n\t"
    "ds_read_b128 v[232:235], v238 offset:1024\n\t"
    "v_mfma_f32_16x16x32_bf16 v[104:107], a[196:199], v[216:219], v[104:107]\n\t"
    "v_exp_f32 v175, v175\n\t"
    "v_mfma_f32_16x16x32_bf16 v[108:111], a[168:171], v[212:215], v[108:111]\n\t"
    "v_cvt_pk_bf16_f32 v205, v174, v175\n\t"
    "v_add_f32 v254, v254, v173\n\t"
    "v_mfma_f32_16x16x32_bf16 v[108:111], a[200:203], v[216:219], v[108:111]\n\t"
    "v_exp_f32 v176, v176\n\t"
    "v_mfma_f32_16x16x32_bf16 v[112:115], a[172:175], v[212:215], v[112:115]\n\t"
    "v_exp_f32 v177, v177\n\t"
    "v_mfma_f32_16x16x32_bf16 v[112:115], a[204:207], v[216:219], v[112:115]\n\t"
    "v_cvt_pk_bf16_f32 v206, v176, v177\n\t"
    "v_add_f32 v254, v254, v174\n\t"
    "v_mfma_f32_16x16x32_bf16 v[116:119], a[176:179], v[212:215], v[116:119]\n\t"
    "v_exp_f32 v178, v178\n\t"
    "v_mfma_f32_16x16x32_bf16 v[116:119], a[208:211], v[216:219], v[116:119]\n\t"
    "v_exp_f32 v179, v179\n\t"
    "v_mfma_f32_16x16x32_bf16 v[120:123], a[180:183], v[212:215], v[120:123]\n\t"
    "v_cvt_pk_bf16_f32 v207, v178, v179\n\t"
    "v_add_f32 v254, v254, v175\n\t"
    "v_mfma_f32_16x16x32_bf16 v[120:123], a[212:215], v[216:219], v[120:123]\n\t"
    "v_exp_f32 v180, v180\n\t"
    "v_mfma_f32_16x16x32_bf16 v[124:127], a[184:187], v[212:215], v[124:127]\n\t"
    "v_exp_f32 v181, v181\n\t"
    "v_mfma_f32_16x16x32_bf16 v[124:127], a[216:219], v[216:219], v[124:127]\n\t"
    "v_cvt_pk_bf16_f32 v208, v180, v181\n\t"
    "v_add_f32 v254, v254, v176\n\t"
    "v_mfma_f32_16x16x32_bf16 v[128:131], a[188:191], v[212:215], v[128:131]\n\t"
    "v_exp_f32 v182, v182\n\t"
    "v_mfma_f32_16x16x32_bf16 v[128:131], a[220:223], v[216:219], v[128:131]\n\t"
    "v_exp_f32 v183, v183\n\t"
    "v_mfma_f32_16x16x32_bf16 a[236:239], a[248:251], v[212:215], a[236:239]\n\t"
    "s_waitcnt lgkmcnt(0)\n\t"
    "v_cvt_pk_bf16_f32 v209, v182, v183\n\t"
    "v_add_f32 v254, v254, v177\n\t"
    "s_sub_u32 s60, s60, 1\n\t"
    "s_cbranch_scc0 LOOP2_%=\n\t"
    "TAIL2_%=:\n\t"
    "v_mfma_f32_16x16x32_bf16 v[188:191], a[96:99], a[0:3], 0\n\t"
    "v_exp_f32 v184, v184\n\t"
    "v_mfma_f32_16x16x32_bf16 v[188:191], a[100:103], a[4:7], v[188:191]\n\t"
    "v_exp_f32 v185, v185\n\t"
    "v_mfma_f32_16x16x32_bf16 v[188:191], a[104:107], a[8:11], v[188:191]\n\t"
    "v_mfma_f32_16x16x32_bf16 v[188:191], a[108:111], a[12:15], v[188:191]\n\t"
    "v_cvt_pk_bf16_f32 v210, v184, v185\n\t"
    "v_add_f32 v254, v254, v178\n\t"
    "v_mfma_f32_16x16x32_bf16 v[192:195], a[112:115], a[0:3], 0\n\t"
    "v_exp_f32 v186, v186\n\t"
    "v_mfma_f32_16x16x32_bf16 v[192:195], a[116:119], a[4:7], v[192:195]\n\t"
    "v_exp_f32 v187, v187\n\t"
    "v_mfma_f32_16x16x32_bf16 v[192:195], a[120:123], a[8:11], v[192:195]\n\t"
    "v_cvt_pk_bf16_f32 v211, v186, v187\n\t"
    "v_add_f32 v254, v254, v179\n\t"
    "v_mfma_f32_16x16x32_bf16 v[192:195], a[124:127], a[12:15], v[192:195]\n\t"
    "v_add_f32 v254, v254, v180\n\t"
    "v_add_f32 v254, v254, v181\n\t"
    "v_mfma_f32_16x16x32_bf16 v[196:199], a[128:131], a[0:3], 0\n\t"
    "v_add_f32 v254, v254, v182\n\t"
    "v_add_f32 v254, v254, v183\n\t"
    "v_mfma_f32_16x16x32_bf16 v[196:199], a[132:135], a[4:7], v[196:199]\n\t"
    "v_add_f32 v254, v254, v184\n\t"
    "v_add_f32 v254, v254, v185\n\t"
    "v_mfma_f32_16x16x32_bf16 v[196:199], a[136:139], a[8:11], v[196:199]\n\t"
    "v_add_f32 v254, v254, v186\n\t"
    "v_add_f32 v254, v254, v187\n\t"
    "v_mfma_f32_16x16x32_bf16 v[196:199], a[140:143], a[12:15], v[196:199]\n\t"
    "v_exp_f32 v188, v188\n\t"
    "v_mfma_f32_16x16x32_bf16 v[200:203], a[144:147], a[0:3], 0\n\t"
    "v_exp_f32 v189, v189\n\t"
    "v_mfma_f32_16x16x32_bf16 v[200:203], a[148:151], a[4:7], v[200:203]\n\t"
    "v_cvt_pk_bf16_f32 v212, v188, v189\n\t"
    "v_add_f32 v250, v250, v188\n\t"
    "v_mfma_f32_16x16x32_bf16 v[200:203], a[152:155], a[8:11], v[200:203]\n\t"
    "v_exp_f32 v190, v190\n\t"
    "v_mfma_f32_16x16x32_bf16 v[200:203], a[156:159], a[12:15], v[200:203]\n\t"
    "v_exp_f32 v191, v191\n\t"
    "v_mfma_f32_16x16x32_bf16 v[164:167], a[176:179], v[228:231], v[164:167]\n\t"
    "v_cvt_pk_bf16_f32 v213, v190, v191\n\t"
    "v_add_f32 v250, v250, v189\n\t"
    "v_mfma_f32_16x16x32_bf16 v[164:167], a[208:211], v[232:235], v[164:167]\n\t"
    "v_exp_f32 v192, v192\n\t"
    "v_mfma_f32_16x16x32_bf16 v[168:171], a[180:183], v[228:231], v[168:171]\n\t"
    "v_mfma_f32_16x16x32_bf16 v[168:171], a[212:215], v[232:235], v[168:171]\n\t"
    "v_exp_f32 v193, v193\n\t"
    "v_mfma_f32_16x16x32_bf16 v[132:135], a[160:163], v[204:207], v[132:135]\n\t"
    "ds_read_b64_tr_b16 a[160:161], v237 offset:16896\n\t"
    "ds_read_b64_tr_b16 a[162:163], v237 offset:17408\n\t"
    "v_accvgpr_write_b32 a0, 0\n\t"
    "v_mfma_f32_16x16x32_bf16 v[132:135], a[192:195], v[208:211], v[132:135]\n\t"
    "v_cvt_pk_bf16_f32 v214, v192, v193\n\t"
    "v_add_f32 v250, v250, v190\n\t"
    "v_accvgpr_write_b32 a1, 0\n\t"
    "v_mfma_f32_16x16x32_bf16 v[136:139], a[164:167], v[204:207], v[136:139]\n\t"
    "ds_read_b64_tr_b16 a[192:193], v237 offset:25344\n\t"
    "ds_read_b64_tr_b16 a[194:195], v237 offset:25856\n\t"
    "v_accvgpr_write_b32 a2, 0\n\t"
    "v_mfma_f32_16x16x32_bf16 v[136:139], a[196:199], v[208:211], v[136:139]\n\t"
    "v_exp_f32 v194, v194\n\t"
    "v_accvgpr_write_b32 a3, 0\n\t"
    "v_mfma_f32_16x16x32_bf16 v[140:143], a[168:171], v[204:207], v[140:143]\n\t"
    "ds_read_b64_tr_b16 a[164:165], v237 offset:16904\n\t"
    "ds_read_b64_tr_b16 a[166:167], v237 offset:17416\n\t"
    "v_accvgpr_write_b32 a4, 0\n\t"
    "v_mfma_f32_16x16x32_bf16 v[140:143], a[200:203], v[208:211], v[140:143]\n\t"
    "v_exp_f32 v195, v195\n\t"
    "v_mfma_f32_16x16x32_bf16 v[144:147], a[172:175], v[204:207], v[144:147]\n\t"
    "ds_read_b64_tr_b16 a[196:197], v237 offset:25352\n\t"
    "ds_read_b64_tr_b16 a[198:199], v237 offset:25864\n\t"
    "v_mfma_f32_16x16x32_bf16 v[144:147], a[204:207], v[208:211], v[144:147]\n\t"
    "v_cvt_pk_bf16_f32 v215, v194, v195\n\t"
    "v_add_f32 v250, v250, v191\n\t"
    "v_mfma_f32_16x16x32_bf16 v[148:151], a[176:179], v[204:207], v[148:151]\n\t"
    "ds_read_b64_tr_b16 a[168:169], v237 offset:16960\n\t"
    "ds_read_b64_tr_b16 a[170:171], v237 offset:17472\n\t"
    "v_mfma_f32_16x16x32_bf16 v[148:151], a[208:211], v[208:211], v[148:151]\n\t"
    "v_exp_f32 v196, v196\n\t"
    "v_mfma_f32_16x16x32_bf16 v[152:155], a[180:183], v[204:207], v[152:155]\n\t"
    "ds_read_b64_tr_b16 a[200:201], v237 offset:25408\n\t"
    "ds_read_b64_tr_b16 a[202:203], v237 offset:25920\n\t"
    "v_mfma_f32_16x16x32_bf16 v[152:155], a[212:215], v[208:211], v[152:155]\n\t"
    "v_exp_f32 v197, v197\n\t"
    "v_mfma_f32_16x16x32_bf16 v[156:159], a[184:187], v[204:207], v[156:159]\n\t"
    "ds_read_b64_tr_b16 a[172:173], v237 offset:16968\n\t"
    "ds_read_b64_tr_b16 a[174:175], v237 offset:17480\n\t"
    "v_mfma_f32_16x16x32_bf16 v[156:159], a[216:219], v[208:211], v[156:159]\n\t"
    "v_cvt_pk_bf16_f32 v216, v196, v197\n\t"
    "v_add_f32 v250, v250, v192\n\t"
    "v_mfma_f32_16x16x32_bf16 v[160:163], a[188:191], v[204:207], v[160:163]\n\t"
    "v_mfma_f32_16x16x32_bf16 v[160:163], a[220:223], v[208:211], v[160:163]\n\t"
    "v_exp_f32 v198, v198\n\t"
    "v_mfma_f32_16x16x32_bf16 v[172:175], a[96:99], a[16:19], 0\n\t"
    "ds_read_b64_tr_b16 a[204:205], v237 offset:25416\n\t"
    "ds_read_b64_tr_b16 a[206:207], v237 offset:25928\n\t"
    "v_mfma_f32_16x16x32_bf16 v[172:175], a[100:103], a[20:23], v[172:175]\n\t"
    "v_exp_f32 v199, v199\n\t"
    "v_mfma_f32_16x16x32_bf16 v[172:175], a[104:107], a[24:27], v[172:175]\n\t"
    "ds_read_b64_tr_b16 a[176:177], v237 offset:17024\n\t"
    "ds_read_b64_tr_b16 a[178:179], v237 offset:17536\n\t"
    "v_mfma_f32_16x16x32_bf16 v[172:175], a[108:111], a[28:31], v[172:175]\n\t"
    "v_cvt_pk_bf16_f32 v217, v198, v199\n\t"
    "v_add_f32 v250, v250, v193\n\t"
    "v_mfma_f32_16x16x32_bf16 v[176:179], a[112:115], a[16:19], 0\n\t"
    "ds_read_b64_tr_b16 a[208:209], v237 offset:25472\n\t"
    "ds_read_b64_tr_b16 a[210:211], v237 offset:25984\n\t"
    "v_mfma_f32_16x16x32_bf16 v[176:179], a[116:119], a[20:23], v[176:179]\n\t"
    "v_exp_f32 v200, v200\n\t"
    "v_mfma_f32_16x16x32_bf16 v[176:179], a[120:123], a[24:27], v[176:179]\n\t"
    "ds_read_b64_tr_b16 a[180:181], v237 offset:17032\n\t"
    "ds_read_b64_tr_b16 a[182:183], v237 offset:17544\n\t"
    "v_mfma_f32_16x16x32_bf16 v[176:179], a[124:127], a[28:31], v[176:179]\n\t"
    "v_exp_f32 v201, v201\n\t"
    "v_mfma_f32_16x16x32_bf16 v[180:183], a[128:131], a[16:19], 0\n\t"
    "ds_read_b64_tr_b16 a[212:213], v237 offset:25480\n\t"
    "ds_read_b64_tr_b16 a[214:215], v237 offset:25992\n\t"
    "v_mfma_f32_16x16x32_bf16 v[180:183], a[132:135], a[20:23], v[180:183]\n\t"
    "v_cvt_pk_bf16_f32 v218, v200, v201\n\t"
    "v_add_f32 v250, v250, v194\n\t"
    "v_mfma_f32_16x16x32_bf16 v[180:183], a[136:139], a[24:27], v[180:183]\n\t"
    "ds_read_b64_tr_b16 a[184:185], v237 offset:17088\n\t"
    "ds_read_b64_tr_b16 a[186:187], v237 offset:17600\n\t"
    "v_mfma_f32_16x16x32_bf16 v[180:183], a[140:143], a[28:31], v[180:183]\n\t"
    "v_exp_f32 v202, v202\n\t"
    "v_mfma_f32_16x16x32_bf16 v[184:187], a[144:147], a[16:19], 0\n\t"
    "ds_read_b64_tr_b16 a[216:217], v237 offset:25536\n\t"
    "ds_read_b64_tr_b16 a[218:219], v237 offset:26048\n\t"
    "v_mfma_f32_16x16x32_bf16 v[184:187], a[148:151], a[20:23], v[184:187]\n\t"
    "v_exp_f32 v203, v203\n\t"
    "v_mfma_f32_16x16x32_bf16 v[184:187], a[152:155], a[24:27], v[184:187]\n\t"
    "ds_read_b64_tr_b16 a[188:189], v237 offset:17096\n\t"
    "ds_read_b64_tr_b16 a[190:191], v237 offset:17608\n\t"
    "v_mfma_f32_16x16x32_bf16 v[184:187], a[156:159], a[28:31], v[184:187]\n\t"
    "v_cvt_pk_bf16_f32 v219, v202, v203\n\t"
    "v_add_f32 v250, v250, v195\n\t"
    "v_mfma_f32_16x16x32_bf16 v[220:223], a[128:131], a[80:83], 0\n\t"
    "ds_read_b64_tr_b16 a[220:221], v237 offset:25544\n\t"
    "ds_read_b64_tr_b16 a[222:223], v237 offset:26056\n\t"
    "v_mfma_f32_16x16x32_bf16 v[220:223], a[132:135], a[84:87], v[220:223]\n\t"
    "v_exp_f32 v172, v172\n\t"
    "v_mfma_f32_16x16x32_bf16 v[220:223], a[136:139], a[88:91], v[220:223]\n\t"
    "v_mfma_f32_16x16x32_bf16 v[220:223], a[140:143], a[92:95], v[220:223]\n\t"
    "s_waitcnt lgkmcnt(4)\n\t"
    "v_exp_f32 v173, v173\n\t"
    "v_mfma_f32_16x16x32_bf16 v[4:7], a[160:163], v[212:215], v[4:7]\n\t"
    "v_cvt_pk_bf16_f32 v204, v172, v173\n\t"
    "v_add_f32 v250, v250, v196\n\t"
    "v_mfma_f32_16x16x32_bf16 v[4:7], a[192:195], v[216:219], v[4:7]\n\t"
    "v_exp_f32 v174, v174\n\t"
    "v_mfma_f32_16x16x32_bf16 v[8:11], a[164:167], v[212:215], v[8:11]\n\t"
    "v_exp_f32 v175, v175\n\t"
    "v_mfma_f32_16x16x32_bf16 v[8:11], a[196:199], v[216:219], v[8:11]\n\t"
    "v_cvt_pk_bf16_f32 v205, v174, v175\n\t"
    "v_add_f32 v250, v250, v197\n\t"
    "v_mfma_f32_16x16x32_bf16 v[12:15], a[168:171], v[212:215], v[12:15]\n\t"
    "v_exp_f32 v176, v176\n\t"
    "v_mfma_f32_16x16x32_bf16 v[12:15], a[200:203], v[216:219], v[12:15]\n\t"
    "v_exp_f32 v177, v177\n\t"
    "v_mfma_f32_16x16x32_bf16 v[16:19], a[172:175], v[212:215], v[16:19]\n\t"
    "v_cvt_pk_bf16_f32 v206, v176, v177\n\t"
    "v_add_f32 v250, v250, v198\n\t"
    "v_mfma_f32_16x16x32_bf16 v[16:19], a[204:207], v[216:219], v[16:19]\n\t"
    "v_exp_f32 v178, v178\n\t"
    "v_mfma_f32_16x16x32_bf16 v[20:23], a[176:179], v[212:215], v[20:23]\n\t"
    "v_exp_f32 v179, v179\n\t"
    "v_mfma_f32_16x16x32_bf16 v[20:23], a[208:211], v[216:219], v[20:23]\n\t"
    "v_cvt_pk_bf16_f32 v207, v178, v179\n\t"
    "v_add_f32 v250, v250, v199\n\t"
    "v_mfma_f32_16x16x32_bf16 v[24:27], a[180:183], v[212:215], v[24:27]\n\t"
    "v_mfma_f32_16x16x32_bf16 v[24:27], a[212:215], v[216:219], v[24:27]\n\t"
    "v_exp_f32 v180, v180\n\t"
    "v_mfma_f32_16x16x32_bf16 v[28:31], a[184:187], v[212:215], v[28:31]\n\t"
    "v_exp_f32 v181, v181\n\t"
    "v_mfma_f32_16x16x32_bf16 v[28:31], a[216:219], v[216:219], v[28:31]\n\t"
    "s_waitcnt lgkmcnt(0)\n\t"
    "v_cvt_pk_bf16_f32 v208, v180, v181\n\t"
    "v_add_f32 v250, v250, v200\n\t"
    "v_mfma_f32_16x16x32_bf16 v[32:35], a[188:191], v[212:215], v[32:35]\n\t"
    "v_exp_f32 v182, v182\n\t"
    "v_mfma_f32_16x16x32_bf16 v[32:35], a[220:223], v[216:219], v[32:35]\n\t"
    "v_exp_f32 v183, v183\n\t"
    "v_mfma_f32_16x16x32_bf16 v[188:191], a[96:99], a[32:35], 0\n\t"
    "v_cvt_pk_bf16_f32 v209, v182, v183\n\t"
    "v_add_f32 v250, v250, v201\n\t"
    "v_mfma_f32_16x16x32_bf16 v[188:191], a[100:103], a[36:39], v[188:191]\n\t"
    "v_exp_f32 v184, v184\n\t"
    "v_mfma_f32_16x16x32_bf16 v[188:191], a[104:107], a[40:43], v[188:191]\n\t"
    "v_exp_f32 v220, v220\n\t"
    "v_mfma_f32_16x16x32_bf16 v[188:191], a[108:111], a[44:47], v[188:191]\n\t"
    "v_exp_f32 v185, v185\n\t"
    "v_mfma_f32_16x16x32_bf16 v[192:195], a[112:115], a[32:35], 0\n\t"
    "v_exp_f32 v221, v221\n\t"
    "v_mfma_f32_16x16x32_bf16 v[192:195], a[116:119], a[36:39], v[192:195]\n\t"
    "v_add_f32 v250, v250, v202\n\t"
    "v_add_f32 v250, v250, v203\n\t"
    "v_mfma_f32_16x16x32_bf16 v[192:195], a[120:123], a[40:43], v[192:195]\n\t"
    "v_cvt_pk_bf16_f32 v210, v184, v185\n\t"
    "v_cvt_pk_bf16_f32 v224, v220, v221\n\t"
    "v_mfma_f32_16x16x4_f32 a[224:227], a252, v250, 0\n\t"
    "v_mfma_f32_16x16x32_bf16 v[192:195], a[124:127], a[44:47], v[192:195]\n\t"
    "v_exp_f32 v186, v186\n\t"
    "v_mfma_f32_16x16x32_bf16 v[196:199], a[128:131], a[32:35], 0\n\t"
    "v_exp_f32 v222, v222\n\t"
    "v_mfma_f32_16x16x32_bf16 v[196:199], a[132:135], a[36:39], v[196:199]\n\t"
    "v_exp_f32 v187, v187\n\t"
    "v_mfma_f32_16x16x32_bf16 v[196:199], a[136:139], a[40:43], v[196:199]\n\t"
    "s_nop 2\n\t"
    "v_accvgpr_read_b32 v242, a224\n\t"
    "v_subrev_u32 v246, 0x2b800000, v242\n\t"
    "v_cmp_lt_u32 vcc, 0x28000000, v246\n\t"
    "s_nop 1\n\t"
    "s_or_b64 s[62:63], s[62:63], vcc\n\t"
    "v_rcp_f32 v243, v242\n\t"
    "s_mov_b32 s64, 40960\n\t"
    "v_mul_f32 v4, v4, v243\n\t"
    "v_mfma_f32_16x16x32_bf16 v[196:199], a[140:143], a[44:47], v[196:199]\n\t"
    "v_exp_f32 v223, v223\n\t"
    "v_mul_f32 v5, v5, v243\n\t"
    "v_mul_f32 v6, v6, v243\n\t"
    "v_mul_f32 v7, v7, v243\n\t"
    "v_mul_f32 v8, v8, v243\n\t"
    "v_mfma_f32_16x16x32_bf16 v[200:203], a[144:147], a[32:35], 0\n\t"
    "v_cvt_pk_bf16_f32 v225, v222, v223\n\t"
    "v_cvt_pk_bf16_f32 v211, v186, v187\n\t"
    "v_mul_f32 v9, v9, v243\n\t"
    "v_mul_f32 v10, v10, v243\n\t"
    "v_mul_f32 v11, v11, v243\n\t"
    "v_cvt_pk_bf16_f32 v4, v4, v5\n\t"
    "v_mfma_f32_16x16x32_bf16 v[200:203], a[148:151], a[36:39], v[200:203]\n\t"
    "ds_write_b64 v238, v[224:225] offset:3072\n\t"
    "v_cvt_pk_bf16_f32 v5, v6, v7\n\t"
    "v_cvt_pk_bf16_f32 v6, v8, v9\n\t"
    "v_cvt_pk_bf16_f32 v7, v10, v11\n\t"
    "s_nop 1\n\t"
    "v_mfma_f32_16x16x32_bf16 a[4:7], a[0:3], v[4:7], a[4:7]\n\t"
    "v_mfma_f32_16x16x32_bf16 v[200:203], a[152:155], a[40:43], v[200:203]\n\t"
    "v_exp_f32 v188, v188\n\t"
    "v_mul_f32 v12, v12, v243\n\t"
    "v_mul_f32 v13, v13, v243\n\t"
    "v_mul_f32 v14, v14, v243\n\t"
    "v_mul_f32 v15, v15, v243\n\t"
    "v_mfma_f32_16x16x32_bf16 v[200:203], a[156:159], a[44:47], v[200:203]\n\t"
    "buffer_store_dwordx4 v[4:7], v241, s[48:51], s64 offen offset:0 sc0 sc1\n\t"
    "v_exp_f32 v189, v189\n\t"
    "v_mul_f32 v16, v16, v243\n\t"
    "v_mul_f32 v17, v17, v243\n\t"
    "v_mul_f32 v18, v18, v243\n\t"
    "v_mul_f32 v19, v19, v243\n\t"
    "v_mfma_f32_16x16x16_bf16 a[244:247], a[248:249], v[224:225], a[244:247]\n\t"
    "v_cvt_pk_bf16_f32 v212, v188, v189\n\t"
    "v_add_f32 v251, v251, v180\n\t"
    "v_cvt_pk_bf16_f32 v12, v12, v13\n\t"
    "v_cvt_pk_bf16_f32 v13, v14, v15\n\t"
    "v_cvt_pk_bf16_f32 v14, v16, v17\n\t"
    "v_cvt_pk_bf16_f32 v15, v18, v19\n\t"
    "v_mfma_f32_16x16x32_bf16 v[36:39], a[160:163], v[204:207], v[36:39]\n\t"
    "v_exp_f32 v190, v190\n\t"
    "v_mfma_f32_16x16x32_bf16 a[4:7], a[0:3], v[12:15], a[4:7]\n\t"
    "v_mul_f32 v20, v20, v243\n\t"
    "v_mul_f32 v21, v21, v243\n\t"
    "v_mul_f32 v22, v22, v243\n\t"
    "v_mfma_f32_16x16x32_bf16 v[36:39], a[192:195], v[208:211], v[36:39]\n\t"
    "v_exp_f32 v191, v191\n\t"
    "v_mul_f32 v23, v23, v243\n\t"
    "v_mul_f32 v24, v24, v243\n\t"
    "v_mul_f32 v25, v25, v243\n\t"
    "v_mul_f32 v26, v26, v243\n\t"
    "v_mfma_f32_16x16x32_bf16 v[40:43], a[164:167], v[204:207], v[40:43]\n\t"
    "buffer_store_dwordx4 v[12:15], v241, s[48:51], s64 offen offset:64 sc0 sc1\n\t"
    "v_cvt_pk_bf16_f32 v213, v190, v191\n\t"
    "v_add_f32 v251, v251, v181\n\t"
    "v_mul_f32 v27, v27, v243\n\t"
    "v_cvt_pk_bf16_f32 v20, v20, v21\n\t"
    "v_cvt_pk_bf16_f32 v21, v22, v23\n\t"
    "v_mfma_f32_16x16x32_bf16 v[40:43], a[196:199], v[208:211], v[40:43]\n\t"
    "v_exp_f32 v192, v192\n\t"
    "v_cvt_pk_bf16_f32 v22, v24, v25\n\t"
    "v_cvt_pk_bf16_f32 v23, v26, v27\n\t"
    "s_nop 1\n\t"
    "v_mfma_f32_16x16x32_bf16 a[4:7], a[0:3], v[20:23], a[4:7]\n\t"
    "v_mul_f32 v28, v28, v243\n\t"
    "v_mfma_f32_16x16x32_bf16 v[44:47], a[168:171], v[204:207], v[44:47]\n\t"
    "v_exp_f32 v193, v193\n\t"
    "v_mul_f32 v29, v29, v243\n\t"
    "v_mul_f32 v30, v30, v243\n\t"
    "v_mul_f32 v31, v31, v243\n\t"
    "v_mul_f32 v32, v32, v243\n\t"
    "v_mfma_f32_16x16x32_bf16 v[44:47], a[200:203], v[208:211], v[44:47]\n\t"
    "buffer_store_dwordx4 v[20:23], v241, s[48:51], s64 offen offset:128 sc0 sc1\n\t"
    "v_cvt_pk_bf16_f32 v214, v192, v193\n\t"
    "v_add_f32 v251, v251, v182\n\t"
    "v_mul_f32 v33, v33, v243\n\t"
    "v_mul_f32 v34, v34, v243\n\t"
    "v_mul_f32 v35, v35, v243\n\t"
    "v_mfma_f32_16x16x32_bf16 v[48:51], a[172:175], v[204:207], v[48:51]\n\t"
    "v_exp_f32 v194, v194\n\t"
    "v_cvt_pk_bf16_f32 v28, v28, v29\n\t"
    "v_cvt_pk_bf16_f32 v29, v30, v31\n\t"
    "v_cvt_pk_bf16_f32 v30, v32, v33\n\t"
    "v_cvt_pk_bf16_f32 v31, v34, v35\n\t"
    "v_mfma_f32_16x16x32_bf16 v[48:51], a[204:207], v[208:211], v[48:51]\n\t"
    "v_exp_f32 v195, v195\n\t"
    "v_mfma_f32_16x16x32_bf16 a[4:7], a[0:3], v[28:31], a[4:7]\n\t"
    "v_mfma_f32_16x16x32_bf16 v[52:55], a[176:179], v[204:207], v[52:55]\n\t"
    "v_cvt_pk_bf16_f32 v215, v194, v195\n\t"
    "v_add_f32 v251, v251, v183\n\t"
    "v_and_b32 v220, 15, v1\n\t"
    "v_mfma_f32_16x16x32_bf16 v[52:55], a[208:211], v[208:211], v[52:55]\n\t"
    "buffer_store_dwordx4 v[28:31], v241, s[48:51], s64 offen offset:192 sc0 sc1\n\t"
    "v_lshlrev_b32 v220, 2, v220\n\t"
    "v_mfma_f32_16x16x32_bf16 v[56:59], a[180:183], v[204:207], v[56:59]\n\t"
    "s_add_u32 s61, s53, 139264\n\t"
    "v_exp_f32 v196, v196\n\t"
    "v_add_u32 v220, s61, v220\n\t"
    "v_mfma_f32_16x16x32_bf16 v[56:59], a[212:215], v[208:211], v[56:59]\n\t"
    "v_exp_f32 v197, v197\n\t"
    "v_accvgpr_read_b32 v221, a244\n\t"
    "v_mfma_f32_16x16x32_bf16 v[60:63], a[184:187], v[204:207], v[60:63]\n\t"
    "ds_write_b32 v220, v221 offset:128\n\t"
    "v_cvt_pk_bf16_f32 v216, v196, v197\n\t"
    "v_add_f32 v251, v251, v184\n\t"
    "v_mfma_f32_16x16x32_bf16 v[60:63], a[216:219], v[208:211], v[60:63]\n\t"
    "v_exp_f32 v198, v198\n\t"
    "v_mfma_f32_16x16x32_bf16 v[64:67], a[188:191], v[204:207], v[64:67]\n\t"
    "v_exp_f32 v199, v199\n\t"
    "v_mfma_f32_16x16x32_bf16 v[64:67], a[220:223], v[208:211], v[64:67]\n\t"
    "v_cvt_pk_bf16_f32 v217, v198, v199\n\t"
    "v_add_f32 v251, v251, v185\n\t"
    "v_mfma_f32_16x16x32_bf16 a[228:231], a[248:251], v[204:207], a[228:231]\n\t"
    "v_exp_f32 v200, v200\n\t"
    "v_mfma_f32_16x16x32_bf16 v[172:175], a[96:99], a[48:51], 0\n\t"
    "v_exp_f32 v201, v201\n\t"
    "v_mfma_f32_16x16x32_bf16 v[172:175], a[100:103], a[52:55], v[172:175]\n\t"
    "v_cvt_pk_bf16_f32 v218, v200, v201\n\t"
    "v_add_f32 v251, v251, v186\n\t"
    "v_mfma_f32_16x16x32_bf16 v[172:175], a[104:107], a[56:59], v[172:175]\n\t"
    "v_exp_f32 v202, v202\n\t"
    "v_mfma_f32_16x16x32_bf16 v[172:175], a[108:111], a[60:63], v[172:175]\n\t"
    "v_add_f32 v251, v251, v187\n\t"
    "v_mfma_f32_16x16x32_bf16 v[176:179], a[112:115], a[48:51], 0\n\t"
    "v_exp_f32 v203, v203\n\t"
    "v_mfma_f32_16x16x32_bf16 v[176:179], a[116:119], a[52:55], v[176:179]\n\t"
    "v_cvt_pk_bf16_f32 v219, v202, v203\n\t"
    "v_mfma_f32_16x16x32_bf16 v[176:179], a[120:123], a[56:59], v[176:179]\n\t"
    "v_add_f32 v252, v252, v196\n\t"
    "v_add_f32 v252, v252, v197\n\t"
    "v_mfma_f32_16x16x4_f32 a[228:231], a252, v251, a[228:231]\n\t"
    "v_mfma_f32_16x16x32_bf16 v[176:179], a[124:127], a[60:63], v[176:179]\n\t"
    "v_add_f32 v252, v252, v198\n\t"
    "v_add_f32 v252, v252, v199\n\t"
    "v_mfma_f32_16x16x32_bf16 v[180:183], a[128:131], a[48:51], 0\n\t"
    "v_mfma_f32_16x16x32_bf16 v[180:183], a[132:135], a[52:55], v[180:183]\n\t"
    "v_add_f32 v252, v252, v200\n\t"
    "v_add_f32 v252, v252, v201\n\t"
    "v_mfma_f32_16x16x32_bf16 v[180:183], a[136:139], a[56:59], v[180:183]\n\t"
    "v_add_f32 v252, v252, v202\n\t"
    "v_add_f32 v252, v252, v203\n\t"
    "v_accvgpr_read_b32 v244, a228\n\t"
    "v_subrev_u32 v246, 0x2b800000, v244\n\t"
    "v_cmp_lt_u32 vcc, 0x28000000, v246\n\t"
    "s_nop 1\n\t"
    "s_or_b64 s[62:63], s[62:63], vcc\n\t"
    "v_mfma_f32_16x16x32_bf16 v[180:183], a[140:143], a[60:63], v[180:183]\n\t"
    "v_exp_f32 v172, v172\n\t"
    "v_rcp_f32 v245, v244\n\t"
    "s_mov_b32 s64, 45056\n\t"
    "v_mul_f32 v36, v36, v245\n\t"
    "v_mul_f32 v37, v37, v245\n\t"
    "v_mfma_f32_16x16x32_bf16 v[184:187], a[144:147], a[48:51], 0\n\t"
    "v_exp_f32 v173, v173\n\t"
    "v_mul_f32 v38, v38, v245\n\t"
    "v_mul_f32 v39, v39, v245\n\t"
    "v_mul_f32 v40, v40, v245\n\t"
    "v_mul_f32 v41, v41, v245\n\t"
    "v_mfma_f32_16x16x32_bf16 v[184:187], a[148:151], a[52:55], v[184:187]\n\t"
    "v_cvt_pk_bf16_f32 v204, v172, v173\n\t"
    "v_mul_f32 v42, v42, v245\n\t"
    "v_mul_f32 v43, v43, v245\n\t"
    "v_cvt_pk_bf16_f32 v36, v36, v37\n\t"
    "v_cvt_pk_bf16_f32 v37, v38, v39\n\t"
    "v_cvt_pk_bf16_f32 v38, v40, v41\n\t"
    "v_mfma_f32_16x16x32_bf16 v[184:187], a[152:155], a[56:59], v[184:187]\n\t"
    "v_exp_f32 v174, v174\n\t"
    "v_cvt_pk_bf16_f32 v39, v42, v43\n\t"
    "s_nop 1\n\t"
    "v_mfma_f32_16x16x32_bf16 a[4:7], a[0:3], v[36:39], a[4:7]\n\t"
    "v_mul_f32 v44, v44, v245\n\t"
    "v_mul_f32 v45, v45, v245\n\t"
    "v_mfma_f32_16x16x32_bf16 v[184:187], a[156:159], a[60:63], v[184:187]\n\t"
    "v_exp_f32 v175, v175\n\t"
    "v_mul_f32 v46, v46, v245\n\t"
    "v_mul_f32 v47, v47, v245\n\t"
    "v_mul_f32 v48, v48, v245\n\t"
    "v_mul_f32 v49, v49, v245\n\t"
    "v_mfma_f32_16x16x32_bf16 v[68:71], a[160:163], v[212:215], v[68:71]\n\t"
    "buffer_store_dwordx4 v[36:39], v241, s[48:51], s64 offen offset:0 sc0 sc1\n\t"
    "v_cvt_pk_bf16_f32 v205, v174, v175\n\t"
    "v_mul_f32 v50, v50, v245\n\t"
    "v_mul_f32 v51, v51, v245\n\t"
    "v_cvt_pk_bf16_f32 v44, v44, v45\n\t"
    "v_cvt_pk_bf16_f32 v45, v46, v47\n\t"
    "v_mfma_f32_16x16x32_bf16 v[68:71], a[192:195], v[216:219], v[68:71]\n\t"
    "v_exp_f32 v176, v176\n\t"
    "v_cvt_pk_bf16_f32 v46, v48, v49\n\t"
    "v_cvt_pk_bf16_f32 v47, v50, v51\n\t"
    "s_nop 1\n\t"
    "v_mfma_f32_16x16x32_bf16 a[4:7], a[0:3], v[44:47], a[4:7]\n\t"
    "v_mul_f32 v52, v52, v245\n\t"
    "v_mfma_f32_16x16x32_bf16 v[72:75], a[164:167], v[212:215], v[72:75]\n\t"
    "v_exp_f32 v177, v177\n\t"
    "v_mul_f32 v53, v53, v245\n\t"
    "v_mul_f32 v54, v54, v245\n\t"
    "v_mul_f32 v55, v55, v245\n\t"
    "v_mul_f32 v56, v56, v245\n\t"
    "v_mfma_f32_16x16x32_bf16 v[72:75], a[196:199], v[216:219], v[72:75]\n\t"
    "buffer_store_dwordx4 v[44:47], v241, s[48:51], s64 offen offset:64 sc0 sc1\n\t"
    "v_cvt_pk_bf16_f32 v206, v176, v177\n\t"
    "v_mul_f32 v57, v57, v245\n\t"
    "v_mul_f32 v58, v58, v245\n\t"
    "v_mul_f32 v59, v59, v245\n\t"
    "v_cvt_pk_bf16_f32 v52, v52, v53\n\t"
    "v_mfma_f32_16x16x32_bf16 v[76:79], a[168:171], v[212:215], v[76:79]\n\t"
    "v_exp_f32 v178, v178\n\t"
    "v_cvt_pk_bf16_f32 v53, v54, v55\n\t"
    "v_cvt_pk_bf16_f32 v54, v56, v57\n\t"
    "v_cvt_pk_bf16_f32 v55, v58, v59\n\t"
    "s_nop 1\n\t"
    "v_mfma_f32_16x16x32_bf16 a[4:7], a[0:3], v[52:55], a[4:7]\n\t"
    "v_mfma_f32_16x16x32_bf16 v[76:79], a[200:203], v[216:219], v[76:79]\n\t"
    "v_exp_f32 v179, v179\n\t"
    "v_mul_f32 v60, v60, v245\n\t"
    "v_mul_f32 v61, v61, v245\n\t"
    "v_mul_f32 v62, v62, v245\n\t"
    "v_mul_f32 v63, v63, v245\n\t"
    "v_mfma_f32_16x16x32_bf16 v[80:83], a[172:175], v[212:215], v[80:83]\n\t"
    "buffer_store_dwordx4 v[52:55], v241, s[48:51], s64 offen offset:128 sc0 sc1\n\t"
    "v_cvt_pk_bf16_f32 v207, v178, v179\n\t"
    "v_mul_f32 v64, v64, v245\n\t"
    "v_mul_f32 v65, v65, v245\n\t"
    "v_mul_f32 v66, v66, v245\n\t"
    "v_mul_f32 v67, v67, v245\n\t"
    "v_mfma_f32_16x16x32_bf16 v[80:83], a[204:207], v[216:219], v[80:83]\n\t"
    "v_exp_f32 v180, v180\n\t"
    "v_cvt_pk_bf16_f32 v60, v60, v61\n\t"
    "v_cvt_pk_bf16_f32 v61, v62, v63\n\t"
    "v_cvt_pk_bf16_f32 v62, v64, v65\n\t"
    "v_cvt_pk_bf16_f32 v63, v66, v67\n\t"
    "v_mfma_f32_16x16x32_bf16 v[84:87], a[176:179], v[212:215], v[84:87]\n\t"
    "v_exp_f32 v181, v181\n\t"
    "v_mfma_f32_16x16x32_bf16 a[4:7], a[0:3], v[60:63], a[4:7]\n\t"
    "v_mfma_f32_16x16x32_bf16 v[84:87], a[208:211], v[216:219], v[84:87]\n\t"
    "v_cvt_pk_bf16_f32 v208, v180, v181\n\t"
    "v_add_f32 v253, v253, v180\n\t"
    "v_mfma_f32_16x16x32_bf16 v[88:91], a[180:183], v[212:215], v[88:91]\n\t"
    "buffer_store_dwordx4 v[60:63], v241, s[48:51], s64 offen offset:192 sc0 sc1\n\t"
    "v_exp_f32 v182, v182\n\t"
    "v_mfma_f32_16x16x32_bf16 v[88:91], a[212:215], v[216:219], v[88:91]\n\t"
    "v_exp_f32 v183, v183\n\t"
    "v_mfma_f32_16x16x32_bf16 v[92:95], a[184:187], v[212:215], v[92:95]\n\t"
    "v_cvt_pk_bf16_f32 v209, v182, v183\n\t"
    "v_add_f32 v253, v253, v181\n\t"
    "v_mfma_f32_16x16x32_bf16 v[92:95], a[216:219], v[216:219], v[92:95]\n\t"
    "v_exp_f32 v184, v184\n\t"
    "v_mfma_f32_16x16x32_bf16 v[96:99], a[188:191], v[212:215], v[96:99]\n\t"
    "v_exp_f32 v185, v185\n\t"
    "v_mfma_f32_16x16x32_bf16 v[96:99], a[220:223], v[216:219], v[96:99]\n\t"
    "v_cvt_pk_bf16_f32 v210, v184, v185\n\t"
    "v_add_f32 v253, v253, v182\n\t"
    "v_mfma_f32_16x16x32_bf16 a[232:235], a[248:251], v[212:215], a[232:235]\n\t"
    "s_waitcnt vmcnt(8) lgkmcnt(0)\n\t"
    "s_barrier\n\t"
    "v_mfma_f32_16x16x32_bf16 v[188:191], a[96:99], a[64:67], 0\n\t"
    "v_add_f32 v253, v253, v183\n\t"
    "v_mfma_f32_16x16x32_bf16 v[188:191], a[100:103], a[68:71], v[188:191]\n\t"
    "v_exp_f32 v186, v186\n\t"
    "v_mfma_f32_16x16x32_bf16 v[188:191], a[104:107], a[72:75], v[188:191]\n\t"
    "v_mfma_f32_16x16x32_bf16 v[188:191], a[108:111], a[76:79], v[188:191]\n\t"
    "v_exp_f32 v187, v187\n\t"
    "v_mfma_f32_16x16x32_bf16 v[192:195], a[112:115], a[64:67], 0\n\t"
    "v_mfma_f32_16x16x32_bf16 v[192:195], a[116:119], a[68:71], v[192:195]\n\t"
    "v_cvt_pk_bf16_f32 v211, v186, v187\n\t"
    "v_add_f32 v253, v253, v184\n\t"
    "v_mfma_f32_16x16x32_bf16 v[192:195], a[120:123], a[72:75], v[192:195]\n\t"
    "v_mfma_f32_16x16x4_f32 a[232:235], a252, v252, a[232:235]\n\t"
    "v_mfma_f32_16x16x32_bf16 v[192:195], a[124:127], a[76:79], v[192:195]\n\t"
    "v_add_f32 v253, v253, v185\n\t"
    "v_add_f32 v253, v253, v186\n\t"
    "v_mfma_f32_16x16x32_bf16 v[196:199], a[128:131], a[64:67], 0\n\t"
    "v_add_f32 v253, v253, v187\n\t"
    "v_mfma_f32_16x16x32_bf16 v[196:199], a[132:135], a[68:71], v[196:199]\n\t"
    "v_mfma_f32_16x16x32_bf16 v[196:199], a[136:139], a[72:75], v[196:199]\n\t"
    "s_nop 2\n\t"
    "v_accvgpr_read_b32 v242, a232\n\t"
    "v_subrev_u32 v246, 0x2b800000, v242\n\t"
    "v_cmp_lt_u32 vcc, 0x28000000, v246\n\t"
    "s_nop 1\n\t"
    "s_or_b64 s[62:63], s[62:63], vcc\n\t"
    "v_rcp_f32 v243, v242\n\t"
    "s_mov_b32 s64, 49152\n\t"
    "v_mul_f32 v68, v68, v243\n\t"
    "v_mfma_f32_16x16x32_bf16 v[196:199], a[140:143], a[76:79], v[196:199]\n\t"
    "v_exp_f32 v188, v188\n\t"
    "v_mul_f32 v69, v69, v243\n\t"
    "v_mul_f32 v70, v70, v243\n\t"
    "v_mul_f32 v71, v71, v243\n\t"
    "v_mul_f32 v72, v72, v243\n\t"
    "v_mfma_f32_16x16x32_bf16 v[200:203], a[144:147], a[64:67], 0\n\t"
    "v_mul_f32 v73, v73, v243\n\t"
    "v_mul_f32 v74, v74, v243\n\t"
    "v_mul_f32 v75, v75, v243\n\t"
    "v_cvt_pk_bf16_f32 v68, v68, v69\n\t"
    "v_cvt_pk_bf16_f32 v69, v70, v71\n\t"
    "v_cvt_pk_bf16_f32 v70, v72, v73\n\t"
    "v_mfma_f32_16x16x32_bf16 v[200:203], a[148:151], a[68:71], v[200:203]\n\t"
    "v_exp_f32 v189, v189\n\t"
    "v_cvt_pk_bf16_f32 v71, v74, v75\n\t"
    "s_nop 1\n\t"
    "v_mfma_f32_16x16x32_bf16 a[4:7], a[0:3], v[68:71], a[4:7]\n\t"
    "v_mul_f32 v76, v76, v243\n\t"
    "v_mul_f32 v77, v77, v243\n\t"
    "v_mfma_f32_16x16x32_bf16 v[200:203], a[152:155], a[72:75], v[200:203]\n\t"
    "v_mul_f32 v78, v78, v243\n\t"
    "v_mul_f32 v79, v79, v243\n\t"
    "v_mul_f32 v80, v80, v243\n\t"
    "v_mul_f32 v81, v81, v243\n\t"
    "v_mul_f32 v82, v82, v243\n\t"
    "v_mul_f32 v83, v83, v243\n\t"
    "v_mfma_f32_16x16x32_bf16 v[200:203], a[156:159], a[76:79], v[200:203]\n\t"
    "buffer_store_dwordx4 v[68:71], v241, s[48:51], s64 offen offset:0 sc0 sc1\n\t"
    "v_cvt_pk_bf16_f32 v212, v188, v189\n\t"
    "v_add_f32 v254, v254, v188\n\t"
    "v_cvt_pk_bf16_f32 v76, v76, v77\n\t"
    "v_cvt_pk_bf16_f32 v77, v78, v79\n\t"
    "v_cvt_pk_bf16_f32 v78, v80, v81\n\t"
    "v_mfma_f32_16x16x32_bf16 v[100:103], a[160:163], v[204:207], v[100:103]\n\t"
    "v_cvt_pk_bf16_f32 v79, v82, v83\n\t"
    "s_nop 1\n\t"
    "v_mfma_f32_16x16x32_bf16 a[4:7], a[0:3], v[76:79], a[4:7]\n\t"
    "v_mul_f32 v84, v84, v243\n\t"
    "v_mul_f32 v85, v85, v243\n\t"
    "v_mul_f32 v86, v86, v243\n\t"
    "v_mul_f32 v87, v87, v243\n\t"
    "v_mfma_f32_16x16x32_bf16 v[100:103], a[192:195], v[208:211], v[100:103]\n\t"
    "v_exp_f32 v190, v190\n\t"
    "v_mul_f32 v88, v88, v243\n\t"
    "v_mul_f32 v89, v89, v243\n\t"
    "v_mul_f32 v90, v90, v243\n\t"
    "v_mul_f32 v91, v91, v243\n\t"
    "v_mfma_f32_16x16x32_bf16 v[104:107], a[164:167], v[204:207], v[104:107]\n\t"
    "ds_read_b128 v[228:231], v238 offset:2048\n\t"
    "ds_read_b128 v[232:235], v238 offset:3072\n\t"
    "v_cvt_pk_bf16_f32 v84, v84, v85\n\t"
    "v_cvt_pk_bf16_f32 v85, v86, v87\n\t"
    "v_cvt_pk_bf16_f32 v86, v88, v89\n\t"
    "v_cvt_pk_bf16_f32 v87, v90, v91\n\t"
    "v_mfma_f32_16x16x32_bf16 v[104:107], a[196:199], v[208:211], v[104:107]\n\t"
    "buffer_store_dwordx4 v[76:79], v241, s[48:51], s64 offen offset:64 sc0 sc1\n\t"
    "v_exp_f32 v191, v191\n\t"
    "v_mfma_f32_16x16x32_bf16 a[4:7], a[0:3], v[84:87], a[4:7]\n\t"
    "v_mul_f32 v92, v92, v243\n\t"
    "v_mul_f32 v93, v93, v243\n\t"
    "v_mul_f32 v94, v94, v243\n\t"
    "v_mfma_f32_16x16x32_bf16 v[108:111], a[168:171], v[204:207], v[108:111]\n\t"
    "v_cvt_pk_bf16_f32 v213, v190, v191\n\t"
    "v_add_f32 v254, v254, v189\n\t"
    "v_mul_f32 v95, v95, v243\n\t"
    "v_mul_f32 v96, v96, v243\n\t"
    "v_mul_f32 v97, v97, v243\n\t"
    "v_mul_f32 v98, v98, v243\n\t"
    "v_mfma_f32_16x16x32_bf16 v[108:111], a[200:203], v[208:211], v[108:111]\n\t"
    "buffer_store_dwordx4 v[84:87], v241, s[48:51], s64 offen offset:128 sc0 sc1\n\t"
    "v_exp_f32 v192, v192\n\t"
    "v_mul_f32 v99, v99, v243\n\t"
    "v_cvt_pk_bf16_f32 v92, v92, v93\n\t"
    "v_cvt_pk_bf16_f32 v93, v94, v95\n\t"
    "v_cvt_pk_bf16_f32 v94, v96, v97\n\t"
    "v_mfma_f32_16x16x32_bf16 v[112:115], a[172:175], v[204:207], v[112:115]\n\t"
    "v_exp_f32 v193, v193\n\t"
    "v_cvt_pk_bf16_f32 v95, v98, v99\n\t"
    "s_nop 1\n\t"
    "v_mfma_f32_16x16x32_bf16 a[4:7], a[0:3], v[92:95], a[4:7]\n\t"
    "ds_read_b32 v222, v220 offset:0\n\t"
    "ds_read_b32 v223, v220 offset:64\n\t"
    "v_mfma_f32_16x16x32_bf16 v[112:115], a[204:207], v[208:211], v[112:115]\n\t"
    "v_cvt_pk_bf16_f32 v214, v192, v193\n\t"
    "v_add_f32 v254, v254, v190\n\t"
    "ds_read_b32 v224, v220 offset:128\n\t"
    "ds_read_b32 v225, v220 offset:192\n\t"
    "s_waitcnt lgkmcnt(2)\n\t"
    "v_add_f32 v222, v222, v223\n\t"
    "s_waitcnt lgkmcnt(0)\n\t"
    "v_add_f32 v224, v224, v225\n\t"
    "v_mfma_f32_16x16x32_bf16 v[116:119], a[176:179], v[204:207], v[116:119]\n\t"
    "buffer_store_dwordx4 v[92:95], v241, s[48:51], s64 offen offset:192 sc0 sc1\n\t"
    "v_exp_f32 v194, v194\n\t"
    "v_add_f32 v248, v222, v224\n\t"
    "v_subrev_u32 v223, 0x2b800000, v248\n\t"
    "v_cmp_lt_u32 vcc, 0x28000000, v223\n\t"
    "s_nop 1\n\t"
    "s_or_b64 s[62:63], s[62:63], vcc\n\t"
    "v_mfma_f32_16x16x32_bf16 v[116:119], a[208:211], v[208:211], v[116:119]\n\t"
    "v_exp_f32 v195, v195\n\t"
    "v_rcp_f32 v249, v248\n\t"
    "v_mfma_f32_16x16x32_bf16 v[120:123], a[180:183], v[204:207], v[120:123]\n\t"
    "v_cvt_pk_bf16_f32 v215, v194, v195\n\t"
    "v_add_f32 v254, v254, v191\n\t"
    "v_mfma_f32_16x16x32_bf16 v[120:123], a[212:215], v[208:211], v[120:123]\n\t"
    "v_exp_f32 v196, v196\n\t"
    "v_mfma_f32_16x16x32_bf16 v[124:127], a[184:187], v[204:207], v[124:127]\n\t"
    "v_exp_f32 v197, v197\n\t"
    "v_mfma_f32_16x16x32_bf16 v[124:127], a[216:219], v[208:211], v[124:127]\n\t"
    "v_cvt_pk_bf16_f32 v216, v196, v197\n\t"
    "v_add_f32 v254, v254, v192\n\t"
    "v_mfma_f32_16x16x32_bf16 v[128:131], a[188:191], v[204:207], v[128:131]\n\t"
    "v_exp_f32 v198, v198\n\t"
    "v_mfma_f32_16x16x32_bf16 v[128:131], a[220:223], v[208:211], v[128:131]\n\t"
    "v_exp_f32 v199, v199\n\t"
    "v_mfma_f32_16x16x32_bf16 a[236:239], a[248:251], v[204:207], a[236:239]\n\t"
    "v_cvt_pk_bf16_f32 v217, v198, v199\n\t"
    "v_add_f32 v254, v254, v193\n\t"
    "v_exp_f32 v200, v200\n\t"
    "v_exp_f32 v201, v201\n\t"
    "s_nop 0\n\t"
    "v_cvt_pk_bf16_f32 v218, v200, v201\n\t"
    "v_add_f32 v254, v254, v194\n\t"
    "v_exp_f32 v202, v202\n\t"
    "v_exp_f32 v203, v203\n\t"
    "s_nop 0\n\t"
    "v_cvt_pk_bf16_f32 v219, v202, v203\n\t"
    "v_add_f32 v254, v254, v195\n\t"
    "v_mfma_f32_16x16x4_f32 a[236:239], a252, v253, a[236:239]\n\t"
    "v_add_f32 v254, v254, v196\n\t"
    "v_add_f32 v254, v254, v197\n\t"
    "v_add_f32 v254, v254, v198\n\t"
    "v_add_f32 v254, v254, v199\n\t"
    "v_add_f32 v254, v254, v200\n\t"
    "v_add_f32 v254, v254, v201\n\t"
    "v_add_f32 v254, v254, v202\n\t"
    "v_add_f32 v254, v254, v203\n\t"
    "s_nop 1\n\t"
    "v_accvgpr_read_b32 v244, a236\n\t"
    "v_subrev_u32 v246, 0x2b800000, v244\n\t"
    "v_cmp_lt_u32 vcc, 0x28000000, v246\n\t"
    "s_nop 1\n\t"
    "s_or_b64 s[62:63], s[62:63], vcc\n\t"
    "v_rcp_f32 v245, v244\n\t"
    "s_mov_b32 s64, 53248\n\t"
    "v_mul_f32 v100, v100, v245\n\t"
    "v_mul_f32 v101, v101, v245\n\t"
    "v_mul_f32 v102, v102, v245\n\t"
    "v_mul_f32 v103, v103, v245\n\t"
    "v_mul_f32 v104, v104, v245\n\t"
    "v_mul_f32 v105, v105, v245\n\t"
    "v_mul_f32 v106, v106, v245\n\t"
    "v_mul_f32 v107, v107, v245\n\t"
    "v_cvt_pk_bf16_f32 v100, v100, v101\n\t"
    "v_cvt_pk_bf16_f32 v101, v102, v103\n\t"
    "v_cvt_pk_bf16_f32 v102, v104, v105\n\t"
    "v_cvt_pk_bf16_f32 v103, v106, v107\n\t"
    "buffer_store_dwordx4 v[100:103], v241, s[48:51], s64 offen offset:0 sc0 sc1\n\t"
    "s_nop 0\n\t"
    "v_mfma_f32_16x16x32_bf16 a[4:7], a[0:3], v[100:103], a[4:7]\n\t"
    "v_mul_f32 v108, v108, v245\n\t"
    "v_mul_f32 v109, v109, v245\n\t"
    "v_mul_f32 v110, v110, v245\n\t"
    "v_mul_f32 v111, v111, v245\n\t"
    "v_mul_f32 v112, v112, v245\n\t"
    "v_mul_f32 v113, v113, v245\n\t"
    "v_mul_f32 v114, v114, v245\n\t"
    "v_mul_f32 v115, v115, v245\n\t"
    "v_cvt_pk_bf16_f32 v108, v108, v109\n\t"
    "v_cvt_pk_bf16_f32 v109, v110, v111\n\t"
    "v_cvt_pk_bf16_f32 v110, v112, v113\n\t"
    "v_cvt_pk_bf16_f32 v111, v114, v115\n\t"
    "buffer_store_dwordx4 v[108:111], v241, s[48:51], s64 offen offset:64 sc0 sc1\n\t"
    "s_nop 0\n\t"
    "v_mfma_f32_16x16x32_bf16 a[4:7], a[0:3], v[108:111], a[4:7]\n\t"
    "v_mul_f32 v116, v116, v245\n\t"
    "v_mul_f32 v117, v117, v245\n\t"
    "v_mul_f32 v118, v118, v245\n\t"
    "v_mfma_f32_16x16x32_bf16 v[164:167], a[176:179], v[228:231], v[164:167]\n\t"
    "v_mul_f32 v119, v119, v245\n\t"
    "v_mul_f32 v120, v120, v245\n\t"
    "v_mul_f32 v121, v121, v245\n\t"
    "v_mul_f32 v122, v122, v245\n\t"
    "v_mul_f32 v123, v123, v245\n\t"
    "v_cvt_pk_bf16_f32 v116, v116, v117\n\t"
    "v_mfma_f32_16x16x32_bf16 v[164:167], a[208:211], v[232:235], v[164:167]\n\t"
    "v_cvt_pk_bf16_f32 v117, v118, v119\n\t"
    "v_cvt_pk_bf16_f32 v118, v120, v121\n\t"
    "v_cvt_pk_bf16_f32 v119, v122, v123\n\t"
    "s_nop 1\n\t"
    "v_mfma_f32_16x16x32_bf16 a[4:7], a[0:3], v[116:119], a[4:7]\n\t"
    "v_mul_f32 v124, v124, v245\n\t"
    "v_mul_f32 v125, v125, v245\n\t"
    "v_mfma_f32_16x16x32_bf16 v[168:171], a[180:183], v[228:231], v[168:171]\n\t"
    "v_mul_f32 v126, v126, v245\n\t"
    "v_mul_f32 v127, v127, v245\n\t"
    "v_mul_f32 v128, v128, v245\n\t"
    "v_mul_f32 v129, v129, v245\n\t"
    "v_mul_f32 v130, v130, v245\n\t"
    "v_mul_f32 v131, v131, v245\n\t"
    "v_mfma_f32_16x16x32_bf16 v[168:171], a[212:215], v[232:235], v[168:171]\n\t"
    "buffer_store_dwordx4 v[116:119], v241, s[48:51], s64 offen offset:128 sc0 sc1\n\t"
    "v_cvt_pk_bf16_f32 v124, v124, v125\n\t"
    "v_cvt_pk_bf16_f32 v125, v126, v127\n\t"
    "v_cvt_pk_bf16_f32 v126, v128, v129\n\t"
    "v_cvt_pk_bf16_f32 v127, v130, v131\n\t"
    "s_nop 1\n\t"
    "v_mfma_f32_16x16x32_bf16 a[4:7], a[0:3], v[124:127], a[4:7]\n\t"
    "v_mfma_f32_16x16x32_bf16 v[132:135], a[160:163], v[212:215], v[132:135]\n\t"
    "v_mfma_f32_16x16x4_f32 a[240:243], a252, v254, 0\n\t"
    "v_mfma_f32_16x16x32_bf16 v[132:135], a[192:195], v[216:219], v[132:135]\n\t"
    "buffer_store_dwordx4 v[124:127], v241, s[48:51], s64 offen offset:192 sc0 sc1\n\t"
    "v_mfma_f32_16x16x32_bf16 v[136:139], a[164:167], v[212:215], v[136:139]\n\t"
    "v_mfma_f32_16x16x32_bf16 v[136:139], a[196:199], v[216:219], v[136:139]\n\t"
    "v_mfma_f32_16x16x32_bf16 v[140:143], a[168:171], v[212:215], v[140:143]\n\t"
    "v_mfma_f32_16x16x32_bf16 v[140:143], a[200:203], v[216:219], v[140:143]\n\t"
    "v_mfma_f32_16x16x32_bf16 v[144:147], a[172:175], v[212:215], v[144:147]\n\t"
    "v_mfma_f32_16x16x32_bf16 v[144:147], a[204:207], v[216:219], v[144:147]\n\t"
    "v_mfma_f32_16x16x32_bf16 v[148:151], a[176:179], v[212:215], v[148:151]\n\t"
    "s_mov_b32 s61, 81920\n\t"
    "v_mul_f32 v164, v164, v249\n\t"
    "v_mul_f32 v165, v165, v249\n\t"
    "v_mul_f32 v166, v166, v249\n\t"
    "v_mul_f32 v167, v167, v249\n\t"
    "v_mul_f32 v168, v168, v249\n\t"
    "v_mul_f32 v169, v169, v249\n\t"
    "v_mfma_f32_16x16x32_bf16 v[148:151], a[208:211], v[216:219], v[148:151]\n\t"
    "v_mul_f32 v170, v170, v249\n\t"
    "v_mul_f32 v171, v171, v249\n\t"
    "v_cvt_pk_bf16_f32 v164, v164, v165\n\t"
    "v_cvt_pk_bf16_f32 v165, v166, v167\n\t"
    "v_cvt_pk_bf16_f32 v166, v168, v169\n\t"
    "v_cvt_pk_bf16_f32 v167, v170, v171\n\t"
    "v_mfma_f32_16x16x32_bf16 v[152:155], a[180:183], v[212:215], v[152:155]\n\t"
    "s_nop 0\n\t"
    "v_mfma_f32_16x16x32_bf16 a[4:7], a[0:3], v[164:167], a[4:7]\n\t"
    "v_accvgpr_read_b32 v242, a240\n\t"
    "v_subrev_u32 v246, 0x2b800000, v242\n\t"
    "v_cmp_lt_u32 vcc, 0x28000000, v246\n\t"
    "s_nop 1\n\t"
    "s_or_b64 s[62:63], s[62:63], vcc\n\t"
    "v_rcp_f32 v243, v242\n\t"
    "v_mfma_f32_16x16x32_bf16 v[152:155], a[212:215], v[216:219], v[152:155]\n\t"
    "v_mfma_f32_16x16x32_bf16 v[156:159], a[184:187], v[212:215], v[156:159]\n\t"
    "buffer_store_dwordx4 v[164:167], v241, s[48:51], s61 offen offset:128 sc0 sc1\n\t"
    "s_mov_b32 s64, 57344\n\t"
    "v_mfma_f32_16x16x32_bf16 v[156:159], a[216:219], v[216:219], v[156:159]\n\t"
    "v_mfma_f32_16x16x32_bf16 v[160:163], a[188:191], v[212:215], v[160:163]\n\t"
    "v_mfma_f32_16x16x32_bf16 v[160:163], a[220:223], v[216:219], v[160:163]\n\t"
    "v_mul_f32 v132, v132, v243\n\t"
    "v_mul_f32 v133, v133, v243\n\t"
    "v_mul_f32 v134, v134, v243\n\t"
    "v_mul_f32 v135, v135, v243\n\t"
    "v_mul_f32 v136, v136, v243\n\t"
    "v_mul_f32 v137, v137, v243\n\t"
    "v_mul_f32 v138, v138, v243\n\t"
    "v_mul_f32 v139, v139, v243\n\t"
    "v_cvt_pk_bf16_f32 v132, v132, v133\n\t"
    "v_cvt_pk_bf16_f32 v133, v134, v135\n\t"
    "v_cvt_pk_bf16_f32 v134, v136, v137\n\t"
    "v_cvt_pk_bf16_f32 v135, v138, v139\n\t"
    "v_mul_f32 v140, v140, v243\n\t"
    "v_mul_f32 v141, v141, v243\n\t"
    "v_mul_f32 v142, v142, v243\n\t"
    "v_mul_f32 v143, v143, v243\n\t"
    "v_mul_f32 v144, v144, v243\n\t"
    "v_mul_f32 v145, v145, v243\n\t"
    "v_mul_f32 v146, v146, v243\n\t"
    "v_mul_f32 v147, v147, v243\n\t"
    "v_cvt_pk_bf16_f32 v140, v140, v141\n\t"
    "v_cvt_pk_bf16_f32 v141, v142, v143\n\t"
    "v_cvt_pk_bf16_f32 v142, v144, v145\n\t"
    "v_cvt_pk_bf16_f32 v143, v146, v147\n\t"
    "buffer_store_dwordx4 v[132:135], v241, s[48:51], s64 offen offset:0 sc0 sc1\n\t"
    "v_mfma_f32_16x16x32_bf16 a[4:7], a[0:3], v[132:135], a[4:7]\n\t"
    "v_mul_f32 v148, v148, v243\n\t"
    "v_mul_f32 v149, v149, v243\n\t"
    "v_mul_f32 v150, v150, v243\n\t"
    "v_mul_f32 v151, v151, v243\n\t"
    "v_mul_f32 v152, v152, v243\n\t"
    "v_mul_f32 v153, v153, v243\n\t"
    "v_mul_f32 v154, v154, v243\n\t"
    "v_mul_f32 v155, v155, v243\n\t"
    "v_cvt_pk_bf16_f32 v148, v148, v149\n\t"
    "v_cvt_pk_bf16_f32 v149, v150, v151\n\t"
    "v_cvt_pk_bf16_f32 v150, v152, v153\n\t"
    "v_cvt_pk_bf16_f32 v151, v154, v155\n\t"
    "buffer_store_dwordx4 v[140:143], v241, s[48:51], s64 offen offset:64 sc0 sc1\n\t"
    "v_mfma_f32_16x16x32_bf16 a[4:7], a[0:3], v[140:143], a[4:7]\n\t"
    "v_mul_f32 v156, v156, v243\n\t"
    "v_mul_f32 v157, v157, v243\n\t"
    "v_mul_f32 v158, v158, v243\n\t"
    "v_mul_f32 v159, v159, v243\n\t"
    "v_mul_f32 v160, v160, v243\n\t"
    "v_mul_f32 v161, v161, v243\n\t"
    "v_mul_f32 v162, v162, v243\n\t"
    "v_mul_f32 v163, v163, v243\n\t"
    "v_cvt_pk_bf16_f32 v156, v156, v157\n\t"
    "v_cvt_pk_bf16_f32 v157, v158, v159\n\t"
    "v_cvt_pk_bf16_f32 v158, v160, v161\n\t"
    "v_cvt_pk_bf16_f32 v159, v162, v163\n\t"
    "buffer_store_dwordx4 v[148:151], v241, s[48:51], s64 offen offset:128 sc0 sc1\n\t"
    "v_mfma_f32_16x16x32_bf16 a[4:7], a[0:3], v[148:151], a[4:7]\n\t"
    "buffer_store_dwordx4 v[156:159], v241, s[48:51], s64 offen offset:192 sc0 sc1\n\t"
    "v_mfma_f32_16x16x32_bf16 a[4:7], a[0:3], v[156:159], a[4:7]\n\t"
    "s_nop 7\n\t"
    "v_accvgpr_read_b32 v246, a4\n\t"
    "v_cmp_u_f32 vcc, v246, v246\n\t"
    "s_nop 1\n\t"
    "s_or_b64 s[62:63], s[62:63], vcc\n\t"
    "s_cmp_lg_u64 s[62:63], 0\n\t"
    "s_cselect_b32 %0, 1, 0\n\t"
    "s_branch END_%=\n\t"
    "WEND2_%=:\n\t"
    "W3_%=:\n\t"
    "WS3_%=:\n\t"
    "CPF3_%=:\n\t"
    "s_add_u32 s56, s53, 70728\n\t"
    "v_add_u32 v226, 4096, v239\n\t"
    "v_add_u32 v227, 8192, v239\n\t"
    "v_add_u32 v3, 12288, v239\n\t"
    "s_mov_b32 s58, 33792\n\t"
    "s_mov_b32 s60, 14\n\t"
    "s_mov_b32 s65, 0x3e0293ee\n\t"
    "s_mov_b64 s[62:63], 0\n\t"
    "v_bfe_u32 v242, v2, 2, 1\n\t"
    "v_and_b32 v243, 3, v2\n\t"
    "v_lshl_add_u32 v242, v242, 2, v243\n\t"
    "v_mul_u32_u24 v242, 0x420, v242\n\t"
    "v_lshrrev_b32 v243, 3, v2\n\t"
    "v_lshl_add_u32 v242, v243, 8, v242\n\t"
    "v_lshl_add_u32 v242, v255, 4, v242\n\t"
    "s_add_u32 s61, s53, 0\n\t"
    "v_add_u32 v236, s61, v242\n\t"
    "v_and_b32 v242, 1, v255\n\t"
    "v_bfe_u32 v243, v1, 2, 2\n\t"
    "v_lshl_add_u32 v242, v242, 2, v243\n\t"
    "v_mul_u32_u24 v242, 0x420, v242\n\t"
    "v_lshrrev_b32 v243, 1, v255\n\t"
    "v_lshl_add_u32 v242, v243, 8, v242\n\t"
    "v_and_b32 v243, 3, v1\n\t"
    "v_lshl_add_u32 v242, v243, 4, v242\n\t"
    "v_bfe_u32 v243, v1, 2, 1\n\t"
    "v_mul_u32_u24 v243, 24, v243\n\t"
    "v_sub_u32 v242, v242, v243\n\t"
    "s_add_u32 s61, s53, 67584\n\t"
    "v_add_u32 v237, s61, v242\n\t"
    "s_add_u32 s61, s53, 135168\n\t"
    "v_add_u32 v238, s61, v239\n\t"
    "v_lshlrev_b32 v241, 8, v2\n\t"
    "v_lshl_add_u32 v241, v255, 4, v241\n\t"
    "v_mov_b32 v242, 0x3f803f80\n\t"
    "v_accvgpr_write_b32 a248, v242\n\t"
    "v_accvgpr_write_b32 a249, v242\n\t"
    "v_accvgpr_write_b32 a250, v242\n\t"
    "v_accvgpr_write_b32 a251, v242\n\t"
    "v_mov_b32 v132, 0\n\t"
    "v_mov_b32 v133, 0\n\t"
    "v_mov_b32 v134, 0\n\t"
    "v_mov_b32 v135, 0\n\t"
    "v_mov_b32 v136, 0\n\t"
    "v_mov_b32 v137, 0\n\t"
    "v_mov_b32 v138, 0\n\t"
    "v_mov_b32 v139, 0\n\t"
    "v_mov_b32 v140, 0\n\t"
    "v_mov_b32 v141, 0\n\t"
    "v_mov_b32 v142, 0\n\t"
    "v_mov_b32 v143, 0\n\t"
    "v_mov_b32 v144, 0\n\t"
    "v_mov_b32 v145, 0\n\t"
    "v_mov_b32 v146, 0\n\t"
    "v_mov_b32 v147, 0\n\t"
    "v_mov_b32 v148, 0\n\t"
    "v_mov_b32 v149, 0\n\t"
    "v_mov_b32 v150, 0\n\t"
    "v_mov_b32 v151, 0\n\t"
    "v_mov_b32 v152, 0\n\t"
    "v_mov_b32 v153, 0\n\t"
    "v_mov_b32 v154, 0\n\t"
    "v_mov_b32 v155, 0\n\t"
    "v_mov_b32 v156, 0\n\t"
    "v_mov_b32 v157, 0\n\t"
    "v_mov_b32 v158, 0\n\t"
    "v_mov_b32 v159, 0\n\t"
    "v_mov_b32 v160, 0\n\t"
    "v_mov_b32 v161, 0\n\t"
    "v_mov_b32 v162, 0\n\t"
    "v_mov_b32 v163, 0\n\t"
    "v_mov_b32 v164, 0\n\t"
    "v_mov_b32 v165, 0\n\t"
    "v_mov_b32 v166, 0\n\t"
    "v_mov_b32 v167, 0\n\t"
    "v_mov_b32 v168, 0\n\t"
    "v_mov_b32 v169, 0\n\t"
    "v_mov_b32 v170, 0\n\t"
    "v_mov_b32 v171, 0\n\t"
    "v_mov_b32 v250, 0\n\t"
    "v_mov_b32 v251, 0\n\t"
    "v_mov_b32 v252, 0\n\t"
    "v_mov_b32 v253, 0\n\t"
    "v_mov_b32 v254, 0\n\t"
    "v_mov_b32 v242, 1.0\n\t"
    "v_accvgpr_write_b32 a252, v242\n\t"
    "s_mov_b32 s64, 65536\n\t"
    "buffer_load_dwordx4 v[116:119], v240, s[36:39], s64 offen offset:0\n\t"
    "buffer_load_dwordx4 v[120:123], v240, s[36:39], s64 offen offset:64\n\t"
    "buffer_load_dwordx4 v[124:127], v240, s[36:39], s64 offen offset:128\n\t"
    "buffer_load_dwordx4 v[128:131], v240, s[36:39], s64 offen offset:192\n\t"
    "s_waitcnt vmcnt(9)\n\t"
    "s_barrier\n\t"
    "ds_read_b128 a[96:99], v236 offset:0\n\t"
    "ds_read_b128 a[100:103], v236 offset:64\n\t"
    "ds_read_b128 a[104:107], v236 offset:128\n\t"
    "ds_read_b128 a[108:111], v236 offset:192\n\t"
    "ds_read_b128 a[112:115], v236 offset:512\n\t"
    "ds_read_b128 a[116:119], v236 offset:576\n\t"
    "ds_read_b128 a[120:123], v236 offset:640\n\t"
    "ds_read_b128 a[124:127], v236 offset:704\n\t"
    "ds_read_b128 a[128:131], v236 offset:8448\n\t"
    "ds_read_b128 a[132:135], v236 offset:8512\n\t"
    "ds_read_b128 a[136:139], v236 offset:8576\n\t"
    "ds_read_b128 a[140:143], v236 offset:8640\n\t"
    "ds_read_b128 a[144:147], v236 offset:8960\n\t"
    "ds_read_b128 a[148:151], v236 offset:9024\n\t"
    "ds_read_b128 a[152:155], v236 offset:9088\n\t"
    "ds_read_b128 a[156:159], v236 offset:9152\n\t"
    "s_add_u32 m0, s56, 0\n\t"
    "s_add_u32 s61, s59, 3072\n\t"
    "s_and_b32 s61, s61, 0x7ffff\n\t"
    "buffer_load_dwordx4 v239, s[44:47], s61 offen offset:0 lds\n\t"
    "s_add_u32 m0, s56, 4224\n\t"
    "s_nop 0\n\t"
    "buffer_load_dwordx4 v226, s[44:47], s61 offen offset:0 lds\n\t"
    "s_add_u32 m0, s56, 8448\n\t"
    "s_nop 0\n\t"
    "buffer_load_dwordx4 v227, s[44:47], s61 offen offset:0 lds\n\t"
    "s_add_u32 m0, s56, 12672\n\t"
    "s_nop 0\n\t"
    "buffer_load_dwordx4 v3, s[44:47], s61 offen offset:0 lds\n\t"
    "s_waitcnt vmcnt(12)\n\t"
    "v_lshlrev_b32 v242, 16, v100\n\t"
    "v_and_b32 v243, 0xffff0000, v100\n\t"
    "v_mul_f32 v242, s65, v242\n\t"
    "v_mul_f32 v243, s65, v243\n\t"
    "v_cvt_pk_bf16_f32 v242, v242, v243\n\t"
    "v_accvgpr_write_b32 a0, v242\n\t"
    "s_mov_b32 s64, 81920\n\t"
    "buffer_load_dwordx4 v[68:71], v240, s[36:39], s64 offen offset:0\n\t"
    "buffer_load_dwordx4 v[72:75], v240, s[36:39], s64 offen offset:64\n\t"
    "buffer_load_dwordx4 v[76:79], v240, s[36:39], s64 offen offset:128\n\t"
    "buffer_load_dwordx4 v[80:83], v240, s[36:39], s64 offen offset:192\n\t"
    "v_lshlrev_b32 v244, 16, v101\n\t"
    "v_and_b32 v245, 0xffff0000, v101\n\t"
    "v_mul_f32 v244, s65, v244\n\t"
    "v_mul_f32 v245, s65, v245\n\t"
    "v_cvt_pk_bf16_f32 v244, v244, v245\n\t"
    "v_accvgpr_write_b32 a1, v244\n\t"
    "v_lshlrev_b32 v246, 16, v102\n\t"
    "v_and_b32 v247, 0xffff0000, v102\n\t"
    "v_mul_f32 v246, s65, v246\n\t"
    "v_mul_f32 v247, s65, v247\n\t"
    "v_cvt_pk_bf16_f32 v246, v246, v247\n\t"
    "v_accvgpr_write_b32 a2, v246\n\t"
    "v_lshlrev_b32 v248, 16, v103\n\t"
    "v_and_b32 v249, 0xffff0000, v103\n\t"
    "v_mul_f32 v248, s65, v248\n\t"
    "v_mul_f32 v249, s65, v249\n\t"
    "v_cvt_pk_bf16_f32 v248, v248, v249\n\t"
    "v_accvgpr_write_b32 a3, v248\n\t"
    "s_waitcnt vmcnt(15)\n\t"
    "v_lshlrev_b32 v242, 16, v104\n\t"
    "v_and_b32 v243, 0xffff0000, v104\n\t"
    "v_mul_f32 v242, s65, v242\n\t"
    "v_mul_f32 v243, s65, v243\n\t"
    "v_cvt_pk_bf16_f32 v242, v242, v243\n\t"
    "v_accvgpr_write_b32 a4, v242\n\t"
    "v_lshlrev_b32 v244, 16, v105\n\t"
    "v_and_b32 v245, 0xffff0000, v105\n\t"
    "v_mul_f32 v244, s65, v244\n\t"
    "v_mul_f32 v245, s65, v245\n\t"
    "v_cvt_pk_bf16_f32 v244, v244, v245\n\t"
    "v_accvgpr_write_b32 a5, v244\n\t"
    "v_lshlrev_b32 v246, 16, v106\n\t"
    "v_and_b32 v247, 0xffff0000, v106\n\t"
    "v_mul_f32 v246, s65, v246\n\t"
    "v_mul_f32 v247, s65, v247\n\t"
    "v_cvt_pk_bf16_f32 v246, v246, v247\n\t"
    "v_accvgpr_write_b32 a6, v246\n\t"
    "v_lshlrev_b32 v248, 16, v107\n\t"
    "v_and_b32 v249, 0xffff0000, v107\n\t"
    "v_mul_f32 v248, s65, v248\n\t"
    "v_mul_f32 v249, s65, v249\n\t"
    "v_cvt_pk_bf16_f32 v248, v248, v249\n\t"
    "v_accvgpr_write_b32 a7, v248\n\t"
    "s_waitcnt vmcnt(14)\n\t"
    "v_lshlrev_b32 v242, 16, v108\n\t"
    "v_and_b32 v243, 0xffff0000, v108\n\t"
    "v_mul_f32 v242, s65, v242\n\t"
    "v_mul_f32 v243, s65, v243\n\t"
    "v_cvt_pk_bf16_f32 v242, v242, v243\n\t"
    "v_accvgpr_write_b32 a8, v242\n\t"
    "v_lshlrev_b32 v244, 16, v109\n\t"
    "v_and_b32 v245, 0xffff0000, v109\n\t"
    "v_mul_f32 v244, s65, v244\n\t"
    "v_mul_f32 v245, s65, v245\n\t"
    "v_cvt_pk_bf16_f32 v244, v244, v245\n\t"
    "v_accvgpr_write_b32 a9, v244\n\t"
    "v_lshlrev_b32 v246, 16, v110\n\t"
    "v_and_b32 v247, 0xffff0000, v110\n\t"
    "v_mul_f32 v246, s65, v246\n\t"
    "v_mul_f32 v247, s65, v247\n\t"
    "v_cvt_pk_bf16_f32 v246, v246, v247\n\t"
    "v_accvgpr_write_b32 a10, v246\n\t"
    "v_lshlrev_b32 v248, 16, v111\n\t"
    "v_and_b32 v249, 0xffff0000, v111\n\t"
    "v_mul_f32 v248, s65, v248\n\t"
    "v_mul_f32 v249, s65, v249\n\t"
    "v_cvt_pk_bf16_f32 v248, v248, v249\n\t"
    "v_accvgpr_write_b32 a11, v248\n\t"
    "s_waitcnt vmcnt(13)\n\t"
    "v_lshlrev_b32 v242, 16, v112\n\t"
    "v_and_b32 v243, 0xffff0000, v112\n\t"
    "v_mul_f32 v242, s65, v242\n\t"
    "v_mul_f32 v243, s65, v243\n\t"
    "v_cvt_pk_bf16_f32 v242, v242, v243\n\t"
    "v_accvgpr_write_b32 a12, v242\n\t"
    "v_lshlrev_b32 v244, 16, v113\n\t"
    "v_and_b32 v245, 0xffff0000, v113\n\t"
    "v_mul_f32 v244, s65, v244\n\t"
    "v_mul_f32 v245, s65, v245\n\t"
    "v_cvt_pk_bf16_f32 v244, v244, v245\n\t"
    "v_accvgpr_write_b32 a13, v244\n\t"
    "v_lshlrev_b32 v246, 16, v114\n\t"
    "v_and_b32 v247, 0xffff0000, v114\n\t"
    "v_mul_f32 v246, s65, v246\n\t"
    "v_mul_f32 v247, s65, v247\n\t"
    "v_cvt_pk_bf16_f32 v246, v246, v247\n\t"
    "v_accvgpr_write_b32 a14, v246\n\t"
    "v_lshlrev_b32 v248, 16, v115\n\t"
    "v_and_b32 v249, 0xffff0000, v115\n\t"
    "v_mul_f32 v248, s65, v248\n\t"
    "v_mul_f32 v249, s65, v249\n\t"
    "v_cvt_pk_bf16_f32 v248, v248, v249\n\t"
    "v_accvgpr_write_b32 a15, v248\n\t"
    "s_waitcnt lgkmcnt(15)\n\t"
    "v_mfma_f32_16x16x32_bf16 v[172:175], a[96:99], a[0:3], 0\n\t"
    "s_waitcnt lgkmcnt(14)\n\t"
    "v_mfma_f32_16x16x32_bf16 v[172:175], a[100:103], a[4:7], v[172:175]\n\t"
    "s_waitcnt lgkmcnt(13)\n\t"
    "s_waitcnt vmcnt(11)\n\t"
    "v_lshlrev_b32 v242, 16, v116\n\t"
    "v_and_b32 v243, 0xffff0000, v116\n\t"
    "v_mul_f32 v242, s65, v242\n\t"
    "v_mul_f32 v243, s65, v243\n\t"
    "v_cvt_pk_bf16_f32 v242, v242, v243\n\t"
    "v_accvgpr_write_b32 a16, v242\n\t"
    "s_mov_b32 s64, 69632\n\t"
    "buffer_load_dwordx4 v[84:87], v240, s[36:39], s64 offen offset:0\n\t"
    "buffer_load_dwordx4 v[88:91], v240, s[36:39], s64 offen offset:64\n\t"
    "buffer_load_dwordx4 v[92:95], v240, s[36:39], s64 offen offset:128\n\t"
    "buffer_load_dwordx4 v[96:99], v240, s[36:39], s64 offen offset:192\n\t"
    "v_mfma_f32_16x16x32_bf16 v[172:175], a[104:107], a[8:11], v[172:175]\n\t"
    "s_waitcnt lgkmcnt(12)\n\t"
    "v_lshlrev_b32 v244, 16, v117\n\t"
    "v_and_b32 v245, 0xffff0000, v117\n\t"
    "v_mul_f32 v244, s65, v244\n\t"
    "v_mul_f32 v245, s65, v245\n\t"
    "v_cvt_pk_bf16_f32 v244, v244, v245\n\t"
    "v_accvgpr_write_b32 a17, v244\n\t"
    "v_mfma_f32_16x16x32_bf16 v[172:175], a[108:111], a[12:15], v[172:175]\n\t"
    "s_waitcnt lgkmcnt(11)\n\t"
    "v_lshlrev_b32 v246, 16, v118\n\t"
    "v_and_b32 v247, 0xffff0000, v118\n\t"
    "v_mul_f32 v246, s65, v246\n\t"
    "v_mul_f32 v247, s65, v247\n\t"
    "v_cvt_pk_bf16_f32 v246, v246, v247\n\t"
    "v_accvgpr_write_b32 a18, v246\n\t"
    "v_lshlrev_b32 v248, 16, v119\n\t"
    "v_mfma_f32_16x16x32_bf16 v[176:179], a[112:115], a[0:3], 0\n\t"
    "s_waitcnt lgkmcnt(10)\n\t"
    "v_and_b32 v249, 0xffff0000, v119\n\t"
    "v_mul_f32 v248, s65, v248\n\t"
    "v_mul_f32 v249, s65, v249\n\t"
    "v_cvt_pk_bf16_f32 v248, v248, v249\n\t"
    "v_accvgpr_write_b32 a19, v248\n\t"
    "s_waitcnt vmcnt(14)\n\t"
    "v_lshlrev_b32 v242, 16, v120\n\t"
    "v_mfma_f32_16x16x32_bf16 v[176:179], a[116:119], a[4:7], v[176:179]\n\t"
    "s_waitcnt lgkmcnt(9)\n\t"
    "v_and_b32 v243, 0xffff0000, v120\n\t"
    "v_mul_f32 v242, s65, v242\n\t"
    "v_mul_f32 v243, s65, v243\n\t"
    "v_cvt_pk_bf16_f32 v242, v242, v243\n\t"
    "v_accvgpr_write_b32 a20, v242\n\t"
    "v_lshlrev_b32 v244, 16, v121\n\t"
    "v_and_b32 v245, 0xffff0000, v121\n\t"
    "v_mfma_f32_16x16x32_bf16 v[176:179], a[120:123], a[8:11], v[176:179]\n\t"
    "s_waitcnt lgkmcnt(0)\n\t"
    "v_mul_f32 v244, s65, v244\n\t"
    "v_mul_f32 v245, s65, v245\n\t"
    "v_cvt_pk_bf16_f32 v244, v244, v245\n\t"
    "v_accvgpr_write_b32 a21, v244\n\t"
    "v_lshlrev_b32 v246, 16, v122\n\t"
    "v_and_b32 v247, 0xffff0000, v122\n\t"
    "v_mfma_f32_16x16x32_bf16 v[176:179], a[124:127], a[12:15], v[176:179]\n\t"
    "v_mul_f32 v246, s65, v246\n\t"
    "v_mul_f32 v247, s65, v247\n\t"
    "v_cvt_pk_bf16_f32 v246, v246, v247\n\t"
    "v_accvgpr_write_b32 a22, v246\n\t"
    "v_lshlrev_b32 v248, 16, v123\n\t"
    "v_and_b32 v249, 0xffff0000, v123\n\t"
    "v_mfma_f32_16x16x32_bf16 v[180:183], a[128:131], a[0:3], 0\n\t"
    "v_mul_f32 v248, s65, v248\n\t"
    "v_mul_f32 v249, s65, v249\n\t"
    "v_cvt_pk_bf16_f32 v248, v248, v249\n\t"
    "v_accvgpr_write_b32 a23, v248\n\t"
    "s_waitcnt vmcnt(13)\n\t"
    "v_lshlrev_b32 v242, 16, v124\n\t"
    "v_and_b32 v243, 0xffff0000, v124\n\t"
    "v_mul_f32 v242, s65, v242\n\t"
    "v_mfma_f32_16x16x32_bf16 v[180:183], a[132:135], a[4:7], v[180:183]\n\t"
    "v_mul_f32 v243, s65, v243\n\t"
    "v_cvt_pk_bf16_f32 v242, v242, v243\n\t"
    "v_accvgpr_write_b32 a24, v242\n\t"
    "v_lshlrev_b32 v244, 16, v125\n\t"
    "v_and_b32 v245, 0xffff0000, v125\n\t"
    "v_mul_f32 v244, s65, v244\n\t"
    "v_mfma_f32_16x16x32_bf16 v[180:183], a[136:139], a[8:11], v[180:183]\n\t"
    "v_mul_f32 v245, s65, v245\n\t"
    "v_cvt_pk_bf16_f32 v244, v244, v245\n\t"
    "v_accvgpr_write_b32 a25, v244\n\t"
    "v_lshlrev_b32 v246, 16, v126\n\t"
    "v_and_b32 v247, 0xffff0000, v126\n\t"
    "v_mul_f32 v246, s65, v246\n\t"
    "v_mul_f32 v247, s65, v247\n\t"
    "v_mfma_f32_16x16x32_bf16 v[180:183], a[140:143], a[12:15], v[180:183]\n\t"
    "v_cvt_pk_bf16_f32 v246, v246, v247\n\t"
    "v_accvgpr_write_b32 a26, v246\n\t"
    "v_lshlrev_b32 v248, 16, v127\n\t"
    "v_and_b32 v249, 0xffff0000, v127\n\t"
    "v_mul_f32 v248, s65, v248\n\t"
    "v_mul_f32 v249, s65, v249\n\t"
    "v_exp_f32 v172, v172\n\t"
    "v_mfma_f32_16x16x32_bf16 v[184:187], a[144:147], a[0:3], 0\n\t"
    "v_cvt_pk_bf16_f32 v248, v248, v249\n\t"
    "v_accvgpr_write_b32 a27, v248\n\t"
    "s_waitcnt vmcnt(12)\n\t"
    "v_lshlrev_b32 v242, 16, v128\n\t"
    "v_and_b32 v243, 0xffff0000, v128\n\t"
    "v_mul_f32 v242, s65, v242\n\t"
    "v_mul_f32 v243, s65, v243\n\t"
    "v_exp_f32 v173, v173\n\t"
    "v_mfma_f32_16x16x32_bf16 v[184:187], a[148:151], a[4:7], v[184:187]\n\t"
    "v_cvt_pk_bf16_f32 v242, v242, v243\n\t"
    "v_accvgpr_write_b32 a28, v242\n\t"
    "v_lshlrev_b32 v244, 16, v129\n\t"
    "v_and_b32 v245, 0xffff0000, v129\n\t"
    "v_mul_f32 v244, s65, v244\n\t"
    "v_mul_f32 v245, s65, v245\n\t"
    "v_cvt_pk_bf16_f32 v244, v244, v245\n\t"
    "v_cvt_pk_bf16_f32 v204, v172, v173\n\t"
    "v_add_f32 v250, v250, v172\n\t"
    "v_mfma_f32_16x16x32_bf16 v[184:187], a[152:155], a[8:11], v[184:187]\n\t"
    "v_accvgpr_write_b32 a29, v244\n\t"
    "v_lshlrev_b32 v246, 16, v130\n\t"
    "v_and_b32 v247, 0xffff0000, v130\n\t"
    "v_mul_f32 v246, s65, v246\n\t"
    "v_mul_f32 v247, s65, v247\n\t"
    "v_cvt_pk_bf16_f32 v246, v246, v247\n\t"
    "v_exp_f32 v174, v174\n\t"
    "v_mfma_f32_16x16x32_bf16 v[184:187], a[156:159], a[12:15], v[184:187]\n\t"
    "v_accvgpr_write_b32 a30, v246\n\t"
    "v_lshlrev_b32 v248, 16, v131\n\t"
    "v_and_b32 v249, 0xffff0000, v131\n\t"
    "v_mul_f32 v248, s65, v248\n\t"
    "v_mul_f32 v249, s65, v249\n\t"
    "v_cvt_pk_bf16_f32 v248, v248, v249\n\t"
    "v_accvgpr_write_b32 a31, v248\n\t"
    "v_exp_f32 v175, v175\n\t"
    "s_waitcnt vmcnt(8)\n\t"
    "s_barrier\n\t"
    "v_cvt_pk_bf16_f32 v205, v174, v175\n\t"
    "v_add_f32 v250, v250, v173\n\t"
    "s_mov_b32 s64, 73728\n\t"
    "buffer_load_dwordx4 v[100:103], v240, s[36:39], s64 offen offset:0\n\t"
    "buffer_load_dwordx4 v[104:107], v240, s[36:39], s64 offen offset:64\n\t"
    "buffer_load_dwordx4 v[108:111], v240, s[36:39], s64 offen offset:128\n\t"
    "buffer_load_dwordx4 v[112:115], v240, s[36:39], s64 offen offset:192\n\t"
    "v_exp_f32 v176, v176\n\t"
    "v_exp_f32 v177, v177\n\t"
    "ds_read_b64_tr_b16 a[160:161], v237 offset:0\n\t"
    "ds_read_b64_tr_b16 a[162:163], v237 offset:512\n\t"
    "v_cvt_pk_bf16_f32 v206, v176, v177\n\t"
    "v_add_f32 v250, v250, v174\n\t"
    "s_waitcnt vmcnt(11)\n\t"
    "v_lshlrev_b32 v242, 16, v68\n\t"
    "v_and_b32 v243, 0xffff0000, v68\n\t"
    "v_mul_f32 v242, s65, v242\n\t"
    "v_mul_f32 v243, s65, v243\n\t"
    "v_cvt_pk_bf16_f32 v242, v242, v243\n\t"
    "v_accvgpr_write_b32 a80, v242\n\t"
    "s_add_u32 m0, s55, 16896\n\t"
    "s_add_u32 s61, s59, 19456\n\t"
    "s_and_b32 s61, s61, 0x7ffff\n\t"
    "buffer_load_dwordx4 v239, s[40:43], s61 offen offset:0 nt lds\n\t"
    "s_add_u32 m0, s55, 21120\n\t"
    "s_nop 0\n\t"
    "buffer_load_dwordx4 v226, s[40:43], s61 offen offset:0 nt lds\n\t"
    "s_add_u32 m0, s55, 25344\n\t"
    "s_nop 0\n\t"
    "buffer_load_dwordx4 v227, s[40:43], s61 offen offset:0 nt lds\n\t"
    "s_add_u32 m0, s55, 29568\n\t"
    "s_nop 0\n\t"
    "buffer_load_dwordx4 v3, s[40:43], s61 offen offset:0 nt lds\n\t"
    "ds_read_b64_tr_b16 a[192:193], v237 offset:8448\n\t"
    "ds_read_b64_tr_b16 a[194:195], v237 offset:8960\n\t"
    "v_lshlrev_b32 v244, 16, v69\n\t"
    "v_and_b32 v245, 0xffff0000, v69\n\t"
    "v_mul_f32 v244, s65, v244\n\t"
    "v_mul_f32 v245, s65, v245\n\t"
    "v_cvt_pk_bf16_f32 v244, v244, v245\n\t"
    "v_accvgpr_write_b32 a81, v244\n\t"
    "v_lshlrev_b32 v246, 16, v70\n\t"
    "v_exp_f32 v178, v178\n\t"
    "v_and_b32 v247, 0xffff0000, v70\n\t"
    "v_mul_f32 v246, s65, v246\n\t"
    "v_mul_f32 v247, s65, v247\n\t"
    "v_cvt_pk_bf16_f32 v246, v246, v247\n\t"
    "v_accvgpr_write_b32 a82, v246\n\t"
    "v_lshlrev_b32 v248, 16, v71\n\t"
    "v_and_b32 v249, 0xffff0000, v71\n\t"
    "ds_read_b64_tr_b16 a[164:165], v237 offset:8\n\t"
    "ds_read_b64_tr_b16 a[166:167], v237 offset:520\n\t"
    "v_mul_f32 v248, s65, v248\n\t"
    "v_mul_f32 v249, s65, v249\n\t"
    "v_cvt_pk_bf16_f32 v248, v248, v249\n\t"
    "v_accvgpr_write_b32 a83, v248\n\t"
    "s_waitcnt vmcnt(14)\n\t"
    "v_lshlrev_b32 v242, 16, v72\n\t"
    "v_and_b32 v243, 0xffff0000, v72\n\t"
    "v_mul_f32 v242, s65, v242\n\t"
    "v_exp_f32 v179, v179\n\t"
    "v_mul_f32 v243, s65, v243\n\t"
    "v_cvt_pk_bf16_f32 v242, v242, v243\n\t"
    "v_accvgpr_write_b32 a84, v242\n\t"
    "v_lshlrev_b32 v244, 16, v73\n\t"
    "v_and_b32 v245, 0xffff0000, v73\n\t"
    "v_mul_f32 v244, s65, v244\n\t"
    "v_mul_f32 v245, s65, v245\n\t"
    "ds_read_b64_tr_b16 a[196:197], v237 offset:8456\n\t"
    "ds_read_b64_tr_b16 a[198:199], v237 offset:8968\n\t"
    "v_cvt_pk_bf16_f32 v244, v244, v245\n\t"
    "v_accvgpr_write_b32 a85, v244\n\t"
    "v_lshlrev_b32 v246, 16, v74\n\t"
    "v_and_b32 v247, 0xffff0000, v74\n\t"
    "v_mul_f32 v246, s65, v246\n\t"
    "v_mul_f32 v247, s65, v247\n\t"
    "v_cvt_pk_bf16_f32 v246, v246, v247\n\t"
    "v_cvt_pk_bf16_f32 v207, v178, v179\n\t"
    "v_add_f32 v250, v250, v175\n\t"
    "v_accvgpr_write_b32 a86, v246\n\t"
    "v_lshlrev_b32 v248, 16, v75\n\t"
    "v_and_b32 v249, 0xffff0000, v75\n\t"
    "v_mul_f32 v248, s65, v248\n\t"
    "v_mul_f32 v249, s65, v249\n\t"
    "v_cvt_pk_bf16_f32 v248, v248, v249\n\t"
    "v_accvgpr_write_b32 a87, v248\n\t"
    "ds_read_b64_tr_b16 a[168:169], v237 offset:64\n\t"
    "ds_read_b64_tr_b16 a[170:171], v237 offset:576\n\t"
    "s_waitcnt vmcnt(13)\n\t"
    "v_lshlrev_b32 v242, 16, v76\n\t"
    "v_and_b32 v243, 0xffff0000, v76\n\t"
    "v_mul_f32 v242, s65, v242\n\t"
    "v_mul_f32 v243, s65, v243\n\t"
    "v_cvt_pk_bf16_f32 v242, v242, v243\n\t"
    "v_accvgpr_write_b32 a88, v242\n\t"
    "v_exp_f32 v180, v180\n\t"
    "v_lshlrev_b32 v244, 16, v77\n\t"
    "v_and_b32 v245, 0xffff0000, v77\n\t"
    "v_mul_f32 v244, s65, v244\n\t"
    "v_mul_f32 v245, s65, v245\n\t"
    "v_cvt_pk_bf16_f32 v244, v244, v245\n\t"
    "v_accvgpr_write_b32 a89, v244\n\t"
    "v_lshlrev_b32 v246, 16, v78\n\t"
    "ds_read_b64_tr_b16 a[200:201], v237 offset:8512\n\t"
    "ds_read_b64_tr_b16 a[202:203], v237 offset:9024\n\t"
    "v_and_b32 v247, 0xffff0000, v78\n\t"
    "v_mul_f32 v246, s65, v246\n\t"
    "v_mul_f32 v247, s65, v247\n\t"
    "v_cvt_pk_bf16_f32 v246, v246, v247\n\t"
    "v_accvgpr_write_b32 a90, v246\n\t"
    "v_lshlrev_b32 v248, 16, v79\n\t"
    "v_and_b32 v249, 0xffff0000, v79\n\t"
    "v_exp_f32 v181, v181\n\t"
    "v_mul_f32 v248, s65, v248\n\t"
    "v_mul_f32 v249, s65, v249\n\t"
    "v_cvt_pk_bf16_f32 v248, v248, v249\n\t"
    "v_accvgpr_write_b32 a91, v248\n\t"
    "s_waitcnt vmcnt(12)\n\t"
    "v_lshlrev_b32 v242, 16, v80\n\t"
    "v_and_b32 v243, 0xffff0000, v80\n\t"
    "v_mul_f32 v242, s65, v242\n\t"
    "ds_read_b64_tr_b16 a[172:173], v237 offset:72\n\t"
    "ds_read_b64_tr_b16 a[174:175], v237 offset:584\n\t"
    "v_mul_f32 v243, s65, v243\n\t"
    "v_cvt_pk_bf16_f32 v242, v242, v243\n\t"
    "v_accvgpr_write_b32 a92, v242\n\t"
    "v_lshlrev_b32 v244, 16, v81\n\t"
    "v_and_b32 v245, 0xffff0000, v81\n\t"
    "v_mul_f32 v244, s65, v244\n\t"
    "v_mul_f32 v245, s65, v245\n\t"
    "v_cvt_pk_bf16_f32 v208, v180, v181\n\t"
    "v_add_f32 v250, v250, v176\n\t"
    "v_cvt_pk_bf16_f32 v244, v244, v245\n\t"
    "v_accvgpr_write_b32 a93, v244\n\t"
    "v_lshlrev_b32 v246, 16, v82\n\t"
    "v_and_b32 v247, 0xffff0000, v82\n\t"
    "v_mul_f32 v246, s65, v246\n\t"
    "v_mul_f32 v247, s65, v247\n\t"
    "v_cvt_pk_bf16_f32 v246, v246, v247\n\t"
    "v_accvgpr_write_b32 a94, v246\n\t"
    "v_lshlrev_b32 v248, 16, v83\n\t"
    "v_and_b32 v249, 0xffff0000, v83\n\t"
    "v_mul_f32 v248, s65, v248\n\t"
    "v_mul_f32 v249, s65, v249\n\t"
    "v_cvt_pk_bf16_f32 v248, v248, v249\n\t"
    "v_accvgpr_write_b32 a95, v248\n\t"
    "v_exp_f32 v182, v182\n\t"
    "v_mfma_f32_16x16x32_bf16 v[188:191], a[96:99], a[16:19], 0\n\t"
    "ds_read_b64_tr_b16 a[204:205], v237 offset:8520\n\t"
    "ds_read_b64_tr_b16 a[206:207], v237 offset:9032\n\t"
    "v_mfma_f32_16x16x32_bf16 v[188:191], a[100:103], a[20:23], v[188:191]\n\t"
    "v_exp_f32 v183, v183\n\t"
    "v_mfma_f32_16x16x32_bf16 v[188:191], a[104:107], a[24:27], v[188:191]\n\t"
    "s_waitcnt vmcnt(11)\n\t"
    "v_lshlrev_b32 v242, 16, v84\n\t"
    "v_and_b32 v243, 0xffff0000, v84\n\t"
    "v_mul_f32 v242, s65, v242\n\t"
    "s_add_u32 m0, s56, 16896\n\t"
    "s_add_u32 s61, s59, 19456\n\t"
    "s_and_b32 s61, s61, 0x7ffff\n\t"
    "buffer_load_dwordx4 v239, s[44:47], s61 offen offset:0 lds\n\t"
    "s_add_u32 m0, s56, 21120\n\t"
    "s_nop 0\n\t"
    "buffer_load_dwordx4 v226, s[44:47], s61 offen offset:0 lds\n\t"
    "s_add_u32 m0, s56, 25344\n\t"
    "s_nop 0\n\t"
    "buffer_load_dwordx4 v227, s[44:47], s61 offen offset:0 lds\n\t"
    "s_add_u32 m0, s56, 29568\n\t"
    "s_nop 0\n\t"
    "buffer_load_dwordx4 v3, s[44:47], s61 offen offset:0 lds\n\t"
    "ds_read_b64_tr_b16 a[176:177], v237 offset:128\n\t"
    "ds_read_b64_tr_b16 a[178:179], v237 offset:640\n\t"
    "v_mfma_f32_16x16x32_bf16 v[188:191], a[108:111], a[28:31], v[188:191]\n\t"
    "v_mul_f32 v243, s65, v243\n\t"
    "v_cvt_pk_bf16_f32 v242, v242, v243\n\t"
    "v_accvgpr_write_b32 a32, v242\n\t"
    "v_cvt_pk_bf16_f32 v209, v182, v183\n\t"
    "v_add_f32 v250, v250, v177\n\t"
    "v_mfma_f32_16x16x32_bf16 v[192:195], a[112:115], a[16:19], 0\n\t"
    "v_lshlrev_b32 v244, 16, v85\n\t"
    "v_and_b32 v245, 0xffff0000, v85\n\t"
    "v_mul_f32 v244, s65, v244\n\t"
    "ds_read_b64_tr_b16 a[208:209], v237 offset:8576\n\t"
    "ds_read_b64_tr_b16 a[210:211], v237 offset:9088\n\t"
    "v_mfma_f32_16x16x32_bf16 v[192:195], a[116:119], a[20:23], v[192:195]\n\t"
    "v_mul_f32 v245, s65, v245\n\t"
    "v_cvt_pk_bf16_f32 v244, v244, v245\n\t"
    "v_accvgpr_write_b32 a33, v244\n\t"
    "v_exp_f32 v184, v184\n\t"
    "v_mfma_f32_16x16x32_bf16 v[192:195], a[120:123], a[24:27], v[192:195]\n\t"
    "v_lshlrev_b32 v246, 16, v86\n\t"
    "v_and_b32 v247, 0xffff0000, v86\n\t"
    "v_mul_f32 v246, s65, v246\n\t"
    "ds_read_b64_tr_b16 a[180:181], v237 offset:136\n\t"
    "ds_read_b64_tr_b16 a[182:183], v237 offset:648\n\t"
    "v_mfma_f32_16x16x32_bf16 v[192:195], a[124:127], a[28:31], v[192:195]\n\t"
    "v_mul_f32 v247, s65, v247\n\t"
    "v_cvt_pk_bf16_f32 v246, v246, v247\n\t"
    "v_accvgpr_write_b32 a34, v246\n\t"
    "v_exp_f32 v185, v185\n\t"
    "v_mfma_f32_16x16x32_bf16 v[196:199], a[128:131], a[16:19], 0\n\t"
    "v_lshlrev_b32 v248, 16, v87\n\t"
    "v_and_b32 v249, 0xffff0000, v87\n\t"
    "v_mul_f32 v248, s65, v248\n\t"
    "ds_read_b64_tr_b16 a[212:213], v237 offset:8584\n\t"
    "ds_read_b64_tr_b16 a[214:215], v237 offset:9096\n\t"
    "v_mfma_f32_16x16x32_bf16 v[196:199], a[132:135], a[20:23], v[196:199]\n\t"
    "v_mul_f32 v249, s65, v249\n\t"
    "v_cvt_pk_bf16_f32 v248, v248, v249\n\t"
    "v_accvgpr_write_b32 a35, v248\n\t"
    "v_cvt_pk_bf16_f32 v210, v184, v185\n\t"
    "v_add_f32 v250, v250, v178\n\t"
    "v_mfma_f32_16x16x32_bf16 v[196:199], a[136:139], a[24:27], v[196:199]\n\t"
    "s_waitcnt vmcnt(14)\n\t"
    "v_lshlrev_b32 v242, 16, v88\n\t"
    "v_and_b32 v243, 0xffff0000, v88\n\t"
    "v_mul_f32 v242, s65, v242\n\t"
    "ds_read_b64_tr_b16 a[184:185], v237 offset:192\n\t"
    "ds_read_b64_tr_b16 a[186:187], v237 offset:704\n\t"
    "v_mfma_f32_16x16x32_bf16 v[196:199], a[140:143], a[28:31], v[196:199]\n\t"
    "v_mul_f32 v243, s65, v243\n\t"
    "v_cvt_pk_bf16_f32 v242, v242, v243\n\t"
    "v_accvgpr_write_b32 a36, v242\n\t"
    "v_exp_f32 v186, v186\n\t"
    "v_mfma_f32_16x16x32_bf16 v[200:203], a[144:147], a[16:19], 0\n\t"
    "v_lshlrev_b32 v244, 16, v89\n\t"
    "v_and_b32 v245, 0xffff0000, v89\n\t"
    "v_mul_f32 v244, s65, v244\n\t"
    "v_mul_f32 v245, s65, v245\n\t"
    "ds_read_b64_tr_b16 a[216:217], v237 offset:8640\n\t"
    "ds_read_b64_tr_b16 a[218:219], v237 offset:9152\n\t"
    "v_mfma_f32_16x16x32_bf16 v[200:203], a[148:151], a[20:23], v[200:203]\n\t"
    "v_cvt_pk_bf16_f32 v244, v244, v245\n\t"
    "v_accvgpr_write_b32 a37, v244\n\t"
    "v_lshlrev_b32 v246, 16, v90\n\t"
    "v_exp_f32 v187, v187\n\t"
    "v_mfma_f32_16x16x32_bf16 v[200:203], a[152:155], a[24:27], v[200:203]\n\t"
    "v_and_b32 v247, 0xffff0000, v90\n\t"
    "v_mul_f32 v246, s65, v246\n\t"
    "v_mul_f32 v247, s65, v247\n\t"
    "ds_read_b64_tr_b16 a[188:189], v237 offset:200\n\t"
    "ds_read_b64_tr_b16 a[190:191], v237 offset:712\n\t"
    "v_mfma_f32_16x16x32_bf16 v[200:203], a[156:159], a[28:31], v[200:203]\n\t"
    "v_cvt_pk_bf16_f32 v246, v246, v247\n\t"
    "v_accvgpr_write_b32 a38, v246\n\t"
    "v_lshlrev_b32 v248, 16, v91\n\t"
    "v_cvt_pk_bf16_f32 v211, v186, v187\n\t"
    "v_add_f32 v250, v250, v179\n\t"
    "v_mfma_f32_16x16x32_bf16 v[220:223], a[144:147], a[80:83], 0\n\t"
    "v_and_b32 v249, 0xffff0000, v91\n\t"
    "v_mul_f32 v248, s65, v248\n\t"
    "v_mul_f32 v249, s65, v249\n\t"
    "ds_read_b64_tr_b16 a[220:221], v237 offset:8648\n\t"
    "ds_read_b64_tr_b16 a[222:223], v237 offset:9160\n\t"
    "v_mfma_f32_16x16x32_bf16 v[220:223], a[148:151], a[84:87], v[220:223]\n\t"
    "v_cvt_pk_bf16_f32 v248, v248, v249\n\t"
    "v_accvgpr_write_b32 a39, v248\n\t"
    "s_waitcnt vmcnt(13)\n\t"
    "v_lshlrev_b32 v242, 16, v92\n\t"
    "v_exp_f32 v188, v188\n\t"
    "v_mfma_f32_16x16x32_bf16 v[220:223], a[152:155], a[88:91], v[220:223]\n\t"
    "v_and_b32 v243, 0xffff0000, v92\n\t"
    "v_mul_f32 v242, s65, v242\n\t"
    "v_mul_f32 v243, s65, v243\n\t"
    "v_mfma_f32_16x16x32_bf16 v[220:223], a[156:159], a[92:95], v[220:223]\n\t"
    "s_waitcnt lgkmcnt(4)\n\t"
    "v_cvt_pk_bf16_f32 v242, v242, v243\n\t"
    "v_accvgpr_write_b32 a40, v242\n\t"
    "v_lshlrev_b32 v244, 16, v93\n\t"
    "v_exp_f32 v189, v189\n\t"
    "v_mfma_f32_16x16x32_bf16 v[4:7], a[160:163], v[204:207], 0\n\t"
    "v_and_b32 v245, 0xffff0000, v93\n\t"
    "v_mul_f32 v244, s65, v244\n\t"
    "v_mul_f32 v245, s65, v245\n\t"
    "v_cvt_pk_bf16_f32 v212, v188, v189\n\t"
    "v_add_f32 v250, v250, v180\n\t"
    "v_mfma_f32_16x16x32_bf16 v[4:7], a[192:195], v[208:211], v[4:7]\n\t"
    "v_cvt_pk_bf16_f32 v244, v244, v245\n\t"
    "v_accvgpr_write_b32 a41, v244\n\t"
    "v_lshlrev_b32 v246, 16, v94\n\t"
    "v_exp_f32 v190, v190\n\t"
    "v_mfma_f32_16x16x32_bf16 v[8:11], a[164:167], v[204:207], 0\n\t"
    "v_and_b32 v247, 0xffff0000, v94\n\t"
    "v_mul_f32 v246, s65, v246\n\t"
    "v_mul_f32 v247, s65, v247\n\t"
    "v_cvt_pk_bf16_f32 v246, v246, v247\n\t"
    "v_exp_f32 v191, v191\n\t"
    "v_mfma_f32_16x16x32_bf16 v[8:11], a[196:199], v[208:211], v[8:11]\n\t"
    "v_accvgpr_write_b32 a42, v246\n\t"
    "v_lshlrev_b32 v248, 16, v95\n\t"
    "v_and_b32 v249, 0xffff0000, v95\n\t"
    "v_cvt_pk_bf16_f32 v213, v190, v191\n\t"
    "v_add_f32 v250, v250, v181\n\t"
    "v_mfma_f32_16x16x32_bf16 v[12:15], a[168:171], v[204:207], 0\n\t"
    "v_mul_f32 v248, s65, v248\n\t"
    "v_mul_f32 v249, s65, v249\n\t"
    "v_cvt_pk_bf16_f32 v248, v248, v249\n\t"
    "v_exp_f32 v192, v192\n\t"
    "v_mfma_f32_16x16x32_bf16 v[12:15], a[200:203], v[208:211], v[12:15]\n\t"
    "v_accvgpr_write_b32 a43, v248\n\t"
    "s_waitcnt vmcnt(12)\n\t"
    "v_lshlrev_b32 v242, 16, v96\n\t"
    "v_and_b32 v243, 0xffff0000, v96\n\t"
    "v_exp_f32 v193, v193\n\t"
    "v_mfma_f32_16x16x32_bf16 v[16:19], a[172:175], v[204:207], 0\n\t"
    "v_mul_f32 v242, s65, v242\n\t"
    "v_mul_f32 v243, s65, v243\n\t"
    "v_cvt_pk_bf16_f32 v242, v242, v243\n\t"
    "v_cvt_pk_bf16_f32 v214, v192, v193\n\t"
    "v_add_f32 v250, v250, v182\n\t"
    "v_mfma_f32_16x16x32_bf16 v[16:19], a[204:207], v[208:211], v[16:19]\n\t"
    "v_accvgpr_write_b32 a44, v242\n\t"
    "v_lshlrev_b32 v244, 16, v97\n\t"
    "v_and_b32 v245, 0xffff0000, v97\n\t"
    "v_exp_f32 v194, v194\n\t"
    "v_mfma_f32_16x16x32_bf16 v[20:23], a[176:179], v[204:207], 0\n\t"
    "v_mul_f32 v244, s65, v244\n\t"
    "v_mul_f32 v245, s65, v245\n\t"
    "v_cvt_pk_bf16_f32 v244, v244, v245\n\t"
    "v_exp_f32 v195, v195\n\t"
    "v_mfma_f32_16x16x32_bf16 v[20:23], a[208:211], v[208:211], v[20:23]\n\t"
    "v_accvgpr_write_b32 a45, v244\n\t"
    "v_lshlrev_b32 v246, 16, v98\n\t"
    "v_and_b32 v247, 0xffff0000, v98\n\t"
    "v_cvt_pk_bf16_f32 v215, v194, v195\n\t"
    "v_add_f32 v250, v250, v183\n\t"
    "v_mfma_f32_16x16x32_bf16 v[24:27], a[180:183], v[204:207], 0\n\t"
    "v_mul_f32 v246, s65, v246\n\t"
    "v_mul_f32 v247, s65, v247\n\t"
    "v_cvt_pk_bf16_f32 v246, v246, v247\n\t"
    "v_mfma_f32_16x16x32_bf16 v[24:27], a[212:215], v[208:211], v[24:27]\n\t"
    "v_accvgpr_write_b32 a46, v246\n\t"
    "v_lshlrev_b32 v248, 16, v99\n\t"
    "v_and_b32 v249, 0xffff0000, v99\n\t"
    "v_exp_f32 v196, v196\n\t"
    "v_mfma_f32_16x16x32_bf16 v[28:31], a[184:187], v[204:207], 0\n\t"
    "v_mul_f32 v248, s65, v248\n\t"
    "v_mul_f32 v249, s65, v249\n\t"
    "v_cvt_pk_bf16_f32 v248, v248, v249\n\t"
    "v_accvgpr_write_b32 a47, v248\n\t"
    "v_exp_f32 v197, v197\n\t"
    "v_mfma_f32_16x16x32_bf16 v[28:31], a[216:219], v[208:211], v[28:31]\n\t"
    "s_waitcnt lgkmcnt(0)\n\t"
    "v_cvt_pk_bf16_f32 v216, v196, v197\n\t"
    "v_add_f32 v250, v250, v184\n\t"
    "v_mfma_f32_16x16x32_bf16 v[32:35], a[188:191], v[204:207], 0\n\t"
    "v_exp_f32 v198, v198\n\t"
    "v_mfma_f32_16x16x32_bf16 v[32:35], a[220:223], v[208:211], v[32:35]\n\t"
    "v_exp_f32 v199, v199\n\t"
    "v_mfma_f32_16x16x32_bf16 v[172:175], a[96:99], a[32:35], 0\n\t"
    "v_cvt_pk_bf16_f32 v217, v198, v199\n\t"
    "v_add_f32 v250, v250, v185\n\t"
    "v_mfma_f32_16x16x32_bf16 v[172:175], a[100:103], a[36:39], v[172:175]\n\t"
    "s_waitcnt vmcnt(11)\n\t"
    "v_lshlrev_b32 v242, 16, v100\n\t"
    "v_and_b32 v243, 0xffff0000, v100\n\t"
    "v_mul_f32 v242, s65, v242\n\t"
    "s_mov_b32 s64, 77824\n\t"
    "buffer_load_dwordx4 v[116:119], v240, s[36:39], s64 offen offset:0\n\t"
    "buffer_load_dwordx4 v[120:123], v240, s[36:39], s64 offen offset:64\n\t"
    "buffer_load_dwordx4 v[124:127], v240, s[36:39], s64 offen offset:128\n\t"
    "buffer_load_dwordx4 v[128:131], v240, s[36:39], s64 offen offset:192\n\t"
    "v_exp_f32 v200, v200\n\t"
    "v_mfma_f32_16x16x32_bf16 v[172:175], a[104:107], a[40:43], v[172:175]\n\t"
    "v_mul_f32 v243, s65, v243\n\t"
    "v_cvt_pk_bf16_f32 v242, v242, v243\n\t"
    "v_accvgpr_write_b32 a48, v242\n\t"
    "v_exp_f32 v220, v220\n\t"
    "v_mfma_f32_16x16x32_bf16 v[172:175], a[108:111], a[44:47], v[172:175]\n\t"
    "v_lshlrev_b32 v244, 16, v101\n\t"
    "v_and_b32 v245, 0xffff0000, v101\n\t"
    "v_mul_f32 v244, s65, v244\n\t"
    "v_exp_f32 v201, v201\n\t"
    "v_mfma_f32_16x16x32_bf16 v[176:179], a[112:115], a[32:35], 0\n\t"
    "v_mul_f32 v245, s65, v245\n\t"
    "v_cvt_pk_bf16_f32 v244, v244, v245\n\t"
    "v_accvgpr_write_b32 a49, v244\n\t"
    "v_exp_f32 v221, v221\n\t"
    "v_mfma_f32_16x16x32_bf16 v[176:179], a[116:119], a[36:39], v[176:179]\n\t"
    "v_lshlrev_b32 v246, 16, v102\n\t"
    "v_and_b32 v247, 0xffff0000, v102\n\t"
    "v_mul_f32 v246, s65, v246\n\t"
    "v_add_f32 v250, v250, v186\n\t"
    "v_add_f32 v250, v250, v187\n\t"
    "v_mfma_f32_16x16x32_bf16 v[176:179], a[120:123], a[40:43], v[176:179]\n\t"
    "v_mul_f32 v247, s65, v247\n\t"
    "v_cvt_pk_bf16_f32 v246, v246, v247\n\t"
    "v_accvgpr_write_b32 a50, v246\n\t"
    "v_cvt_pk_bf16_f32 v218, v200, v201\n\t"
    "v_cvt_pk_bf16_f32 v224, v220, v221\n\t"
    "v_mfma_f32_16x16x32_bf16 v[176:179], a[124:127], a[44:47], v[176:179]\n\t"
    "v_lshlrev_b32 v248, 16, v103\n\t"
    "v_and_b32 v249, 0xffff0000, v103\n\t"
    "v_mul_f32 v248, s65, v248\n\t"
    "v_exp_f32 v202, v202\n\t"
    "v_mfma_f32_16x16x32_bf16 v[180:183], a[128:131], a[32:35], 0\n\t"
    "v_mul_f32 v249, s65, v249\n\t"
    "v_cvt_pk_bf16_f32 v248, v248, v249\n\t"
    "v_accvgpr_write_b32 a51, v248\n\t"
    "v_exp_f32 v222, v222\n\t"
    "v_mfma_f32_16x16x32_bf16 v[180:183], a[132:135], a[36:39], v[180:183]\n\t"
    "s_waitcnt vmcnt(14)\n\t"
    "v_lshlrev_b32 v242, 16, v104\n\t"
    "v_and_b32 v243, 0xffff0000, v104\n\t"
    "v_mul_f32 v242, s65, v242\n\t"
    "v_exp_f32 v203, v203\n\t"
    "v_mfma_f32_16x16x32_bf16 v[180:183], a[136:139], a[40:43], v[180:183]\n\t"
    "v_mul_f32 v243, s65, v243\n\t"
    "v_cvt_pk_bf16_f32 v242, v242, v243\n\t"
    "v_accvgpr_write_b32 a52, v242\n\t"
    "v_mfma_f32_16x16x32_bf16 v[180:183], a[140:143], a[44:47], v[180:183]\n\t"
    "v_lshlrev_b32 v244, 16, v105\n\t"
    "v_and_b32 v245, 0xffff0000, v105\n\t"
    "v_mul_f32 v244, s65, v244\n\t"
    "v_mul_f32 v245, s65, v245\n\t"
    "v_exp_f32 v223, v223\n\t"
    "v_mfma_f32_16x16x32_bf16 v[184:187], a[144:147], a[32:35], 0\n\t"
    "v_cvt_pk_bf16_f32 v244, v244, v245\n\t"
    "v_accvgpr_write_b32 a53, v244\n\t"
    "v_lshlrev_b32 v246, 16, v106\n\t"
    "v_cvt_pk_bf16_f32 v225, v222, v223\n\t"
    "v_cvt_pk_bf16_f32 v219, v202, v203\n\t"
    "v_mfma_f32_16x16x32_bf16 v[184:187], a[148:151], a[36:39], v[184:187]\n\t"
    "v_and_b32 v247, 0xffff0000, v106\n\t"
    "v_mul_f32 v246, s65, v246\n\t"
    "v_mul_f32 v247, s65, v247\n\t"
    "ds_write_b64 v238, v[224:225] offset:1032\n\t"
    "v_mfma_f32_16x16x32_bf16 v[184:187], a[152:155], a[40:43], v[184:187]\n\t"
    "v_cvt_pk_bf16_f32 v246, v246, v247\n\t"
    "v_accvgpr_write_b32 a54, v246\n\t"
    "v_lshlrev_b32 v248, 16, v107\n\t"
    "v_exp_f32 v172, v172\n\t"
    "v_mfma_f32_16x16x32_bf16 v[184:187], a[156:159], a[44:47], v[184:187]\n\t"
    "v_and_b32 v249, 0xffff0000, v107\n\t"
    "v_mul_f32 v248, s65, v248\n\t"
    "v_mul_f32 v249, s65, v249\n\t"
    "v_exp_f32 v173, v173\n\t"
    "v_mfma_f32_16x16x16_bf16 a[244:247], a[248:249], v[224:225], 0\n\t"
    "v_cvt_pk_bf16_f32 v248, v248, v249\n\t"
    "v_accvgpr_write_b32 a55, v248\n\t"
    "s_waitcnt vmcnt(13)\n\t"
    "v_lshlrev_b32 v242, 16, v108\n\t"
    "v_cvt_pk_bf16_f32 v204, v172, v173\n\t"
    "v_add_f32 v251, v251, v196\n\t"
    "v_mfma_f32_16x16x32_bf16 v[36:39], a[160:163], v[212:215], 0\n\t"
    "v_and_b32 v243, 0xffff0000, v108\n\t"
    "v_mul_f32 v242, s65, v242\n\t"
    "v_mul_f32 v243, s65, v243\n\t"
    "v_exp_f32 v174, v174\n\t"
    "v_mfma_f32_16x16x32_bf16 v[36:39], a[192:195], v[216:219], v[36:39]\n\t"
    "v_cvt_pk_bf16_f32 v242, v242, v243\n\t"
    "v_accvgpr_write_b32 a56, v242\n\t"
    "v_lshlrev_b32 v244, 16, v109\n\t"
    "v_exp_f32 v175, v175\n\t"
    "v_mfma_f32_16x16x32_bf16 v[40:43], a[164:167], v[212:215], 0\n\t"
    "v_and_b32 v245, 0xffff0000, v109\n\t"
    "v_mul_f32 v244, s65, v244\n\t"
    "v_mul_f32 v245, s65, v245\n\t"
    "v_cvt_pk_bf16_f32 v205, v174, v175\n\t"
    "v_add_f32 v251, v251, v197\n\t"
    "v_mfma_f32_16x16x32_bf16 v[40:43], a[196:199], v[216:219], v[40:43]\n\t"
    "v_cvt_pk_bf16_f32 v244, v244, v245\n\t"
    "v_accvgpr_write_b32 a57, v244\n\t"
    "v_lshlrev_b32 v246, 16, v110\n\t"
    "v_exp_f32 v176, v176\n\t"
    "v_mfma_f32_16x16x32_bf16 v[44:47], a[168:171], v[212:215], 0\n\t"
    "v_and_b32 v247, 0xffff0000, v110\n\t"
    "v_mul_f32 v246, s65, v246\n\t"
    "v_mul_f32 v247, s65, v247\n\t"
    "v_cvt_pk_bf16_f32 v246, v246, v247\n\t"
    "v_exp_f32 v177, v177\n\t"
    "v_mfma_f32_16x16x32_bf16 v[44:47], a[200:203], v[216:219], v[44:47]\n\t"
    "v_accvgpr_write_b32 a58, v246\n\t"
    "v_lshlrev_b32 v248, 16, v111\n\t"
    "v_and_b32 v249, 0xffff0000, v111\n\t"
    "v_cvt_pk_bf16_f32 v206, v176, v177\n\t"
    "v_add_f32 v251, v251, v198\n\t"
    "v_mfma_f32_16x16x32_bf16 v[48:51], a[172:175], v[212:215], 0\n\t"
    "v_mul_f32 v248, s65, v248\n\t"
    "v_mul_f32 v249, s65, v249\n\t"
    "v_cvt_pk_bf16_f32 v248, v248, v249\n\t"
    "v_exp_f32 v178, v178\n\t"
    "v_mfma_f32_16x16x32_bf16 v[48:51], a[204:207], v[216:219], v[48:51]\n\t"
    "v_accvgpr_write_b32 a59, v248\n\t"
    "s_waitcnt vmcnt(12)\n\t"
    "v_lshlrev_b32 v242, 16, v112\n\t"
    "v_and_b32 v243, 0xffff0000, v112\n\t"
    "v_exp_f32 v179, v179\n\t"
    "v_mfma_f32_16x16x32_bf16 v[52:55], a[176:179], v[212:215], 0\n\t"
    "v_mul_f32 v242, s65, v242\n\t"
    "v_mul_f32 v243, s65, v243\n\t"
    "v_cvt_pk_bf16_f32 v242, v242, v243\n\t"
    "v_cvt_pk_bf16_f32 v207, v178, v179\n\t"
    "v_add_f32 v251, v251, v199\n\t"
    "v_mfma_f32_16x16x32_bf16 v[52:55], a[208:211], v[216:219], v[52:55]\n\t"
    "v_accvgpr_write_b32 a60, v242\n\t"
    "v_lshlrev_b32 v244, 16, v113\n\t"
    "v_and_b32 v245, 0xffff0000, v113\n\t"
    "v_mfma_f32_16x16x32_bf16 v[56:59], a[180:183], v[212:215], 0\n\t"
    "v_mul_f32 v244, s65, v244\n\t"
    "v_mul_f32 v245, s65, v245\n\t"
    "v_cvt_pk_bf16_f32 v244, v244, v245\n\t"
    "v_exp_f32 v180, v180\n\t"
    "v_mfma_f32_16x16x32_bf16 v[56:59], a[212:215], v[216:219], v[56:59]\n\t"
    "v_accvgpr_write_b32 a61, v244\n\t"
    "v_lshlrev_b32 v246, 16, v114\n\t"
    "v_and_b32 v247, 0xffff0000, v114\n\t"
    "v_exp_f32 v181, v181\n\t"
    "v_mfma_f32_16x16x32_bf16 v[60:63], a[184:187], v[212:215], 0\n\t"
    "v_mul_f32 v246, s65, v246\n\t"
    "v_mul_f32 v247, s65, v247\n\t"
    "v_cvt_pk_bf16_f32 v246, v246, v247\n\t"
    "v_cvt_pk_bf16_f32 v208, v180, v181\n\t"
    "v_add_f32 v251, v251, v200\n\t"
    "v_mfma_f32_16x16x32_bf16 v[60:63], a[216:219], v[216:219], v[60:63]\n\t"
    "v_accvgpr_write_b32 a62, v246\n\t"
    "v_lshlrev_b32 v248, 16, v115\n\t"
    "v_and_b32 v249, 0xffff0000, v115\n\t"
    "v_exp_f32 v182, v182\n\t"
    "v_mfma_f32_16x16x32_bf16 v[64:67], a[188:191], v[212:215], 0\n\t"
    "v_mul_f32 v248, s65, v248\n\t"
    "v_mul_f32 v249, s65, v249\n\t"
    "v_cvt_pk_bf16_f32 v248, v248, v249\n\t"
    "v_accvgpr_write_b32 a63, v248\n\t"
    "v_exp_f32 v183, v183\n\t"
    "v_mfma_f32_16x16x32_bf16 v[64:67], a[220:223], v[216:219], v[64:67]\n\t"
    "v_cvt_pk_bf16_f32 v209, v182, v183\n\t"
    "v_add_f32 v251, v251, v201\n\t"
    "v_mfma_f32_16x16x32_bf16 a[228:231], a[248:251], v[212:215], 0\n\t"
    "v_exp_f32 v184, v184\n\t"
    "v_mfma_f32_16x16x32_bf16 v[188:191], a[96:99], a[48:51], 0\n\t"
    "v_exp_f32 v185, v185\n\t"
    "v_mfma_f32_16x16x32_bf16 v[188:191], a[100:103], a[52:55], v[188:191]\n\t"
    "s_waitcnt vmcnt(3)\n\t"
    "v_lshlrev_b32 v242, 16, v116\n\t"
    "v_and_b32 v243, 0xffff0000, v116\n\t"
    "v_mul_f32 v242, s65, v242\n\t"
    "v_cvt_pk_bf16_f32 v210, v184, v185\n\t"
    "v_add_f32 v251, v251, v202\n\t"
    "v_mfma_f32_16x16x32_bf16 v[188:191], a[104:107], a[56:59], v[188:191]\n\t"
    "v_mul_f32 v243, s65, v243\n\t"
    "v_cvt_pk_bf16_f32 v242, v242, v243\n\t"
    "v_accvgpr_write_b32 a64, v242\n\t"
    "s_add_u32 m0, s55, 33792\n\t"
    "s_add_u32 s61, s59, 35840\n\t"
    "s_and_b32 s61, s61, 0x7ffff\n\t"
    "buffer_load_dwordx4 v239, s[40:43], s61 offen offset:0 nt lds\n\t"
    "s_add_u32 m0, s55, 38016\n\t"
    "s_nop 0\n\t"
    "buffer_load_dwordx4 v226, s[40:43], s61 offen offset:0 nt lds\n\t"
    "s_add_u32 m0, s55, 42240\n\t"
    "s_nop 0\n\t"
    "buffer_load_dwordx4 v227, s[40:43], s61 offen offset:0 nt lds\n\t"
    "s_add_u32 m0, s55, 46464\n\t"
    "s_nop 0\n\t"
    "buffer_load_dwordx4 v3, s[40:43], s61 offen offset:0 nt lds\n\t"
    "v_exp_f32 v186, v186\n\t"
    "v_mfma_f32_16x16x32_bf16 v[188:191], a[108:111], a[60:63], v[188:191]\n\t"
    "v_lshlrev_b32 v244, 16, v117\n\t"
    "v_and_b32 v245, 0xffff0000, v117\n\t"
    "v_mul_f32 v244, s65, v244\n\t"
    "v_add_f32 v251, v251, v203\n\t"
    "v_mfma_f32_16x16x32_bf16 v[192:195], a[112:115], a[48:51], 0\n\t"
    "v_mul_f32 v245, s65, v245\n\t"
    "v_cvt_pk_bf16_f32 v244, v244, v245\n\t"
    "v_accvgpr_write_b32 a65, v244\n\t"
    "v_exp_f32 v187, v187\n\t"
    "v_mfma_f32_16x16x32_bf16 v[192:195], a[116:119], a[52:55], v[192:195]\n\t"
    "v_lshlrev_b32 v246, 16, v118\n\t"
    "v_and_b32 v247, 0xffff0000, v118\n\t"
    "v_mul_f32 v246, s65, v246\n\t"
    "v_mul_f32 v247, s65, v247\n\t"
    "v_cvt_pk_bf16_f32 v211, v186, v187\n\t"
    "v_mfma_f32_16x16x32_bf16 v[192:195], a[120:123], a[56:59], v[192:195]\n\t"
    "v_cvt_pk_bf16_f32 v246, v246, v247\n\t"
    "v_accvgpr_write_b32 a66, v246\n\t"
    "v_lshlrev_b32 v248, 16, v119\n\t"
    "v_add_f32 v252, v252, v180\n\t"
    "v_add_f32 v252, v252, v181\n\t"
    "v_mfma_f32_16x16x32_bf16 v[192:195], a[124:127], a[60:63], v[192:195]\n\t"
    "v_and_b32 v249, 0xffff0000, v119\n\t"
    "v_mul_f32 v248, s65, v248\n\t"
    "v_mul_f32 v249, s65, v249\n\t"
    "v_add_f32 v252, v252, v182\n\t"
    "v_add_f32 v252, v252, v183\n\t"
    "v_mfma_f32_16x16x32_bf16 v[196:199], a[128:131], a[48:51], 0\n\t"
    "v_cvt_pk_bf16_f32 v248, v248, v249\n\t"
    "v_accvgpr_write_b32 a67, v248\n\t"
    "s_waitcnt vmcnt(6)\n\t"
    "v_lshlrev_b32 v242, 16, v120\n\t"
    "v_mfma_f32_16x16x32_bf16 v[196:199], a[132:135], a[52:55], v[196:199]\n\t"
    "v_and_b32 v243, 0xffff0000, v120\n\t"
    "v_mul_f32 v242, s65, v242\n\t"
    "v_mul_f32 v243, s65, v243\n\t"
    "s_add_u32 m0, s56, 33792\n\t"
    "s_add_u32 s61, s59, 35840\n\t"
    "s_and_b32 s61, s61, 0x7ffff\n\t"
    "buffer_load_dwordx4 v239, s[44:47], s61 offen offset:0 lds\n\t"
    "s_add_u32 m0, s56, 38016\n\t"
    "s_nop 0\n\t"
    "buffer_load_dwordx4 v226, s[44:47], s61 offen offset:0 lds\n\t"
    "s_add_u32 m0, s56, 42240\n\t"
    "s_nop 0\n\t"
    "buffer_load_dwordx4 v227, s[44:47], s61 offen offset:0 lds\n\t"
    "s_add_u32 m0, s56, 46464\n\t"
    "s_nop 0\n\t"
    "buffer_load_dwordx4 v3, s[44:47], s61 offen offset:0 lds\n\t"
    "v_add_f32 v252, v252, v184\n\t"
    "v_add_f32 v252, v252, v185\n\t"
    "v_mfma_f32_16x16x32_bf16 v[196:199], a[136:139], a[56:59], v[196:199]\n\t"
    "v_cvt_pk_bf16_f32 v242, v242, v243\n\t"
    "v_accvgpr_write_b32 a68, v242\n\t"
    "v_lshlrev_b32 v244, 16, v121\n\t"
    "v_and_b32 v245, 0xffff0000, v121\n\t"
    "v_add_f32 v252, v252, v186\n\t"
    "v_add_f32 v252, v252, v187\n\t"
    "v_mfma_f32_16x16x32_bf16 v[196:199], a[140:143], a[60:63], v[196:199]\n\t"
    "v_mul_f32 v244, s65, v244\n\t"
    "v_mul_f32 v245, s65, v245\n\t"
    "v_cvt_pk_bf16_f32 v244, v244, v245\n\t"
    "v_exp_f32 v188, v188\n\t"
    "v_mfma_f32_16x16x32_bf16 v[200:203], a[144:147], a[48:51], 0\n\t"
    "v_accvgpr_write_b32 a69, v244\n\t"
    "v_lshlrev_b32 v246, 16, v122\n\t"
    "v_and_b32 v247, 0xffff0000, v122\n\t"
    "v_exp_f32 v189, v189\n\t"
    "v_mfma_f32_16x16x32_bf16 v[200:203], a[148:151], a[52:55], v[200:203]\n\t"
    "v_mul_f32 v246, s65, v246\n\t"
    "v_mul_f32 v247, s65, v247\n\t"
    "v_cvt_pk_bf16_f32 v246, v246, v247\n\t"
    "v_cvt_pk_bf16_f32 v212, v188, v189\n\t"
    "v_mfma_f32_16x16x32_bf16 v[200:203], a[152:155], a[56:59], v[200:203]\n\t"
    "v_accvgpr_write_b32 a70, v246\n\t"
    "v_lshlrev_b32 v248, 16, v123\n\t"
    "v_and_b32 v249, 0xffff0000, v123\n\t"
    "v_exp_f32 v190, v190\n\t"
    "v_mfma_f32_16x16x32_bf16 v[200:203], a[156:159], a[60:63], v[200:203]\n\t"
    "v_mul_f32 v248, s65, v248\n\t"
    "v_mul_f32 v249, s65, v249\n\t"
    "v_cvt_pk_bf16_f32 v248, v248, v249\n\t"
    "v_accvgpr_write_b32 a71, v248\n\t"
    "v_exp_f32 v191, v191\n\t"
    "v_mfma_f32_16x16x32_bf16 v[68:71], a[160:163], v[204:207], 0\n\t"
    "s_waitcnt vmcnt(9)\n\t"
    "v_lshlrev_b32 v242, 16, v124\n\t"
    "v_and_b32 v243, 0xffff0000, v124\n\t"
    "v_mul_f32 v242, s65, v242\n\t"
    "v_cvt_pk_bf16_f32 v213, v190, v191\n\t"
    "v_mfma_f32_16x16x32_bf16 v[68:71], a[192:195], v[208:211], v[68:71]\n\t"
    "v_mul_f32 v243, s65, v243\n\t"
    "v_cvt_pk_bf16_f32 v242, v242, v243\n\t"
    "v_accvgpr_write_b32 a72, v242\n\t"
    "v_exp_f32 v192, v192\n\t"
    "v_mfma_f32_16x16x32_bf16 v[72:75], a[164:167], v[204:207], 0\n\t"
    "v_lshlrev_b32 v244, 16, v125\n\t"
    "v_and_b32 v245, 0xffff0000, v125\n\t"
    "v_mul_f32 v244, s65, v244\n\t"
    "v_exp_f32 v193, v193\n\t"
    "v_mfma_f32_16x16x32_bf16 v[72:75], a[196:199], v[208:211], v[72:75]\n\t"
    "v_mul_f32 v245, s65, v245\n\t"
    "v_cvt_pk_bf16_f32 v244, v244, v245\n\t"
    "v_accvgpr_write_b32 a73, v244\n\t"
    "v_cvt_pk_bf16_f32 v214, v192, v193\n\t"
    "v_mfma_f32_16x16x32_bf16 v[76:79], a[168:171], v[204:207], 0\n\t"
    "v_lshlrev_b32 v246, 16, v126\n\t"
    "v_and_b32 v247, 0xffff0000, v126\n\t"
    "v_mul_f32 v246, s65, v246\n\t"
    "v_mul_f32 v247, s65, v247\n\t"
    "v_exp_f32 v194, v194\n\t"
    "v_mfma_f32_16x16x32_bf16 v[76:79], a[200:203], v[208:211], v[76:79]\n\t"
    "v_cvt_pk_bf16_f32 v246, v246, v247\n\t"
    "v_accvgpr_write_b32 a74, v246\n\t"
    "v_lshlrev_b32 v248, 16, v127\n\t"
    "v_exp_f32 v195, v195\n\t"
    "v_mfma_f32_16x16x32_bf16 v[80:83], a[172:175], v[204:207], 0\n\t"
    "v_and_b32 v249, 0xffff0000, v127\n\t"
    "v_mul_f32 v248, s65, v248\n\t"
    "v_mul_f32 v249, s65, v249\n\t"
    "v_cvt_pk_bf16_f32 v215, v194, v195\n\t"
    "v_mfma_f32_16x16x32_bf16 v[80:83], a[204:207], v[208:211], v[80:83]\n\t"
    "v_cvt_pk_bf16_f32 v248, v248, v249\n\t"
    "v_accvgpr_write_b32 a75, v248\n\t"
    "s_waitcnt vmcnt(8)\n\t"
    "v_lshlrev_b32 v242, 16, v128\n\t"
    "v_exp_f32 v196, v196\n\t"
    "v_mfma_f32_16x16x32_bf16 v[84:87], a[176:179], v[204:207], 0\n\t"
    "v_and_b32 v243, 0xffff0000, v128\n\t"
    "v_mul_f32 v242, s65, v242\n\t"
    "v_mul_f32 v243, s65, v243\n\t"
    "v_exp_f32 v197, v197\n\t"
    "v_mfma_f32_16x16x32_bf16 v[84:87], a[208:211], v[208:211], v[84:87]\n\t"
    "v_cvt_pk_bf16_f32 v242, v242, v243\n\t"
    "v_accvgpr_write_b32 a76, v242\n\t"
    "v_lshlrev_b32 v244, 16, v129\n\t"
    "v_and_b32 v245, 0xffff0000, v129\n\t"
    "v_cvt_pk_bf16_f32 v216, v196, v197\n\t"
    "v_add_f32 v253, v253, v196\n\t"
    "v_mfma_f32_16x16x32_bf16 v[88:91], a[180:183], v[204:207], 0\n\t"
    "v_mul_f32 v244, s65, v244\n\t"
    "v_mul_f32 v245, s65, v245\n\t"
    "v_cvt_pk_bf16_f32 v244, v244, v245\n\t"
    "v_exp_f32 v198, v198\n\t"
    "v_mfma_f32_16x16x32_bf16 v[88:91], a[212:215], v[208:211], v[88:91]\n\t"
    "v_accvgpr_write_b32 a77, v244\n\t"
    "v_lshlrev_b32 v246, 16, v130\n\t"
    "v_and_b32 v247, 0xffff0000, v130\n\t"
    "v_exp_f32 v199, v199\n\t"
    "v_mfma_f32_16x16x32_bf16 v[92:95], a[184:187], v[204:207], 0\n\t"
    "v_mul_f32 v246, s65, v246\n\t"
    "v_mul_f32 v247, s65, v247\n\t"
    "v_cvt_pk_bf16_f32 v246, v246, v247\n\t"
    "v_cvt_pk_bf16_f32 v217, v198, v199\n\t"
    "v_add_f32 v253, v253, v197\n\t"
    "v_mfma_f32_16x16x32_bf16 v[92:95], a[216:219], v[208:211], v[92:95]\n\t"
    "v_accvgpr_write_b32 a78, v246\n\t"
    "v_lshlrev_b32 v248, 16, v131\n\t"
    "v_and_b32 v249, 0xffff0000, v131\n\t"
    "v_exp_f32 v200, v200\n\t"
    "v_mfma_f32_16x16x32_bf16 v[96:99], a[188:191], v[204:207], 0\n\t"
    "v_mul_f32 v248, s65, v248\n\t"
    "v_mul_f32 v249, s65, v249\n\t"
    "v_cvt_pk_bf16_f32 v248, v248, v249\n\t"
    "v_accvgpr_write_b32 a79, v248\n\t"
    "v_exp_f32 v201, v201\n\t"
    "v_mfma_f32_16x16x32_bf16 v[96:99], a[220:223], v[208:211], v[96:99]\n\t"
    "s_add_u32 s54, s59, 68608\n\t"
    "s_and_b32 s54, s54, 0x7ffff\n\t"
    "s_mov_b32 s57, 33792\n\t"
    "v_cvt_pk_bf16_f32 v218, v200, v201\n\t"
    "v_add_f32 v253, v253, v198\n\t"
    "v_mfma_f32_16x16x32_bf16 a[232:235], a[248:251], v[204:207], 0\n\t"
    "s_waitcnt lgkmcnt(0)\n\t"
    "s_barrier\n\t"
    "v_mfma_f32_16x16x32_bf16 v[172:175], a[96:99], a[64:67], 0\n\t"
    "ds_read_b128 a[96:99], v236 offset:16896\n\t"
    "v_add_f32 v253, v253, v199\n\t"
    "v_mfma_f32_16x16x32_bf16 v[172:175], a[100:103], a[68:71], v[172:175]\n\t"
    "v_exp_f32 v202, v202\n\t"
    "v_mfma_f32_16x16x32_bf16 v[172:175], a[104:107], a[72:75], v[172:175]\n\t"
    "s_add_u32 m0, s55, 50688\n\t"
    "s_add_u32 s61, s59, 52224\n\t"
    "s_and_b32 s61, s61, 0x7ffff\n\t"
    "buffer_load_dwordx4 v239, s[40:43], s61 offen offset:0 nt lds\n\t"
    "s_add_u32 m0, s55, 54912\n\t"
    "s_nop 0\n\t"
    "buffer_load_dwordx4 v226, s[40:43], s61 offen offset:0 nt lds\n\t"
    "s_add_u32 m0, s55, 59136\n\t"
    "s_nop 0\n\t"
    "buffer_load_dwordx4 v227, s[40:43], s61 offen offset:0 nt lds\n\t"
    "s_add_u32 m0, s55, 63360\n\t"
    "s_nop 0\n\t"
    "buffer_load_dwordx4 v3, s[40:43], s61 offen offset:0 nt lds\n\t"
    "ds_read_b128 a[100:103], v236 offset:16960\n\t"
    "ds_read_b128 a[104:107], v236 offset:17024\n\t"
    "v_mfma_f32_16x16x32_bf16 v[172:175], a[108:111], a[76:79], v[172:175]\n\t"
    "v_exp_f32 v203, v203\n\t"
    "v_mfma_f32_16x16x32_bf16 v[176:179], a[112:115], a[64:67], 0\n\t"
    "ds_read_b128 a[108:111], v236 offset:17088\n\t"
    "ds_read_b128 a[112:115], v236 offset:17408\n\t"
    "v_mfma_f32_16x16x32_bf16 v[176:179], a[116:119], a[68:71], v[176:179]\n\t"
    "v_cvt_pk_bf16_f32 v219, v202, v203\n\t"
    "v_add_f32 v253, v253, v200\n\t"
    "v_mfma_f32_16x16x32_bf16 v[176:179], a[120:123], a[72:75], v[176:179]\n\t"
    "ds_read_b128 a[116:119], v236 offset:17472\n\t"
    "ds_read_b128 a[120:123], v236 offset:17536\n\t"
    "v_mfma_f32_16x16x32_bf16 v[176:179], a[124:127], a[76:79], v[176:179]\n\t"
    "v_add_f32 v253, v253, v201\n\t"
    "v_add_f32 v253, v253, v202\n\t"
    "v_mfma_f32_16x16x32_bf16 v[180:183], a[128:131], a[64:67], 0\n\t"
    "ds_read_b128 a[124:127], v236 offset:17600\n\t"
    "v_add_f32 v253, v253, v203\n\t"
    "v_mfma_f32_16x16x32_bf16 v[180:183], a[132:135], a[68:71], v[180:183]\n\t"
    "ds_read_b128 a[128:131], v236 offset:25344\n\t"
    "v_mfma_f32_16x16x32_bf16 v[180:183], a[136:139], a[72:75], v[180:183]\n\t"
    "ds_read_b128 a[132:135], v236 offset:25408\n\t"
    "v_mfma_f32_16x16x32_bf16 v[180:183], a[140:143], a[76:79], v[180:183]\n\t"
    "v_exp_f32 v172, v172\n\t"
    "v_mfma_f32_16x16x32_bf16 v[184:187], a[144:147], a[64:67], 0\n\t"
    "ds_read_b128 a[136:139], v236 offset:25472\n\t"
    "ds_read_b128 a[140:143], v236 offset:25536\n\t"
    "v_mfma_f32_16x16x32_bf16 v[184:187], a[148:151], a[68:71], v[184:187]\n\t"
    "v_exp_f32 v173, v173\n\t"
    "v_mfma_f32_16x16x32_bf16 v[184:187], a[152:155], a[72:75], v[184:187]\n\t"
    "ds_read_b128 a[144:147], v236 offset:25856\n\t"
    "ds_read_b128 a[148:151], v236 offset:25920\n\t"
    "v_mfma_f32_16x16x32_bf16 v[184:187], a[156:159], a[76:79], v[184:187]\n\t"
    "s_add_u32 m0, s56, 50688\n\t"
    "s_add_u32 s61, s59, 52224\n\t"
    "s_and_b32 s61, s61, 0x7ffff\n\t"
    "buffer_load_dwordx4 v239, s[44:47], s61 offen offset:0 lds\n\t"
    "s_add_u32 m0, s56, 54912\n\t"
    "s_nop 0\n\t"
    "buffer_load_dwordx4 v226, s[44:47], s61 offen offset:0 lds\n\t"
    "s_add_u32 m0, s56, 59136\n\t"
    "s_nop 0\n\t"
    "buffer_load_dwordx4 v227, s[44:47], s61 offen offset:0 lds\n\t"
    "s_add_u32 m0, s56, 63360\n\t"
    "s_nop 0\n\t"
    "buffer_load_dwordx4 v3, s[44:47], s61 offen offset:0 lds\n\t"
    "v_cvt_pk_bf16_f32 v204, v172, v173\n\t"
    "v_add_f32 v254, v254, v172\n\t"
    "v_mfma_f32_16x16x32_bf16 v[100:103], a[160:163], v[212:215], 0\n\t"
    "ds_read_b128 a[152:155], v236 offset:25984\n\t"
    "ds_read_b128 a[156:159], v236 offset:26048\n\t"
    "v_mfma_f32_16x16x32_bf16 v[100:103], a[192:195], v[216:219], v[100:103]\n\t"
    "v_exp_f32 v174, v174\n\t"
    "v_mfma_f32_16x16x32_bf16 v[104:107], a[164:167], v[212:215], 0\n\t"
    "ds_read_b128 v[228:231], v238 offset:0\n\t"
    "ds_read_b128 v[232:235], v238 offset:1024\n\t"
    "v_mfma_f32_16x16x32_bf16 v[104:107], a[196:199], v[216:219], v[104:107]\n\t"
    "v_exp_f32 v175, v175\n\t"
    "v_mfma_f32_16x16x32_bf16 v[108:111], a[168:171], v[212:215], 0\n\t"
    "v_cvt_pk_bf16_f32 v205, v174, v175\n\t"
    "v_add_f32 v254, v254, v173\n\t"
    "v_mfma_f32_16x16x32_bf16 v[108:111], a[200:203], v[216:219], v[108:111]\n\t"
    "v_exp_f32 v176, v176\n\t"
    "v_mfma_f32_16x16x32_bf16 v[112:115], a[172:175], v[212:215], 0\n\t"
    "v_exp_f32 v177, v177\n\t"
    "v_mfma_f32_16x16x32_bf16 v[112:115], a[204:207], v[216:219], v[112:115]\n\t"
    "v_cvt_pk_bf16_f32 v206, v176, v177\n\t"
    "v_add_f32 v254, v254, v174\n\t"
    "v_mfma_f32_16x16x32_bf16 v[116:119], a[176:179], v[212:215], 0\n\t"
    "v_exp_f32 v178, v178\n\t"
    "v_mfma_f32_16x16x32_bf16 v[116:119], a[208:211], v[216:219], v[116:119]\n\t"
    "v_exp_f32 v179, v179\n\t"
    "v_mfma_f32_16x16x32_bf16 v[120:123], a[180:183], v[212:215], 0\n\t"
    "v_cvt_pk_bf16_f32 v207, v178, v179\n\t"
    "v_add_f32 v254, v254, v175\n\t"
    "v_mfma_f32_16x16x32_bf16 v[120:123], a[212:215], v[216:219], v[120:123]\n\t"
    "v_exp_f32 v180, v180\n\t"
    "v_mfma_f32_16x16x32_bf16 v[124:127], a[184:187], v[212:215], 0\n\t"
    "v_exp_f32 v181, v181\n\t"
    "v_mfma_f32_16x16x32_bf16 v[124:127], a[216:219], v[216:219], v[124:127]\n\t"
    "v_cvt_pk_bf16_f32 v208, v180, v181\n\t"
    "v_add_f32 v254, v254, v176\n\t"
    "v_mfma_f32_16x16x32_bf16 v[128:131], a[188:191], v[212:215], 0\n\t"
    "v_exp_f32 v182, v182\n\t"
    "v_mfma_f32_16x16x32_bf16 v[128:131], a[220:223], v[216:219], v[128:131]\n\t"
    "v_exp_f32 v183, v183\n\t"
    "v_mfma_f32_16x16x32_bf16 a[236:239], a[248:251], v[212:215], 0\n\t"
    "s_waitcnt lgkmcnt(0)\n\t"
    "v_cvt_pk_bf16_f32 v209, v182, v183\n\t"
    "v_add_f32 v254, v254, v177\n\t"
    "LOOP3_%=:\n\t"
    "s_cmp_lg_u32 s60, 0\n\t"
    "s_cbranch_scc1 NOPF3_%=\n\t"
    "s_mov_b32 s42, 0\n\t"
    "s_mov_b32 s46, 0\n\t"
    "s_mov_b32 s61, TAIL3_%=-CPF3_%=\n\t"
    "v_lshlrev_b32 v2, 7, v1\n\t"
    "buffer_load_dword v255, v2, s[96:99], s61 offen\n\t"
    "NOPF3_%=:\n\t"
    "v_mfma_f32_16x16x32_bf16 v[188:191], a[96:99], a[0:3], 0\n\t"
    "v_exp_f32 v184, v184\n\t"
    "v_mfma_f32_16x16x32_bf16 v[188:191], a[100:103], a[4:7], v[188:191]\n\t"
    "s_add_u32 m0, s55, 0\n\t"
    "v_exp_f32 v185, v185\n\t"
    "v_mfma_f32_16x16x32_bf16 v[188:191], a[104:107], a[8:11], v[188:191]\n\t"
    "buffer_load_dwordx4 v239, s[40:43], s54 offen offset:0 nt lds\n\t"
    "v_mfma_f32_16x16x32_bf16 v[188:191], a[108:111], a[12:15], v[188:191]\n\t"
    "v_cvt_pk_bf16_f32 v210, v184, v185\n\t"
    "v_add_f32 v254, v254, v178\n\t"
    "v_mfma_f32_16x16x32_bf16 v[192:195], a[112:115], a[0:3], 0\n\t"
    "v_exp_f32 v186, v186\n\t"
    "v_mfma_f32_16x16x32_bf16 v[192:195], a[116:119], a[4:7], v[192:195]\n\t"
    "v_exp_f32 v187, v187\n\t"
    "v_mfma_f32_16x16x32_bf16 v[192:195], a[120:123], a[8:11], v[192:195]\n\t"
    "v_cvt_pk_bf16_f32 v211, v186, v187\n\t"
    "v_add_f32 v254, v254, v179\n\t"
    "v_mfma_f32_16x16x32_bf16 v[192:195], a[124:127], a[12:15], v[192:195]\n\t"
    "v_add_f32 v254, v254, v180\n\t"
    "v_add_f32 v254, v254, v181\n\t"
    "v_mfma_f32_16x16x32_bf16 v[196:199], a[128:131], a[0:3], 0\n\t"
    "v_add_f32 v254, v254, v182\n\t"
    "v_add_f32 v254, v254, v183\n\t"
    "v_mfma_f32_16x16x32_bf16 v[196:199], a[132:135], a[4:7], v[196:199]\n\t"
    "v_add_f32 v254, v254, v184\n\t"
    "v_add_f32 v254, v254, v185\n\t"
    "v_mfma_f32_16x16x32_bf16 v[196:199], a[136:139], a[8:11], v[196:199]\n\t"
    "v_add_f32 v254, v254, v186\n\t"
    "v_add_f32 v254, v254, v187\n\t"
    "v_mfma_f32_16x16x32_bf16 v[196:199], a[140:143], a[12:15], v[196:199]\n\t"
    "v_exp_f32 v188, v188\n\t"
    "v_mfma_f32_16x16x32_bf16 v[200:203], a[144:147], a[0:3], 0\n\t"
    "v_exp_f32 v189, v189\n\t"
    "v_mfma_f32_16x16x32_bf16 v[200:203], a[148:151], a[4:7], v[200:203]\n\t"
    "v_cvt_pk_bf16_f32 v212, v188, v189\n\t"
    "v_add_f32 v250, v250, v188\n\t"
    "v_mfma_f32_16x16x32_bf16 v[200:203], a[152:155], a[8:11], v[200:203]\n\t"
    "v_exp_f32 v190, v190\n\t"
    "v_mfma_f32_16x16x32_bf16 v[200:203], a[156:159], a[12:15], v[200:203]\n\t"
    "v_exp_f32 v191, v191\n\t"
    "v_mfma_f32_16x16x32_bf16 v[164:167], a[184:187], v[228:231], v[164:167]\n\t"
    "v_cvt_pk_bf16_f32 v213, v190, v191\n\t"
    "v_add_f32 v250, v250, v189\n\t"
    "v_mfma_f32_16x16x32_bf16 v[164:167], a[216:219], v[232:235], v[164:167]\n\t"
    "s_add_u32 m0, s56, 0\n\t"
    "v_exp_f32 v192, v192\n\t"
    "v_mfma_f32_16x16x32_bf16 v[168:171], a[188:191], v[228:231], v[168:171]\n\t"
    "buffer_load_dwordx4 v239, s[44:47], s54 offen offset:0 lds\n\t"
    "v_mfma_f32_16x16x32_bf16 v[168:171], a[220:223], v[232:235], v[168:171]\n\t"
    "v_exp_f32 v193, v193\n\t"
    "v_mfma_f32_16x16x32_bf16 v[132:135], a[160:163], v[204:207], v[132:135]\n\t"
    "ds_read_b64_tr_b16 a[160:161], v237 offset:16896\n\t"
    "ds_read_b64_tr_b16 a[162:163], v237 offset:17408\n\t"
    "v_mfma_f32_16x16x32_bf16 v[132:135], a[192:195], v[208:211], v[132:135]\n\t"
    "v_cvt_pk_bf16_f32 v214, v192, v193\n\t"
    "v_add_f32 v250, v250, v190\n\t"
    "v_mfma_f32_16x16x32_bf16 v[136:139], a[164:167], v[204:207], v[136:139]\n\t"
    "ds_read_b64_tr_b16 a[192:193], v237 offset:25344\n\t"
    "ds_read_b64_tr_b16 a[194:195], v237 offset:25856\n\t"
    "v_mfma_f32_16x16x32_bf16 v[136:139], a[196:199], v[208:211], v[136:139]\n\t"
    "v_exp_f32 v194, v194\n\t"
    "v_mfma_f32_16x16x32_bf16 v[140:143], a[168:171], v[204:207], v[140:143]\n\t"
    "ds_read_b64_tr_b16 a[164:165], v237 offset:16904\n\t"
    "ds_read_b64_tr_b16 a[166:167], v237 offset:17416\n\t"
    "v_mfma_f32_16x16x32_bf16 v[140:143], a[200:203], v[208:211], v[140:143]\n\t"
    "v_exp_f32 v195, v195\n\t"
    "v_mfma_f32_16x16x32_bf16 v[144:147], a[172:175], v[204:207], v[144:147]\n\t"
    "ds_read_b64_tr_b16 a[196:197], v237 offset:25352\n\t"
    "ds_read_b64_tr_b16 a[198:199], v237 offset:25864\n\t"
    "v_mfma_f32_16x16x32_bf16 v[144:147], a[204:207], v[208:211], v[144:147]\n\t"
    "v_cvt_pk_bf16_f32 v215, v194, v195\n\t"
    "v_add_f32 v250, v250, v191\n\t"
    "v_mfma_f32_16x16x32_bf16 v[148:151], a[176:179], v[204:207], v[148:151]\n\t"
    "ds_read_b64_tr_b16 a[168:169], v237 offset:16960\n\t"
    "ds_read_b64_tr_b16 a[170:171], v237 offset:17472\n\t"
    "v_mfma_f32_16x16x32_bf16 v[148:151], a[208:211], v[208:211], v[148:151]\n\t"
    "v_exp_f32 v196, v196\n\t"
    "v_mfma_f32_16x16x32_bf16 v[152:155], a[180:183], v[204:207], v[152:155]\n\t"
    "ds_read_b64_tr_b16 a[200:201], v237 offset:25408\n\t"
    "ds_read_b64_tr_b16 a[202:203], v237 offset:25920\n\t"
    "v_mfma_f32_16x16x32_bf16 v[152:155], a[212:215], v[208:211], v[152:155]\n\t"
    "v_exp_f32 v197, v197\n\t"
    "v_mfma_f32_16x16x32_bf16 v[156:159], a[184:187], v[204:207], v[156:159]\n\t"
    "ds_read_b64_tr_b16 a[172:173], v237 offset:16968\n\t"
    "ds_read_b64_tr_b16 a[174:175], v237 offset:17480\n\t"
    "v_mfma_f32_16x16x32_bf16 v[156:159], a[216:219], v[208:211], v[156:159]\n\t"
    "s_add_u32 m0, s55, 4224\n\t"
    "v_cvt_pk_bf16_f32 v216, v196, v197\n\t"
    "v_add_f32 v250, v250, v192\n\t"
    "v_mfma_f32_16x16x32_bf16 v[160:163], a[188:191], v[204:207], v[160:163]\n\t"
    "buffer_load_dwordx4 v226, s[40:43], s54 offen offset:0 nt lds\n\t"
    "v_mfma_f32_16x16x32_bf16 v[160:163], a[220:223], v[208:211], v[160:163]\n\t"
    "v_exp_f32 v198, v198\n\t"
    "v_mfma_f32_16x16x32_bf16 v[172:175], a[96:99], a[16:19], 0\n\t"
    "ds_read_b64_tr_b16 a[204:205], v237 offset:25416\n\t"
    "ds_read_b64_tr_b16 a[206:207], v237 offset:25928\n\t"
    "v_mfma_f32_16x16x32_bf16 v[172:175], a[100:103], a[20:23], v[172:175]\n\t"
    "v_exp_f32 v199, v199\n\t"
    "v_mfma_f32_16x16x32_bf16 v[172:175], a[104:107], a[24:27], v[172:175]\n\t"
    "ds_read_b64_tr_b16 a[176:177], v237 offset:17024\n\t"
    "ds_read_b64_tr_b16 a[178:179], v237 offset:17536\n\t"
    "v_mfma_f32_16x16x32_bf16 v[172:175], a[108:111], a[28:31], v[172:175]\n\t"
    "v_cvt_pk_bf16_f32 v217, v198, v199\n\t"
    "v_add_f32 v250, v250, v193\n\t"
    "v_mfma_f32_16x16x32_bf16 v[176:179], a[112:115], a[16:19], 0\n\t"
    "ds_read_b64_tr_b16 a[208:209], v237 offset:25472\n\t"
    "ds_read_b64_tr_b16 a[210:211], v237 offset:25984\n\t"
    "v_mfma_f32_16x16x32_bf16 v[176:179], a[116:119], a[20:23], v[176:179]\n\t"
    "v_exp_f32 v200, v200\n\t"
    "v_mfma_f32_16x16x32_bf16 v[176:179], a[120:123], a[24:27], v[176:179]\n\t"
    "ds_read_b64_tr_b16 a[180:181], v237 offset:17032\n\t"
    "ds_read_b64_tr_b16 a[182:183], v237 offset:17544\n\t"
    "v_mfma_f32_16x16x32_bf16 v[176:179], a[124:127], a[28:31], v[176:179]\n\t"
    "v_exp_f32 v201, v201\n\t"
    "v_mfma_f32_16x16x32_bf16 v[180:183], a[128:131], a[16:19], 0\n\t"
    "ds_read_b64_tr_b16 a[212:213], v237 offset:25480\n\t"
    "ds_read_b64_tr_b16 a[214:215], v237 offset:25992\n\t"
    "v_mfma_f32_16x16x32_bf16 v[180:183], a[132:135], a[20:23], v[180:183]\n\t"
    "v_cvt_pk_bf16_f32 v218, v200, v201\n\t"
    "v_add_f32 v250, v250, v194\n\t"
    "v_mfma_f32_16x16x32_bf16 v[180:183], a[136:139], a[24:27], v[180:183]\n\t"
    "ds_read_b64_tr_b16 a[184:185], v237 offset:17088\n\t"
    "ds_read_b64_tr_b16 a[186:187], v237 offset:17600\n\t"
    "v_mfma_f32_16x16x32_bf16 v[180:183], a[140:143], a[28:31], v[180:183]\n\t"
    "v_exp_f32 v202, v202\n\t"
    "v_mfma_f32_16x16x32_bf16 v[184:187], a[144:147], a[16:19], 0\n\t"
    "ds_read_b64_tr_b16 a[216:217], v237 offset:25536\n\t"
    "ds_read_b64_tr_b16 a[218:219], v237 offset:26048\n\t"
    "v_mfma_f32_16x16x32_bf16 v[184:187], a[148:151], a[20:23], v[184:187]\n\t"
    "v_exp_f32 v203, v203\n\t"
    "v_mfma_f32_16x16x32_bf16 v[184:187], a[152:155], a[24:27], v[184:187]\n\t"
    "ds_read_b64_tr_b16 a[188:189], v237 offset:17096\n\t"
    "ds_read_b64_tr_b16 a[190:191], v237 offset:17608\n\t"
    "v_mfma_f32_16x16x32_bf16 v[184:187], a[156:159], a[28:31], v[184:187]\n\t"
    "v_cvt_pk_bf16_f32 v219, v202, v203\n\t"
    "v_add_f32 v250, v250, v195\n\t"
    "v_mfma_f32_16x16x32_bf16 v[220:223], a[144:147], a[80:83], 0\n\t"
    "ds_read_b64_tr_b16 a[220:221], v237 offset:25544\n\t"
    "ds_read_b64_tr_b16 a[222:223], v237 offset:26056\n\t"
    "v_mfma_f32_16x16x32_bf16 v[220:223], a[148:151], a[84:87], v[220:223]\n\t"
    "s_add_u32 m0, s56, 4224\n\t"
    "v_exp_f32 v172, v172\n\t"
    "v_mfma_f32_16x16x32_bf16 v[220:223], a[152:155], a[88:91], v[220:223]\n\t"
    "buffer_load_dwordx4 v226, s[44:47], s54 offen offset:0 lds\n\t"
    "v_mfma_f32_16x16x32_bf16 v[220:223], a[156:159], a[92:95], v[220:223]\n\t"
    "s_waitcnt lgkmcnt(4)\n\t"
    "v_exp_f32 v173, v173\n\t"
    "v_mfma_f32_16x16x32_bf16 v[4:7], a[160:163], v[212:215], v[4:7]\n\t"
    "v_cvt_pk_bf16_f32 v204, v172, v173\n\t"
    "v_add_f32 v250, v250, v196\n\t"
    "v_mfma_f32_16x16x32_bf16 v[4:7], a[192:195], v[216:219], v[4:7]\n\t"
    "v_exp_f32 v174, v174\n\t"
    "v_mfma_f32_16x16x32_bf16 v[8:11], a[164:167], v[212:215], v[8:11]\n\t"
    "v_exp_f32 v175, v175\n\t"
    "v_mfma_f32_16x16x32_bf16 v[8:11], a[196:199], v[216:219], v[8:11]\n\t"
    "v_cvt_pk_bf16_f32 v205, v174, v175\n\t"
    "v_add_f32 v250, v250, v197\n\t"
    "v_mfma_f32_16x16x32_bf16 v[12:15], a[168:171], v[212:215], v[12:15]\n\t"
    "v_exp_f32 v176, v176\n\t"
    "v_mfma_f32_16x16x32_bf16 v[12:15], a[200:203], v[216:219], v[12:15]\n\t"
    "v_exp_f32 v177, v177\n\t"
    "v_mfma_f32_16x16x32_bf16 v[16:19], a[172:175], v[212:215], v[16:19]\n\t"
    "v_cvt_pk_bf16_f32 v206, v176, v177\n\t"
    "v_add_f32 v250, v250, v198\n\t"
    "v_mfma_f32_16x16x32_bf16 v[16:19], a[204:207], v[216:219], v[16:19]\n\t"
    "v_exp_f32 v178, v178\n\t"
    "v_mfma_f32_16x16x32_bf16 v[20:23], a[176:179], v[212:215], v[20:23]\n\t"
    "v_exp_f32 v179, v179\n\t"
    "v_mfma_f32_16x16x32_bf16 v[20:23], a[208:211], v[216:219], v[20:23]\n\t"
    "s_add_u32 m0, s55, 8448\n\t"
    "v_cvt_pk_bf16_f32 v207, v178, v179\n\t"
    "v_add_f32 v250, v250, v199\n\t"
    "v_mfma_f32_16x16x32_bf16 v[24:27], a[180:183], v[212:215], v[24:27]\n\t"
    "buffer_load_dwordx4 v227, s[40:43], s54 offen offset:0 nt lds\n\t"
    "v_mfma_f32_16x16x32_bf16 v[24:27], a[212:215], v[216:219], v[24:27]\n\t"
    "v_exp_f32 v180, v180\n\t"
    "v_mfma_f32_16x16x32_bf16 v[28:31], a[184:187], v[212:215], v[28:31]\n\t"
    "v_exp_f32 v181, v181\n\t"
    "v_mfma_f32_16x16x32_bf16 v[28:31], a[216:219], v[216:219], v[28:31]\n\t"
    "s_waitcnt lgkmcnt(0)\n\t"
    "v_cvt_pk_bf16_f32 v208, v180, v181\n\t"
    "v_add_f32 v250, v250, v200\n\t"
    "v_mfma_f32_16x16x32_bf16 v[32:35], a[188:191], v[212:215], v[32:35]\n\t"
    "v_exp_f32 v182, v182\n\t"
    "v_mfma_f32_16x16x32_bf16 v[32:35], a[220:223], v[216:219], v[32:35]\n\t"
    "v_exp_f32 v183, v183\n\t"
    "v_mfma_f32_16x16x32_bf16 v[188:191], a[96:99], a[32:35], 0\n\t"
    "v_cvt_pk_bf16_f32 v209, v182, v183\n\t"
    "v_add_f32 v250, v250, v201\n\t"
    "v_mfma_f32_16x16x32_bf16 v[188:191], a[100:103], a[36:39], v[188:191]\n\t"
    "v_exp_f32 v184, v184\n\t"
    "v_mfma_f32_16x16x32_bf16 v[188:191], a[104:107], a[40:43], v[188:191]\n\t"
    "v_exp_f32 v220, v220\n\t"
    "v_mfma_f32_16x16x32_bf16 v[188:191], a[108:111], a[44:47], v[188:191]\n\t"
    "v_exp_f32 v185, v185\n\t"
    "v_mfma_f32_16x16x32_bf16 v[192:195], a[112:115], a[32:35], 0\n\t"
    "v_exp_f32 v221, v221\n\t"
    "v_mfma_f32_16x16x32_bf16 v[192:195], a[116:119], a[36:39], v[192:195]\n\t"
    "v_add_f32 v250, v250, v202\n\t"
    "v_add_f32 v250, v250, v203\n\t"
    "v_mfma_f32_16x16x32_bf16 v[192:195], a[120:123], a[40:43], v[192:195]\n\t"
    "v_cvt_pk_bf16_f32 v210, v184, v185\n\t"
    "v_cvt_pk_bf16_f32 v224, v220, v221\n\t"
    "v_mfma_f32_16x16x32_bf16 v[192:195], a[124:127], a[44:47], v[192:195]\n\t"
    "v_exp_f32 v186, v186\n\t"
    "v_mfma_f32_16x16x32_bf16 v[196:199], a[128:131], a[32:35], 0\n\t"
    "v_exp_f32 v222, v222\n\t"
    "v_mfma_f32_16x16x32_bf16 v[196:199], a[132:135], a[36:39], v[196:199]\n\t"
    "s_add_u32 m0, s56, 8448\n\t"
    "v_exp_f32 v187, v187\n\t"
    "v_mfma_f32_16x16x32_bf16 v[196:199], a[136:139], a[40:43], v[196:199]\n\t"
    "buffer_load_dwordx4 v227, s[44:47], s54 offen offset:0 lds\n\t"
    "v_mfma_f32_16x16x32_bf16 v[196:199], a[140:143], a[44:47], v[196:199]\n\t"
    "v_exp_f32 v223, v223\n\t"
    "v_mfma_f32_16x16x32_bf16 v[200:203], a[144:147], a[32:35], 0\n\t"
    "v_cvt_pk_bf16_f32 v225, v222, v223\n\t"
    "v_cvt_pk_bf16_f32 v211, v186, v187\n\t"
    "v_mfma_f32_16x16x32_bf16 v[200:203], a[148:151], a[36:39], v[200:203]\n\t"
    "ds_write_b64 v238, v[224:225] offset:3080\n\t"
    "v_mfma_f32_16x16x32_bf16 v[200:203], a[152:155], a[40:43], v[200:203]\n\t"
    "v_exp_f32 v188, v188\n\t"
    "v_mfma_f32_16x16x32_bf16 v[200:203], a[156:159], a[44:47], v[200:203]\n\t"
    "v_exp_f32 v189, v189\n\t"
    "v_mfma_f32_16x16x16_bf16 a[244:247], a[248:249], v[224:225], a[244:247]\n\t"
    "v_cvt_pk_bf16_f32 v212, v188, v189\n\t"
    "v_add_f32 v251, v251, v180\n\t"
    "v_mfma_f32_16x16x32_bf16 v[36:39], a[160:163], v[204:207], v[36:39]\n\t"
    "v_exp_f32 v190, v190\n\t"
    "v_mfma_f32_16x16x32_bf16 v[36:39], a[192:195], v[208:211], v[36:39]\n\t"
    "v_exp_f32 v191, v191\n\t"
    "v_mfma_f32_16x16x32_bf16 v[40:43], a[164:167], v[204:207], v[40:43]\n\t"
    "v_cvt_pk_bf16_f32 v213, v190, v191\n\t"
    "v_add_f32 v251, v251, v181\n\t"
    "v_mfma_f32_16x16x32_bf16 v[40:43], a[196:199], v[208:211], v[40:43]\n\t"
    "v_exp_f32 v192, v192\n\t"
    "v_mfma_f32_16x16x32_bf16 v[44:47], a[168:171], v[204:207], v[44:47]\n\t"
    "v_exp_f32 v193, v193\n\t"
    "v_mfma_f32_16x16x32_bf16 v[44:47], a[200:203], v[208:211], v[44:47]\n\t"
    "v_cvt_pk_bf16_f32 v214, v192, v193\n\t"
    "v_add_f32 v251, v251, v182\n\t"
    "v_mfma_f32_16x16x32_bf16 v[48:51], a[172:175], v[204:207], v[48:51]\n\t"
    "v_exp_f32 v194, v194\n\t"
    "v_mfma_f32_16x16x32_bf16 v[48:51], a[204:207], v[208:211], v[48:51]\n\t"
    "v_exp_f32 v195, v195\n\t"
    "v_mfma_f32_16x16x32_bf16 v[52:55], a[176:179], v[204:207], v[52:55]\n\t"
    "s_add_u32 m0, s55, 12672\n\t"
    "v_cvt_pk_bf16_f32 v215, v194, v195\n\t"
    "v_add_f32 v251, v251, v183\n\t"
    "v_mfma_f32_16x16x32_bf16 v[52:55], a[208:211], v[208:211], v[52:55]\n\t"
    "buffer_load_dwordx4 v3, s[40:43], s54 offen offset:0 nt lds\n\t"
    "v_mfma_f32_16x16x32_bf16 v[56:59], a[180:183], v[204:207], v[56:59]\n\t"
    "v_exp_f32 v196, v196\n\t"
    "v_mfma_f32_16x16x32_bf16 v[56:59], a[212:215], v[208:211], v[56:59]\n\t"
    "v_exp_f32 v197, v197\n\t"
    "v_mfma_f32_16x16x32_bf16 v[60:63], a[184:187], v[204:207], v[60:63]\n\t"
    "v_cvt_pk_bf16_f32 v216, v196, v197\n\t"
    "v_add_f32 v251, v251, v184\n\t"
    "v_mfma_f32_16x16x32_bf16 v[60:63], a[216:219], v[208:211], v[60:63]\n\t"
    "v_exp_f32 v198, v198\n\t"
    "v_mfma_f32_16x16x32_bf16 v[64:67], a[188:191], v[204:207], v[64:67]\n\t"
    "v_exp_f32 v199, v199\n\t"
    "v_mfma_f32_16x16x32_bf16 v[64:67], a[220:223], v[208:211], v[64:67]\n\t"
    "v_cvt_pk_bf16_f32 v217, v198, v199\n\t"
    "v_add_f32 v251, v251, v185\n\t"
    "v_mfma_f32_16x16x32_bf16 a[228:231], a[248:251], v[204:207], a[228:231]\n\t"
    "v_exp_f32 v200, v200\n\t"
    "v_mfma_f32_16x16x32_bf16 v[172:175], a[96:99], a[48:51], 0\n\t"
    "v_exp_f32 v201, v201\n\t"
    "v_mfma_f32_16x16x32_bf16 v[172:175], a[100:103], a[52:55], v[172:175]\n\t"
    "v_cvt_pk_bf16_f32 v218, v200, v201\n\t"
    "v_add_f32 v251, v251, v186\n\t"
    "v_mfma_f32_16x16x32_bf16 v[172:175], a[104:107], a[56:59], v[172:175]\n\t"
    "v_exp_f32 v202, v202\n\t"
    "v_mfma_f32_16x16x32_bf16 v[172:175], a[108:111], a[60:63], v[172:175]\n\t"
    "v_add_f32 v251, v251, v187\n\t"
    "v_add_u32 v236, s58, v236\n\t"
    "v_mfma_f32_16x16x32_bf16 v[176:179], a[112:115], a[48:51], 0\n\t"
    "v_exp_f32 v203, v203\n\t"
    "v_mfma_f32_16x16x32_bf16 v[176:179], a[116:119], a[52:55], v[176:179]\n\t"
    "v_cvt_pk_bf16_f32 v219, v202, v203\n\t"
    "v_add_u32 v237, s58, v237\n\t"
    "v_mfma_f32_16x16x32_bf16 v[176:179], a[120:123], a[56:59], v[176:179]\n\t"
    "v_add_f32 v252, v252, v196\n\t"
    "v_add_f32 v252, v252, v197\n\t"
    "v_mfma_f32_16x16x32_bf16 v[176:179], a[124:127], a[60:63], v[176:179]\n\t"
    "s_add_u32 m0, s56, 12672\n\t"
    "v_add_f32 v252, v252, v198\n\t"
    "v_add_f32 v252, v252, v199\n\t"
    "v_mfma_f32_16x16x32_bf16 v[180:183], a[128:131], a[48:51], 0\n\t"
    "buffer_load_dwordx4 v3, s[44:47], s54 offen offset:0 lds\n\t"
    "v_mfma_f32_16x16x32_bf16 v[180:183], a[132:135], a[52:55], v[180:183]\n\t"
    "v_add_f32 v252, v252, v200\n\t"
    "v_add_f32 v252, v252, v201\n\t"
    "v_mfma_f32_16x16x32_bf16 v[180:183], a[136:139], a[56:59], v[180:183]\n\t"
    "v_add_f32 v252, v252, v202\n\t"
    "v_add_f32 v252, v252, v203\n\t"
    "v_mfma_f32_16x16x32_bf16 v[180:183], a[140:143], a[60:63], v[180:183]\n\t"
    "v_exp_f32 v172, v172\n\t"
    "v_mfma_f32_16x16x32_bf16 v[184:187], a[144:147], a[48:51], 0\n\t"
    "v_exp_f32 v173, v173\n\t"
    "v_mfma_f32_16x16x32_bf16 v[184:187], a[148:151], a[52:55], v[184:187]\n\t"
    "v_cvt_pk_bf16_f32 v204, v172, v173\n\t"
    "v_mfma_f32_16x16x32_bf16 v[184:187], a[152:155], a[56:59], v[184:187]\n\t"
    "v_exp_f32 v174, v174\n\t"
    "v_mfma_f32_16x16x32_bf16 v[184:187], a[156:159], a[60:63], v[184:187]\n\t"
    "v_exp_f32 v175, v175\n\t"
    "v_mfma_f32_16x16x32_bf16 v[68:71], a[160:163], v[212:215], v[68:71]\n\t"
    "v_cvt_pk_bf16_f32 v205, v174, v175\n\t"
    "v_mfma_f32_16x16x32_bf16 v[68:71], a[192:195], v[216:219], v[68:71]\n\t"
    "v_exp_f32 v176, v176\n\t"
    "v_mfma_f32_16x16x32_bf16 v[72:75], a[164:167], v[212:215], v[72:75]\n\t"
    "v_exp_f32 v177, v177\n\t"
    "v_mfma_f32_16x16x32_bf16 v[72:75], a[196:199], v[216:219], v[72:75]\n\t"
    "v_cvt_pk_bf16_f32 v206, v176, v177\n\t"
    "v_mfma_f32_16x16x32_bf16 v[76:79], a[168:171], v[212:215], v[76:79]\n\t"
    "v_exp_f32 v178, v178\n\t"
    "v_mfma_f32_16x16x32_bf16 v[76:79], a[200:203], v[216:219], v[76:79]\n\t"
    "v_exp_f32 v179, v179\n\t"
    "v_mfma_f32_16x16x32_bf16 v[80:83], a[172:175], v[212:215], v[80:83]\n\t"
    "v_cvt_pk_bf16_f32 v207, v178, v179\n\t"
    "v_mfma_f32_16x16x32_bf16 v[80:83], a[204:207], v[216:219], v[80:83]\n\t"
    "v_exp_f32 v180, v180\n\t"
    "v_mfma_f32_16x16x32_bf16 v[84:87], a[176:179], v[212:215], v[84:87]\n\t"
    "v_exp_f32 v181, v181\n\t"
    "v_mfma_f32_16x16x32_bf16 v[84:87], a[208:211], v[216:219], v[84:87]\n\t"
    "v_cvt_pk_bf16_f32 v208, v180, v181\n\t"
    "v_add_f32 v253, v253, v180\n\t"
    "v_mfma_f32_16x16x32_bf16 v[88:91], a[180:183], v[212:215], v[88:91]\n\t"
    "v_exp_f32 v182, v182\n\t"
    "v_mfma_f32_16x16x32_bf16 v[88:91], a[212:215], v[216:219], v[88:91]\n\t"
    "v_exp_f32 v183, v183\n\t"
    "v_mfma_f32_16x16x32_bf16 v[92:95], a[184:187], v[212:215], v[92:95]\n\t"
    "v_cvt_pk_bf16_f32 v209, v182, v183\n\t"
    "v_add_f32 v253, v253, v181\n\t"
    "v_mfma_f32_16x16x32_bf16 v[92:95], a[216:219], v[216:219], v[92:95]\n\t"
    "v_exp_f32 v184, v184\n\t"
    "v_mfma_f32_16x16x32_bf16 v[96:99], a[188:191], v[212:215], v[96:99]\n\t"
    "v_exp_f32 v185, v185\n\t"
    "v_mfma_f32_16x16x32_bf16 v[96:99], a[220:223], v[216:219], v[96:99]\n\t"
    "s_add_u32 s54, s54, 16384\n\t"
    "v_cvt_pk_bf16_f32 v210, v184, v185\n\t"
    "v_add_f32 v253, v253, v182\n\t"
    "v_mfma_f32_16x16x32_bf16 a[232:235], a[248:251], v[212:215], a[232:235]\n\t"
    "s_waitcnt vmcnt(16) lgkmcnt(0)\n\t"
    "s_barrier\n\t"
    "v_mfma_f32_16x16x32_bf16 v[188:191], a[96:99], a[64:67], 0\n\t"
    "ds_read_b128 a[96:99], v236 offset:0\n\t"
    "v_add_f32 v253, v253, v183\n\t"
    "v_mfma_f32_16x16x32_bf16 v[188:191], a[100:103], a[68:71], v[188:191]\n\t"
    "s_sub_u32 s58, 0, s58\n\t"
    "v_exp_f32 v186, v186\n\t"
    "v_mfma_f32_16x16x32_bf16 v[188:191], a[104:107], a[72:75], v[188:191]\n\t"
    "ds_read_b128 a[100:103], v236 offset:64\n\t"
    "ds_read_b128 a[104:107], v236 offset:128\n\t"
    "v_mfma_f32_16x16x32_bf16 v[188:191], a[108:111], a[76:79], v[188:191]\n\t"
    "s_and_b32 s54, s54, 0x7ffff\n\t"
    "v_exp_f32 v187, v187\n\t"
    "v_mfma_f32_16x16x32_bf16 v[192:195], a[112:115], a[64:67], 0\n\t"
    "ds_read_b128 a[108:111], v236 offset:192\n\t"
    "ds_read_b128 a[112:115], v236 offset:512\n\t"
    "v_mfma_f32_16x16x32_bf16 v[192:195], a[116:119], a[68:71], v[192:195]\n\t"
    "v_cvt_pk_bf16_f32 v211, v186, v187\n\t"
    "v_add_f32 v253, v253, v184\n\t"
    "v_mfma_f32_16x16x32_bf16 v[192:195], a[120:123], a[72:75], v[192:195]\n\t"
    "ds_read_b128 a[116:119], v236 offset:576\n\t"
    "ds_read_b128 a[120:123], v236 offset:640\n\t"
    "v_mfma_f32_16x16x32_bf16 v[192:195], a[124:127], a[76:79], v[192:195]\n\t"
    "v_add_f32 v253, v253, v185\n\t"
    "v_add_f32 v253, v253, v186\n\t"
    "v_mfma_f32_16x16x32_bf16 v[196:199], a[128:131], a[64:67], 0\n\t"
    "ds_read_b128 a[124:127], v236 offset:704\n\t"
    "v_add_f32 v253, v253, v187\n\t"
    "v_mfma_f32_16x16x32_bf16 v[196:199], a[132:135], a[68:71], v[196:199]\n\t"
    "ds_read_b128 a[128:131], v236 offset:8448\n\t"
    "v_mfma_f32_16x16x32_bf16 v[196:199], a[136:139], a[72:75], v[196:199]\n\t"
    "ds_read_b128 a[132:135], v236 offset:8512\n\t"
    "v_mfma_f32_16x16x32_bf16 v[196:199], a[140:143], a[76:79], v[196:199]\n\t"
    "v_exp_f32 v188, v188\n\t"
    "v_mfma_f32_16x16x32_bf16 v[200:203], a[144:147], a[64:67], 0\n\t"
    "ds_read_b128 a[136:139], v236 offset:8576\n\t"
    "ds_read_b128 a[140:143], v236 offset:8640\n\t"
    "v_mfma_f32_16x16x32_bf16 v[200:203], a[148:151], a[68:71], v[200:203]\n\t"
    "v_exp_f32 v189, v189\n\t"
    "v_mfma_f32_16x16x32_bf16 v[200:203], a[152:155], a[72:75], v[200:203]\n\t"
    "ds_read_b128 a[144:147], v236 offset:8960\n\t"
    "ds_read_b128 a[148:151], v236 offset:9024\n\t"
    "v_mfma_f32_16x16x32_bf16 v[200:203], a[156:159], a[76:79], v[200:203]\n\t"
    "v_cvt_pk_bf16_f32 v212, v188, v189\n\t"
    "v_add_f32 v254, v254, v188\n\t"
    "v_mfma_f32_16x16x32_bf16 v[100:103], a[160:163], v[204:207], v[100:103]\n\t"
    "ds_read_b128 a[152:155], v236 offset:9088\n\t"
    "ds_read_b128 a[156:159], v236 offset:9152\n\t"
    "v_mfma_f32_16x16x32_bf16 v[100:103], a[192:195], v[208:211], v[100:103]\n\t"
    "v_exp_f32 v190, v190\n\t"
    "v_mfma_f32_16x16x32_bf16 v[104:107], a[164:167], v[204:207], v[104:107]\n\t"
    "ds_read_b128 v[228:231], v238 offset:2048\n\t"
    "ds_read_b128 v[232:235], v238 offset:3072\n\t"
    "v_mfma_f32_16x16x32_bf16 v[104:107], a[196:199], v[208:211], v[104:107]\n\t"
    "v_exp_f32 v191, v191\n\t"
    "v_mfma_f32_16x16x32_bf16 v[108:111], a[168:171], v[204:207], v[108:111]\n\t"
    "v_cvt_pk_bf16_f32 v213, v190, v191\n\t"
    "v_add_f32 v254, v254, v189\n\t"
    "v_mfma_f32_16x16x32_bf16 v[108:111], a[200:203], v[208:211], v[108:111]\n\t"
    "v_exp_f32 v192, v192\n\t"
    "v_mfma_f32_16x16x32_bf16 v[112:115], a[172:175], v[204:207], v[112:115]\n\t"
    "v_exp_f32 v193, v193\n\t"
    "v_mfma_f32_16x16x32_bf16 v[112:115], a[204:207], v[208:211], v[112:115]\n\t"
    "v_cvt_pk_bf16_f32 v214, v192, v193\n\t"
    "v_add_f32 v254, v254, v190\n\t"
    "v_mfma_f32_16x16x32_bf16 v[116:119], a[176:179], v[204:207], v[116:119]\n\t"
    "v_exp_f32 v194, v194\n\t"
    "v_mfma_f32_16x16x32_bf16 v[116:119], a[208:211], v[208:211], v[116:119]\n\t"
    "v_exp_f32 v195, v195\n\t"
    "v_mfma_f32_16x16x32_bf16 v[120:123], a[180:183], v[204:207], v[120:123]\n\t"
    "v_cvt_pk_bf16_f32 v215, v194, v195\n\t"
    "v_add_f32 v254, v254, v191\n\t"
    "v_mfma_f32_16x16x32_bf16 v[120:123], a[212:215], v[208:211], v[120:123]\n\t"
    "v_exp_f32 v196, v196\n\t"
    "v_mfma_f32_16x16x32_bf16 v[124:127], a[184:187], v[204:207], v[124:127]\n\t"
    "v_exp_f32 v197, v197\n\t"
    "v_mfma_f32_16x16x32_bf16 v[124:127], a[216:219], v[208:211], v[124:127]\n\t"
    "v_cvt_pk_bf16_f32 v216, v196, v197\n\t"
    "v_add_f32 v254, v254, v192\n\t"
    "v_mfma_f32_16x16x32_bf16 v[128:131], a[188:191], v[204:207], v[128:131]\n\t"
    "v_exp_f32 v198, v198\n\t"
    "v_mfma_f32_16x16x32_bf16 v[128:131], a[220:223], v[208:211], v[128:131]\n\t"
    "v_exp_f32 v199, v199\n\t"
    "v_mfma_f32_16x16x32_bf16 a[236:239], a[248:251], v[204:207], a[236:239]\n\t"
    "s_waitcnt lgkmcnt(0)\n\t"
    "v_cvt_pk_bf16_f32 v217, v198, v199\n\t"
    "v_add_f32 v254, v254, v193\n\t"
    "v_mfma_f32_16x16x32_bf16 v[172:175], a[96:99], a[0:3], 0\n\t"
    "v_exp_f32 v200, v200\n\t"
    "v_mfma_f32_16x16x32_bf16 v[172:175], a[100:103], a[4:7], v[172:175]\n\t"
    "s_add_u32 m0, s55, 16896\n\t"
    "v_exp_f32 v201, v201\n\t"
    "v_mfma_f32_16x16x32_bf16 v[172:175], a[104:107], a[8:11], v[172:175]\n\t"
    "buffer_load_dwordx4 v239, s[40:43], s54 offen offset:0 nt lds\n\t"
    "v_mfma_f32_16x16x32_bf16 v[172:175], a[108:111], a[12:15], v[172:175]\n\t"
    "v_cvt_pk_bf16_f32 v218, v200, v201\n\t"
    "v_add_f32 v254, v254, v194\n\t"
    "v_mfma_f32_16x16x32_bf16 v[176:179], a[112:115], a[0:3], 0\n\t"
    "v_exp_f32 v202, v202\n\t"
    "v_mfma_f32_16x16x32_bf16 v[176:179], a[116:119], a[4:7], v[176:179]\n\t"
    "v_exp_f32 v203, v203\n\t"
    "v_mfma_f32_16x16x32_bf16 v[176:179], a[120:123], a[8:11], v[176:179]\n\t"
    "v_cvt_pk_bf16_f32 v219, v202, v203\n\t"
    "v_add_f32 v254, v254, v195\n\t"
    "v_mfma_f32_16x16x32_bf16 v[176:179], a[124:127], a[12:15], v[176:179]\n\t"
    "v_add_f32 v254, v254, v196\n\t"
    "v_add_f32 v254, v254, v197\n\t"
    "v_mfma_f32_16x16x32_bf16 v[180:183], a[128:131], a[0:3], 0\n\t"
    "v_add_f32 v254, v254, v198\n\t"
    "v_add_f32 v254, v254, v199\n\t"
    "v_mfma_f32_16x16x32_bf16 v[180:183], a[132:135], a[4:7], v[180:183]\n\t"
    "v_add_f32 v254, v254, v200\n\t"
    "v_add_f32 v254, v254, v201\n\t"
    "v_mfma_f32_16x16x32_bf16 v[180:183], a[136:139], a[8:11], v[180:183]\n\t"
    "v_add_f32 v254, v254, v202\n\t"
    "v_add_f32 v254, v254, v203\n\t"
    "v_mfma_f32_16x16x32_bf16 v[180:183], a[140:143], a[12:15], v[180:183]\n\t"
    "v_exp_f32 v172, v172\n\t"
    "v_mfma_f32_16x16x32_bf16 v[184:187], a[144:147], a[0:3], 0\n\t"
    "v_exp_f32 v173, v173\n\t"
    "v_mfma_f32_16x16x32_bf16 v[184:187], a[148:151], a[4:7], v[184:187]\n\t"
    "v_cvt_pk_bf16_f32 v204, v172, v173\n\t"
    "v_add_f32 v250, v250, v172\n\t"
    "v_mfma_f32_16x16x32_bf16 v[184:187], a[152:155], a[8:11], v[184:187]\n\t"
    "v_exp_f32 v174, v174\n\t"
    "v_mfma_f32_16x16x32_bf16 v[184:187], a[156:159], a[12:15], v[184:187]\n\t"
    "v_exp_f32 v175, v175\n\t"
    "v_mfma_f32_16x16x32_bf16 v[164:167], a[184:187], v[228:231], v[164:167]\n\t"
    "v_cvt_pk_bf16_f32 v205, v174, v175\n\t"
    "v_add_f32 v250, v250, v173\n\t"
    "v_mfma_f32_16x16x32_bf16 v[164:167], a[216:219], v[232:235], v[164:167]\n\t"
    "s_add_u32 m0, s56, 16896\n\t"
    "v_exp_f32 v176, v176\n\t"
    "v_mfma_f32_16x16x32_bf16 v[168:171], a[188:191], v[228:231], v[168:171]\n\t"
    "buffer_load_dwordx4 v239, s[44:47], s54 offen offset:0 lds\n\t"
    "v_mfma_f32_16x16x32_bf16 v[168:171], a[220:223], v[232:235], v[168:171]\n\t"
    "v_exp_f32 v177, v177\n\t"
    "v_mfma_f32_16x16x32_bf16 v[132:135], a[160:163], v[212:215], v[132:135]\n\t"
    "ds_read_b64_tr_b16 a[160:161], v237 offset:0\n\t"
    "ds_read_b64_tr_b16 a[162:163], v237 offset:512\n\t"
    "v_mfma_f32_16x16x32_bf16 v[132:135], a[192:195], v[216:219], v[132:135]\n\t"
    "v_cvt_pk_bf16_f32 v206, v176, v177\n\t"
    "v_add_f32 v250, v250, v174\n\t"
    "v_mfma_f32_16x16x32_bf16 v[136:139], a[164:167], v[212:215], v[136:139]\n\t"
    "ds_read_b64_tr_b16 a[192:193], v237 offset:8448\n\t"
    "ds_read_b64_tr_b16 a[194:195], v237 offset:8960\n\t"
    "v_mfma_f32_16x16x32_bf16 v[136:139], a[196:199], v[216:219], v[136:139]\n\t"
    "v_exp_f32 v178, v178\n\t"
    "v_mfma_f32_16x16x32_bf16 v[140:143], a[168:171], v[212:215], v[140:143]\n\t"
    "ds_read_b64_tr_b16 a[164:165], v237 offset:8\n\t"
    "ds_read_b64_tr_b16 a[166:167], v237 offset:520\n\t"
    "v_mfma_f32_16x16x32_bf16 v[140:143], a[200:203], v[216:219], v[140:143]\n\t"
    "v_exp_f32 v179, v179\n\t"
    "v_mfma_f32_16x16x32_bf16 v[144:147], a[172:175], v[212:215], v[144:147]\n\t"
    "ds_read_b64_tr_b16 a[196:197], v237 offset:8456\n\t"
    "ds_read_b64_tr_b16 a[198:199], v237 offset:8968\n\t"
    "v_mfma_f32_16x16x32_bf16 v[144:147], a[204:207], v[216:219], v[144:147]\n\t"
    "v_cvt_pk_bf16_f32 v207, v178, v179\n\t"
    "v_add_f32 v250, v250, v175\n\t"
    "v_mfma_f32_16x16x32_bf16 v[148:151], a[176:179], v[212:215], v[148:151]\n\t"
    "ds_read_b64_tr_b16 a[168:169], v237 offset:64\n\t"
    "ds_read_b64_tr_b16 a[170:171], v237 offset:576\n\t"
    "v_mfma_f32_16x16x32_bf16 v[148:151], a[208:211], v[216:219], v[148:151]\n\t"
    "v_exp_f32 v180, v180\n\t"
    "v_mfma_f32_16x16x32_bf16 v[152:155], a[180:183], v[212:215], v[152:155]\n\t"
    "ds_read_b64_tr_b16 a[200:201], v237 offset:8512\n\t"
    "ds_read_b64_tr_b16 a[202:203], v237 offset:9024\n\t"
    "v_mfma_f32_16x16x32_bf16 v[152:155], a[212:215], v[216:219], v[152:155]\n\t"
    "v_exp_f32 v181, v181\n\t"
    "v_mfma_f32_16x16x32_bf16 v[156:159], a[184:187], v[212:215], v[156:159]\n\t"
    "ds_read_b64_tr_b16 a[172:173], v237 offset:72\n\t"
    "ds_read_b64_tr_b16 a[174:175], v237 offset:584\n\t"
    "v_mfma_f32_16x16x32_bf16 v[156:159], a[216:219], v[216:219], v[156:159]\n\t"
    "s_add_u32 m0, s55, 21120\n\t"
    "v_cvt_pk_bf16_f32 v208, v180, v181\n\t"
    "v_add_f32 v250, v250, v176\n\t"
    "v_mfma_f32_16x16x32_bf16 v[160:163], a[188:191], v[212:215], v[160:163]\n\t"
    "buffer_load_dwordx4 v226, s[40:43], s54 offen offset:0 nt lds\n\t"
    "v_mfma_f32_16x16x32_bf16 v[160:163], a[220:223], v[216:219], v[160:163]\n\t"
    "v_exp_f32 v182, v182\n\t"
    "v_mfma_f32_16x16x32_bf16 v[188:191], a[96:99], a[16:19], 0\n\t"
    "ds_read_b64_tr_b16 a[204:205], v237 offset:8520\n\t"
    "ds_read_b64_tr_b16 a[206:207], v237 offset:9032\n\t"
    "v_mfma_f32_16x16x32_bf16 v[188:191], a[100:103], a[20:23], v[188:191]\n\t"
    "v_exp_f32 v183, v183\n\t"
    "v_mfma_f32_16x16x32_bf16 v[188:191], a[104:107], a[24:27], v[188:191]\n\t"
    "ds_read_b64_tr_b16 a[176:177], v237 offset:128\n\t"
    "ds_read_b64_tr_b16 a[178:179], v237 offset:640\n\t"
    "v_mfma_f32_16x16x32_bf16 v[188:191], a[108:111], a[28:31], v[188:191]\n\t"
    "v_cvt_pk_bf16_f32 v209, v182, v183\n\t"
    "v_add_f32 v250, v250, v177\n\t"
    "v_mfma_f32_16x16x32_bf16 v[192:195], a[112:115], a[16:19], 0\n\t"
    "ds_read_b64_tr_b16 a[208:209], v237 offset:8576\n\t"
    "ds_read_b64_tr_b16 a[210:211], v237 offset:9088\n\t"
    "v_mfma_f32_16x16x32_bf16 v[192:195], a[116:119], a[20:23], v[192:195]\n\t"
    "v_exp_f32 v184, v184\n\t"
    "v_mfma_f32_16x16x32_bf16 v[192:195], a[120:123], a[24:27], v[192:195]\n\t"
    "ds_read_b64_tr_b16 a[180:181], v237 offset:136\n\t"
    "ds_read_b64_tr_b16 a[182:183], v237 offset:648\n\t"
    "v_mfma_f32_16x16x32_bf16 v[192:195], a[124:127], a[28:31], v[192:195]\n\t"
    "v_exp_f32 v185, v185\n\t"
    "v_mfma_f32_16x16x32_bf16 v[196:199], a[128:131], a[16:19], 0\n\t"
    "ds_read_b64_tr_b16 a[212:213], v237 offset:8584\n\t"
    "ds_read_b64_tr_b16 a[214:215], v237 offset:9096\n\t"
    "v_mfma_f32_16x16x32_bf16 v[196:199], a[132:135], a[20:23], v[196:199]\n\t"
    "v_cvt_pk_bf16_f32 v210, v184, v185\n\t"
    "v_add_f32 v250, v250, v178\n\t"
    "v_mfma_f32_16x16x32_bf16 v[196:199], a[136:139], a[24:27], v[196:199]\n\t"
    "ds_read_b64_tr_b16 a[184:185], v237 offset:192\n\t"
    "ds_read_b64_tr_b16 a[186:187], v237 offset:704\n\t"
    "v_mfma_f32_16x16x32_bf16 v[196:199], a[140:143], a[28:31], v[196:199]\n\t"
    "v_exp_f32 v186, v186\n\t"
    "v_mfma_f32_16x16x32_bf16 v[200:203], a[144:147], a[16:19], 0\n\t"
    "ds_read_b64_tr_b16 a[216:217], v237 offset:8640\n\t"
    "ds_read_b64_tr_b16 a[218:219], v237 offset:9152\n\t"
    "v_mfma_f32_16x16x32_bf16 v[200:203], a[148:151], a[20:23], v[200:203]\n\t"
    "v_exp_f32 v187, v187\n\t"
    "v_mfma_f32_16x16x32_bf16 v[200:203], a[152:155], a[24:27], v[200:203]\n\t"
    "ds_read_b64_tr_b16 a[188:189], v237 offset:200\n\t"
    "ds_read_b64_tr_b16 a[190:191], v237 offset:712\n\t"
    "v_mfma_f32_16x16x32_bf16 v[200:203], a[156:159], a[28:31], v[200:203]\n\t"
    "v_cvt_pk_bf16_f32 v211, v186, v187\n\t"
    "v_add_f32 v250, v250, v179\n\t"
    "v_mfma_f32_16x16x32_bf16 v[220:223], a[144:147], a[80:83], 0\n\t"
    "ds_read_b64_tr_b16 a[220:221], v237 offset:8648\n\t"
    "ds_read_b64_tr_b16 a[222:223], v237 offset:9160\n\t"
    "v_mfma_f32_16x16x32_bf16 v[220:223], a[148:151], a[84:87], v[220:223]\n\t"
    "s_add_u32 m0, s56, 21120\n\t"
    "v_exp_f32 v188, v188\n\t"
    "v_mfma_f32_16x16x32_bf16 v[220:223], a[152:155], a[88:91], v[220:223]\n\t"
    "buffer_load_dwordx4 v226, s[44:47], s54 offen offset:0 lds\n\t"
    "v_mfma_f32_16x16x32_bf16 v[220:223], a[156:159], a[92:95], v[220:223]\n\t"
    "s_waitcnt lgkmcnt(4)\n\t"
    "v_exp_f32 v189, v189\n\t"
    "v_mfma_f32_16x16x32_bf16 v[4:7], a[160:163], v[204:207], v[4:7]\n\t"
    "v_cvt_pk_bf16_f32 v212, v188, v189\n\t"
    "v_add_f32 v250, v250, v180\n\t"
    "v_mfma_f32_16x16x32_bf16 v[4:7], a[192:195], v[208:211], v[4:7]\n\t"
    "v_exp_f32 v190, v190\n\t"
    "v_mfma_f32_16x16x32_bf16 v[8:11], a[164:167], v[204:207], v[8:11]\n\t"
    "v_exp_f32 v191, v191\n\t"
    "v_mfma_f32_16x16x32_bf16 v[8:11], a[196:199], v[208:211], v[8:11]\n\t"
    "v_cvt_pk_bf16_f32 v213, v190, v191\n\t"
    "v_add_f32 v250, v250, v181\n\t"
    "v_mfma_f32_16x16x32_bf16 v[12:15], a[168:171], v[204:207], v[12:15]\n\t"
    "v_exp_f32 v192, v192\n\t"
    "v_mfma_f32_16x16x32_bf16 v[12:15], a[200:203], v[208:211], v[12:15]\n\t"
    "v_exp_f32 v193, v193\n\t"
    "v_mfma_f32_16x16x32_bf16 v[16:19], a[172:175], v[204:207], v[16:19]\n\t"
    "v_cvt_pk_bf16_f32 v214, v192, v193\n\t"
    "v_add_f32 v250, v250, v182\n\t"
    "v_mfma_f32_16x16x32_bf16 v[16:19], a[204:207], v[208:211], v[16:19]\n\t"
    "v_exp_f32 v194, v194\n\t"
    "v_mfma_f32_16x16x32_bf16 v[20:23], a[176:179], v[204:207], v[20:23]\n\t"
    "v_exp_f32 v195, v195\n\t"
    "v_mfma_f32_16x16x32_bf16 v[20:23], a[208:211], v[208:211], v[20:23]\n\t"
    "s_add_u32 m0, s55, 25344\n\t"
    "v_cvt_pk_bf16_f32 v215, v194, v195\n\t"
    "v_add_f32 v250, v250, v183\n\t"
    "v_mfma_f32_16x16x32_bf16 v[24:27], a[180:183], v[204:207], v[24:27]\n\t"
    "buffer_load_dwordx4 v227, s[40:43], s54 offen offset:0 nt lds\n\t"
    "v_mfma_f32_16x16x32_bf16 v[24:27], a[212:215], v[208:211], v[24:27]\n\t"
    "v_exp_f32 v196, v196\n\t"
    "v_mfma_f32_16x16x32_bf16 v[28:31], a[184:187], v[204:207], v[28:31]\n\t"
    "v_exp_f32 v197, v197\n\t"
    "v_mfma_f32_16x16x32_bf16 v[28:31], a[216:219], v[208:211], v[28:31]\n\t"
    "s_waitcnt lgkmcnt(0)\n\t"
    "v_cvt_pk_bf16_f32 v216, v196, v197\n\t"
    "v_add_f32 v250, v250, v184\n\t"
    "v_mfma_f32_16x16x32_bf16 v[32:35], a[188:191], v[204:207], v[32:35]\n\t"
    "v_exp_f32 v198, v198\n\t"
    "v_mfma_f32_16x16x32_bf16 v[32:35], a[220:223], v[208:211], v[32:35]\n\t"
    "v_exp_f32 v199, v199\n\t"
    "v_mfma_f32_16x16x32_bf16 v[172:175], a[96:99], a[32:35], 0\n\t"
    "v_cvt_pk_bf16_f32 v217, v198, v199\n\t"
    "v_add_f32 v250, v250, v185\n\t"
    "v_mfma_f32_16x16x32_bf16 v[172:175], a[100:103], a[36:39], v[172:175]\n\t"
    "v_exp_f32 v200, v200\n\t"
    "v_mfma_f32_16x16x32_bf16 v[172:175], a[104:107], a[40:43], v[172:175]\n\t"
    "v_exp_f32 v220, v220\n\t"
    "v_mfma_f32_16x16x32_bf16 v[172:175], a[108:111], a[44:47], v[172:175]\n\t"
    "v_exp_f32 v201, v201\n\t"
    "v_mfma_f32_16x16x32_bf16 v[176:179], a[112:115], a[32:35], 0\n\t"
    "v_exp_f32 v221, v221\n\t"
    "v_mfma_f32_16x16x32_bf16 v[176:179], a[116:119], a[36:39], v[176:179]\n\t"
    "v_add_f32 v250, v250, v186\n\t"
    "v_add_f32 v250, v250, v187\n\t"
    "v_mfma_f32_16x16x32_bf16 v[176:179], a[120:123], a[40:43], v[176:179]\n\t"
    "v_cvt_pk_bf16_f32 v218, v200, v201\n\t"
    "v_cvt_pk_bf16_f32 v224, v220, v221\n\t"
    "v_mfma_f32_16x16x32_bf16 v[176:179], a[124:127], a[44:47], v[176:179]\n\t"
    "v_exp_f32 v202, v202\n\t"
    "v_mfma_f32_16x16x32_bf16 v[180:183], a[128:131], a[32:35], 0\n\t"
    "v_exp_f32 v222, v222\n\t"
    "v_mfma_f32_16x16x32_bf16 v[180:183], a[132:135], a[36:39], v[180:183]\n\t"
    "s_add_u32 m0, s56, 25344\n\t"
    "v_exp_f32 v203, v203\n\t"
    "v_mfma_f32_16x16x32_bf16 v[180:183], a[136:139], a[40:43], v[180:183]\n\t"
    "buffer_load_dwordx4 v227, s[44:47], s54 offen offset:0 lds\n\t"
    "v_mfma_f32_16x16x32_bf16 v[180:183], a[140:143], a[44:47], v[180:183]\n\t"
    "v_exp_f32 v223, v223\n\t"
    "v_mfma_f32_16x16x32_bf16 v[184:187], a[144:147], a[32:35], 0\n\t"
    "v_cvt_pk_bf16_f32 v225, v222, v223\n\t"
    "v_cvt_pk_bf16_f32 v219, v202, v203\n\t"
    "v_mfma_f32_16x16x32_bf16 v[184:187], a[148:151], a[36:39], v[184:187]\n\t"
    "ds_write_b64 v238, v[224:225] offset:1032\n\t"
    "v_mfma_f32_16x16x32_bf16 v[184:187], a[152:155], a[40:43], v[184:187]\n\t"
    "v_exp_f32 v172, v172\n\t"
    "v_mfma_f32_16x16x32_bf16 v[184:187], a[156:159], a[44:47], v[184:187]\n\t"
    "v_exp_f32 v173, v173\n\t"
    "v_mfma_f32_16x16x16_bf16 a[244:247], a[248:249], v[224:225], a[244:247]\n\t"
    "v_cvt_pk_bf16_f32 v204, v172, v173\n\t"
    "v_add_f32 v251, v251, v196\n\t"
    "v_mfma_f32_16x16x32_bf16 v[36:39], a[160:163], v[212:215], v[36:39]\n\t"
    "v_exp_f32 v174, v174\n\t"
    "v_mfma_f32_16x16x32_bf16 v[36:39], a[192:195], v[216:219], v[36:39]\n\t"
    "v_exp_f32 v175, v175\n\t"
    "v_mfma_f32_16x16x32_bf16 v[40:43], a[164:167], v[212:215], v[40:43]\n\t"
    "v_cvt_pk_bf16_f32 v205, v174, v175\n\t"
    "v_add_f32 v251, v251, v197\n\t"
    "v_mfma_f32_16x16x32_bf16 v[40:43], a[196:199], v[216:219], v[40:43]\n\t"
    "v_exp_f32 v176, v176\n\t"
    "v_mfma_f32_16x16x32_bf16 v[44:47], a[168:171], v[212:215], v[44:47]\n\t"
    "v_exp_f32 v177, v177\n\t"
    "v_mfma_f32_16x16x32_bf16 v[44:47], a[200:203], v[216:219], v[44:47]\n\t"
    "v_cvt_pk_bf16_f32 v206, v176, v177\n\t"
    "v_add_f32 v251, v251, v198\n\t"
    "v_mfma_f32_16x16x32_bf16 v[48:51], a[172:175], v[212:215], v[48:51]\n\t"
    "v_exp_f32 v178, v178\n\t"
    "v_mfma_f32_16x16x32_bf16 v[48:51], a[204:207], v[216:219], v[48:51]\n\t"
    "v_exp_f32 v179, v179\n\t"
    "v_mfma_f32_16x16x32_bf16 v[52:55], a[176:179], v[212:215], v[52:55]\n\t"
    "s_add_u32 m0, s55, 29568\n\t"
    "v_cvt_pk_bf16_f32 v207, v178, v179\n\t"
    "v_add_f32 v251, v251, v199\n\t"
    "v_mfma_f32_16x16x32_bf16 v[52:55], a[208:211], v[216:219], v[52:55]\n\t"
    "buffer_load_dwordx4 v3, s[40:43], s54 offen offset:0 nt lds\n\t"
    "v_mfma_f32_16x16x32_bf16 v[56:59], a[180:183], v[212:215], v[56:59]\n\t"
    "v_exp_f32 v180, v180\n\t"
    "v_mfma_f32_16x16x32_bf16 v[56:59], a[212:215], v[216:219], v[56:59]\n\t"
    "v_exp_f32 v181, v181\n\t"
    "v_mfma_f32_16x16x32_bf16 v[60:63], a[184:187], v[212:215], v[60:63]\n\t"
    "v_cvt_pk_bf16_f32 v208, v180, v181\n\t"
    "v_add_f32 v251, v251, v200\n\t"
    "v_mfma_f32_16x16x32_bf16 v[60:63], a[216:219], v[216:219], v[60:63]\n\t"
    "v_exp_f32 v182, v182\n\t"
    "v_mfma_f32_16x16x32_bf16 v[64:67], a[188:191], v[212:215], v[64:67]\n\t"
    "v_exp_f32 v183, v183\n\t"
    "v_mfma_f32_16x16x32_bf16 v[64:67], a[220:223], v[216:219], v[64:67]\n\t"
    "v_cvt_pk_bf16_f32 v209, v182, v183\n\t"
    "v_add_f32 v251, v251, v201\n\t"
    "v_mfma_f32_16x16x32_bf16 a[228:231], a[248:251], v[212:215], a[228:231]\n\t"
    "v_exp_f32 v184, v184\n\t"
    "v_mfma_f32_16x16x32_bf16 v[188:191], a[96:99], a[48:51], 0\n\t"
    "v_exp_f32 v185, v185\n\t"
    "v_mfma_f32_16x16x32_bf16 v[188:191], a[100:103], a[52:55], v[188:191]\n\t"
    "v_cvt_pk_bf16_f32 v210, v184, v185\n\t"
    "v_add_f32 v251, v251, v202\n\t"
    "v_mfma_f32_16x16x32_bf16 v[188:191], a[104:107], a[56:59], v[188:191]\n\t"
    "v_exp_f32 v186, v186\n\t"
    "v_mfma_f32_16x16x32_bf16 v[188:191], a[108:111], a[60:63], v[188:191]\n\t"
    "v_add_f32 v251, v251, v203\n\t"
    "v_mfma_f32_16x16x32_bf16 v[192:195], a[112:115], a[48:51], 0\n\t"
    "v_exp_f32 v187, v187\n\t"
    "v_mfma_f32_16x16x32_bf16 v[192:195], a[116:119], a[52:55], v[192:195]\n\t"
    "v_cvt_pk_bf16_f32 v211, v186, v187\n\t"
    "v_mfma_f32_16x16x32_bf16 v[192:195], a[120:123], a[56:59], v[192:195]\n\t"
    "v_add_f32 v252, v252, v180\n\t"
    "v_add_f32 v252, v252, v181\n\t"
    "v_mfma_f32_16x16x32_bf16 v[192:195], a[124:127], a[60:63], v[192:195]\n\t"
    "s_add_u32 m0, s56, 29568\n\t"
    "v_add_f32 v252, v252, v182\n\t"
    "v_add_f32 v252, v252, v183\n\t"
    "v_mfma_f32_16x16x32_bf16 v[196:199], a[128:131], a[48:51], 0\n\t"
    "buffer_load_dwordx4 v3, s[44:47], s54 offen offset:0 lds\n\t"
    "v_mfma_f32_16x16x32_bf16 v[196:199], a[132:135], a[52:55], v[196:199]\n\t"
    "v_add_f32 v252, v252, v184\n\t"
    "v_add_f32 v252, v252, v185\n\t"
    "v_mfma_f32_16x16x32_bf16 v[196:199], a[136:139], a[56:59], v[196:199]\n\t"
    "v_add_f32 v252, v252, v186\n\t"
    "v_add_f32 v252, v252, v187\n\t"
    "v_mfma_f32_16x16x32_bf16 v[196:199], a[140:143], a[60:63], v[196:199]\n\t"
    "v_exp_f32 v188, v188\n\t"
    "v_mfma_f32_16x16x32_bf16 v[200:203], a[144:147], a[48:51], 0\n\t"
    "v_exp_f32 v189, v189\n\t"
    "v_mfma_f32_16x16x32_bf16 v[200:203], a[148:151], a[52:55], v[200:203]\n\t"
    "v_cvt_pk_bf16_f32 v212, v188, v189\n\t"
    "v_mfma_f32_16x16x32_bf16 v[200:203], a[152:155], a[56:59], v[200:203]\n\t"
    "v_exp_f32 v190, v190\n\t"
    "v_mfma_f32_16x16x32_bf16 v[200:203], a[156:159], a[60:63], v[200:203]\n\t"
    "v_exp_f32 v191, v191\n\t"
    "v_mfma_f32_16x16x32_bf16 v[68:71], a[160:163], v[204:207], v[68:71]\n\t"
    "v_cvt_pk_bf16_f32 v213, v190, v191\n\t"
    "v_mfma_f32_16x16x32_bf16 v[68:71], a[192:195], v[208:211], v[68:71]\n\t"
    "v_exp_f32 v192, v192\n\t"
    "v_mfma_f32_16x16x32_bf16 v[72:75], a[164:167], v[204:207], v[72:75]\n\t"
    "v_exp_f32 v193, v193\n\t"
    "v_mfma_f32_16x16x32_bf16 v[72:75], a[196:199], v[208:211], v[72:75]\n\t"
    "v_cvt_pk_bf16_f32 v214, v192, v193\n\t"
    "v_mfma_f32_16x16x32_bf16 v[76:79], a[168:171], v[204:207], v[76:79]\n\t"
    "v_exp_f32 v194, v194\n\t"
    "v_mfma_f32_16x16x32_bf16 v[76:79], a[200:203], v[208:211], v[76:79]\n\t"
    "v_exp_f32 v195, v195\n\t"
    "v_mfma_f32_16x16x32_bf16 v[80:83], a[172:175], v[204:207], v[80:83]\n\t"
    "v_cvt_pk_bf16_f32 v215, v194, v195\n\t"
    "v_mfma_f32_16x16x32_bf16 v[80:83], a[204:207], v[208:211], v[80:83]\n\t"
    "v_exp_f32 v196, v196\n\t"
    "v_mfma_f32_16x16x32_bf16 v[84:87], a[176:179], v[204:207], v[84:87]\n\t"
    "v_exp_f32 v197, v197\n\t"
    "v_mfma_f32_16x16x32_bf16 v[84:87], a[208:211], v[208:211], v[84:87]\n\t"
    "v_cvt_pk_bf16_f32 v216, v196, v197\n\t"
    "v_add_f32 v253, v253, v196\n\t"
    "v_mfma_f32_16x16x32_bf16 v[88:91], a[180:183], v[204:207], v[88:91]\n\t"
    "v_exp_f32 v198, v198\n\t"
    "v_mfma_f32_16x16x32_bf16 v[88:91], a[212:215], v[208:211], v[88:91]\n\t"
    "v_exp_f32 v199, v199\n\t"
    "v_mfma_f32_16x16x32_bf16 v[92:95], a[184:187], v[204:207], v[92:95]\n\t"
    "v_cvt_pk_bf16_f32 v217, v198, v199\n\t"
    "v_add_f32 v253, v253, v197\n\t"
    "v_mfma_f32_16x16x32_bf16 v[92:95], a[216:219], v[208:211], v[92:95]\n\t"
    "v_exp_f32 v200, v200\n\t"
    "v_mfma_f32_16x16x32_bf16 v[96:99], a[188:191], v[204:207], v[96:99]\n\t"
    "v_exp_f32 v201, v201\n\t"
    "v_mfma_f32_16x16x32_bf16 v[96:99], a[220:223], v[208:211], v[96:99]\n\t"
    "s_add_u32 s54, s54, 16384\n\t"
    "v_cvt_pk_bf16_f32 v218, v200, v201\n\t"
    "v_add_f32 v253, v253, v198\n\t"
    "v_mfma_f32_16x16x32_bf16 a[232:235], a[248:251], v[204:207], a[232:235]\n\t"
    "s_waitcnt vmcnt(16) lgkmcnt(0)\n\t"
    "s_barrier\n\t"
    "v_mfma_f32_16x16x32_bf16 v[172:175], a[96:99], a[64:67], 0\n\t"
    "ds_read_b128 a[96:99], v236 offset:16896\n\t"
    "v_add_f32 v253, v253, v199\n\t"
    "v_mfma_f32_16x16x32_bf16 v[172:175], a[100:103], a[68:71], v[172:175]\n\t"
    "v_exp_f32 v202, v202\n\t"
    "v_mfma_f32_16x16x32_bf16 v[172:175], a[104:107], a[72:75], v[172:175]\n\t"
    "ds_read_b128 a[100:103], v236 offset:16960\n\t"
    "ds_read_b128 a[104:107], v236 offset:17024\n\t"
    "v_mfma_f32_16x16x32_bf16 v[172:175], a[108:111], a[76:79], v[172:175]\n\t"
    "s_and_b32 s54, s54, 0x7ffff\n\t"
    "v_exp_f32 v203, v203\n\t"
    "v_mfma_f32_16x16x32_bf16 v[176:179], a[112:115], a[64:67], 0\n\t"
    "ds_read_b128 a[108:111], v236 offset:17088\n\t"
    "ds_read_b128 a[112:115], v236 offset:17408\n\t"
    "v_mfma_f32_16x16x32_bf16 v[176:179], a[116:119], a[68:71], v[176:179]\n\t"
    "v_cvt_pk_bf16_f32 v219, v202, v203\n\t"
    "v_add_f32 v253, v253, v200\n\t"
    "v_mfma_f32_16x16x32_bf16 v[176:179], a[120:123], a[72:75], v[176:179]\n\t"
    "ds_read_b128 a[116:119], v236 offset:17472\n\t"
    "ds_read_b128 a[120:123], v236 offset:17536\n\t"
    "v_mfma_f32_16x16x32_bf16 v[176:179], a[124:127], a[76:79], v[176:179]\n\t"
    "v_add_f32 v253, v253, v201\n\t"
    "v_add_f32 v253, v253, v202\n\t"
    "v_mfma_f32_16x16x32_bf16 v[180:183], a[128:131], a[64:67], 0\n\t"
    "ds_read_b128 a[124:127], v236 offset:17600\n\t"
    "v_add_f32 v253, v253, v203\n\t"
    "v_mfma_f32_16x16x32_bf16 v[180:183], a[132:135], a[68:71], v[180:183]\n\t"
    "ds_read_b128 a[128:131], v236 offset:25344\n\t"
    "v_mfma_f32_16x16x32_bf16 v[180:183], a[136:139], a[72:75], v[180:183]\n\t"
    "ds_read_b128 a[132:135], v236 offset:25408\n\t"
    "v_mfma_f32_16x16x32_bf16 v[180:183], a[140:143], a[76:79], v[180:183]\n\t"
    "s_add_u32 s55, s55, s57\n\t"
    "v_exp_f32 v172, v172\n\t"
    "v_mfma_f32_16x16x32_bf16 v[184:187], a[144:147], a[64:67], 0\n\t"
    "ds_read_b128 a[136:139], v236 offset:25472\n\t"
    "ds_read_b128 a[140:143], v236 offset:25536\n\t"
    "v_mfma_f32_16x16x32_bf16 v[184:187], a[148:151], a[68:71], v[184:187]\n\t"
    "s_add_u32 s56, s56, s57\n\t"
    "v_exp_f32 v173, v173\n\t"
    "v_mfma_f32_16x16x32_bf16 v[184:187], a[152:155], a[72:75], v[184:187]\n\t"
    "ds_read_b128 a[144:147], v236 offset:25856\n\t"
    "ds_read_b128 a[148:151], v236 offset:25920\n\t"
    "v_mfma_f32_16x16x32_bf16 v[184:187], a[156:159], a[76:79], v[184:187]\n\t"
    "s_sub_u32 s57, 0, s57\n\t"
    "v_cvt_pk_bf16_f32 v204, v172, v173\n\t"
    "v_add_f32 v254, v254, v172\n\t"
    "v_mfma_f32_16x16x32_bf16 v[100:103], a[160:163], v[212:215], v[100:103]\n\t"
    "ds_read_b128 a[152:155], v236 offset:25984\n\t"
    "ds_read_b128 a[156:159], v236 offset:26048\n\t"
    "v_mfma_f32_16x16x32_bf16 v[100:103], a[192:195], v[216:219], v[100:103]\n\t"
    "v_exp_f32 v174, v174\n\t"
    "v_mfma_f32_16x16x32_bf16 v[104:107], a[164:167], v[212:215], v[104:107]\n\t"
    "ds_read_b128 v[228:231], v238 offset:0\n\t"
    "ds_read_b128 v[232:235], v238 offset:1024\n\t"
    "v_mfma_f32_16x16x32_bf16 v[104:107], a[196:199], v[216:219], v[104:107]\n\t"
    "v_exp_f32 v175, v175\n\t"
    "v_mfma_f32_16x16x32_bf16 v[108:111], a[168:171], v[212:215], v[108:111]\n\t"
    "v_cvt_pk_bf16_f32 v205, v174, v175\n\t"
    "v_add_f32 v254, v254, v173\n\t"
    "v_mfma_f32_16x16x32_bf16 v[108:111], a[200:203], v[216:219], v[108:111]\n\t"
    "v_exp_f32 v176, v176\n\t"
    "v_mfma_f32_16x16x32_bf16 v[112:115], a[172:175], v[212:215], v[112:115]\n\t"
    "v_exp_f32 v177, v177\n\t"
    "v_mfma_f32_16x16x32_bf16 v[112:115], a[204:207], v[216:219], v[112:115]\n\t"
    "v_cvt_pk_bf16_f32 v206, v176, v177\n\t"
    "v_add_f32 v254, v254, v174\n\t"
    "v_mfma_f32_16x16x32_bf16 v[116:119], a[176:179], v[212:215], v[116:119]\n\t"
    "v_exp_f32 v178, v178\n\t"
    "v_mfma_f32_16x16x32_bf16 v[116:119], a[208:211], v[216:219], v[116:119]\n\t"
    "v_exp_f32 v179, v179\n\t"
    "v_mfma_f32_16x16x32_bf16 v[120:123], a[180:183], v[212:215], v[120:123]\n\t"
    "v_cvt_pk_bf16_f32 v207, v178, v179\n\t"
    "v_add_f32 v254, v254, v175\n\t"
    "v_mfma_f32_16x16x32_bf16 v[120:123], a[212:215], v[216:219], v[120:123]\n\t"
    "v_exp_f32 v180, v180\n\t"
    "v_mfma_f32_16x16x32_bf16 v[124:127], a[184:187], v[212:215], v[124:127]\n\t"
    "v_exp_f32 v181, v181\n\t"
    "v_mfma_f32_16x16x32_bf16 v[124:127], a[216:219], v[216:219], v[124:127]\n\t"
    "v_cvt_pk_bf16_f32 v208, v180, v181\n\t"
    "v_add_f32 v254, v254, v176\n\t"
    "v_mfma_f32_16x16x32_bf16 v[128:131], a[188:191], v[212:215], v[128:131]\n\t"
    "v_exp_f32 v182, v182\n\t"
    "v_mfma_f32_16x16x32_bf16 v[128:131], a[220:223], v[216:219], v[128:131]\n\t"
    "v_exp_f32 v183, v183\n\t"
    "v_mfma_f32_16x16x32_bf16 a[236:239], a[248:251], v[212:215], a[236:239]\n\t"
    "s_waitcnt lgkmcnt(0)\n\t"
    "v_cvt_pk_bf16_f32 v209, v182, v183\n\t"
    "v_add_f32 v254, v254, v177\n\t"
    "s_sub_u32 s60, s60, 1\n\t"
    "s_cbranch_scc0 LOOP3_%=\n\t"
    "TAIL3_%=:\n\t"
    "v_mfma_f32_16x16x32_bf16 v[188:191], a[96:99], a[0:3], 0\n\t"
    "v_exp_f32 v184, v184\n\t"
    "v_mfma_f32_16x16x32_bf16 v[188:191], a[100:103], a[4:7], v[188:191]\n\t"
    "v_exp_f32 v185, v185\n\t"
    "v_mfma_f32_16x16x32_bf16 v[188:191], a[104:107], a[8:11], v[188:191]\n\t"
    "v_mfma_f32_16x16x32_bf16 v[188:191], a[108:111], a[12:15], v[188:191]\n\t"
    "v_cvt_pk_bf16_f32 v210, v184, v185\n\t"
    "v_add_f32 v254, v254, v178\n\t"
    "v_mfma_f32_16x16x32_bf16 v[192:195], a[112:115], a[0:3], 0\n\t"
    "v_exp_f32 v186, v186\n\t"
    "v_mfma_f32_16x16x32_bf16 v[192:195], a[116:119], a[4:7], v[192:195]\n\t"
    "v_exp_f32 v187, v187\n\t"
    "v_mfma_f32_16x16x32_bf16 v[192:195], a[120:123], a[8:11], v[192:195]\n\t"
    "v_cvt_pk_bf16_f32 v211, v186, v187\n\t"
    "v_add_f32 v254, v254, v179\n\t"
    "v_mfma_f32_16x16x32_bf16 v[192:195], a[124:127], a[12:15], v[192:195]\n\t"
    "v_add_f32 v254, v254, v180\n\t"
    "v_add_f32 v254, v254, v181\n\t"
    "v_mfma_f32_16x16x32_bf16 v[196:199], a[128:131], a[0:3], 0\n\t"
    "v_add_f32 v254, v254, v182\n\t"
    "v_add_f32 v254, v254, v183\n\t"
    "v_mfma_f32_16x16x32_bf16 v[196:199], a[132:135], a[4:7], v[196:199]\n\t"
    "v_add_f32 v254, v254, v184\n\t"
    "v_add_f32 v254, v254, v185\n\t"
    "v_mfma_f32_16x16x32_bf16 v[196:199], a[136:139], a[8:11], v[196:199]\n\t"
    "v_add_f32 v254, v254, v186\n\t"
    "v_add_f32 v254, v254, v187\n\t"
    "v_mfma_f32_16x16x32_bf16 v[196:199], a[140:143], a[12:15], v[196:199]\n\t"
    "v_exp_f32 v188, v188\n\t"
    "v_mfma_f32_16x16x32_bf16 v[200:203], a[144:147], a[0:3], 0\n\t"
    "v_exp_f32 v189, v189\n\t"
    "v_mfma_f32_16x16x32_bf16 v[200:203], a[148:151], a[4:7], v[200:203]\n\t"
    "v_cvt_pk_bf16_f32 v212, v188, v189\n\t"
    "v_add_f32 v250, v250, v188\n\t"
    "v_mfma_f32_16x16x32_bf16 v[200:203], a[152:155], a[8:11], v[200:203]\n\t"
    "v_exp_f32 v190, v190\n\t"
    "v_mfma_f32_16x16x32_bf16 v[200:203], a[156:159], a[12:15], v[200:203]\n\t"
    "v_exp_f32 v191, v191\n\t"
    "v_mfma_f32_16x16x32_bf16 v[164:167], a[184:187], v[228:231], v[164:167]\n\t"
    "v_cvt_pk_bf16_f32 v213, v190, v191\n\t"
    "v_add_f32 v250, v250, v189\n\t"
    "v_mfma_f32_16x16x32_bf16 v[164:167], a[216:219], v[232:235], v[164:167]\n\t"
    "v_exp_f32 v192, v192\n\t"
    "v_mfma_f32_16x16x32_bf16 v[168:171], a[188:191], v[228:231], v[168:171]\n\t"
    "v_mfma_f32_16x16x32_bf16 v[168:171], a[220:223], v[232:235], v[168:171]\n\t"
    "v_exp_f32 v193, v193\n\t"
    "v_mfma_f32_16x16x32_bf16 v[132:135], a[160:163], v[204:207], v[132:135]\n\t"
    "ds_read_b64_tr_b16 a[160:161], v237 offset:16896\n\t"
    "ds_read_b64_tr_b16 a[162:163], v237 offset:17408\n\t"
    "v_accvgpr_write_b32 a0, 0\n\t"
    "v_mfma_f32_16x16x32_bf16 v[132:135], a[192:195], v[208:211], v[132:135]\n\t"
    "v_cvt_pk_bf16_f32 v214, v192, v193\n\t"
    "v_add_f32 v250, v250, v190\n\t"
    "v_accvgpr_write_b32 a1, 0\n\t"
    "v_mfma_f32_16x16x32_bf16 v[136:139], a[164:167], v[204:207], v[136:139]\n\t"
    "ds_read_b64_tr_b16 a[192:193], v237 offset:25344\n\t"
    "ds_read_b64_tr_b16 a[194:195], v237 offset:25856\n\t"
    "v_accvgpr_write_b32 a2, 0\n\t"
    "v_mfma_f32_16x16x32_bf16 v[136:139], a[196:199], v[208:211], v[136:139]\n\t"
    "v_exp_f32 v194, v194\n\t"
    "v_accvgpr_write_b32 a3, 0\n\t"
    "v_mfma_f32_16x16x32_bf16 v[140:143], a[168:171], v[204:207], v[140:143]\n\t"
    "ds_read_b64_tr_b16 a[164:165], v237 offset:16904\n\t"
    "ds_read_b64_tr_b16 a[166:167], v237 offset:17416\n\t"
    "v_accvgpr_write_b32 a4, 0\n\t"
    "v_mfma_f32_16x16x32_bf16 v[140:143], a[200:203], v[208:211], v[140:143]\n\t"
    "v_exp_f32 v195, v195\n\t"
    "v_mfma_f32_16x16x32_bf16 v[144:147], a[172:175], v[204:207], v[144:147]\n\t"
    "ds_read_b64_tr_b16 a[196:197], v237 offset:25352\n\t"
    "ds_read_b64_tr_b16 a[198:199], v237 offset:25864\n\t"
    "v_mfma_f32_16x16x32_bf16 v[144:147], a[204:207], v[208:211], v[144:147]\n\t"
    "v_cvt_pk_bf16_f32 v215, v194, v195\n\t"
    "v_add_f32 v250, v250, v191\n\t"
    "v_mfma_f32_16x16x32_bf16 v[148:151], a[176:179], v[204:207], v[148:151]\n\t"
    "ds_read_b64_tr_b16 a[168:169], v237 offset:16960\n\t"
    "ds_read_b64_tr_b16 a[170:171], v237 offset:17472\n\t"
    "v_mfma_f32_16x16x32_bf16 v[148:151], a[208:211], v[208:211], v[148:151]\n\t"
    "v_exp_f32 v196, v196\n\t"
    "v_mfma_f32_16x16x32_bf16 v[152:155], a[180:183], v[204:207], v[152:155]\n\t"
    "ds_read_b64_tr_b16 a[200:201], v237 offset:25408\n\t"
    "ds_read_b64_tr_b16 a[202:203], v237 offset:25920\n\t"
    "v_mfma_f32_16x16x32_bf16 v[152:155], a[212:215], v[208:211], v[152:155]\n\t"
    "v_exp_f32 v197, v197\n\t"
    "v_mfma_f32_16x16x32_bf16 v[156:159], a[184:187], v[204:207], v[156:159]\n\t"
    "ds_read_b64_tr_b16 a[172:173], v237 offset:16968\n\t"
    "ds_read_b64_tr_b16 a[174:175], v237 offset:17480\n\t"
    "v_mfma_f32_16x16x32_bf16 v[156:159], a[216:219], v[208:211], v[156:159]\n\t"
    "v_cvt_pk_bf16_f32 v216, v196, v197\n\t"
    "v_add_f32 v250, v250, v192\n\t"
    "v_mfma_f32_16x16x32_bf16 v[160:163], a[188:191], v[204:207], v[160:163]\n\t"
    "v_mfma_f32_16x16x32_bf16 v[160:163], a[220:223], v[208:211], v[160:163]\n\t"
    "v_exp_f32 v198, v198\n\t"
    "v_mfma_f32_16x16x32_bf16 v[172:175], a[96:99], a[16:19], 0\n\t"
    "ds_read_b64_tr_b16 a[204:205], v237 offset:25416\n\t"
    "ds_read_b64_tr_b16 a[206:207], v237 offset:25928\n\t"
    "v_mfma_f32_16x16x32_bf16 v[172:175], a[100:103], a[20:23], v[172:175]\n\t"
    "v_exp_f32 v199, v199\n\t"
    "v_mfma_f32_16x16x32_bf16 v[172:175], a[104:107], a[24:27], v[172:175]\n\t"
    "ds_read_b64_tr_b16 a[176:177], v237 offset:17024\n\t"
    "ds_read_b64_tr_b16 a[178:179], v237 offset:17536\n\t"
    "v_mfma_f32_16x16x32_bf16 v[172:175], a[108:111], a[28:31], v[172:175]\n\t"
    "v_cvt_pk_bf16_f32 v217, v198, v199\n\t"
    "v_add_f32 v250, v250, v193\n\t"
    "v_mfma_f32_16x16x32_bf16 v[176:179], a[112:115], a[16:19], 0\n\t"
    "ds_read_b64_tr_b16 a[208:209], v237 offset:25472\n\t"
    "ds_read_b64_tr_b16 a[210:211], v237 offset:25984\n\t"
    "v_mfma_f32_16x16x32_bf16 v[176:179], a[116:119], a[20:23], v[176:179]\n\t"
    "v_exp_f32 v200, v200\n\t"
    "v_mfma_f32_16x16x32_bf16 v[176:179], a[120:123], a[24:27], v[176:179]\n\t"
    "ds_read_b64_tr_b16 a[180:181], v237 offset:17032\n\t"
    "ds_read_b64_tr_b16 a[182:183], v237 offset:17544\n\t"
    "v_mfma_f32_16x16x32_bf16 v[176:179], a[124:127], a[28:31], v[176:179]\n\t"
    "v_exp_f32 v201, v201\n\t"
    "v_mfma_f32_16x16x32_bf16 v[180:183], a[128:131], a[16:19], 0\n\t"
    "ds_read_b64_tr_b16 a[212:213], v237 offset:25480\n\t"
    "ds_read_b64_tr_b16 a[214:215], v237 offset:25992\n\t"
    "v_mfma_f32_16x16x32_bf16 v[180:183], a[132:135], a[20:23], v[180:183]\n\t"
    "v_cvt_pk_bf16_f32 v218, v200, v201\n\t"
    "v_add_f32 v250, v250, v194\n\t"
    "v_mfma_f32_16x16x32_bf16 v[180:183], a[136:139], a[24:27], v[180:183]\n\t"
    "ds_read_b64_tr_b16 a[184:185], v237 offset:17088\n\t"
    "ds_read_b64_tr_b16 a[186:187], v237 offset:17600\n\t"
    "v_mfma_f32_16x16x32_bf16 v[180:183], a[140:143], a[28:31], v[180:183]\n\t"
    "v_exp_f32 v202, v202\n\t"
    "v_mfma_f32_16x16x32_bf16 v[184:187], a[144:147], a[16:19], 0\n\t"
    "ds_read_b64_tr_b16 a[216:217], v237 offset:25536\n\t"
    "ds_read_b64_tr_b16 a[218:219], v237 offset:26048\n\t"
    "v_mfma_f32_16x16x32_bf16 v[184:187], a[148:151], a[20:23], v[184:187]\n\t"
    "v_exp_f32 v203, v203\n\t"
    "v_mfma_f32_16x16x32_bf16 v[184:187], a[152:155], a[24:27], v[184:187]\n\t"
    "ds_read_b64_tr_b16 a[188:189], v237 offset:17096\n\t"
    "ds_read_b64_tr_b16 a[190:191], v237 offset:17608\n\t"
    "v_mfma_f32_16x16x32_bf16 v[184:187], a[156:159], a[28:31], v[184:187]\n\t"
    "v_cvt_pk_bf16_f32 v219, v202, v203\n\t"
    "v_add_f32 v250, v250, v195\n\t"
    "v_mfma_f32_16x16x32_bf16 v[220:223], a[144:147], a[80:83], 0\n\t"
    "ds_read_b64_tr_b16 a[220:221], v237 offset:25544\n\t"
    "ds_read_b64_tr_b16 a[222:223], v237 offset:26056\n\t"
    "v_mfma_f32_16x16x32_bf16 v[220:223], a[148:151], a[84:87], v[220:223]\n\t"
    "v_exp_f32 v172, v172\n\t"
    "v_mfma_f32_16x16x32_bf16 v[220:223], a[152:155], a[88:91], v[220:223]\n\t"
    "v_mfma_f32_16x16x32_bf16 v[220:223], a[156:159], a[92:95], v[220:223]\n\t"
    "s_waitcnt lgkmcnt(4)\n\t"
    "v_exp_f32 v173, v173\n\t"
    "v_mfma_f32_16x16x32_bf16 v[4:7], a[160:163], v[212:215], v[4:7]\n\t"
    "v_cvt_pk_bf16_f32 v204, v172, v173\n\t"
    "v_add_f32 v250, v250, v196\n\t"
    "v_mfma_f32_16x16x32_bf16 v[4:7], a[192:195], v[216:219], v[4:7]\n\t"
    "v_exp_f32 v174, v174\n\t"
    "v_mfma_f32_16x16x32_bf16 v[8:11], a[164:167], v[212:215], v[8:11]\n\t"
    "v_exp_f32 v175, v175\n\t"
    "v_mfma_f32_16x16x32_bf16 v[8:11], a[196:199], v[216:219], v[8:11]\n\t"
    "v_cvt_pk_bf16_f32 v205, v174, v175\n\t"
    "v_add_f32 v250, v250, v197\n\t"
    "v_mfma_f32_16x16x32_bf16 v[12:15], a[168:171], v[212:215], v[12:15]\n\t"
    "v_exp_f32 v176, v176\n\t"
    "v_mfma_f32_16x16x32_bf16 v[12:15], a[200:203], v[216:219], v[12:15]\n\t"
    "v_exp_f32 v177, v177\n\t"
    "v_mfma_f32_16x16x32_bf16 v[16:19], a[172:175], v[212:215], v[16:19]\n\t"
    "v_cvt_pk_bf16_f32 v206, v176, v177\n\t"
    "v_add_f32 v250, v250, v198\n\t"
    "v_mfma_f32_16x16x32_bf16 v[16:19], a[204:207], v[216:219], v[16:19]\n\t"
    "v_exp_f32 v178, v178\n\t"
    "v_mfma_f32_16x16x32_bf16 v[20:23], a[176:179], v[212:215], v[20:23]\n\t"
    "v_exp_f32 v179, v179\n\t"
    "v_mfma_f32_16x16x32_bf16 v[20:23], a[208:211], v[216:219], v[20:23]\n\t"
    "v_cvt_pk_bf16_f32 v207, v178, v179\n\t"
    "v_add_f32 v250, v250, v199\n\t"
    "v_mfma_f32_16x16x32_bf16 v[24:27], a[180:183], v[212:215], v[24:27]\n\t"
    "v_mfma_f32_16x16x32_bf16 v[24:27], a[212:215], v[216:219], v[24:27]\n\t"
    "v_exp_f32 v180, v180\n\t"
    "v_mfma_f32_16x16x32_bf16 v[28:31], a[184:187], v[212:215], v[28:31]\n\t"
    "v_exp_f32 v181, v181\n\t"
    "v_mfma_f32_16x16x32_bf16 v[28:31], a[216:219], v[216:219], v[28:31]\n\t"
    "s_waitcnt lgkmcnt(0)\n\t"
    "v_cvt_pk_bf16_f32 v208, v180, v181\n\t"
    "v_add_f32 v250, v250, v200\n\t"
    "v_mfma_f32_16x16x32_bf16 v[32:35], a[188:191], v[212:215], v[32:35]\n\t"
    "v_exp_f32 v182, v182\n\t"
    "v_mfma_f32_16x16x32_bf16 v[32:35], a[220:223], v[216:219], v[32:35]\n\t"
    "v_exp_f32 v183, v183\n\t"
    "v_mfma_f32_16x16x32_bf16 v[188:191], a[96:99], a[32:35], 0\n\t"
    "v_cvt_pk_bf16_f32 v209, v182, v183\n\t"
    "v_add_f32 v250, v250, v201\n\t"
    "v_mfma_f32_16x16x32_bf16 v[188:191], a[100:103], a[36:39], v[188:191]\n\t"
    "v_exp_f32 v184, v184\n\t"
    "v_mfma_f32_16x16x32_bf16 v[188:191], a[104:107], a[40:43], v[188:191]\n\t"
    "v_exp_f32 v220, v220\n\t"
    "v_mfma_f32_16x16x32_bf16 v[188:191], a[108:111], a[44:47], v[188:191]\n\t"
    "v_exp_f32 v185, v185\n\t"
    "v_mfma_f32_16x16x32_bf16 v[192:195], a[112:115], a[32:35], 0\n\t"
    "v_exp_f32 v221, v221\n\t"
    "v_mfma_f32_16x16x32_bf16 v[192:195], a[116:119], a[36:39], v[192:195]\n\t"
    "v_add_f32 v250, v250, v202\n\t"
    "v_add_f32 v250, v250, v203\n\t"
    "v_mfma_f32_16x16x32_bf16 v[192:195], a[120:123], a[40:43], v[192:195]\n\t"
    "v_cvt_pk_bf16_f32 v210, v184, v185\n\t"
    "v_cvt_pk_bf16_f32 v224, v220, v221\n\t"
    "v_mfma_f32_16x16x4_f32 a[224:227], a252, v250, 0\n\t"
    "v_mfma_f32_16x16x32_bf16 v[192:195], a[124:127], a[44:47], v[192:195]\n\t"
    "v_exp_f32 v186, v186\n\t"
    "v_mfma_f32_16x16x32_bf16 v[196:199], a[128:131], a[32:35], 0\n\t"
    "v_exp_f32 v222, v222\n\t"
    "v_mfma_f32_16x16x32_bf16 v[196:199], a[132:135], a[36:39], v[196:199]\n\t"
    "v_exp_f32 v187, v187\n\t"
    "v_mfma_f32_16x16x32_bf16 v[196:199], a[136:139], a[40:43], v[196:199]\n\t"
    "s_nop 2\n\t"
    "v_accvgpr_read_b32 v242, a224\n\t"
    "v_subrev_u32 v246, 0x2b800000, v242\n\t"
    "v_cmp_lt_u32 vcc, 0x28000000, v246\n\t"
    "s_nop 1\n\t"
    "s_or_b64 s[62:63], s[62:63], vcc\n\t"
    "v_rcp_f32 v243, v242\n\t"
    "s_mov_b32 s64, 61440\n\t"
    "v_mul_f32 v4, v4, v243\n\t"
    "v_mfma_f32_16x16x32_bf16 v[196:199], a[140:143], a[44:47], v[196:199]\n\t"
    "v_exp_f32 v223, v223\n\t"
    "v_mul_f32 v5, v5, v243\n\t"
    "v_mul_f32 v6, v6, v243\n\t"
    "v_mul_f32 v7, v7, v243\n\t"
    "v_mul_f32 v8, v8, v243\n\t"
    "v_mfma_f32_16x16x32_bf16 v[200:203], a[144:147], a[32:35], 0\n\t"
    "v_cvt_pk_bf16_f32 v225, v222, v223\n\t"
    "v_cvt_pk_bf16_f32 v211, v186, v187\n\t"
    "v_mul_f32 v9, v9, v243\n\t"
    "v_mul_f32 v10, v10, v243\n\t"
    "v_mul_f32 v11, v11, v243\n\t"
    "v_cvt_pk_bf16_f32 v4, v4, v5\n\t"
    "v_mfma_f32_16x16x32_bf16 v[200:203], a[148:151], a[36:39], v[200:203]\n\t"
    "ds_write_b64 v238, v[224:225] offset:3080\n\t"
    "v_cvt_pk_bf16_f32 v5, v6, v7\n\t"
    "v_cvt_pk_bf16_f32 v6, v8, v9\n\t"
    "v_cvt_pk_bf16_f32 v7, v10, v11\n\t"
    "s_nop 1\n\t"
    "v_mfma_f32_16x16x32_bf16 a[4:7], a[0:3], v[4:7], a[4:7]\n\t"
    "v_mfma_f32_16x16x32_bf16 v[200:203], a[152:155], a[40:43], v[200:203]\n\t"
    "v_exp_f32 v188, v188\n\t"
    "v_mul_f32 v12, v12, v243\n\t"
    "v_mul_f32 v13, v13, v243\n\t"
    "v_mul_f32 v14, v14, v243\n\t"
    "v_mul_f32 v15, v15, v243\n\t"
    "v_mfma_f32_16x16x32_bf16 v[200:203], a[156:159], a[44:47], v[200:203]\n\t"
    "buffer_store_dwordx4 v[4:7], v241, s[48:51], s64 offen offset:0 sc0 sc1\n\t"
    "v_exp_f32 v189, v189\n\t"
    "v_mul_f32 v16, v16, v243\n\t"
    "v_mul_f32 v17, v17, v243\n\t"
    "v_mul_f32 v18, v18, v243\n\t"
    "v_mul_f32 v19, v19, v243\n\t"
    "v_mfma_f32_16x16x16_bf16 a[244:247], a[248:249], v[224:225], a[244:247]\n\t"
    "v_cvt_pk_bf16_f32 v212, v188, v189\n\t"
    "v_add_f32 v251, v251, v180\n\t"
    "v_cvt_pk_bf16_f32 v12, v12, v13\n\t"
    "v_cvt_pk_bf16_f32 v13, v14, v15\n\t"
    "v_cvt_pk_bf16_f32 v14, v16, v17\n\t"
    "v_cvt_pk_bf16_f32 v15, v18, v19\n\t"
    "v_mfma_f32_16x16x32_bf16 v[36:39], a[160:163], v[204:207], v[36:39]\n\t"
    "v_exp_f32 v190, v190\n\t"
    "v_mfma_f32_16x16x32_bf16 a[4:7], a[0:3], v[12:15], a[4:7]\n\t"
    "v_mul_f32 v20, v20, v243\n\t"
    "v_mul_f32 v21, v21, v243\n\t"
    "v_mul_f32 v22, v22, v243\n\t"
    "v_mfma_f32_16x16x32_bf16 v[36:39], a[192:195], v[208:211], v[36:39]\n\t"
    "v_exp_f32 v191, v191\n\t"
    "v_mul_f32 v23, v23, v243\n\t"
    "v_mul_f32 v24, v24, v243\n\t"
    "v_mul_f32 v25, v25, v243\n\t"
    "v_mul_f32 v26, v26, v243\n\t"
    "v_mfma_f32_16x16x32_bf16 v[40:43], a[164:167], v[204:207], v[40:43]\n\t"
    "buffer_store_dwordx4 v[12:15], v241, s[48:51], s64 offen offset:64 sc0 sc1\n\t"
    "v_cvt_pk_bf16_f32 v213, v190, v191\n\t"
    "v_add_f32 v251, v251, v181\n\t"
    "v_mul_f32 v27, v27, v243\n\t"
    "v_cvt_pk_bf16_f32 v20, v20, v21\n\t"
    "v_cvt_pk_bf16_f32 v21, v22, v23\n\t"
    "v_mfma_f32_16x16x32_bf16 v[40:43], a[196:199], v[208:211], v[40:43]\n\t"
    "v_exp_f32 v192, v192\n\t"
    "v_cvt_pk_bf16_f32 v22, v24, v25\n\t"
    "v_cvt_pk_bf16_f32 v23, v26, v27\n\t"
    "s_nop 1\n\t"
    "v_mfma_f32_16x16x32_bf16 a[4:7], a[0:3], v[20:23], a[4:7]\n\t"
    "v_mul_f32 v28, v28, v243\n\t"
    "v_mfma_f32_16x16x32_bf16 v[44:47], a[168:171], v[204:207], v[44:47]\n\t"
    "v_exp_f32 v193, v193\n\t"
    "v_mul_f32 v29, v29, v243\n\t"
    "v_mul_f32 v30, v30, v243\n\t"
    "v_mul_f32 v31, v31, v243\n\t"
    "v_mul_f32 v32, v32, v243\n\t"
    "v_mfma_f32_16x16x32_bf16 v[44:47], a[200:203], v[208:211], v[44:47]\n\t"
    "buffer_store_dwordx4 v[20:23], v241, s[48:51], s64 offen offset:128 sc0 sc1\n\t"
    "v_cvt_pk_bf16_f32 v214, v192, v193\n\t"
    "v_add_f32 v251, v251, v182\n\t"
    "v_mul_f32 v33, v33, v243\n\t"
    "v_mul_f32 v34, v34, v243\n\t"
    "v_mul_f32 v35, v35, v243\n\t"
    "v_mfma_f32_16x16x32_bf16 v[48:51], a[172:175], v[204:207], v[48:51]\n\t"
    "v_exp_f32 v194, v194\n\t"
    "v_cvt_pk_bf16_f32 v28, v28, v29\n\t"
    "v_cvt_pk_bf16_f32 v29, v30, v31\n\t"
    "v_cvt_pk_bf16_f32 v30, v32, v33\n\t"
    "v_cvt_pk_bf16_f32 v31, v34, v35\n\t"
    "v_mfma_f32_16x16x32_bf16 v[48:51], a[204:207], v[208:211], v[48:51]\n\t"
    "v_exp_f32 v195, v195\n\t"
    "v_mfma_f32_16x16x32_bf16 a[4:7], a[0:3], v[28:31], a[4:7]\n\t"
    "v_mfma_f32_16x16x32_bf16 v[52:55], a[176:179], v[204:207], v[52:55]\n\t"
    "v_cvt_pk_bf16_f32 v215, v194, v195\n\t"
    "v_add_f32 v251, v251, v183\n\t"
    "v_and_b32 v220, 15, v1\n\t"
    "v_mfma_f32_16x16x32_bf16 v[52:55], a[208:211], v[208:211], v[52:55]\n\t"
    "buffer_store_dwordx4 v[28:31], v241, s[48:51], s64 offen offset:192 sc0 sc1\n\t"
    "v_lshlrev_b32 v220, 2, v220\n\t"
    "v_mfma_f32_16x16x32_bf16 v[56:59], a[180:183], v[204:207], v[56:59]\n\t"
    "s_add_u32 s61, s53, 139264\n\t"
    "v_exp_f32 v196, v196\n\t"
    "v_add_u32 v220, s61, v220\n\t"
    "v_mfma_f32_16x16x32_bf16 v[56:59], a[212:215], v[208:211], v[56:59]\n\t"
    "v_exp_f32 v197, v197\n\t"
    "v_accvgpr_read_b32 v221, a244\n\t"
    "v_mfma_f32_16x16x32_bf16 v[60:63], a[184:187], v[204:207], v[60:63]\n\t"
    "ds_write_b32 v220, v221 offset:192\n\t"
    "v_cvt_pk_bf16_f32 v216, v196, v197\n\t"
    "v_add_f32 v251, v251, v184\n\t"
    "v_mfma_f32_16x16x32_bf16 v[60:63], a[216:219], v[208:211], v[60:63]\n\t"
    "v_exp_f32 v198, v198\n\t"
    "v_mfma_f32_16x16x32_bf16 v[64:67], a[188:191], v[204:207], v[64:67]\n\t"
    "v_exp_f32 v199, v199\n\t"
    "v_mfma_f32_16x16x32_bf16 v[64:67], a[220:223], v[208:211], v[64:67]\n\t"
    "v_cvt_pk_bf16_f32 v217, v198, v199\n\t"
    "v_add_f32 v251, v251, v185\n\t"
    "v_mfma_f32_16x16x32_bf16 a[228:231], a[248:251], v[204:207], a[228:231]\n\t"
    "v_exp_f32 v200, v200\n\t"
    "v_mfma_f32_16x16x32_bf16 v[172:175], a[96:99], a[48:51], 0\n\t"
    "v_exp_f32 v201, v201\n\t"
    "v_mfma_f32_16x16x32_bf16 v[172:175], a[100:103], a[52:55], v[172:175]\n\t"
    "v_cvt_pk_bf16_f32 v218, v200, v201\n\t"
    "v_add_f32 v251, v251, v186\n\t"
    "v_mfma_f32_16x16x32_bf16 v[172:175], a[104:107], a[56:59], v[172:175]\n\t"
    "v_exp_f32 v202, v202\n\t"
    "v_mfma_f32_16x16x32_bf16 v[172:175], a[108:111], a[60:63], v[172:175]\n\t"
    "v_add_f32 v251, v251, v187\n\t"
    "v_mfma_f32_16x16x32_bf16 v[176:179], a[112:115], a[48:51], 0\n\t"
    "v_exp_f32 v203, v203\n\t"
    "v_mfma_f32_16x16x32_bf16 v[176:179], a[116:119], a[52:55], v[176:179]\n\t"
    "v_cvt_pk_bf16_f32 v219, v202, v203\n\t"
    "v_mfma_f32_16x16x32_bf16 v[176:179], a[120:123], a[56:59], v[176:179]\n\t"
    "v_add_f32 v252, v252, v196\n\t"
    "v_add_f32 v252, v252, v197\n\t"
    "v_mfma_f32_16x16x4_f32 a[228:231], a252, v251, a[228:231]\n\t"
    "v_mfma_f32_16x16x32_bf16 v[176:179], a[124:127], a[60:63], v[176:179]\n\t"
    "v_add_f32 v252, v252, v198\n\t"
    "v_add_f32 v252, v252, v199\n\t"
    "v_mfma_f32_16x16x32_bf16 v[180:183], a[128:131], a[48:51], 0\n\t"
    "v_mfma_f32_16x16x32_bf16 v[180:183], a[132:135], a[52:55], v[180:183]\n\t"
    "v_add_f32 v252, v252, v200\n\t"
    "v_add_f32 v252, v252, v201\n\t"
    "v_mfma_f32_16x16x32_bf16 v[180:183], a[136:139], a[56:59], v[180:183]\n\t"
    "v_add_f32 v252, v252, v202\n\t"
    "v_add_f32 v252, v252, v203\n\t"
    "v_accvgpr_read_b32 v244, a228\n\t"
    "v_subrev_u32 v246, 0x2b800000, v244\n\t"
    "v_cmp_lt_u32 vcc, 0x28000000, v246\n\t"
    "s_nop 1\n\t"
    "s_or_b64 s[62:63], s[62:63], vcc\n\t"
    "v_mfma_f32_16x16x32_bf16 v[180:183], a[140:143], a[60:63], v[180:183]\n\t"
    "v_exp_f32 v172, v172\n\t"
    "v_rcp_f32 v245, v244\n\t"
    "s_mov_b32 s64, 65536\n\t"
    "v_mul_f32 v36, v36, v245\n\t"
    "v_mul_f32 v37, v37, v245\n\t"
    "v_mfma_f32_16x16x32_bf16 v[184:187], a[144:147], a[48:51], 0\n\t"
    "v_exp_f32 v173, v173\n\t"
    "v_mul_f32 v38, v38, v245\n\t"
    "v_mul_f32 v39, v39, v245\n\t"
    "v_mul_f32 v40, v40, v245\n\t"
    "v_mul_f32 v41, v41, v245\n\t"
    "v_mfma_f32_16x16x32_bf16 v[184:187], a[148:151], a[52:55], v[184:187]\n\t"
    "v_cvt_pk_bf16_f32 v204, v172, v173\n\t"
    "v_mul_f32 v42, v42, v245\n\t"
    "v_mul_f32 v43, v43, v245\n\t"
    "v_cvt_pk_bf16_f32 v36, v36, v37\n\t"
    "v_cvt_pk_bf16_f32 v37, v38, v39\n\t"
    "v_cvt_pk_bf16_f32 v38, v40, v41\n\t"
    "v_mfma_f32_16x16x32_bf16 v[184:187], a[152:155], a[56:59], v[184:187]\n\t"
    "v_exp_f32 v174, v174\n\t"
    "v_cvt_pk_bf16_f32 v39, v42, v43\n\t"
    "s_nop 1\n\t"
    "v_mfma_f32_16x16x32_bf16 a[4:7], a[0:3], v[36:39], a[4:7]\n\t"
    "v_mul_f32 v44, v44, v245\n\t"
    "v_mul_f32 v45, v45, v245\n\t"
    "v_mfma_f32_16x16x32_bf16 v[184:187], a[156:159], a[60:63], v[184:187]\n\t"
    "v_exp_f32 v175, v175\n\t"
    "v_mul_f32 v46, v46, v245\n\t"
    "v_mul_f32 v47, v47, v245\n\t"
    "v_mul_f32 v48, v48, v245\n\t"
    "v_mul_f32 v49, v49, v245\n\t"
    "v_mfma_f32_16x16x32_bf16 v[68:71], a[160:163], v[212:215], v[68:71]\n\t"
    "buffer_store_dwordx4 v[36:39], v241, s[48:51], s64 offen offset:0 sc0 sc1\n\t"
    "v_cvt_pk_bf16_f32 v205, v174, v175\n\t"
    "v_mul_f32 v50, v50, v245\n\t"
    "v_mul_f32 v51, v51, v245\n\t"
    "v_cvt_pk_bf16_f32 v44, v44, v45\n\t"
    "v_cvt_pk_bf16_f32 v45, v46, v47\n\t"
    "v_mfma_f32_16x16x32_bf16 v[68:71], a[192:195], v[216:219], v[68:71]\n\t"
    "v_exp_f32 v176, v176\n\t"
    "v_cvt_pk_bf16_f32 v46, v48, v49\n\t"
    "v_cvt_pk_bf16_f32 v47, v50, v51\n\t"
    "s_nop 1\n\t"
    "v_mfma_f32_16x16x32_bf16 a[4:7], a[0:3], v[44:47], a[4:7]\n\t"
    "v_mul_f32 v52, v52, v245\n\t"
    "v_mfma_f32_16x16x32_bf16 v[72:75], a[164:167], v[212:215], v[72:75]\n\t"
    "v_exp_f32 v177, v177\n\t"
    "v_mul_f32 v53, v53, v245\n\t"
    "v_mul_f32 v54, v54, v245\n\t"
    "v_mul_f32 v55, v55, v245\n\t"
    "v_mul_f32 v56, v56, v245\n\t"
    "v_mfma_f32_16x16x32_bf16 v[72:75], a[196:199], v[216:219], v[72:75]\n\t"
    "buffer_store_dwordx4 v[44:47], v241, s[48:51], s64 offen offset:64 sc0 sc1\n\t"
    "v_cvt_pk_bf16_f32 v206, v176, v177\n\t"
    "v_mul_f32 v57, v57, v245\n\t"
    "v_mul_f32 v58, v58, v245\n\t"
    "v_mul_f32 v59, v59, v245\n\t"
    "v_cvt_pk_bf16_f32 v52, v52, v53\n\t"
    "v_mfma_f32_16x16x32_bf16 v[76:79], a[168:171], v[212:215], v[76:79]\n\t"
    "v_exp_f32 v178, v178\n\t"
    "v_cvt_pk_bf16_f32 v53, v54, v55\n\t"
    "v_cvt_pk_bf16_f32 v54, v56, v57\n\t"
    "v_cvt_pk_bf16_f32 v55, v58, v59\n\t"
    "s_nop 1\n\t"
    "v_mfma_f32_16x16x32_bf16 a[4:7], a[0:3], v[52:55], a[4:7]\n\t"
    "v_mfma_f32_16x16x32_bf16 v[76:79], a[200:203], v[216:219], v[76:79]\n\t"
    "v_exp_f32 v179, v179\n\t"
    "v_mul_f32 v60, v60, v245\n\t"
    "v_mul_f32 v61, v61, v245\n\t"
    "v_mul_f32 v62, v62, v245\n\t"
    "v_mul_f32 v63, v63, v245\n\t"
    "v_mfma_f32_16x16x32_bf16 v[80:83], a[172:175], v[212:215], v[80:83]\n\t"
    "buffer_store_dwordx4 v[52:55], v241, s[48:51], s64 offen offset:128 sc0 sc1\n\t"
    "v_cvt_pk_bf16_f32 v207, v178, v179\n\t"
    "v_mul_f32 v64, v64, v245\n\t"
    "v_mul_f32 v65, v65, v245\n\t"
    "v_mul_f32 v66, v66, v245\n\t"
    "v_mul_f32 v67, v67, v245\n\t"
    "v_mfma_f32_16x16x32_bf16 v[80:83], a[204:207], v[216:219], v[80:83]\n\t"
    "v_exp_f32 v180, v180\n\t"
    "v_cvt_pk_bf16_f32 v60, v60, v61\n\t"
    "v_cvt_pk_bf16_f32 v61, v62, v63\n\t"
    "v_cvt_pk_bf16_f32 v62, v64, v65\n\t"
    "v_cvt_pk_bf16_f32 v63, v66, v67\n\t"
    "v_mfma_f32_16x16x32_bf16 v[84:87], a[176:179], v[212:215], v[84:87]\n\t"
    "v_exp_f32 v181, v181\n\t"
    "v_mfma_f32_16x16x32_bf16 a[4:7], a[0:3], v[60:63], a[4:7]\n\t"
    "v_mfma_f32_16x16x32_bf16 v[84:87], a[208:211], v[216:219], v[84:87]\n\t"
    "v_cvt_pk_bf16_f32 v208, v180, v181\n\t"
    "v_add_f32 v253, v253, v180\n\t"
    "v_mfma_f32_16x16x32_bf16 v[88:91], a[180:183], v[212:215], v[88:91]\n\t"
    "buffer_store_dwordx4 v[60:63], v241, s[48:51], s64 offen offset:192 sc0 sc1\n\t"
    "v_exp_f32 v182, v182\n\t"
    "v_mfma_f32_16x16x32_bf16 v[88:91], a[212:215], v[216:219], v[88:91]\n\t"
    "v_exp_f32 v183, v183\n\t"
    "v_mfma_f32_16x16x32_bf16 v[92:95], a[184:187], v[212:215], v[92:95]\n\t"
    "v_cvt_pk_bf16_f32 v209, v182, v183\n\t"
    "v_add_f32 v253, v253, v181\n\t"
    "v_mfma_f32_16x16x32_bf16 v[92:95], a[216:219], v[216:219], v[92:95]\n\t"
    "v_exp_f32 v184, v184\n\t"
    "v_mfma_f32_16x16x32_bf16 v[96:99], a[188:191], v[212:215], v[96:99]\n\t"
    "v_exp_f32 v185, v185\n\t"
    "v_mfma_f32_16x16x32_bf16 v[96:99], a[220:223], v[216:219], v[96:99]\n\t"
    "v_cvt_pk_bf16_f32 v210, v184, v185\n\t"
    "v_add_f32 v253, v253, v182\n\t"
    "v_mfma_f32_16x16x32_bf16 a[232:235], a[248:251], v[212:215], a[232:235]\n\t"
    "s_waitcnt vmcnt(8) lgkmcnt(0)\n\t"
    "s_barrier\n\t"
    "v_mfma_f32_16x16x32_bf16 v[188:191], a[96:99], a[64:67], 0\n\t"
    "v_add_f32 v253, v253, v183\n\t"
    "v_mfma_f32_16x16x32_bf16 v[188:191], a[100:103], a[68:71], v[188:191]\n\t"
    "v_exp_f32 v186, v186\n\t"
    "v_mfma_f32_16x16x32_bf16 v[188:191], a[104:107], a[72:75], v[188:191]\n\t"
    "v_mfma_f32_16x16x32_bf16 v[188:191], a[108:111], a[76:79], v[188:191]\n\t"
    "v_exp_f32 v187, v187\n\t"
    "v_mfma_f32_16x16x32_bf16 v[192:195], a[112:115], a[64:67], 0\n\t"
    "v_mfma_f32_16x16x32_bf16 v[192:195], a[116:119], a[68:71], v[192:195]\n\t"
    "v_cvt_pk_bf16_f32 v211, v186, v187\n\t"
    "v_add_f32 v253, v253, v184\n\t"
    "v_mfma_f32_16x16x32_bf16 v[192:195], a[120:123], a[72:75], v[192:195]\n\t"
    "v_mfma_f32_16x16x4_f32 a[232:235], a252, v252, a[232:235]\n\t"
    "v_mfma_f32_16x16x32_bf16 v[192:195], a[124:127], a[76:79], v[192:195]\n\t"
    "v_add_f32 v253, v253, v185\n\t"
    "v_add_f32 v253, v253, v186\n\t"
    "v_mfma_f32_16x16x32_bf16 v[196:199], a[128:131], a[64:67], 0\n\t"
    "v_add_f32 v253, v253, v187\n\t"
    "v_mfma_f32_16x16x32_bf16 v[196:199], a[132:135], a[68:71], v[196:199]\n\t"
    "v_mfma_f32_16x16x32_bf16 v[196:199], a[136:139], a[72:75], v[196:199]\n\t"
    "s_nop 2\n\t"
    "v_accvgpr_read_b32 v242, a232\n\t"
    "v_subrev_u32 v246, 0x2b800000, v242\n\t"
    "v_cmp_lt_u32 vcc, 0x28000000, v246\n\t"
    "s_nop 1\n\t"
    "s_or_b64 s[62:63], s[62:63], vcc\n\t"
    "v_rcp_f32 v243, v242\n\t"
    "s_mov_b32 s64, 69632\n\t"
    "v_mul_f32 v68, v68, v243\n\t"
    "v_mfma_f32_16x16x32_bf16 v[196:199], a[140:143], a[76:79], v[196:199]\n\t"
    "v_exp_f32 v188, v188\n\t"
    "v_mul_f32 v69, v69, v243\n\t"
    "v_mul_f32 v70, v70, v243\n\t"
    "v_mul_f32 v71, v71, v243\n\t"
    "v_mul_f32 v72, v72, v243\n\t"
    "v_mfma_f32_16x16x32_bf16 v[200:203], a[144:147], a[64:67], 0\n\t"
    "v_mul_f32 v73, v73, v243\n\t"
    "v_mul_f32 v74, v74, v243\n\t"
    "v_mul_f32 v75, v75, v243\n\t"
    "v_cvt_pk_bf16_f32 v68, v68, v69\n\t"
    "v_cvt_pk_bf16_f32 v69, v70, v71\n\t"
    "v_cvt_pk_bf16_f32 v70, v72, v73\n\t"
    "v_mfma_f32_16x16x32_bf16 v[200:203], a[148:151], a[68:71], v[200:203]\n\t"
    "v_exp_f32 v189, v189\n\t"
    "v_cvt_pk_bf16_f32 v71, v74, v75\n\t"
    "s_nop 1\n\t"
    "v_mfma_f32_16x16x32_bf16 a[4:7], a[0:3], v[68:71], a[4:7]\n\t"
    "v_mul_f32 v76, v76, v243\n\t"
    "v_mul_f32 v77, v77, v243\n\t"
    "v_mfma_f32_16x16x32_bf16 v[200:203], a[152:155], a[72:75], v[200:203]\n\t"
    "v_mul_f32 v78, v78, v243\n\t"
    "v_mul_f32 v79, v79, v243\n\t"
    "v_mul_f32 v80, v80, v243\n\t"
    "v_mul_f32 v81, v81, v243\n\t"
    "v_mul_f32 v82, v82, v243\n\t"
    "v_mul_f32 v83, v83, v243\n\t"
    "v_mfma_f32_16x16x32_bf16 v[200:203], a[156:159], a[76:79], v[200:203]\n\t"
    "buffer_store_dwordx4 v[68:71], v241, s[48:51], s64 offen offset:0 sc0 sc1\n\t"
    "v_cvt_pk_bf16_f32 v212, v188, v189\n\t"
    "v_add_f32 v254, v254, v188\n\t"
    "v_cvt_pk_bf16_f32 v76, v76, v77\n\t"
    "v_cvt_pk_bf16_f32 v77, v78, v79\n\t"
    "v_cvt_pk_bf16_f32 v78, v80, v81\n\t"
    "v_mfma_f32_16x16x32_bf16 v[100:103], a[160:163], v[204:207], v[100:103]\n\t"
    "v_cvt_pk_bf16_f32 v79, v82, v83\n\t"
    "s_nop 1\n\t"
    "v_mfma_f32_16x16x32_bf16 a[4:7], a[0:3], v[76:79], a[4:7]\n\t"
    "v_mul_f32 v84, v84, v243\n\t"
    "v_mul_f32 v85, v85, v243\n\t"
    "v_mul_f32 v86, v86, v243\n\t"
    "v_mul_f32 v87, v87, v243\n\t"
    "v_mfma_f32_16x16x32_bf16 v[100:103], a[192:195], v[208:211], v[100:103]\n\t"
    "v_exp_f32 v190, v190\n\t"
    "v_mul_f32 v88, v88, v243\n\t"
    "v_mul_f32 v89, v89, v243\n\t"
    "v_mul_f32 v90, v90, v243\n\t"
    "v_mul_f32 v91, v91, v243\n\t"
    "v_mfma_f32_16x16x32_bf16 v[104:107], a[164:167], v[204:207], v[104:107]\n\t"
    "ds_read_b128 v[228:231], v238 offset:2048\n\t"
    "ds_read_b128 v[232:235], v238 offset:3072\n\t"
    "v_cvt_pk_bf16_f32 v84, v84, v85\n\t"
    "v_cvt_pk_bf16_f32 v85, v86, v87\n\t"
    "v_cvt_pk_bf16_f32 v86, v88, v89\n\t"
    "v_cvt_pk_bf16_f32 v87, v90, v91\n\t"
    "v_mfma_f32_16x16x32_bf16 v[104:107], a[196:199], v[208:211], v[104:107]\n\t"
    "buffer_store_dwordx4 v[76:79], v241, s[48:51], s64 offen offset:64 sc0 sc1\n\t"
    "v_exp_f32 v191, v191\n\t"
    "v_mfma_f32_16x16x32_bf16 a[4:7], a[0:3], v[84:87], a[4:7]\n\t"
    "v_mul_f32 v92, v92, v243\n\t"
    "v_mul_f32 v93, v93, v243\n\t"
    "v_mul_f32 v94, v94, v243\n\t"
    "v_mfma_f32_16x16x32_bf16 v[108:111], a[168:171], v[204:207], v[108:111]\n\t"
    "v_cvt_pk_bf16_f32 v213, v190, v191\n\t"
    "v_add_f32 v254, v254, v189\n\t"
    "v_mul_f32 v95, v95, v243\n\t"
    "v_mul_f32 v96, v96, v243\n\t"
    "v_mul_f32 v97, v97, v243\n\t"
    "v_mul_f32 v98, v98, v243\n\t"
    "v_mfma_f32_16x16x32_bf16 v[108:111], a[200:203], v[208:211], v[108:111]\n\t"
    "buffer_store_dwordx4 v[84:87], v241, s[48:51], s64 offen offset:128 sc0 sc1\n\t"
    "v_exp_f32 v192, v192\n\t"
    "v_mul_f32 v99, v99, v243\n\t"
    "v_cvt_pk_bf16_f32 v92, v92, v93\n\t"
    "v_cvt_pk_bf16_f32 v93, v94, v95\n\t"
    "v_cvt_pk_bf16_f32 v94, v96, v97\n\t"
    "v_mfma_f32_16x16x32_bf16 v[112:115], a[172:175], v[204:207], v[112:115]\n\t"
    "v_exp_f32 v193, v193\n\t"
    "v_cvt_pk_bf16_f32 v95, v98, v99\n\t"
    "s_nop 1\n\t"
    "v_mfma_f32_16x16x32_bf16 a[4:7], a[0:3], v[92:95], a[4:7]\n\t"
    "ds_read_b32 v222, v220 offset:0\n\t"
    "ds_read_b32 v223, v220 offset:64\n\t"
    "v_mfma_f32_16x16x32_bf16 v[112:115], a[204:207], v[208:211], v[112:115]\n\t"
    "v_cvt_pk_bf16_f32 v214, v192, v193\n\t"
    "v_add_f32 v254, v254, v190\n\t"
    "ds_read_b32 v224, v220 offset:128\n\t"
    "ds_read_b32 v225, v220 offset:192\n\t"
    "s_waitcnt lgkmcnt(2)\n\t"
    "v_add_f32 v222, v222, v223\n\t"
    "s_waitcnt lgkmcnt(0)\n\t"
    "v_add_f32 v224, v224, v225\n\t"
    "v_mfma_f32_16x16x32_bf16 v[116:119], a[176:179], v[204:207], v[116:119]\n\t"
    "buffer_store_dwordx4 v[92:95], v241, s[48:51], s64 offen offset:192 sc0 sc1\n\t"
    "v_exp_f32 v194, v194\n\t"
    "v_add_f32 v248, v222, v224\n\t"
    "v_subrev_u32 v223, 0x2b800000, v248\n\t"
    "v_cmp_lt_u32 vcc, 0x28000000, v223\n\t"
    "s_nop 1\n\t"
    "s_or_b64 s[62:63], s[62:63], vcc\n\t"
    "v_mfma_f32_16x16x32_bf16 v[116:119], a[208:211], v[208:211], v[116:119]\n\t"
    "v_exp_f32 v195, v195\n\t"
    "v_rcp_f32 v249, v248\n\t"
    "v_mfma_f32_16x16x32_bf16 v[120:123], a[180:183], v[204:207], v[120:123]\n\t"
    "v_cvt_pk_bf16_f32 v215, v194, v195\n\t"
    "v_add_f32 v254, v254, v191\n\t"
    "v_mfma_f32_16x16x32_bf16 v[120:123], a[212:215], v[208:211], v[120:123]\n\t"
    "v_exp_f32 v196, v196\n\t"
    "v_mfma_f32_16x16x32_bf16 v[124:127], a[184:187], v[204:207], v[124:127]\n\t"
    "v_exp_f32 v197, v197\n\t"
    "v_mfma_f32_16x16x32_bf16 v[124:127], a[216:219], v[208:211], v[124:127]\n\t"
    "v_cvt_pk_bf16_f32 v216, v196, v197\n\t"
    "v_add_f32 v254, v254, v192\n\t"
    "v_mfma_f32_16x16x32_bf16 v[128:131], a[188:191], v[204:207], v[128:131]\n\t"
    "v_exp_f32 v198, v198\n\t"
    "v_mfma_f32_16x16x32_bf16 v[128:131], a[220:223], v[208:211], v[128:131]\n\t"
    "v_exp_f32 v199, v199\n\t"
    "v_mfma_f32_16x16x32_bf16 a[236:239], a[248:251], v[204:207], a[236:239]\n\t"
    "v_cvt_pk_bf16_f32 v217, v198, v199\n\t"
    "v_add_f32 v254, v254, v193\n\t"
    "v_exp_f32 v200, v200\n\t"
    "v_exp_f32 v201, v201\n\t"
    "s_nop 0\n\t"
    "v_cvt_pk_bf16_f32 v218, v200, v201\n\t"
    "v_add_f32 v254, v254, v194\n\t"
    "v_exp_f32 v202, v202\n\t"
    "v_exp_f32 v203, v203\n\t"
    "s_nop 0\n\t"
    "v_cvt_pk_bf16_f32 v219, v202, v203\n\t"
    "v_add_f32 v254, v254, v195\n\t"
    "v_mfma_f32_16x16x4_f32 a[236:239], a252, v253, a[236:239]\n\t"
    "v_add_f32 v254, v254, v196\n\t"
    "v_add_f32 v254, v254, v197\n\t"
    "v_add_f32 v254, v254, v198\n\t"
    "v_add_f32 v254, v254, v199\n\t"
    "v_add_f32 v254, v254, v200\n\t"
    "v_add_f32 v254, v254, v201\n\t"
    "v_add_f32 v254, v254, v202\n\t"
    "v_add_f32 v254, v254, v203\n\t"
    "s_nop 1\n\t"
    "v_accvgpr_read_b32 v244, a236\n\t"
    "v_subrev_u32 v246, 0x2b800000, v244\n\t"
    "v_cmp_lt_u32 vcc, 0x28000000, v246\n\t"
    "s_nop 1\n\t"
    "s_or_b64 s[62:63], s[62:63], vcc\n\t"
    "v_rcp_f32 v245, v244\n\t"
    "s_mov_b32 s64, 73728\n\t"
    "v_mul_f32 v100, v100, v245\n\t"
    "v_mul_f32 v101, v101, v245\n\t"
    "v_mul_f32 v102, v102, v245\n\t"
    "v_mul_f32 v103, v103, v245\n\t"
    "v_mul_f32 v104, v104, v245\n\t"
    "v_mul_f32 v105, v105, v245\n\t"
    "v_mul_f32 v106, v106, v245\n\t"
    "v_mul_f32 v107, v107, v245\n\t"
    "v_cvt_pk_bf16_f32 v100, v100, v101\n\t"
    "v_cvt_pk_bf16_f32 v101, v102, v103\n\t"
    "v_cvt_pk_bf16_f32 v102, v104, v105\n\t"
    "v_cvt_pk_bf16_f32 v103, v106, v107\n\t"
    "buffer_store_dwordx4 v[100:103], v241, s[48:51], s64 offen offset:0 sc0 sc1\n\t"
    "s_nop 0\n\t"
    "v_mfma_f32_16x16x32_bf16 a[4:7], a[0:3], v[100:103], a[4:7]\n\t"
    "v_mul_f32 v108, v108, v245\n\t"
    "v_mul_f32 v109, v109, v245\n\t"
    "v_mul_f32 v110, v110, v245\n\t"
    "v_mul_f32 v111, v111, v245\n\t"
    "v_mul_f32 v112, v112, v245\n\t"
    "v_mul_f32 v113, v113, v245\n\t"
    "v_mul_f32 v114, v114, v245\n\t"
    "v_mul_f32 v115, v115, v245\n\t"
    "v_cvt_pk_bf16_f32 v108, v108, v109\n\t"
    "v_cvt_pk_bf16_f32 v109, v110, v111\n\t"
    "v_cvt_pk_bf16_f32 v110, v112, v113\n\t"
    "v_cvt_pk_bf16_f32 v111, v114, v115\n\t"
    "buffer_store_dwordx4 v[108:111], v241, s[48:51], s64 offen offset:64 sc0 sc1\n\t"
    "s_nop 0\n\t"
    "v_mfma_f32_16x16x32_bf16 a[4:7], a[0:3], v[108:111], a[4:7]\n\t"
    "v_mul_f32 v116, v116, v245\n\t"
    "v_mul_f32 v117, v117, v245\n\t"
    "v_mul_f32 v118, v118, v245\n\t"
    "v_mfma_f32_16x16x32_bf16 v[164:167], a[184:187], v[228:231], v[164:167]\n\t"
    "v_mul_f32 v119, v119, v245\n\t"
    "v_mul_f32 v120, v120, v245\n\t"
    "v_mul_f32 v121, v121, v245\n\t"
    "v_mul_f32 v122, v122, v245\n\t"
    "v_mul_f32 v123, v123, v245\n\t"
    "v_cvt_pk_bf16_f32 v116, v116, v117\n\t"
    "v_mfma_f32_16x16x32_bf16 v[164:167], a[216:219], v[232:235], v[164:167]\n\t"
    "v_cvt_pk_bf16_f32 v117, v118, v119\n\t"
    "v_cvt_pk_bf16_f32 v118, v120, v121\n\t"
    "v_cvt_pk_bf16_f32 v119, v122, v123\n\t"
    "s_nop 1\n\t"
    "v_mfma_f32_16x16x32_bf16 a[4:7], a[0:3], v[116:119], a[4:7]\n\t"
    "v_mul_f32 v124, v124, v245\n\t"
    "v_mul_f32 v125, v125, v245\n\t"
    "v_mfma_f32_16x16x32_bf16 v[168:171], a[188:191], v[228:231], v[168:171]\n\t"
    "v_mul_f32 v126, v126, v245\n\t"
    "v_mul_f32 v127, v127, v245\n\t"
    "v_mul_f32 v128, v128, v245\n\t"
    "v_mul_f32 v129, v129, v245\n\t"
    "v_mul_f32 v130, v130, v245\n\t"
    "v_mul_f32 v131, v131, v245\n\t"
    "v_mfma_f32_16x16x32_bf16 v[168:171], a[220:223], v[232:235], v[168:171]\n\t"
    "buffer_store_dwordx4 v[116:119], v241, s[48:51], s64 offen offset:128 sc0 sc1\n\t"
    "v_cvt_pk_bf16_f32 v124, v124, v125\n\t"
    "v_cvt_pk_bf16_f32 v125, v126, v127\n\t"
    "v_cvt_pk_bf16_f32 v126, v128, v129\n\t"
    "v_cvt_pk_bf16_f32 v127, v130, v131\n\t"
    "s_nop 1\n\t"
    "v_mfma_f32_16x16x32_bf16 a[4:7], a[0:3], v[124:127], a[4:7]\n\t"
    "v_mfma_f32_16x16x32_bf16 v[132:135], a[160:163], v[212:215], v[132:135]\n\t"
    "v_mfma_f32_16x16x4_f32 a[240:243], a252, v254, 0\n\t"
    "v_mfma_f32_16x16x32_bf16 v[132:135], a[192:195], v[216:219], v[132:135]\n\t"
    "buffer_store_dwordx4 v[124:127], v241, s[48:51], s64 offen offset:192 sc0 sc1\n\t"
    "v_mfma_f32_16x16x32_bf16 v[136:139], a[164:167], v[212:215], v[136:139]\n\t"
    "v_mfma_f32_16x16x32_bf16 v[136:139], a[196:199], v[216:219], v[136:139]\n\t"
    "v_mfma_f32_16x16x32_bf16 v[140:143], a[168:171], v[212:215], v[140:143]\n\t"
    "v_mfma_f32_16x16x32_bf16 v[140:143], a[200:203], v[216:219], v[140:143]\n\t"
    "v_mfma_f32_16x16x32_bf16 v[144:147], a[172:175], v[212:215], v[144:147]\n\t"
    "v_mfma_f32_16x16x32_bf16 v[144:147], a[204:207], v[216:219], v[144:147]\n\t"
    "v_mfma_f32_16x16x32_bf16 v[148:151], a[176:179], v[212:215], v[148:151]\n\t"
    "s_mov_b32 s61, 81920\n\t"
    "v_mul_f32 v164, v164, v249\n\t"
    "v_mul_f32 v165, v165, v249\n\t"
    "v_mul_f32 v166, v166, v249\n\t"
    "v_mul_f32 v167, v167, v249\n\t"
    "v_mul_f32 v168, v168, v249\n\t"
    "v_mul_f32 v169, v169, v249\n\t"
    "v_mfma_f32_16x16x32_bf16 v[148:151], a[208:211], v[216:219], v[148:151]\n\t"
    "v_mul_f32 v170, v170, v249\n\t"
    "v_mul_f32 v171, v171, v249\n\t"
    "v_cvt_pk_bf16_f32 v164, v164, v165\n\t"
    "v_cvt_pk_bf16_f32 v165, v166, v167\n\t"
    "v_cvt_pk_bf16_f32 v166, v168, v169\n\t"
    "v_cvt_pk_bf16_f32 v167, v170, v171\n\t"
    "v_mfma_f32_16x16x32_bf16 v[152:155], a[180:183], v[212:215], v[152:155]\n\t"
    "s_nop 0\n\t"
    "v_mfma_f32_16x16x32_bf16 a[4:7], a[0:3], v[164:167], a[4:7]\n\t"
    "v_accvgpr_read_b32 v242, a240\n\t"
    "v_subrev_u32 v246, 0x2b800000, v242\n\t"
    "v_cmp_lt_u32 vcc, 0x28000000, v246\n\t"
    "s_nop 1\n\t"
    "s_or_b64 s[62:63], s[62:63], vcc\n\t"
    "v_rcp_f32 v243, v242\n\t"
    "v_mfma_f32_16x16x32_bf16 v[152:155], a[212:215], v[216:219], v[152:155]\n\t"
    "v_mfma_f32_16x16x32_bf16 v[156:159], a[184:187], v[212:215], v[156:159]\n\t"
    "buffer_store_dwordx4 v[164:167], v241, s[48:51], s61 offen offset:192 sc0 sc1\n\t"
    "s_mov_b32 s64, 77824\n\t"
    "v_mfma_f32_16x16x32_bf16 v[156:159], a[216:219], v[216:219], v[156:159]\n\t"
    "v_mfma_f32_16x16x32_bf16 v[160:163], a[188:191], v[212:215], v[160:163]\n\t"
    "v_mfma_f32_16x16x32_bf16 v[160:163], a[220:223], v[216:219], v[160:163]\n\t"
    "v_mul_f32 v132, v132, v243\n\t"
    "v_mul_f32 v133, v133, v243\n\t"
    "v_mul_f32 v134, v134, v243\n\t"
    "v_mul_f32 v135, v135, v243\n\t"
    "v_mul_f32 v136, v136, v243\n\t"
    "v_mul_f32 v137, v137, v243\n\t"
    "v_mul_f32 v138, v138, v243\n\t"
    "v_mul_f32 v139, v139, v243\n\t"
    "v_cvt_pk_bf16_f32 v132, v132, v133\n\t"
    "v_cvt_pk_bf16_f32 v133, v134, v135\n\t"
    "v_cvt_pk_bf16_f32 v134, v136, v137\n\t"
    "v_cvt_pk_bf16_f32 v135, v138, v139\n\t"
    "v_mul_f32 v140, v140, v243\n\t"
    "v_mul_f32 v141, v141, v243\n\t"
    "v_mul_f32 v142, v142, v243\n\t"
    "v_mul_f32 v143, v143, v243\n\t"
    "v_mul_f32 v144, v144, v243\n\t"
    "v_mul_f32 v145, v145, v243\n\t"
    "v_mul_f32 v146, v146, v243\n\t"
    "v_mul_f32 v147, v147, v243\n\t"
    "v_cvt_pk_bf16_f32 v140, v140, v141\n\t"
    "v_cvt_pk_bf16_f32 v141, v142, v143\n\t"
    "v_cvt_pk_bf16_f32 v142, v144, v145\n\t"
    "v_cvt_pk_bf16_f32 v143, v146, v147\n\t"
    "buffer_store_dwordx4 v[132:135], v241, s[48:51], s64 offen offset:0 sc0 sc1\n\t"
    "v_mfma_f32_16x16x32_bf16 a[4:7], a[0:3], v[132:135], a[4:7]\n\t"
    "v_mul_f32 v148, v148, v243\n\t"
    "v_mul_f32 v149, v149, v243\n\t"
    "v_mul_f32 v150, v150, v243\n\t"
    "v_mul_f32 v151, v151, v243\n\t"
    "v_mul_f32 v152, v152, v243\n\t"
    "v_mul_f32 v153, v153, v243\n\t"
    "v_mul_f32 v154, v154, v243\n\t"
    "v_mul_f32 v155, v155, v243\n\t"
    "v_cvt_pk_bf16_f32 v148, v148, v149\n\t"
    "v_cvt_pk_bf16_f32 v149, v150, v151\n\t"
    "v_cvt_pk_bf16_f32 v150, v152, v153\n\t"
    "v_cvt_pk_bf16_f32 v151, v154, v155\n\t"
    "buffer_store_dwordx4 v[140:143], v241, s[48:51], s64 offen offset:64 sc0 sc1\n\t"
    "v_mfma_f32_16x16x32_bf16 a[4:7], a[0:3], v[140:143], a[4:7]\n\t"
    "v_mul_f32 v156, v156, v243\n\t"
    "v_mul_f32 v157, v157, v243\n\t"
    "v_mul_f32 v158, v158, v243\n\t"
    "v_mul_f32 v159, v159, v243\n\t"
    "v_mul_f32 v160, v160, v243\n\t"
    "v_mul_f32 v161, v161, v243\n\t"
    "v_mul_f32 v162, v162, v243\n\t"
    "v_mul_f32 v163, v163, v243\n\t"
    "v_cvt_pk_bf16_f32 v156, v156, v157\n\t"
    "v_cvt_pk_bf16_f32 v157, v158, v159\n\t"
    "v_cvt_pk_bf16_f32 v158, v160, v161\n\t"
    "v_cvt_pk_bf16_f32 v159, v162, v163\n\t"
    "buffer_store_dwordx4 v[148:151], v241, s[48:51], s64 offen offset:128 sc0 sc1\n\t"
    "v_mfma_f32_16x16x32_bf16 a[4:7], a[0:3], v[148:151], a[4:7]\n\t"
    "buffer_store_dwordx4 v[156:159], v241, s[48:51], s64 offen offset:192 sc0 sc1\n\t"
    "v_mfma_f32_16x16x32_bf16 a[4:7], a[0:3], v[156:159], a[4:7]\n\t"
    "s_nop 7\n\t"
    "v_accvgpr_read_b32 v246, a4\n\t"
    "v_cmp_u_f32 vcc, v246, v246\n\t"
    "s_nop 1\n\t"
    "s_or_b64 s[62:63], s[62:63], vcc\n\t"
    "s_cmp_lg_u64 s[62:63], 0\n\t"
    "s_cselect_b32 %0, 1, 0\n\t"
    "s_branch END_%=\n\t"
    "WEND3_%=:\n\t"
    "END_%=:\n\t"
    : "=&s"(redo) : "s"(Q), "s"(K), "s"(V), "s"(O), "s"(bh), "s"(wave), "s"(ldsb), "s"((unsigned)(size_t)K), "s"((unsigned)((size_t)K >> 32)), "s"((unsigned)(size_t)Q), "s"((unsigned)((size_t)Q >> 32)) : "v1", "v2", "v3", "v4", "v5", "v6", "v7", "v8", "v9", "v10", "v11", "v12", "v13", "v14", "v15", "v16", "v17", "v18", "v19", "v20", "v21", "v22", "v23", "v24", "v25", "v26", "v27", "v28", "v29", "v30", "v31", "v32", "v33", "v34", "v35", "v36", "v37", "v38", "v39", "v40", "v41", "v42", "v43", "v44", "v45", "v46", "v47", "v48", "v49", "v50", "v51", "v52", "v53", "v54", "v55", "v56", "v57", "v58", "v59", "v60", "v61", "v62", "v63", "v64", "v65", "v66", "v67", "v68", "v69", "v70", "v71", "v72", "v73", "v74", "v75", "v76", "v77", "v78", "v79", "v80", "v81", "v82", "v83", "v84", "v85", "v86", "v87", "v88", "v89", "v90", "v91", "v92", "v93", "v94", "v95", "v96", "v97", "v98", "v99", "v100", "v101", "v102", "v103", "v104", "v105", "v106", "v107", "v108", "v109", "v110", "v111", "v112", "v113", "v114", "v115", "v116", "v117", "v118", "v119", "v120", "v121", "v122", "v123", "v124", "v125", "v126", "v127", "v128", "v129", "v130", "v131", "v132", "v133", "v134", "v135", "v136", "v137", "v138", "v139", "v140", "v141", "v142", "v143", "v144", "v145", "v146", "v147", "v148", "v149", "v150", "v151", "v152", "v153", "v154", "v155", "v156", "v157", "v158", "v159", "v160", "v161", "v162", "v163", "v164", "v165", "v166", "v167", "v168", "v169", "v170", "v171", "v172", "v173", "v174", "v175", "v176", "v177", "v178", "v179", "v180", "v181", "v182", "v183", "v184", "v185", "v186", "v187", "v188", "v189", "v190", "v191", "v192", "v193", "v194", "v195", "v196", "v197", "v198", "v199", "v200", "v201", "v202", "v203", "v204", "v205", "v206", "v207", "v208", "v209", "v210", "v211", "v212", "v213", "v214", "v215", "v216", "v217", "v218", "v219", "v220", "v221", "v222", "v223", "v224", "v225", "v226", "v227", "v228", "v229", "v230", "v231", "v232", "v233", "v234", "v235", "v236", "v237", "v238", "v239", "v240", "v241", "v242", "v243", "v244", "v245", "v246", "v247", "v248", "v249", "v250", "v251", "v252", "v253", "v254", "v255", "a0", "a1", "a2", "a3", "a4", "a5", "a6", "a7", "a8", "a9", "a10", "a11", "a12", "a13", "a14", "a15", "a16", "a17", "a18", "a19", "a20", "a21", "a22", "a23", "a24", "a25", "a26", "a27", "a28", "a29", "a30", "a31", "a32", "a33", "a34", "a35", "a36", "a37", "a38", "a39", "a40", "a41", "a42", "a43", "a44", "a45", "a46", "a47", "a48", "a49", "a50", "a51", "a52", "a53", "a54", "a55", "a56", "a57", "a58", "a59", "a60", "a61", "a62", "a63", "a64", "a65", "a66", "a67", "a68", "a69", "a70", "a71", "a72", "a73", "a74", "a75", "a76", "a77", "a78", "a79", "a80", "a81", "a82", "a83", "a84", "a85", "a86", "a87", "a88", "a89", "a90", "a91", "a92", "a93", "a94", "a95", "a96", "a97", "a98", "a99", "a100", "a101", "a102", "a103", "a104", "a105", "a106", "a107", "a108", "a109", "a110", "a111", "a112", "a113", "a114", "a115", "a116", "a117", "a118", "a119", "a120", "a121", "a122", "a123", "a124", "a125", "a126", "a127", "a128", "a129", "a130", "a131", "a132", "a133", "a134", "a135", "a136", "a137", "a138", "a139", "a140", "a141", "a142", "a143", "a144", "a145", "a146", "a147", "a148", "a149", "a150", "a151", "a152", "a153", "a154", "a155", "a156", "a157", "a158", "a159", "a160", "a161", "a162", "a163", "a164", "a165", "a166", "a167", "a168", "a169", "a170", "a171", "a172", "a173", "a174", "a175", "a176", "a177", "a178", "a179", "a180", "a181", "a182", "a183", "a184", "a185", "a186", "a187", "a188", "a189", "a190", "a191", "a192", "a193", "a194", "a195", "a196", "a197", "a198", "a199", "a200", "a201", "a202", "a203", "a204", "a205", "a206", "a207", "a208", "a209", "a210", "a211", "a212", "a213", "a214", "a215", "a216", "a217", "a218", "a219", "a220", "a221", "a222", "a223", "a224", "a225", "a226", "a227", "a228", "a229", "a230", "a231", "a232", "a233", "a234", "a235", "a236", "a237", "a238", "a239", "a240", "a241", "a242", "a243", "a244", "a245", "a246", "a247", "a248", "a249", "a250", "a251", "a252", "a253", "a254", "a255", "s34", "s35", "s36", "s37", "s38", "s39", "s40", "s41", "s42", "s43", "s44", "s45", "s46", "s47", "s48", "s49", "s50", "s51", "s52", "s53", "s54", "s55", "s56", "s57", "s58", "s59", "s60", "s61", "s62", "s63", "s64", "s65", "s66", "s67", "s68", "s69", "s70", "s71", "s72", "s73", "s74", "s75", "s76", "s77", "s78", "s79", "s80", "s81", "s82", "s83", "s84", "s85", "s86", "s87", "s88", "s89", "s90", "s91", "s92", "s93", "s94", "s95", "s96", "s97", "s98", "s99", "vcc", "scc", "memory");
  if (redo) {
    asm volatile("s_waitcnt vmcnt(0) lgkmcnt(0)" ::: "memory");
    safe_attn(Q + (size_t)bh * SQ * D, K + (size_t)bh * SK * D, V + (size_t)bh * SK * D, O + (size_t)bh * SQ * D, lds + wave * 8704, wave, threadIdx.x & 63);
  }
}

extern "C" void kernel_launch(void* const* d_in, const int* in_sizes, int n_in, void* d_out, int out_size, void* d_ws, size_t ws_size, hipStream_t stream) {
  attn_fast<<<256, 256, 0, stream>>>((const unsigned short*)d_in[0], (const unsigned short*)d_in[1], (const unsigned short*)d_in[2], (unsigned short*)d_out);
}
```
